```python
import torch

def _softmax(x, axis=-1):
    return torch.softmax(x, dim=axis)
def _gelu(x):
    return torch.nn.functional.gelu(x, approximate="tanh")
def _silu(x):
    return torch.nn.functional.silu(x)
def _sigmoid(x):
    return torch.sigmoid(x)
def _layernorm(x, axis=-1, eps=1e-5):
    m = x.mean(dim=axis, keepdim=True)
    v = x.var(dim=axis, keepdim=True, unbiased=False)
    return (x - m) / torch.sqrt(v + eps)
def _scatter_add(tgt, idx, src, axis=0):
    return tgt.clone().index_add_(axis, idx, src)
def _segment_sum(src, seg, n):
    out = torch.zeros((n,) + src.shape[1:], dtype=src.dtype)
    return out.index_add_(0, seg, src)
def _pad(x, pw):
    flat = tuple(v for pair in reversed(pw) for v in pair)
    return torch.nn.functional.pad(x, flat)

kernel_name = 'interleave_layers__sliding_block_cached__moe_arch_mod__decode'

def setup_inputs():

    _w = (lambda a: (lambda t: t.to(torch.bfloat16) if t.is_floating_point() else t)(torch.as_tensor(a)))
    def _A_setup():
        B, Sq, D = 4, 1, 640
        Hq, Hkv, Sk, Dh = 32, 32, 4096, 192
        assert Hq % Hkv == 0, (Hq, Hkv)
        return {
            "x": _w((lambda *s: torch.randn(*s, dtype=torch.bfloat16) * 0.05)(B, Sq, D)),
            "k_cache": _w((lambda *s: torch.randn(*s, dtype=torch.bfloat16) * 1.6)(B, Hkv, Sk, Dh)),
            "v_cache": _w((lambda *s: torch.randn(*s, dtype=torch.bfloat16) * 1.6)(B, Hkv, Sk, Dh)),
            "wq": _w((lambda *s: torch.randn(*s, dtype=torch.bfloat16) * 1.6)(D, Hq * Dh) * (D ** -0.5)),
            "wo": _w((lambda *s: torch.randn(*s, dtype=torch.bfloat16) * 1.6)(Hq * Dh, D) * ((Hq * Dh) ** -0.5)),
        }
    _A_in = _A_setup()

    def _B_setup():
        B, S, D, F, C = 4, 1, 640, 1536, 1
        return {
            "x": _w((lambda *s: torch.randn(*s, dtype=torch.bfloat16) * 0.05)(B, S, D)),
            "w_r": _w((lambda *s: torch.randn(*s, dtype=torch.bfloat16) * 0.05)(D)),
            "w_gate": _w((lambda *s: torch.randn(*s, dtype=torch.bfloat16) * 0.05)(D, F)),
            "w_up": _w((lambda *s: torch.randn(*s, dtype=torch.bfloat16) * 0.05)(D, F)),
            "w_down": _w((lambda *s: torch.randn(*s, dtype=torch.bfloat16) * 0.05)(F, D)),
        }
    _B_in = _B_setup()

    return {
        "A_x": _A_in["x"],
        "A_k_cache": _A_in["k_cache"],
        "A_v_cache": _A_in["v_cache"],
        "A_wq": _A_in["wq"],
        "A_wo": _A_in["wo"],
        "B_x": _B_in["x"],
        "B_w_r": _B_in["w_r"],
        "B_w_gate": _B_in["w_gate"],
        "B_w_up": _B_in["w_up"],
        "B_w_down": _B_in["w_down"],
    }

def reference(A_x, A_k_cache, A_v_cache, A_wq, A_wo, B_x, B_w_r, B_w_gate, B_w_up, B_w_down):
    def _A_ref(x, k_cache, v_cache, wq, wo):
        B, Sq, D = x.shape
        Hq, Dh = 32, 192
        Hkv, Sk = k_cache.shape[1], k_cache.shape[2]
        W = min(4096, max(256, Sk // 4))
        rep = Hq // Hkv
        q = (lambda x, *d: x.permute(*d))((x @ wq).reshape(B, Sq, Hq, Dh), 0, 2, 1, 3)
        k = (lambda x, n, axis: torch.repeat_interleave(x, n, dim=axis))(k_cache[:, :, Sk - W:, :], rep, axis=1)
        v = (lambda x, n, axis: torch.repeat_interleave(x, n, dim=axis))(v_cache[:, :, Sk - W:, :], rep, axis=1)
        scores = torch.einsum("bhqd,bhkd->bhqk", q, k) * (Dh ** -0.5)
        p = _softmax(scores, axis=-1)
        o = torch.einsum("bhqk,bhkd->bhqd", p, v)
        return (lambda x, *d: x.permute(*d))(o, 0, 2, 1, 3).reshape(B, Sq, Hq * Dh) @ wo

    def _B_ref(x, w_r, w_gate, w_up, w_down):
        B, S, D = x.shape
        C = 1
        r = x @ w_r
        idx = torch.argsort(-r, axis=-1)[:, :C]
        idx_e = (lambda x, n, axis: torch.repeat_interleave(x, n, dim=axis))(idx.reshape(B, C, 1), D, axis=2)
        x_sel = (lambda x, i, axis: torch.take_along_dim(x, i, dim=axis))(x, idx_e, axis=1)
        g = _silu(x_sel @ w_gate)
        u = x_sel @ w_up
        y = (g * u) @ w_down
        r_sel = _sigmoid((lambda x, i, axis: torch.take_along_dim(x, i, dim=axis))(r, idx, axis=1))
        y = y * r_sel.reshape(B, C, 1)
        b_off = (torch.arange(B) * S).reshape(B, 1)
        flat_idx = (idx + b_off).reshape(B * C)
        zero = (x * 0.0).reshape(B * S, D)
        upd = _scatter_add(zero, flat_idx, y.reshape(B * C, D), axis=0)
        return x + upd.reshape(B, S, D)

    x = A_x
    x = x + _A_ref(_layernorm(x, axis=-1), A_k_cache, A_v_cache, A_wq, A_wo)
    x = x + _B_ref(_layernorm(x, axis=-1), B_w_r, B_w_gate, B_w_up, B_w_down)
    x = x + _A_ref(_layernorm(x, axis=-1), A_k_cache, A_v_cache, A_wq, A_wo)
    x = x + _B_ref(_layernorm(x, axis=-1), B_w_r, B_w_gate, B_w_up, B_w_down)
    return x
```

```cpp
#define SCHED {136,244,312,56,222,298,346,442,10,11,490,7,0,262,14,350,8,1,1,0,84,58,567,60,6,21,0,0,70,40,16,40,2,12,12,256,455,30,0,0,120,42,44,46}
#define WOMODE 0
#define KAL 4096
#define P_LN 7
#define P_QP 9
#define F7 4
#define DHI 4
#define WQP 4
#define EPF 3
#define LDR 38
#define CA 8
#define A7 13
#define Q7 22
#define L7 25
#define QG 35
#define QW 6
#define ECX "sc1"
#define NN7 18
#define WK 26
#define DNW 21
#define PADN 133
#include <hip/hip_runtime.h>
#include <hip/hip_bf16.h>
#include <stdint.h>

typedef unsigned short u16;
typedef unsigned u32x4 __attribute__((ext_vector_type(4)));
typedef unsigned u32x2 __attribute__((ext_vector_type(2)));
typedef unsigned u32x8 __attribute__((ext_vector_type(8)));
typedef float f32x4 __attribute__((ext_vector_type(4)));
typedef short s16x4 __attribute__((ext_vector_type(4)));
typedef __attribute__((ext_vector_type(4))) __bf16 bf16x4;
typedef __attribute__((ext_vector_type(8))) __bf16 bf16x8;
#define LAS __attribute__((address_space(3)))

constexpr int DM = 640, HQ = 32, SK = 4096, DH = 192, WIN = 1024;
constexpr int QD = HQ * DH;
constexpr float SCALE = 0.07216878364870322f;
constexpr float L2E = 1.44269504088896341f;
constexpr unsigned kSpinCap = 1u << 16;
constexpr int NMLP = 96;
#define TDEPTH 4

#define POL 0
#define WPOL " nt"
enum { S_XA0, S_H50, S_XB, S_H1, S_H2, S_H3, S_XA1, S_H51, S_GAP, S_PA, S_PD, S_WT, S_WD, S_CL, S_H4, S_GS, S_TS, S_TW, S_TR, S_C5, S_H30, S_H20, S_PM, S_EX, S_N0, S_N1, S_NT, S_ND, S_NR, S_PT, S_FL, S_BT, S_G1, S_G2, S_G3, S_X3, S_X4, S_X5, S_X6, S_X7, S_Y0, S_Y1, S_Y2, S_Y3, S_N };
#define S_LT S_GS
#define S_EXB S_NR
#define S_PO S_BT
#define S_MO S_PT
#define S_P7 S_X4
#define S_O7 S_X5

constexpr unsigned LB = 2097152, GP = 16384;
constexpr unsigned WS_XA = 80 * GP;
constexpr unsigned WS_H2 = 84 * GP;
constexpr unsigned WS_XB = 92 * GP;
constexpr unsigned WS_TS = 96 * GP;
constexpr unsigned WS_H1B = LB + 92 * GP;
constexpr unsigned WS_PR = 124 * GP;
constexpr unsigned WS_EP = 2 * LB;
constexpr unsigned WS_AR = WS_EP + 1024;
constexpr unsigned WS_GO = WS_EP + 4096;
constexpr unsigned WS_H1A = WS_EP + 8192;
constexpr unsigned WS_SINK = WS_H1A + 32 * 5 * 3840;
__device__ __forceinline__ unsigned a_rep(int r) { return (unsigned)((r & 3) * GP + (r >> 2) * 8192); }
__device__ __forceinline__ unsigned a_own(int o) { return (unsigned)(o * GP - (o >= 80 ? 80 * GP - 8192 : 0)); }
__device__ __forceinline__ unsigned a_h2(int h) { return WS_H2 + (unsigned)(((h & 15) >> 1) * GP + (h >> 4) * 8192 + (h & 1) * 4096); }
__device__ __forceinline__ unsigned a_hf(int h) { return WS_XA + a_rep(h >> 2) + (unsigned)((h & 2) * 6 * GP + 6144 + (h & 1) * 1024); }
__device__ __forceinline__ unsigned a_ts(int r) { return WS_TS + (unsigned)(((r & 3) >> 1) * GP + (r >> 2) * 8192 + (r & 1) * 4096); }
__device__ __forceinline__ unsigned a_h1(int h, int row) { return WS_H1B + (unsigned)(((h & 15) * 2 + (row >> 1)) * GP + (h >> 4) * 8192 + (row & 1) * 3840); }
#define CX(layer) ((pol << (12 - (layer))) & 8192u)
constexpr int LEADER = LDR;
#define RST 128
constexpr unsigned WS_DBG = 6680576;
static_assert(WS_SINK + 256 * 128 <= WS_DBG, "ws map");

constexpr int L_WQ = 0;
constexpr int L_WO = 49152;
constexpr int L_GU = 0;
constexpr int L_WD = 40960;
constexpr int OPS = 3200;
constexpr int L_OP = 98304;
constexpr int L_P = L_OP + 8 * OPS;
constexpr int L_Q = L_P + 71 * 192;
constexpr int L_CS = L_Q + 512;
constexpr int L_ML = L_CS + 1536;
constexpr int L_XR = L_ML + 64;
constexpr int L_CNT = L_XR + 256;
constexpr int L_FW = L_CNT + 64;
constexpr int L_PF = L_FW + 64;
constexpr int L_POL = L_PF + 256;
constexpr int L_WR = L_POL + 32;
constexpr int L_DBG = L_WR + 1536;
constexpr int L_END = L_DBG + 4096;
static_assert(L_CNT + 32 - L_ML == 352, "stats offset");
static_assert(L_END <= 163840 && L_END > 81920 && L_Q % 16 == 0, "LDS");

typedef __attribute__((ext_vector_type(2))) __bf16 bf16x2;
typedef float f32x2 __attribute__((ext_vector_type(2)));
__device__ __forceinline__ unsigned pk2(float a, float b) { return __builtin_bit_cast(unsigned, __builtin_convertvector((f32x2){a, b}, bf16x2)); }
__device__ __forceinline__ float lo16(unsigned w) { return __uint_as_float(w << 16); }
__device__ __forceinline__ float hi16(unsigned w) { return __uint_as_float(w & 0xffff0000u); }
__device__ __forceinline__ float bfr(float f) { return lo16(pk2(f, f)); }
template <int CTRL> __device__ __forceinline__ float dpp(float x) {
    return __builtin_bit_cast(float, __builtin_amdgcn_mov_dpp(__builtin_bit_cast(int, x), CTRL, 0xf, 0xf, true));
}
template <int CTRL> __device__ __forceinline__ unsigned dppu(unsigned x) { return (unsigned)__builtin_amdgcn_mov_dpp((int)x, CTRL, 0xf, 0xf, true); }
__device__ __forceinline__ float row16_sum(float v) {
    v += dpp<0xB1>(v); v += dpp<0x4E>(v); v += dpp<0x141>(v); v += dpp<0x140>(v); return v;
}
__device__ __forceinline__ float sw16_add(float a, float b) { auto s = __builtin_amdgcn_permlane16_swap(__float_as_uint(a), __float_as_uint(b), false, false); return __uint_as_float(s[0]) + __uint_as_float(s[1]); }
__device__ __forceinline__ float sw32_add(float a, float b) { auto s = __builtin_amdgcn_permlane32_swap(__float_as_uint(a), __float_as_uint(b), false, false); return __uint_as_float(s[0]) + __uint_as_float(s[1]); }
__device__ __forceinline__ float wave_sum(float v) { v = row16_sum(v); v = sw16_add(v, v); return sw32_add(v, v); }
__device__ __forceinline__ int imax(int a, int b) { return a > b ? a : b; }
__device__ __forceinline__ unsigned umax(unsigned a, unsigned b) { return a > b ? a : b; }
__device__ __forceinline__ int wave_imax(int v) {
    v = imax(v, __builtin_amdgcn_mov_dpp(v, 0xB1, 0xf, 0xf, true)); v = imax(v, __builtin_amdgcn_mov_dpp(v, 0x4E, 0xf, 0xf, true));
    v = imax(v, __builtin_amdgcn_mov_dpp(v, 0x141, 0xf, 0xf, true)); v = imax(v, __builtin_amdgcn_mov_dpp(v, 0x140, 0xf, 0xf, true));
    return imax(imax(__builtin_amdgcn_readlane(v, 0), __builtin_amdgcn_readlane(v, 16)), imax(__builtin_amdgcn_readlane(v, 32), __builtin_amdgcn_readlane(v, 48)));
}
__device__ __forceinline__ int fkey(int bits) { return bits ^ ((bits >> 31) & 0x7fffffff); }

#define BAR() asm volatile("s_waitcnt lgkmcnt(0)\n\ts_barrier" ::: "memory")
#define VMW(n) asm volatile("s_waitcnt vmcnt(%0)" :: "i"(n) : "memory")

__device__ __forceinline__ u32x4 ld16_sc1(const void* sbase, unsigned voff) {
    u32x4 r; asm volatile("global_load_dwordx4 %0, %1, %2 sc1" : "=v"(r) : "v"(voff), "s"(sbase) : "memory"); return r;
}
template <int IMM> __device__ __forceinline__ u32x4 ld16_sc1o(const void* sbase, unsigned voff) {
    u32x4 r; asm volatile("global_load_dwordx4 %0, %1, %2 offset:%3 sc1" : "=v"(r) : "v"(voff), "s"(sbase), "i"(IMM) : "memory"); return r;
}
template <int IMM> __device__ __forceinline__ u32x2 ld8_sc1o(const void* sbase, unsigned voff) {
    u32x2 r; asm volatile("global_load_dwordx2 %0, %1, %2 offset:%3 sc1" : "=v"(r) : "v"(voff), "s"(sbase), "i"(IMM) : "memory"); return r;
}
__device__ __forceinline__ unsigned ld4_sc1(const void* sbase, unsigned voff) {
    unsigned r; asm volatile("global_load_dword %0, %1, %2 sc1" : "=v"(r) : "v"(voff), "s"(sbase) : "memory"); return r;
}
__device__ __forceinline__ void st16_sc1(void* sbase, unsigned voff, u32x4 v) {
    asm volatile("global_store_dwordx4 %0, %1, %2 sc1\n\ts_nop 1" :: "v"(voff), "v"(v), "s"(sbase) : "memory");
}
#define XSX "sc1 nt"
#define XLX "sc1 nt"
#define LCX "sc1"
__device__ __forceinline__ void st16_e(void* sbase, unsigned voff, u32x4 v) {
    asm volatile("global_store_dwordx4 %0, %1, %2 " ECX "\n\ts_nop 1" :: "v"(voff), "v"(v), "s"(sbase) : "memory");
}
__device__ __forceinline__ void st16_l(void* sbase, unsigned voff, u32x4 v) {
    asm volatile("global_store_dwordx4 %0, %1, %2 " LCX "\n\ts_nop 1" :: "v"(voff), "v"(v), "s"(sbase) : "memory");
}
__device__ __forceinline__ void st16_x(void* sbase, unsigned voff, u32x4 v) {
    asm volatile("global_store_dwordx4 %0, %1, %2 " XSX "\n\ts_nop 1" :: "v"(voff), "v"(v), "s"(sbase) : "memory");
}
template <int IMM> __device__ __forceinline__ u32x4 ld16_xo(const void* sbase, unsigned voff) {
    u32x4 r; asm volatile("global_load_dwordx4 %0, %1, %2 offset:%3 " XLX : "=v"(r) : "v"(voff), "s"(sbase), "i"(IMM) : "memory"); return r;
}
__device__ __forceinline__ void st8_sc1(void* sbase, unsigned voff, u32x2 v) {
    asm volatile("global_store_dwordx2 %0, %1, %2 sc1" :: "v"(voff), "v"(v), "s"(sbase) : "memory");
}
__device__ __forceinline__ void st4_sc1(void* sbase, unsigned voff, unsigned v) {
    asm volatile("global_store_dword %0, %1, %2 sc1" :: "v"(voff), "v"(v), "s"(sbase) : "memory");
}
__device__ __forceinline__ void st8(void* sbase, unsigned voff, u32x2 v) {
    asm volatile("global_store_dwordx2 %0, %1, %2" :: "v"(voff), "v"(v), "s"(sbase) : "memory");
}
template <int IMM> __device__ __forceinline__ u32x4 ld16_nt(const void* sbase, unsigned voff) {
    return __builtin_nontemporal_load(reinterpret_cast<const u32x4*>(reinterpret_cast<const unsigned char*>(sbase) + voff + IMM));
}
__device__ __forceinline__ u32x4 ld16_v(const void* sbase, unsigned voff) {
    return *reinterpret_cast<const u32x4*>(reinterpret_cast<const unsigned char*>(sbase) + voff);
}
__device__ __forceinline__ void dma16(const void* sbase, unsigned voff, unsigned lds_dst) {
    unsigned keep;
    asm volatile("s_mov_b32 %0, m0\n\ts_mov_b32 m0, %3\n\ts_nop 0\n\tglobal_load_lds_dwordx4 %1, %2" WPOL "\n\ts_mov_b32 m0, %0"
                 : "=&s"(keep) : "v"(voff), "s"(sbase), "s"(lds_dst) : "memory");
}
__device__ __forceinline__ void dma16p(const void* sbase, unsigned voff, unsigned lds_dst) {
    unsigned keep;
    asm volatile("s_mov_b32 %0, m0\n\ts_mov_b32 m0, %3\n\ts_nop 0\n\tglobal_load_lds_dwordx4 %1, %2\n\ts_mov_b32 m0, %0"
                 : "=&s"(keep) : "v"(voff), "s"(sbase), "s"(lds_dst) : "memory");
}
__device__ __forceinline__ void dma4(const void* sbase, unsigned voff, unsigned lds_dst) {
    unsigned keep;
    asm volatile("s_mov_b32 %0, m0\n\ts_mov_b32 m0, %3\n\ts_nop 0\n\tglobal_load_lds_dword %1, %2\n\ts_mov_b32 m0, %0"
                 : "=&s"(keep) : "v"(voff), "s"(sbase), "s"(lds_dst) : "memory");
}
__device__ __forceinline__ void dma4n(const void* sbase, unsigned voff, unsigned lds_dst) {
    unsigned keep;
    asm volatile("s_mov_b32 %0, m0\n\ts_mov_b32 m0, %3\n\ts_nop 0\n\tglobal_load_lds_dword %1, %2 nt\n\ts_mov_b32 m0, %0"
                 : "=&s"(keep) : "v"(voff), "s"(sbase), "s"(lds_dst) : "memory");
}
#define KEEPY " sc1"
__device__ __forceinline__ void pull(const void* sbase, unsigned voff, unsigned lds_dst) {
    unsigned keep;
    asm volatile("s_mov_b32 %0, m0\n\ts_mov_b32 m0, %3\n\ts_nop 0\n\tglobal_load_lds_dword %1, %2 nt\n\tglobal_load_lds_dword %1, %2" KEEPY "\n\ts_mov_b32 m0, %0"
                 : "=&s"(keep) : "v"(voff), "s"(sbase), "s"(lds_dst) : "memory");
}
__device__ __forceinline__ unsigned poll4(void* sbase, unsigned voff) {
    unsigned r;
    asm volatile("global_load_dword %0, %1, %2 sc1\n\ts_waitcnt vmcnt(0)" : "=v"(r) : "v"(voff), "s"(sbase) : "memory");
    return r;
}
__device__ __forceinline__ unsigned enc(unsigned w) { return w < 0xfffffffeu ? w : 0xfffffffeu; }
__device__ __forceinline__ u32x4 enc4(f32x4 c) { return (u32x4){enc(__float_as_uint(c[0])), enc(__float_as_uint(c[1])), enc(__float_as_uint(c[2])), enc(__float_as_uint(c[3]))}; }
__device__ __forceinline__ unsigned mx4(u32x4 a) { return umax(umax(umax(a[0], a[1]), a[2]), a[3]); }
__device__ __forceinline__ unsigned mx4(u32x4 a, unsigned m) { return umax(umax(umax(umax(m, a[0]), a[1]), a[2]), a[3]); }
__device__ __forceinline__ bool allok(unsigned m) { return __builtin_amdgcn_ballot_w64(m == 0xffffffffu) == 0ull; }
__device__ __forceinline__ u32x4 tag4(u32x4 a, unsigned t8) { return (a & 0xffffff00u) | t8; }
__device__ __forceinline__ unsigned or4(u32x4 a) { return a[0] | a[1] | a[2] | a[3]; }
__device__ __forceinline__ bool tagok(unsigned m) { return __builtin_amdgcn_ballot_w64((m & 0xffu) != 0u) == 0ull; }
__device__ __forceinline__ f32x4 asf(u32x4 a) { return __builtin_bit_cast(f32x4, a); }

__device__ __forceinline__ bf16x4 tr64(unsigned lds_addr) { return __builtin_amdgcn_ds_read_tr16_b64_v4bf16((LAS bf16x4*)(size_t)lds_addr); }
__device__ __forceinline__ bf16x8 cat8(bf16x4 a, bf16x4 b) { return __builtin_shufflevector(a, b, 0, 1, 2, 3, 4, 5, 6, 7); }
__device__ __forceinline__ f32x4 mfma32(bf16x8 a, bf16x8 b, f32x4 c) { return __builtin_amdgcn_mfma_f32_16x16x32_bf16(a, b, c, 0, 0, 0); }
__device__ __forceinline__ f32x4 mfma32u(u32x4 a, u32x4 b, f32x4 c) { return mfma32(__builtin_bit_cast(bf16x8, a), __builtin_bit_cast(bf16x8, b), c); }
__device__ __forceinline__ f32x4 mfma32w(bf16x8 a, u32x4 b, f32x4 c) { return mfma32(a, __builtin_bit_cast(bf16x8, b), c); }
__device__ __forceinline__ f32x4 mfma16(bf16x4 a, u32x2 b, f32x4 c) {
    return __builtin_amdgcn_mfma_f32_16x16x16bf16_1k(__builtin_bit_cast(s16x4, a), __builtin_bit_cast(s16x4, b), c, 0, 0, 0);
}
__device__ __forceinline__ unsigned now32() { return (unsigned)__builtin_amdgcn_s_memrealtime(); }
#define WSL 1
__device__ __forceinline__ void wait_until(unsigned t) {
    asm volatile("s_branch 2f\n1:\n\ts_sleep 2\n2:\n\ts_memrealtime vcc\n\ts_waitcnt lgkmcnt(0)\n\ts_sub_i32 vcc_lo, vcc_lo, %0\n\ts_cmp_lt_i32 vcc_lo, -14\n\ts_cbranch_scc1 1b\n\ts_cmp_lt_i32 vcc_lo, 0\n\ts_cbranch_scc0 4f\n"
                 "3:\n\ts_memrealtime vcc\n\ts_waitcnt lgkmcnt(0)\n\ts_sub_i32 vcc_lo, vcc_lo, %0\n\ts_cmp_lt_i32 vcc_lo, 0\n\ts_cbranch_scc1 3b\n4:" :: "s"(t) : "memory", "scc", "vcc");
}
__device__ __forceinline__ unsigned lds_ld(unsigned addr) { unsigned t; asm volatile("ds_read_b32 %0, %1\n\ts_waitcnt lgkmcnt(0)\n\ts_nop 0" : "=v"(t) : "v"(addr) : "memory"); return __builtin_amdgcn_readfirstlane(t); }
__device__ __forceinline__ void lds_wait_ge(unsigned addr, unsigned v) {
#pragma unroll 1
    for (unsigned spins = 0; spins < (kSpinCap << 6); ++spins) { if (lds_ld(addr) >= v) break; __builtin_amdgcn_s_sleep(1); }
}

#define STAMP(i) do {} while (0)
#define STAMPV(i, t) do {} while (0)
#define RETRY(i, n) do {} while (0)
#define RETRY_ASM ""
#define RETRY_ASM3 ""
#define RETRY_ASM2 ""

#define CY(v) do {} while (0)
#define CYW(slot, a, b) do {} while (0)
#define CYS(slot, a, b) do {} while (0)
__device__ __forceinline__ int swz(int r) { return r ^ (((r >> 3) & 1) << 2); }
__device__ __forceinline__ f32x2 unpk(unsigned w) { return (f32x2){lo16(w), hi16(w)}; }
__device__ __forceinline__ unsigned pk2v(f32x2 v) { return __builtin_bit_cast(unsigned, __builtin_convertvector(v, bf16x2)); }
__device__ __forceinline__ float frcp(float x) { return __builtin_amdgcn_rcpf(x); }
__device__ __forceinline__ float fexp(float x) { return __builtin_amdgcn_exp2f(x * L2E); }

__device__ __forceinline__ void ln_consts(float mean, float var, float& mb, float& rden) {
    const unsigned mv = pk2(mean, __builtin_amdgcn_fmed3f(var, 0.f, 3.0e38f));
    mb = lo16(mv);
    rden = frcp(bfr(__builtin_amdgcn_sqrtf(bfr(hi16(mv) + 1e-5f))));
}
__device__ __forceinline__ void row_stats(u32x4 a, u32x4 c, int lane, float& mb, float& rden) {
    const float c0 = __uint_as_float((unsigned)__builtin_amdgcn_readfirstlane((int)a[0]) << 16);
    const f32x2 c2 = {c0, c0};
    f32x2 s2 = {0.f, 0.f}, q2 = s2, t2 = s2, u2 = s2;
#pragma unroll
    for (int i = 0; i < 4; ++i) { const f32x2 d = unpk(a[i]) - c2, e = unpk(c[i]) - c2; s2 += d; q2 += d * d; t2 += e; u2 += e * e; }
    float s = s2[0] + s2[1], q = q2[0] + q2[1];
    if (lane < 16) { s += t2[0] + t2[1]; q += u2[0] + u2[1]; }
    s = wave_sum(s); q = wave_sum(q);
    const float ms = s * (1.f / DM);
    ln_consts(c0 + ms, (q - s * ms) * (1.f / DM), mb, rden);
}
__device__ __forceinline__ float red3(f32x4 v) { float z = sw16_add(sw32_add(v[0], v[2]), sw32_add(v[1], v[3])); return z + dpp<0x128>(z); }
__device__ __forceinline__ float red4(f32x4 v) { float z = red3(v); return z + dpp<0x124>(z); }
__device__ __forceinline__ float sum_x4(float v) { v += dpp<0x124>(v); v += dpp<0x128>(v); v = sw16_add(v, v); return sw32_add(v, v); }
__device__ __forceinline__ float dot2(unsigned a, unsigned b, float c) { return __builtin_amdgcn_fdot2_f32_bf16(__builtin_bit_cast(bf16x2, a), __builtin_bit_cast(bf16x2, b), c, false); }
__device__ __forceinline__ int kpos(int r) { return (r & 7) | (((r >> 5) & 3) << 3) | (((r >> 3) & 3) << 5); }
__device__ __forceinline__ int gpos(int r) { const int qt = r / 160, i = r - 160 * qt; return 160 * qt + (i < 128 ? kpos(i) : i); }
__device__ __forceinline__ float vmax3(float a, float b, float c) { float r; asm("v_max3_f32 %0, %1, %2, %3" : "=v"(r) : "v"(a), "v"(b), "v"(c)); return r; }
template <unsigned LO, unsigned HI> __device__ __forceinline__ void st16_m(void* sbase, unsigned voff, u32x4 v) {
    asm volatile("s_mov_b32 exec_lo, %3\n\ts_mov_b32 exec_hi, %4\n\tglobal_store_dwordx4 %0, %1, %2 sc1\n\ts_mov_b64 exec, -1\n\ts_nop 0" :: "v"(voff), "v"(v), "s"(sbase), "i"(LO), "i"(HI) : "memory");
}
template <unsigned LO, unsigned HI> __device__ __forceinline__ void st4_m(void* sbase, unsigned voff, unsigned v) {
    asm volatile("s_mov_b32 exec_lo, %3\n\ts_mov_b32 exec_hi, %4\n\tglobal_store_dword %0, %1, %2 sc1\n\ts_mov_b64 exec, -1" :: "v"(voff), "v"(v), "s"(sbase), "i"(LO), "i"(HI) : "memory");
}

#define GATHER(sid, LOADS, OK) do { LOADS; if (__builtin_expect(!(OK), 0)) { for (unsigned spins_ = 1; spins_ < kSpinCap; ++spins_) { RETRY(sid, spins_); __builtin_amdgcn_s_sleep(1); LOADS; if (OK) break; } } } while (0)
#define SCH(i) (sched_c[i])
#define PRIO_MODE 0
#define TAILN 8
#define ABL 0
#define DIRECT 0
#define GMODE 0
#define GP_ 0
#define GID blockIdx.x
#define H1ROW 1
#define P_BA 3
#define WO_AT 7

#define ASM_QK \
    "ds_read_b128 %4, %10 offset:0\n\t" \
    "ds_read_b128 %5, %10 offset:64\n\t" \
    "ds_read_b128 %6, %10 offset:128\n\t" \
    "ds_read_b128 %7, %10 offset:192\n\t" \
    "ds_read_b128 %8, %10 offset:256\n\t" \
    "ds_read_b128 %9, %10 offset:320\n\t" \
    "s_waitcnt lgkmcnt(5)\n\t" \
    "v_mfma_f32_16x16x32_bf16 %0, %4, v[64:67], 0\n\t" \
    "v_mfma_f32_16x16x32_bf16 %1, %4, v[88:91], 0\n\t" \
    "v_mfma_f32_16x16x32_bf16 %2, %4, v[112:115], 0\n\t" \
    "v_mfma_f32_16x16x32_bf16 %3, %4, v[136:139], 0\n\t" \
    "s_waitcnt lgkmcnt(4)\n\t" \
    "v_mfma_f32_16x16x32_bf16 %0, %5, v[76:79], %0\n\t" \
    "v_mfma_f32_16x16x32_bf16 %1, %5, v[100:103], %1\n\t" \
    "v_mfma_f32_16x16x32_bf16 %2, %5, v[124:127], %2\n\t" \
    "v_mfma_f32_16x16x32_bf16 %3, %5, v[148:151], %3\n\t" \
    "s_waitcnt lgkmcnt(3)\n\t" \
    "v_mfma_f32_16x16x32_bf16 %0, %6, v[68:71], %0\n\t" \
    "v_mfma_f32_16x16x32_bf16 %1, %6, v[92:95], %1\n\t" \
    "v_mfma_f32_16x16x32_bf16 %2, %6, v[116:119], %2\n\t" \
    "v_mfma_f32_16x16x32_bf16 %3, %6, v[140:143], %3\n\t" \
    "s_waitcnt lgkmcnt(2)\n\t" \
    "v_mfma_f32_16x16x32_bf16 %0, %7, v[80:83], %0\n\t" \
    "v_mfma_f32_16x16x32_bf16 %1, %7, v[104:107], %1\n\t" \
    "v_mfma_f32_16x16x32_bf16 %2, %7, v[128:131], %2\n\t" \
    "v_mfma_f32_16x16x32_bf16 %3, %7, v[152:155], %3\n\t" \
    "s_waitcnt lgkmcnt(1)\n\t" \
    "v_mfma_f32_16x16x32_bf16 %0, %8, v[72:75], %0\n\t" \
    "v_mfma_f32_16x16x32_bf16 %1, %8, v[96:99], %1\n\t" \
    "v_mfma_f32_16x16x32_bf16 %2, %8, v[120:123], %2\n\t" \
    "v_mfma_f32_16x16x32_bf16 %3, %8, v[144:147], %3\n\t" \
    "s_waitcnt lgkmcnt(0)\n\t" \
    "v_mfma_f32_16x16x32_bf16 %0, %9, v[84:87], %0\n\t" \
    "v_mfma_f32_16x16x32_bf16 %1, %9, v[108:111], %1\n\t" \
    "v_mfma_f32_16x16x32_bf16 %2, %9, v[132:135], %2\n\t" \
    "v_mfma_f32_16x16x32_bf16 %3, %9, v[156:159], %3\n\t" \
    "s_nop 7\n\t"
#define ASM_PV3 \
    "s_waitcnt lgkmcnt(8)\n\t" \
    "v_mfma_f32_16x16x32_bf16 %0, v[160:163], %3, 0\n\t" \
    "v_mfma_f32_16x16x32_bf16 %1, v[192:195], %3, 0\n\t" \
    "v_mfma_f32_16x16x32_bf16 %2, v[224:227], %3, 0\n\t" \
    "s_waitcnt lgkmcnt(7)\n\t" \
    "v_mfma_f32_16x16x32_bf16 %0, v[164:167], %4, %0\n\t" \
    "v_mfma_f32_16x16x32_bf16 %1, v[196:199], %4, %1\n\t" \
    "v_mfma_f32_16x16x32_bf16 %2, v[228:231], %4, %2\n\t" \
    "s_waitcnt lgkmcnt(6)\n\t" \
    "v_mfma_f32_16x16x32_bf16 %0, v[168:171], %5, %0\n\t" \
    "v_mfma_f32_16x16x32_bf16 %1, v[200:203], %5, %1\n\t" \
    "v_mfma_f32_16x16x32_bf16 %2, v[232:235], %5, %2\n\t" \
    "s_waitcnt lgkmcnt(5)\n\t" \
    "v_mfma_f32_16x16x32_bf16 %0, v[172:175], %6, %0\n\t" \
    "v_mfma_f32_16x16x32_bf16 %1, v[204:207], %6, %1\n\t" \
    "v_mfma_f32_16x16x32_bf16 %2, v[236:239], %6, %2\n\t" \
    "s_waitcnt lgkmcnt(4)\n\t" \
    "v_mfma_f32_16x16x32_bf16 %0, v[176:179], %7, %0\n\t" \
    "v_mfma_f32_16x16x32_bf16 %1, v[208:211], %7, %1\n\t" \
    "v_mfma_f32_16x16x32_bf16 %2, v[240:243], %7, %2\n\t" \
    "s_waitcnt lgkmcnt(3)\n\t" \
    "v_mfma_f32_16x16x32_bf16 %0, v[180:183], %8, %0\n\t" \
    "v_mfma_f32_16x16x32_bf16 %1, v[212:215], %8, %1\n\t" \
    "v_mfma_f32_16x16x32_bf16 %2, v[244:247], %8, %2\n\t" \
    "s_waitcnt lgkmcnt(2)\n\t" \
    "v_mfma_f32_16x16x32_bf16 %0, v[184:187], %9, %0\n\t" \
    "v_mfma_f32_16x16x32_bf16 %1, v[216:219], %9, %1\n\t" \
    "v_mfma_f32_16x16x32_bf16 %2, v[248:251], %9, %2\n\t" \
    "s_waitcnt lgkmcnt(1)\n\t" \
    "v_mfma_f32_16x16x32_bf16 %0, v[188:191], %10, %0\n\t" \
    "v_mfma_f32_16x16x32_bf16 %1, v[220:223], %10, %1\n\t" \
    "v_mfma_f32_16x16x32_bf16 %2, v[252:255], %10, %2\n\t" \
    "s_nop 5\n\t" \
    "ds_write_b128 %11, %0 offset:0\n\t" \
    "ds_write_b128 %11, %1 offset:1024\n\t" \
    "ds_write_b128 %11, %2 offset:2048\n\t"
#define ASM_PV01 \
    "s_waitcnt lgkmcnt(8)\n\t" \
    "v_mfma_f32_16x16x32_bf16 %0, v[160:163], %2, 0\n\t" \
    "v_mfma_f32_16x16x32_bf16 %1, v[192:195], %2, 0\n\t" \
    "s_waitcnt lgkmcnt(7)\n\t" \
    "v_mfma_f32_16x16x32_bf16 %0, v[164:167], %3, %0\n\t" \
    "v_mfma_f32_16x16x32_bf16 %1, v[196:199], %3, %1\n\t" \
    "s_waitcnt lgkmcnt(6)\n\t" \
    "v_mfma_f32_16x16x32_bf16 %0, v[168:171], %4, %0\n\t" \
    "v_mfma_f32_16x16x32_bf16 %1, v[200:203], %4, %1\n\t" \
    "s_waitcnt lgkmcnt(5)\n\t" \
    "v_mfma_f32_16x16x32_bf16 %0, v[172:175], %5, %0\n\t" \
    "v_mfma_f32_16x16x32_bf16 %1, v[204:207], %5, %1\n\t" \
    "s_waitcnt lgkmcnt(4)\n\t" \
    "v_mfma_f32_16x16x32_bf16 %0, v[176:179], %6, %0\n\t" \
    "v_mfma_f32_16x16x32_bf16 %1, v[208:211], %6, %1\n\t" \
    "s_waitcnt lgkmcnt(3)\n\t" \
    "v_mfma_f32_16x16x32_bf16 %0, v[180:183], %7, %0\n\t" \
    "v_mfma_f32_16x16x32_bf16 %1, v[212:215], %7, %1\n\t" \
    "s_waitcnt lgkmcnt(2)\n\t" \
    "v_mfma_f32_16x16x32_bf16 %0, v[184:187], %8, %0\n\t" \
    "v_mfma_f32_16x16x32_bf16 %1, v[216:219], %8, %1\n\t" \
    "s_waitcnt lgkmcnt(1)\n\t" \
    "v_mfma_f32_16x16x32_bf16 %0, v[188:191], %9, %0\n\t" \
    "v_mfma_f32_16x16x32_bf16 %1, v[220:223], %9, %1\n\t" \
    "s_nop 6\n\t" \
    "ds_write_b128 %10, %0 offset:0\n\t" \
    "ds_write_b128 %10, %1 offset:1024\n\t"
#define ASM_PV2 \
    "s_nop 1\n\t" \
    "v_mfma_f32_16x16x32_bf16 %0, v[224:227], %1, 0\n\t" \
    "v_mfma_f32_16x16x32_bf16 %0, v[228:231], %2, %0\n\t" \
    "v_mfma_f32_16x16x32_bf16 %0, v[232:235], %3, %0\n\t" \
    "v_mfma_f32_16x16x32_bf16 %0, v[236:239], %4, %0\n\t" \
    "v_mfma_f32_16x16x32_bf16 %0, v[240:243], %5, %0\n\t" \
    "v_mfma_f32_16x16x32_bf16 %0, v[244:247], %6, %0\n\t" \
    "v_mfma_f32_16x16x32_bf16 %0, v[248:251], %7, %0\n\t" \
    "v_mfma_f32_16x16x32_bf16 %0, v[252:255], %8, %0\n\t" \
    "s_nop 7\n\t" \
    "ds_write_b128 %9, %0 offset:2048\n\t"
#define ASM_PVB0 \
    "s_nop 1\n\t" \
    "v_mfma_f32_16x16x32_bf16 %0, v[160:163], %1, 0\n\t" \
    "v_mfma_f32_16x16x32_bf16 %0, v[164:167], %2, %0\n\t" \
    "v_mfma_f32_16x16x32_bf16 %0, v[168:171], %3, %0\n\t" \
    "v_mfma_f32_16x16x32_bf16 %0, v[172:175], %4, %0\n\t" \
    "v_mfma_f32_16x16x32_bf16 %0, v[176:179], %5, %0\n\t" \
    "v_mfma_f32_16x16x32_bf16 %0, v[180:183], %6, %0\n\t" \
    "v_mfma_f32_16x16x32_bf16 %0, v[184:187], %7, %0\n\t" \
    "v_mfma_f32_16x16x32_bf16 %0, v[188:191], %8, %0\n\t" \
    "s_nop 7\n\t" \
    "ds_write_b128 %9, %0 offset:0\n\t"
#define ASM_PVB1 \
    "s_nop 1\n\t" \
    "v_mfma_f32_16x16x32_bf16 %0, v[192:195], %1, 0\n\t" \
    "v_mfma_f32_16x16x32_bf16 %0, v[196:199], %2, %0\n\t" \
    "v_mfma_f32_16x16x32_bf16 %0, v[200:203], %3, %0\n\t" \
    "v_mfma_f32_16x16x32_bf16 %0, v[204:207], %4, %0\n\t" \
    "v_mfma_f32_16x16x32_bf16 %0, v[208:211], %5, %0\n\t" \
    "v_mfma_f32_16x16x32_bf16 %0, v[212:215], %6, %0\n\t" \
    "v_mfma_f32_16x16x32_bf16 %0, v[216:219], %7, %0\n\t" \
    "v_mfma_f32_16x16x32_bf16 %0, v[220:223], %8, %0\n\t" \
    "s_nop 7\n\t" \
    "ds_write_b128 %9, %0 offset:1024\n\t"
#define ASM_TR00 \
    "v_perm_b32 %0, v164, v160, %2\n\t" \
    "v_perm_b32 v164, v164, v160, %3\n\t" \
    "v_mov_b32 v160, %0\n\t"
#define ASM_TR01 \
    "v_perm_b32 %0, v173, v169, %2\n\t" \
    "v_perm_b32 v173, v173, v169, %3\n\t" \
    "v_mov_b32 v169, %0\n\t" \
    "v_perm_b32 %0, v172, v168, %2\n\t" \
    "v_perm_b32 %1, v172, v168, %3\n\t" \
    "v_perm_b32 v168, v165, v161, %2\n\t" \
    "v_perm_b32 v172, v165, v161, %3\n\t" \
    "v_mov_b32 v161, %0\n\t" \
    "v_mov_b32 v165, %1\n\t"
#define ASM_TR02 \
    "v_perm_b32 %0, v182, v178, %2\n\t" \
    "v_perm_b32 v182, v182, v178, %3\n\t" \
    "v_mov_b32 v178, %0\n\t" \
    "v_perm_b32 %0, v180, v176, %2\n\t" \
    "v_perm_b32 %1, v180, v176, %3\n\t" \
    "v_perm_b32 v176, v166, v162, %2\n\t" \
    "v_perm_b32 v180, v166, v162, %3\n\t" \
    "v_mov_b32 v162, %0\n\t" \
    "v_mov_b32 v166, %1\n\t" \
    "v_perm_b32 %0, v181, v177, %2\n\t" \
    "v_perm_b32 %1, v181, v177, %3\n\t" \
    "v_perm_b32 v177, v174, v170, %2\n\t" \
    "v_perm_b32 v181, v174, v170, %3\n\t" \
    "v_mov_b32 v170, %0\n\t" \
    "v_mov_b32 v174, %1\n\t"
#define ASM_TR03 \
    "v_perm_b32 %0, v191, v187, %2\n\t" \
    "v_perm_b32 v191, v191, v187, %3\n\t" \
    "v_mov_b32 v187, %0\n\t" \
    "v_perm_b32 %0, v188, v184, %2\n\t" \
    "v_perm_b32 %1, v188, v184, %3\n\t" \
    "v_perm_b32 v184, v167, v163, %2\n\t" \
    "v_perm_b32 v188, v167, v163, %3\n\t" \
    "v_mov_b32 v163, %0\n\t" \
    "v_mov_b32 v167, %1\n\t" \
    "v_perm_b32 %0, v189, v185, %2\n\t" \
    "v_perm_b32 %1, v189, v185, %3\n\t" \
    "v_perm_b32 v185, v175, v171, %2\n\t" \
    "v_perm_b32 v189, v175, v171, %3\n\t" \
    "v_mov_b32 v171, %0\n\t" \
    "v_mov_b32 v175, %1\n\t" \
    "v_perm_b32 %0, v190, v186, %2\n\t" \
    "v_perm_b32 %1, v190, v186, %3\n\t" \
    "v_perm_b32 v186, v183, v179, %2\n\t" \
    "v_perm_b32 v190, v183, v179, %3\n\t" \
    "v_mov_b32 v179, %0\n\t" \
    "v_mov_b32 v183, %1\n\t"
#define ASM_TR10 \
    "v_perm_b32 %0, v196, v192, %2\n\t" \
    "v_perm_b32 v196, v196, v192, %3\n\t" \
    "v_mov_b32 v192, %0\n\t"
#define ASM_TR11 \
    "v_perm_b32 %0, v205, v201, %2\n\t" \
    "v_perm_b32 v205, v205, v201, %3\n\t" \
    "v_mov_b32 v201, %0\n\t" \
    "v_perm_b32 %0, v204, v200, %2\n\t" \
    "v_perm_b32 %1, v204, v200, %3\n\t" \
    "v_perm_b32 v200, v197, v193, %2\n\t" \
    "v_perm_b32 v204, v197, v193, %3\n\t" \
    "v_mov_b32 v193, %0\n\t" \
    "v_mov_b32 v197, %1\n\t"
#define ASM_TR12 \
    "v_perm_b32 %0, v214, v210, %2\n\t" \
    "v_perm_b32 v214, v214, v210, %3\n\t" \
    "v_mov_b32 v210, %0\n\t" \
    "v_perm_b32 %0, v212, v208, %2\n\t" \
    "v_perm_b32 %1, v212, v208, %3\n\t" \
    "v_perm_b32 v208, v198, v194, %2\n\t" \
    "v_perm_b32 v212, v198, v194, %3\n\t" \
    "v_mov_b32 v194, %0\n\t" \
    "v_mov_b32 v198, %1\n\t" \
    "v_perm_b32 %0, v213, v209, %2\n\t" \
    "v_perm_b32 %1, v213, v209, %3\n\t" \
    "v_perm_b32 v209, v206, v202, %2\n\t" \
    "v_perm_b32 v213, v206, v202, %3\n\t" \
    "v_mov_b32 v202, %0\n\t" \
    "v_mov_b32 v206, %1\n\t"
#define ASM_TR13 \
    "v_perm_b32 %0, v223, v219, %2\n\t" \
    "v_perm_b32 v223, v223, v219, %3\n\t" \
    "v_mov_b32 v219, %0\n\t" \
    "v_perm_b32 %0, v220, v216, %2\n\t" \
    "v_perm_b32 %1, v220, v216, %3\n\t" \
    "v_perm_b32 v216, v199, v195, %2\n\t" \
    "v_perm_b32 v220, v199, v195, %3\n\t" \
    "v_mov_b32 v195, %0\n\t" \
    "v_mov_b32 v199, %1\n\t" \
    "v_perm_b32 %0, v221, v217, %2\n\t" \
    "v_perm_b32 %1, v221, v217, %3\n\t" \
    "v_perm_b32 v217, v207, v203, %2\n\t" \
    "v_perm_b32 v221, v207, v203, %3\n\t" \
    "v_mov_b32 v203, %0\n\t" \
    "v_mov_b32 v207, %1\n\t" \
    "v_perm_b32 %0, v222, v218, %2\n\t" \
    "v_perm_b32 %1, v222, v218, %3\n\t" \
    "v_perm_b32 v218, v215, v211, %2\n\t" \
    "v_perm_b32 v222, v215, v211, %3\n\t" \
    "v_mov_b32 v211, %0\n\t" \
    "v_mov_b32 v215, %1\n\t"
#define ASM_TR20 \
    "v_perm_b32 %0, v228, v224, %2\n\t" \
    "v_perm_b32 v228, v228, v224, %3\n\t" \
    "v_mov_b32 v224, %0\n\t"
#define ASM_TR21 \
    "v_perm_b32 %0, v237, v233, %2\n\t" \
    "v_perm_b32 v237, v237, v233, %3\n\t" \
    "v_mov_b32 v233, %0\n\t" \
    "v_perm_b32 %0, v236, v232, %2\n\t" \
    "v_perm_b32 %1, v236, v232, %3\n\t" \
    "v_perm_b32 v232, v229, v225, %2\n\t" \
    "v_perm_b32 v236, v229, v225, %3\n\t" \
    "v_mov_b32 v225, %0\n\t" \
    "v_mov_b32 v229, %1\n\t"
#define ASM_TR22 \
    "v_perm_b32 %0, v246, v242, %2\n\t" \
    "v_perm_b32 v246, v246, v242, %3\n\t" \
    "v_mov_b32 v242, %0\n\t" \
    "v_perm_b32 %0, v244, v240, %2\n\t" \
    "v_perm_b32 %1, v244, v240, %3\n\t" \
    "v_perm_b32 v240, v230, v226, %2\n\t" \
    "v_perm_b32 v244, v230, v226, %3\n\t" \
    "v_mov_b32 v226, %0\n\t" \
    "v_mov_b32 v230, %1\n\t" \
    "v_perm_b32 %0, v245, v241, %2\n\t" \
    "v_perm_b32 %1, v245, v241, %3\n\t" \
    "v_perm_b32 v241, v238, v234, %2\n\t" \
    "v_perm_b32 v245, v238, v234, %3\n\t" \
    "v_mov_b32 v234, %0\n\t" \
    "v_mov_b32 v238, %1\n\t"
#define ASM_TR23 \
    "v_perm_b32 %0, v255, v251, %2\n\t" \
    "v_perm_b32 v255, v255, v251, %3\n\t" \
    "v_mov_b32 v251, %0\n\t" \
    "v_perm_b32 %0, v252, v248, %2\n\t" \
    "v_perm_b32 %1, v252, v248, %3\n\t" \
    "v_perm_b32 v248, v231, v227, %2\n\t" \
    "v_perm_b32 v252, v231, v227, %3\n\t" \
    "v_mov_b32 v227, %0\n\t" \
    "v_mov_b32 v231, %1\n\t" \
    "v_perm_b32 %0, v253, v249, %2\n\t" \
    "v_perm_b32 %1, v253, v249, %3\n\t" \
    "v_perm_b32 v249, v239, v235, %2\n\t" \
    "v_perm_b32 v253, v239, v235, %3\n\t" \
    "v_mov_b32 v235, %0\n\t" \
    "v_mov_b32 v239, %1\n\t" \
    "v_perm_b32 %0, v254, v250, %2\n\t" \
    "v_perm_b32 %1, v254, v250, %3\n\t" \
    "v_perm_b32 v250, v247, v243, %2\n\t" \
    "v_perm_b32 v254, v247, v243, %3\n\t" \
    "v_mov_b32 v243, %0\n\t" \
    "v_mov_b32 v247, %1\n\t"
#define ASM_ATT_A \
    "s_setprio 2\n\t" \
    "v_mov_b32 v16, 0\n\t" \
    "v_mov_b32 v17, 0\n\t" \
    "v_mov_b32 v18, 0\n\t" \
    "v_mov_b32 v19, 0\n\t" \
    "v_mov_b32 v20, %[ma]\n\t" \
    "s_cmp_eq_u32 %[tz], 0\n\t" \
    "s_cbranch_scc1 23f\n\t" \
    "s_branch 22f\n\t" \
    "21:\n\t" \
    "s_sleep 3\n\t" \
    "22:\n\t" \
    "s_memrealtime vcc\n\t" \
    "s_waitcnt lgkmcnt(0)\n\t" \
    "s_sub_i32 vcc_lo, vcc_lo, %[tz]\n\t" \
    "s_cmp_lt_i32 vcc_lo, 0\n\t" \
    "s_cbranch_scc1 21b\n\t" \
    "23:\n\t" \
    "s_mov_b32 vcc_hi, 0x400000\n\t" \
    "0:\n\t" \
    "ds_read_b32 v21, v20 offset:340\n\t" \
    "s_sub_u32 vcc_hi, vcc_hi, 1\n\t" \
    "s_cbranch_scc1 4f\n\t" \
    "s_waitcnt lgkmcnt(0)\n\t" \
    "v_readfirstlane_b32 %[st], v21\n\t" \
    "s_cmp_eq_u32 %[st], 0\n\t" \
    "s_cbranch_scc1 0b\n\t" \
    "s_add_u32 %[st], %[st], %[tg]\n\t" \
    "4:\n\t" \
    "s_memrealtime vcc\n\t" \
    "s_waitcnt lgkmcnt(0)\n\t" \
    "s_sub_i32 vcc_lo, vcc_lo, %[st]\n\t" \
    "s_cmp_lt_i32 vcc_lo, -4000\n\t" \
    "s_cbranch_scc1 10f\n\t" \
    "s_cmp_lt_i32 vcc_lo, 0\n\t" \
    "s_cbranch_scc1 4b\n\t" \
    "10:\n\t" \
    "s_mov_b32 exec_hi, 0xff\n\t" \
    "global_load_dwordx4 v[16:19], %[go], %[ws] sc1\n\t" \
    "s_mov_b32 exec_hi, -1\n\t" \
    "s_barrier\n\t" \
    "ds_read_b64 v[22:23], v20 offset:352\n\t" \
    "s_mov_b32 %[st], 0x10000\n\t" \
    "s_waitcnt lgkmcnt(0)\n\t" \
    "v_mul_f32 v22, v22, %[cs]\n\t" \
    "v_mul_f32_e64 v22, -v22, v23\n\t" \
    "5:\n\t" \
    "s_waitcnt vmcnt(0)\n\t" \
    "v_max3_u32 v24, v16, v17, v18\n\t" \
    "v_max_u32 v24, v24, v19\n\t" \
    "v_cmp_eq_u32 vcc, -1, v24\n\t" \
    "v_permlane32_swap_b32 v16, v18\n\t" \
    "v_permlane32_swap_b32 v17, v19\n\t" \
    "v_pk_add_f32 v[16:17], v[16:17], v[18:19]\n\t" \
    "s_nop 0\n\t" \
    "s_cbranch_vccnz 6f\n\t" \
    "7:\n\t" \
    "v_permlane16_swap_b32 v16, v17\n\t" \
    "v_add_f32 v16, v16, v17\n\t" \
    "s_nop 1\n\t" \
    "v_add_f32_dpp v16, v16, v16 row_ror:8 row_mask:0xf bank_mask:0xf\n\t" \
    "v_fmac_f32 v22, v16, v23\n\t" \
    "v_cvt_pk_bf16_f32 v22, v22, v22\n\t" \
    "ds_write_b16 %[qo], v22\n\t" \
    "s_waitcnt lgkmcnt(0)\n\t" \
    "s_barrier\n\t" \
    "ds_read_b128 v[16:19], %[qa] offset:0\n\t" \
    "ds_read_b128 v[20:23], %[qa] offset:64\n\t" \
    "ds_read_b128 v[24:27], %[qa] offset:128\n\t" \
    "ds_read_b128 v[28:31], %[qa] offset:192\n\t" \
    "ds_read_b128 v[32:35], %[qa] offset:256\n\t" \
    "ds_read_b128 v[36:39], %[qa] offset:320\n\t" \
    "s_waitcnt lgkmcnt(5)\n\t" \
    "v_mfma_f32_16x16x32_bf16 v[40:43], v[16:19], v[64:67], 0\n\t" \
    "v_mfma_f32_16x16x32_bf16 v[44:47], v[16:19], v[88:91], 0\n\t" \
    "v_mfma_f32_16x16x32_bf16 v[48:51], v[16:19], v[112:115], 0\n\t" \
    "v_mfma_f32_16x16x32_bf16 v[52:55], v[16:19], v[136:139], 0\n\t" \
    "s_waitcnt lgkmcnt(4)\n\t" \
    "v_mfma_f32_16x16x32_bf16 v[40:43], v[20:23], v[76:79], v[40:43]\n\t" \
    "v_mfma_f32_16x16x32_bf16 v[44:47], v[20:23], v[100:103], v[44:47]\n\t" \
    "v_mfma_f32_16x16x32_bf16 v[48:51], v[20:23], v[124:127], v[48:51]\n\t" \
    "v_mfma_f32_16x16x32_bf16 v[52:55], v[20:23], v[148:151], v[52:55]\n\t" \
    "s_waitcnt lgkmcnt(3)\n\t" \
    "v_mfma_f32_16x16x32_bf16 v[40:43], v[24:27], v[68:71], v[40:43]\n\t" \
    "v_mfma_f32_16x16x32_bf16 v[44:47], v[24:27], v[92:95], v[44:47]\n\t" \
    "v_mfma_f32_16x16x32_bf16 v[48:51], v[24:27], v[116:119], v[48:51]\n\t" \
    "v_mfma_f32_16x16x32_bf16 v[52:55], v[24:27], v[140:143], v[52:55]\n\t" \
    "s_waitcnt lgkmcnt(2)\n\t" \
    "v_mfma_f32_16x16x32_bf16 v[40:43], v[28:31], v[80:83], v[40:43]\n\t" \
    "v_mfma_f32_16x16x32_bf16 v[44:47], v[28:31], v[104:107], v[44:47]\n\t" \
    "v_mfma_f32_16x16x32_bf16 v[48:51], v[28:31], v[128:131], v[48:51]\n\t" \
    "v_mfma_f32_16x16x32_bf16 v[52:55], v[28:31], v[152:155], v[52:55]\n\t" \
    "s_waitcnt lgkmcnt(1)\n\t" \
    "v_mfma_f32_16x16x32_bf16 v[40:43], v[32:35], v[72:75], v[40:43]\n\t" \
    "v_mfma_f32_16x16x32_bf16 v[44:47], v[32:35], v[96:99], v[44:47]\n\t" \
    "v_mfma_f32_16x16x32_bf16 v[48:51], v[32:35], v[120:123], v[48:51]\n\t" \
    "v_mfma_f32_16x16x32_bf16 v[52:55], v[32:35], v[144:147], v[52:55]\n\t" \
    "s_waitcnt lgkmcnt(0)\n\t" \
    "v_mfma_f32_16x16x32_bf16 v[40:43], v[36:39], v[84:87], v[40:43]\n\t" \
    "v_mfma_f32_16x16x32_bf16 v[44:47], v[36:39], v[108:111], v[44:47]\n\t" \
    "v_mfma_f32_16x16x32_bf16 v[48:51], v[36:39], v[132:135], v[48:51]\n\t" \
    "v_mfma_f32_16x16x32_bf16 v[52:55], v[36:39], v[156:159], v[52:55]\n\t" \
    "s_nop 5\n\t" \
    "v_cndmask_b32_e64 v60, v40, v44, %[mo_]\n\t" \
    "s_nop 0\n\t" \
    "v_cndmask_b32_e64 v61, v48, v52, %[mo_]\n\t" \
    "v_cndmask_b32_e64 v60, v60, v61, %[mh_]\n\t" \
    "v_mul_f32 v60, 0x3dd53b94, v60\n\t" \
    "v_mov_b32 v63, %[mlw]\n\t" \
    "s_nop 0\n\t" \
    "v_max_f32_dpp v61, v60, v60 quad_perm:[1,0,3,2] row_mask:0xf bank_mask:0xf\n\t" \
    "s_nop 1\n\t" \
    "v_max_f32_dpp v61, v61, v61 quad_perm:[2,3,0,1] row_mask:0xf bank_mask:0xf\n\t" \
    "s_nop 1\n\t" \
    "v_max_f32_dpp v61, v61, v61 row_half_mirror row_mask:0xf bank_mask:0xf\n\t" \
    "s_nop 1\n\t" \
    "v_max_f32_dpp v61, v61, v61 row_mirror row_mask:0xf bank_mask:0xf\n\t" \
    "s_nop 1\n\t" \
    "v_max_f32_dpp v61, v61, v61 row_bcast:15 row_mask:0xa bank_mask:0xf\n\t" \
    "s_nop 1\n\t" \
    "v_max_f32_dpp v61, v61, v61 row_bcast:31 row_mask:0xc bank_mask:0xf\n\t" \
    "s_nop 0\n\t" \
    "v_readlane_b32 %[st], v61, 63\n\t" \
    "s_mov_b32 exec_lo, 0\n\t" \
    "s_brev_b32 exec_hi, 1\n\t" \
    "ds_write_b32 v63, v61\n\t" \
    "s_mov_b64 exec, -1\n\t" \
    "v_subrev_f32 v62, %[st], v60\n\t" \
    "v_exp_f32 v62, v62\n\t" \
    "s_nop 0\n\t" \
    "v_cvt_pk_bf16_f32 v60, v62, v62\n\t" \
    "ds_write_b16 %[pw], v60\n\t" \
    "ds_read_b128 v[16:19], %[pr] offset:0\n\t" \
    "ds_read_b128 v[20:23], %[pr] offset:192\n\t" \
    "ds_read_b128 v[24:27], %[pr] offset:384\n\t" \
    "ds_read_b128 v[28:31], %[pr] offset:576\n\t" \
    "ds_read_b128 v[32:35], %[pr] offset:768\n\t" \
    "ds_read_b128 v[36:39], %[pr] offset:960\n\t" \
    "ds_read_b128 v[40:43], %[pr] offset:1152\n\t" \
    "ds_read_b128 v[44:47], %[pr] offset:1344\n\t" \
    "v_mov_b32 v60, %[ma]\n\t" \
    "s_setprio 0\n\t" \
    "s_waitcnt lgkmcnt(7)\n\t" \
    "v_mfma_f32_16x16x32_bf16 v[48:51], v[160:163], v[16:19], 0\n\t" \
    "v_mfma_f32_16x16x32_bf16 v[52:55], v[192:195], v[16:19], 0\n\t" \
    "v_mfma_f32_16x16x32_bf16 v[56:59], v[224:227], v[16:19], 0\n\t" \
    "v_add_f32_dpp v62, v62, v62 quad_perm:[1,0,3,2] row_mask:0xf bank_mask:0xf\n\t" \
    "s_waitcnt lgkmcnt(6)\n\t" \
    "v_mfma_f32_16x16x32_bf16 v[48:51], v[164:167], v[20:23], v[48:51]\n\t" \
    "v_mfma_f32_16x16x32_bf16 v[52:55], v[196:199], v[20:23], v[52:55]\n\t" \
    "v_mfma_f32_16x16x32_bf16 v[56:59], v[228:231], v[20:23], v[56:59]\n\t" \
    "v_add_f32_dpp v62, v62, v62 quad_perm:[2,3,0,1] row_mask:0xf bank_mask:0xf\n\t" \
    "s_waitcnt lgkmcnt(5)\n\t" \
    "v_mfma_f32_16x16x32_bf16 v[48:51], v[168:171], v[24:27], v[48:51]\n\t" \
    "v_mfma_f32_16x16x32_bf16 v[52:55], v[200:203], v[24:27], v[52:55]\n\t" \
    "v_mfma_f32_16x16x32_bf16 v[56:59], v[232:235], v[24:27], v[56:59]\n\t" \
    "v_add_f32_dpp v62, v62, v62 row_half_mirror row_mask:0xf bank_mask:0xf\n\t" \
    "s_waitcnt lgkmcnt(4)\n\t" \
    "v_mfma_f32_16x16x32_bf16 v[48:51], v[172:175], v[28:31], v[48:51]\n\t" \
    "v_mfma_f32_16x16x32_bf16 v[52:55], v[204:207], v[28:31], v[52:55]\n\t" \
    "v_mfma_f32_16x16x32_bf16 v[56:59], v[236:239], v[28:31], v[56:59]\n\t" \
    "v_add_f32_dpp v62, v62, v62 row_mirror row_mask:0xf bank_mask:0xf\n\t" \
    "s_waitcnt lgkmcnt(3)\n\t" \
    "v_mfma_f32_16x16x32_bf16 v[48:51], v[176:179], v[32:35], v[48:51]\n\t" \
    "v_mfma_f32_16x16x32_bf16 v[52:55], v[208:211], v[32:35], v[52:55]\n\t" \
    "v_mfma_f32_16x16x32_bf16 v[56:59], v[240:243], v[32:35], v[56:59]\n\t" \
    "v_add_f32_dpp v62, v62, v62 row_bcast:15 row_mask:0xa bank_mask:0xf\n\t" \
    "s_waitcnt lgkmcnt(2)\n\t" \
    "v_mfma_f32_16x16x32_bf16 v[48:51], v[180:183], v[36:39], v[48:51]\n\t" \
    "v_mfma_f32_16x16x32_bf16 v[52:55], v[212:215], v[36:39], v[52:55]\n\t" \
    "v_mfma_f32_16x16x32_bf16 v[56:59], v[244:247], v[36:39], v[56:59]\n\t" \
    "v_add_f32_dpp v62, v62, v62 row_bcast:31 row_mask:0xc bank_mask:0xf\n\t" \
    "s_waitcnt lgkmcnt(1)\n\t" \
    "v_mfma_f32_16x16x32_bf16 v[48:51], v[184:187], v[40:43], v[48:51]\n\t" \
    "v_mfma_f32_16x16x32_bf16 v[52:55], v[216:219], v[40:43], v[52:55]\n\t" \
    "v_mfma_f32_16x16x32_bf16 v[56:59], v[248:251], v[40:43], v[56:59]\n\t" \
    "s_waitcnt lgkmcnt(0)\n\t" \
    "v_mfma_f32_16x16x32_bf16 v[48:51], v[188:191], v[44:47], v[48:51]\n\t" \
    "v_mfma_f32_16x16x32_bf16 v[52:55], v[220:223], v[44:47], v[52:55]\n\t" \
    "v_mfma_f32_16x16x32_bf16 v[56:59], v[252:255], v[44:47], v[56:59]\n\t" \
    "s_barrier\n\t" \
    "ds_read_b128 v[16:19], v60\n\t" \
    "ds_read_b128 v[20:23], v60 offset:16\n\t" \
    "ds_read_b32 v24, %[mo]\n\t" \
    "s_mov_b32 exec_lo, 0\n\t" \
    "s_brev_b32 exec_hi, 1\n\t" \
    "ds_write_b32 v63, v62 offset:32\n\t" \
    "s_mov_b64 exec, -1\n\t" \
    "v_mov_b32_dpp v48, v52 quad_perm:[0,1,2,3] row_mask:0xf bank_mask:0xc\n\t" \
    "v_mov_b32_dpp v49, v53 quad_perm:[0,1,2,3] row_mask:0xf bank_mask:0xc\n\t" \
    "v_mov_b32_dpp v50, v54 quad_perm:[0,1,2,3] row_mask:0xf bank_mask:0xc\n\t" \
    "v_mov_b32_dpp v51, v55 quad_perm:[0,1,2,3] row_mask:0xf bank_mask:0xc\n\t" \
    "ds_write_b128 %[ow], v[48:51] offset:0\n\t" \
    "s_mov_b32 exec_lo, 0x00ff00ff\n\t" \
    "s_mov_b32 exec_hi, 0xff00ff00\n\t" \
    "ds_write_b128 %[ow], v[56:59] offset:2048\n\t" \
    "s_mov_b64 exec, -1\n\t" \
    "s_waitcnt lgkmcnt(3)\n\t" \
    "v_max3_f32 v16, v16, v17, v18\n\t" \
    "v_max3_f32 v19, v19, v20, v21\n\t" \
    "v_max3_f32 v16, v16, v22, v23\n\t" \
    "v_max_f32 v26, v16, v19\n\t" \
    "v_sub_f32 v60, v24, v26\n\t" \
    "v_exp_f32 v60, v60\n\t" \
    "s_nop 0\n\t" \
    "ds_write2_b32 %[mo], v60, v26 offset0:96 offset1:104\n\t" \
    "s_waitcnt lgkmcnt(0)\n\t" \
    "s_barrier\n\t" \
    "ds_read_b128 v[16:19], %[r0]\n\t" \
    "ds_read_b128 v[20:23], %[r0] offset:128\n\t" \
    "s_waitcnt lgkmcnt(0)\n\t" \
    "v_pk_add_f32 v[16:17], v[16:17], v[20:21]\n\t" \
    "v_pk_add_f32 v[18:19], v[18:19], v[22:23]\n\t" \
    "v_pk_mul_f32 v[16:17], v[16:17], v[60:61] op_sel_hi:[1,0]\n\t" \
    "v_pk_mul_f32 v[18:19], v[18:19], v[60:61] op_sel_hi:[1,0]\n\t" \
    "s_nop 1\n\t" \
    "v_permlane32_swap_b32 v16, v18\n\t" \
    "v_permlane32_swap_b32 v17, v19\n\t" \
    "v_pk_add_f32 v[16:17], v[16:17], v[18:19]\n\t" \
    "s_nop 1\n\t" \
    "v_permlane16_swap_b32 v16, v17\n\t" \
    "v_add_f32 v16, v16, v17\n\t" \
    "s_nop 1\n\t" \
    "v_add_f32_dpp v17, v16, v16 row_ror:8 row_mask:0xf bank_mask:0xf\n\t" \
    "v_add_f32_dpp v16, v16, v16 row_ror:8 row_mask:0xf bank_mask:0xf\n\t" \
    "s_nop 1\n\t" \
    "v_permlane16_swap_b32 v16, v17\n\t" \
    "s_mov_b32 exec_lo, 0xff\n\t" \
    "v_cvt_pk_bf16_f32 v16, v16, v17\n\t" \
    "s_mov_b32 exec_hi, 0xff\n\t" \
    "v_min_u32 v16, -2, v16\n\t" \
    "global_store_dword %[so], v16, %[ws] sc1\n\t" \
    "s_mov_b64 exec, -1\n\t" \
    "s_branch 2f\n\t" \
    "6:\n\t" \
    "s_sub_u32 %[st], %[st], 1\n\t" \
    "s_cbranch_scc1 7b\n\t" \
    RETRY_ASM \
    "v_mov_b32 v16, 0\n\t" \
    "v_mov_b32 v17, 0\n\t" \
    "v_mov_b32 v18, 0\n\t" \
    "v_mov_b32 v19, 0\n\t" \
    "s_mov_b32 exec_hi, 0xff\n\t" \
    "global_load_dwordx4 v[16:19], %[go], %[ws] sc1\n\t" \
    "s_mov_b32 exec_hi, -1\n\t" \
    "s_branch 5b\n\t" \
    "2:\n\t"
#define ASM_ATT_B \
    "s_setprio 1\n\t" \
    "v_mov_b32 v56, -1\n\t" \
    "v_mov_b32 v57, -1\n\t" \
    "v_mov_b32 v58, -1\n\t" \
    "v_mov_b32 v59, -1\n\t" \
    "s_cmp_gt_u32 %[wv], 5\n\t" \
    "s_cbranch_scc0 8f\n\t" \
    "s_cmp_eq_u32 %[wv], 7\n\t" \
    "s_cbranch_scc1 9f\n\t" \
    "s_barrier\n\t" \
    "s_branch 9f\n\t" \
    "8:\n\t" \
    "v_mov_b32 v16, 0\n\t" \
    "v_mov_b32 v17, 0\n\t" \
    "v_mov_b32 v18, 0\n\t" \
    "v_mov_b32 v19, 0\n\t" \
    "v_mov_b32 v20, %[ma]\n\t" \
    "s_cmp_eq_u32 %[tz], 0\n\t" \
    "s_cbranch_scc1 23f\n\t" \
    "s_branch 22f\n\t" \
    "21:\n\t" \
    "s_sleep 3\n\t" \
    "22:\n\t" \
    "s_memrealtime vcc\n\t" \
    "s_waitcnt lgkmcnt(0)\n\t" \
    "s_sub_i32 vcc_lo, vcc_lo, %[tz]\n\t" \
    "s_cmp_lt_i32 vcc_lo, 0\n\t" \
    "s_cbranch_scc1 21b\n\t" \
    "23:\n\t" \
    "s_mov_b32 vcc_hi, 0x400000\n\t" \
    "0:\n\t" \
    "ds_read_b32 v21, v20 offset:340\n\t" \
    "s_sub_u32 vcc_hi, vcc_hi, 1\n\t" \
    "s_cbranch_scc1 4f\n\t" \
    "s_waitcnt lgkmcnt(0)\n\t" \
    "v_readfirstlane_b32 %[st], v21\n\t" \
    "s_cmp_eq_u32 %[st], 0\n\t" \
    "s_cbranch_scc1 0b\n\t" \
    "s_add_u32 %[st], %[st], %[tg]\n\t" \
    "4:\n\t" \
    "s_memrealtime vcc\n\t" \
    "s_waitcnt lgkmcnt(0)\n\t" \
    "s_sub_i32 vcc_lo, vcc_lo, %[st]\n\t" \
    "s_cmp_lt_i32 vcc_lo, -4000\n\t" \
    "s_cbranch_scc1 10f\n\t" \
    "s_cmp_lt_i32 vcc_lo, 0\n\t" \
    "s_cbranch_scc1 4b\n\t" \
    "10:\n\t" \
    "s_mov_b32 exec_hi, 0xff\n\t" \
    "global_load_dwordx4 v[16:19], %[go], %[ws] sc1\n\t" \
    "s_mov_b32 exec_hi, -1\n\t" \
    "s_barrier\n\t" \
    "ds_read_b64 v[22:23], v20 offset:352\n\t" \
    "s_mov_b32 %[st], 0x10000\n\t" \
    "s_waitcnt lgkmcnt(0)\n\t" \
    "v_mul_f32 v22, v22, %[cs]\n\t" \
    "v_mul_f32_e64 v22, -v22, v23\n\t" \
    "5:\n\t" \
    "s_waitcnt vmcnt(0)\n\t" \
    "v_max3_u32 v24, v16, v17, v18\n\t" \
    "v_max_u32 v24, v24, v19\n\t" \
    "v_cmp_eq_u32 vcc, -1, v24\n\t" \
    "v_permlane32_swap_b32 v16, v18\n\t" \
    "v_permlane32_swap_b32 v17, v19\n\t" \
    "v_pk_add_f32 v[16:17], v[16:17], v[18:19]\n\t" \
    "s_nop 0\n\t" \
    "s_cbranch_vccnz 6f\n\t" \
    "7:\n\t" \
    "v_permlane16_swap_b32 v16, v17\n\t" \
    "v_add_f32 v16, v16, v17\n\t" \
    "s_nop 1\n\t" \
    "v_add_f32_dpp v16, v16, v16 row_ror:8 row_mask:0xf bank_mask:0xf\n\t" \
    "v_fmac_f32 v22, v16, v23\n\t" \
    "v_cvt_pk_bf16_f32 v22, v22, v22\n\t" \
    "ds_write_b16 %[qo], v22\n\t" \
    "9:\n\t" \
    "s_waitcnt lgkmcnt(0)\n\t" \
    "s_barrier\n\t" \
    "ds_read_b128 v[16:19], %[qa] offset:0\n\t" \
    "ds_read_b128 v[20:23], %[qa] offset:64\n\t" \
    "ds_read_b128 v[24:27], %[qa] offset:128\n\t" \
    "ds_read_b128 v[28:31], %[qa] offset:192\n\t" \
    "ds_read_b128 v[32:35], %[qa] offset:256\n\t" \
    "ds_read_b128 v[36:39], %[qa] offset:320\n\t" \
    "s_cmp_eq_u32 %[cl], 0\n\t" \
    "s_cbranch_scc1 3f\n\t" \
    "global_store_dwordx4 %[go], v[56:59], %[ws] offset:0 sc1\n\t" \
    "global_store_dwordx4 %[go], v[56:59], %[ws] offset:1024 sc1\n\t" \
    "global_store_dwordx4 %[go], v[56:59], %[ws] offset:2048 sc1\n\t" \
    "3:\n\t" \
    "s_waitcnt lgkmcnt(5)\n\t" \
    "v_mfma_f32_16x16x32_bf16 v[40:43], v[16:19], v[64:67], 0\n\t" \
    "v_mfma_f32_16x16x32_bf16 v[44:47], v[16:19], v[88:91], 0\n\t" \
    "v_mfma_f32_16x16x32_bf16 v[48:51], v[16:19], v[112:115], 0\n\t" \
    "v_mfma_f32_16x16x32_bf16 v[52:55], v[16:19], v[136:139], 0\n\t" \
    "s_waitcnt lgkmcnt(4)\n\t" \
    "v_mfma_f32_16x16x32_bf16 v[40:43], v[20:23], v[76:79], v[40:43]\n\t" \
    "v_mfma_f32_16x16x32_bf16 v[44:47], v[20:23], v[100:103], v[44:47]\n\t" \
    "v_mfma_f32_16x16x32_bf16 v[48:51], v[20:23], v[124:127], v[48:51]\n\t" \
    "v_mfma_f32_16x16x32_bf16 v[52:55], v[20:23], v[148:151], v[52:55]\n\t" \
    "s_waitcnt lgkmcnt(3)\n\t" \
    "v_mfma_f32_16x16x32_bf16 v[40:43], v[24:27], v[68:71], v[40:43]\n\t" \
    "v_mfma_f32_16x16x32_bf16 v[44:47], v[24:27], v[92:95], v[44:47]\n\t" \
    "v_mfma_f32_16x16x32_bf16 v[48:51], v[24:27], v[116:119], v[48:51]\n\t" \
    "v_mfma_f32_16x16x32_bf16 v[52:55], v[24:27], v[140:143], v[52:55]\n\t" \
    "s_waitcnt lgkmcnt(2)\n\t" \
    "v_mfma_f32_16x16x32_bf16 v[40:43], v[28:31], v[80:83], v[40:43]\n\t" \
    "v_mfma_f32_16x16x32_bf16 v[44:47], v[28:31], v[104:107], v[44:47]\n\t" \
    "v_mfma_f32_16x16x32_bf16 v[48:51], v[28:31], v[128:131], v[48:51]\n\t" \
    "v_mfma_f32_16x16x32_bf16 v[52:55], v[28:31], v[152:155], v[52:55]\n\t" \
    "s_waitcnt lgkmcnt(1)\n\t" \
    "v_mfma_f32_16x16x32_bf16 v[40:43], v[32:35], v[72:75], v[40:43]\n\t" \
    "v_mfma_f32_16x16x32_bf16 v[44:47], v[32:35], v[96:99], v[44:47]\n\t" \
    "v_mfma_f32_16x16x32_bf16 v[48:51], v[32:35], v[120:123], v[48:51]\n\t" \
    "v_mfma_f32_16x16x32_bf16 v[52:55], v[32:35], v[144:147], v[52:55]\n\t" \
    "s_waitcnt lgkmcnt(0)\n\t" \
    "v_mfma_f32_16x16x32_bf16 v[40:43], v[36:39], v[84:87], v[40:43]\n\t" \
    "v_mfma_f32_16x16x32_bf16 v[44:47], v[36:39], v[108:111], v[44:47]\n\t" \
    "v_mfma_f32_16x16x32_bf16 v[48:51], v[36:39], v[132:135], v[48:51]\n\t" \
    "v_mfma_f32_16x16x32_bf16 v[52:55], v[36:39], v[156:159], v[52:55]\n\t" \
    "s_nop 5\n\t" \
    "v_cndmask_b32_e64 v60, v40, v44, %[mo_]\n\t" \
    "s_nop 0\n\t" \
    "v_cndmask_b32_e64 v61, v48, v52, %[mo_]\n\t" \
    "v_cndmask_b32_e64 v60, v60, v61, %[mh_]\n\t" \
    "v_mul_f32 v60, 0x3dd53b94, v60\n\t" \
    "v_mov_b32 v63, %[mlw]\n\t" \
    "s_nop 0\n\t" \
    "v_max_f32_dpp v61, v60, v60 quad_perm:[1,0,3,2] row_mask:0xf bank_mask:0xf\n\t" \
    "s_nop 1\n\t" \
    "v_max_f32_dpp v61, v61, v61 quad_perm:[2,3,0,1] row_mask:0xf bank_mask:0xf\n\t" \
    "s_nop 1\n\t" \
    "v_max_f32_dpp v61, v61, v61 row_half_mirror row_mask:0xf bank_mask:0xf\n\t" \
    "s_nop 1\n\t" \
    "v_max_f32_dpp v61, v61, v61 row_mirror row_mask:0xf bank_mask:0xf\n\t" \
    "s_nop 1\n\t" \
    "v_max_f32_dpp v61, v61, v61 row_bcast:15 row_mask:0xa bank_mask:0xf\n\t" \
    "s_nop 1\n\t" \
    "v_max_f32_dpp v61, v61, v61 row_bcast:31 row_mask:0xc bank_mask:0xf\n\t" \
    "s_nop 0\n\t" \
    "v_readlane_b32 %[st], v61, 63\n\t" \
    "s_mov_b32 exec_lo, 0\n\t" \
    "s_brev_b32 exec_hi, 1\n\t" \
    "ds_write_b32 v63, v61\n\t" \
    "s_mov_b64 exec, -1\n\t" \
    "v_subrev_f32 v62, %[st], v60\n\t" \
    "v_exp_f32 v62, v62\n\t" \
    "s_nop 0\n\t" \
    "v_cvt_pk_bf16_f32 v60, v62, v62\n\t" \
    "ds_write_b16 %[pw], v60\n\t" \
    "ds_read_b128 v[16:19], %[pr] offset:0\n\t" \
    "ds_read_b128 v[20:23], %[pr] offset:192\n\t" \
    "ds_read_b128 v[24:27], %[pr] offset:384\n\t" \
    "ds_read_b128 v[28:31], %[pr] offset:576\n\t" \
    "ds_read_b128 v[32:35], %[pr] offset:768\n\t" \
    "ds_read_b128 v[36:39], %[pr] offset:960\n\t" \
    "ds_read_b128 v[40:43], %[pr] offset:1152\n\t" \
    "ds_read_b128 v[44:47], %[pr] offset:1344\n\t" \
    "v_mov_b32 v60, %[ma]\n\t" \
    "s_setprio 0\n\t" \
    "s_waitcnt lgkmcnt(7)\n\t" \
    "s_barrier\n\t" \
    "v_mfma_f32_16x16x32_bf16 v[48:51], v[160:163], v[16:19], 0\n\t" \
    "v_mfma_f32_16x16x32_bf16 v[52:55], v[192:195], v[16:19], 0\n\t" \
    "v_mfma_f32_16x16x32_bf16 v[56:59], v[224:227], v[16:19], 0\n\t" \
    "v_add_f32_dpp v62, v62, v62 quad_perm:[1,0,3,2] row_mask:0xf bank_mask:0xf\n\t" \
    "s_waitcnt lgkmcnt(6)\n\t" \
    "v_mfma_f32_16x16x32_bf16 v[48:51], v[164:167], v[20:23], v[48:51]\n\t" \
    "v_mfma_f32_16x16x32_bf16 v[52:55], v[196:199], v[20:23], v[52:55]\n\t" \
    "v_mfma_f32_16x16x32_bf16 v[56:59], v[228:231], v[20:23], v[56:59]\n\t" \
    "v_add_f32_dpp v62, v62, v62 quad_perm:[2,3,0,1] row_mask:0xf bank_mask:0xf\n\t" \
    "s_waitcnt lgkmcnt(5)\n\t" \
    "v_mfma_f32_16x16x32_bf16 v[48:51], v[168:171], v[24:27], v[48:51]\n\t" \
    "v_mfma_f32_16x16x32_bf16 v[52:55], v[200:203], v[24:27], v[52:55]\n\t" \
    "v_mfma_f32_16x16x32_bf16 v[56:59], v[232:235], v[24:27], v[56:59]\n\t" \
    "v_add_f32_dpp v62, v62, v62 row_half_mirror row_mask:0xf bank_mask:0xf\n\t" \
    "s_waitcnt lgkmcnt(4)\n\t" \
    "v_mfma_f32_16x16x32_bf16 v[48:51], v[172:175], v[28:31], v[48:51]\n\t" \
    "v_mfma_f32_16x16x32_bf16 v[52:55], v[204:207], v[28:31], v[52:55]\n\t" \
    "v_mfma_f32_16x16x32_bf16 v[56:59], v[236:239], v[28:31], v[56:59]\n\t" \
    "v_add_f32_dpp v62, v62, v62 row_mirror row_mask:0xf bank_mask:0xf\n\t" \
    "s_waitcnt lgkmcnt(3)\n\t" \
    "v_mfma_f32_16x16x32_bf16 v[48:51], v[176:179], v[32:35], v[48:51]\n\t" \
    "v_mfma_f32_16x16x32_bf16 v[52:55], v[208:211], v[32:35], v[52:55]\n\t" \
    "v_mfma_f32_16x16x32_bf16 v[56:59], v[240:243], v[32:35], v[56:59]\n\t" \
    "v_add_f32_dpp v62, v62, v62 row_bcast:15 row_mask:0xa bank_mask:0xf\n\t" \
    "s_waitcnt lgkmcnt(2)\n\t" \
    "v_mfma_f32_16x16x32_bf16 v[48:51], v[180:183], v[36:39], v[48:51]\n\t" \
    "v_mfma_f32_16x16x32_bf16 v[52:55], v[212:215], v[36:39], v[52:55]\n\t" \
    "v_mfma_f32_16x16x32_bf16 v[56:59], v[244:247], v[36:39], v[56:59]\n\t" \
    "v_add_f32_dpp v62, v62, v62 row_bcast:31 row_mask:0xc bank_mask:0xf\n\t" \
    "s_waitcnt lgkmcnt(1)\n\t" \
    "v_mfma_f32_16x16x32_bf16 v[48:51], v[184:187], v[40:43], v[48:51]\n\t" \
    "v_mfma_f32_16x16x32_bf16 v[52:55], v[216:219], v[40:43], v[52:55]\n\t" \
    "v_mfma_f32_16x16x32_bf16 v[56:59], v[248:251], v[40:43], v[56:59]\n\t" \
    "s_waitcnt lgkmcnt(0)\n\t" \
    "v_mfma_f32_16x16x32_bf16 v[48:51], v[188:191], v[44:47], v[48:51]\n\t" \
    "v_mfma_f32_16x16x32_bf16 v[52:55], v[220:223], v[44:47], v[52:55]\n\t" \
    "v_mfma_f32_16x16x32_bf16 v[56:59], v[252:255], v[44:47], v[56:59]\n\t" \
    "s_mov_b32 exec_lo, 0\n\t" \
    "s_brev_b32 exec_hi, 1\n\t" \
    "ds_write_b32 v63, v62 offset:32\n\t" \
    "s_mov_b64 exec, -1\n\t" \
    "s_cmp_gt_u32 %[wv], 5\n\t" \
    "s_nop 1\n\t" \
    "v_mov_b32_dpp v48, v52 quad_perm:[0,1,2,3] row_mask:0xf bank_mask:0xc\n\t" \
    "v_mov_b32_dpp v49, v53 quad_perm:[0,1,2,3] row_mask:0xf bank_mask:0xc\n\t" \
    "v_mov_b32_dpp v50, v54 quad_perm:[0,1,2,3] row_mask:0xf bank_mask:0xc\n\t" \
    "v_mov_b32_dpp v51, v55 quad_perm:[0,1,2,3] row_mask:0xf bank_mask:0xc\n\t" \
    "ds_write_b128 %[ow], v[48:51] offset:0\n\t" \
    "s_mov_b32 exec_lo, 0x00ff00ff\n\t" \
    "s_mov_b32 exec_hi, 0xff00ff00\n\t" \
    "ds_write_b128 %[ow], v[56:59] offset:2048\n\t" \
    "s_mov_b64 exec, -1\n\t" \
    "s_cbranch_scc1 1f\n\t" \
    "s_waitcnt lgkmcnt(0)\n\t" \
    "s_barrier\n\t" \
    "ds_read_b128 v[16:19], %[r0]\n\t" \
    "ds_read_b128 v[20:23], %[r0] offset:640\n\t" \
    "ds_read_b32 v60, %[mo] offset:384\n\t" \
    "s_waitcnt lgkmcnt(1)\n\t" \
    "v_pk_add_f32 v[16:17], v[16:17], v[20:21]\n\t" \
    "v_pk_add_f32 v[18:19], v[18:19], v[22:23]\n\t" \
    "s_waitcnt lgkmcnt(0)\n\t" \
    "v_pk_mul_f32 v[16:17], v[16:17], v[60:61] op_sel_hi:[1,0]\n\t" \
    "v_pk_mul_f32 v[18:19], v[18:19], v[60:61] op_sel_hi:[1,0]\n\t" \
    "s_nop 1\n\t" \
    "v_permlane32_swap_b32 v16, v18\n\t" \
    "v_permlane32_swap_b32 v17, v19\n\t" \
    "v_pk_add_f32 v[16:17], v[16:17], v[18:19]\n\t" \
    "s_nop 1\n\t" \
    "v_permlane16_swap_b32 v16, v17\n\t" \
    "v_add_f32 v16, v16, v17\n\t" \
    "s_nop 1\n\t" \
    "v_add_f32_dpp v17, v16, v16 row_ror:8 row_mask:0xf bank_mask:0xf\n\t" \
    "v_add_f32_dpp v16, v16, v16 row_ror:8 row_mask:0xf bank_mask:0xf\n\t" \
    "s_nop 1\n\t" \
    "v_permlane16_swap_b32 v16, v17\n\t" \
    "s_mov_b32 exec_lo, 0xff\n\t" \
    "v_cvt_pk_bf16_f32 v16, v16, v17\n\t" \
    "s_mov_b32 exec_hi, 0xff\n\t" \
    "v_min_u32 v16, -2, v16\n\t" \
    "global_store_dword %[so], v16, %[ws] sc1\n\t" \
    "s_mov_b64 exec, -1\n\t" \
    "s_branch 2f\n\t" \
    "6:\n\t" \
    "s_sub_u32 %[st], %[st], 1\n\t" \
    "s_cbranch_scc1 7b\n\t" \
    RETRY_ASM \
    "v_mov_b32 v16, 0\n\t" \
    "v_mov_b32 v17, 0\n\t" \
    "v_mov_b32 v18, 0\n\t" \
    "v_mov_b32 v19, 0\n\t" \
    "s_mov_b32 exec_hi, 0xff\n\t" \
    "global_load_dwordx4 v[16:19], %[go], %[ws] sc1\n\t" \
    "s_mov_b32 exec_hi, -1\n\t" \
    "s_branch 5b\n\t" \
    "1:\n\t" \
    "s_cmp_eq_u32 %[wv], 7\n\t" \
    "s_waitcnt vmcnt(0) lgkmcnt(0)\n\t" \
    "s_barrier\n\t" \
    "s_cbranch_scc1 2f\n\t" \
    "ds_read_b32 v16, %[mo] offset:32\n\t" \
    "ds_read_b32 v60, %[mo] offset:384\n\t" \
    "ds_read_b32 v26, %[mo] offset:416\n\t" \
    "s_waitcnt lgkmcnt(0)\n\t" \
    "v_min_u32 v26, -2, v26\n\t" \
    "v_mul_f32 v16, v16, v60\n\t" \
    "s_nop 1\n\t" \
    "v_add_f32_dpp v17, v16, v16 row_ror:8 row_mask:0xf bank_mask:0xf\n\t" \
    "v_add_f32_dpp v16, v16, v16 row_ror:8 row_mask:0xf bank_mask:0xf\n\t" \
    "s_nop 1\n\t" \
    "v_permlane16_swap_b32 v16, v17\n\t" \
    "v_add_f32 v18, v16, v17\n\t" \
    "v_add_f32 v16, v16, v17\n\t" \
    "s_nop 1\n\t" \
    "v_permlane32_swap_b32 v16, v18\n\t" \
    "s_mov_b64 exec, 3\n\t" \
    "v_add_f32 v27, v16, v18\n\t" \
    "v_min_u32 v27, -2, v27\n\t" \
    "global_store_dwordx2 %[so], v[26:27], %[ws] sc1\n\t" \
    "s_mov_b64 exec, -1\n\t" \
    "2:\n\t"
#define ASM_QP \
    "s_mov_b32 %[st], 0x10000\n\t" \
    "5:\n\t" \
    "ds_read_b64_tr_b16 v[16:17], %[a0] offset:0\n\t" \
    "ds_read_b64_tr_b16 v[18:19], %[a0] offset:1536\n\t" \
    "ds_read_b64_tr_b16 v[20:21], %[a1] offset:0\n\t" \
    "ds_read_b64_tr_b16 v[22:23], %[a1] offset:1536\n\t" \
    "ds_read_b64_tr_b16 v[24:25], %[a0] offset:12288\n\t" \
    "ds_read_b64_tr_b16 v[26:27], %[a0] offset:13824\n\t" \
    "ds_read_b64_tr_b16 v[28:29], %[a1] offset:12288\n\t" \
    "ds_read_b64_tr_b16 v[30:31], %[a1] offset:13824\n\t" \
    "ds_read_b64_tr_b16 v[32:33], %[a0] offset:24576\n\t" \
    "ds_read_b64_tr_b16 v[34:35], %[a0] offset:26112\n\t" \
    "ds_read_b64_tr_b16 v[36:37], %[a1] offset:24576\n\t" \
    "ds_read_b64_tr_b16 v[38:39], %[a1] offset:26112\n\t" \
    "ds_read_b64_tr_b16 v[40:41], %[a0] offset:36864\n\t" \
    "ds_read_b64_tr_b16 v[42:43], %[a0] offset:38400\n\t" \
    "ds_read_b64_tr_b16 v[44:45], %[a1] offset:36864\n\t" \
    "ds_read_b64_tr_b16 v[46:47], %[a1] offset:38400\n\t" \
    "v_mov_b32 v48, %[fl]\n\t" \
    "v_add_u32 v50, -52, v48\n\t" \
    "s_mov_b32 vcc_hi, 0x100000\n\t" \
    "s_branch 25f\n\t" \
    "24:\n\t" \
    "s_sleep 4\n\t" \
    "25:\n\t" \
    "ds_read_b32 v49, v50\n\t" \
    "s_sub_u32 vcc_hi, vcc_hi, 1\n\t" \
    "s_cbranch_scc1 23f\n\t" \
    "s_waitcnt lgkmcnt(0)\n\t" \
    "v_readfirstlane_b32 %[rf], v49\n\t" \
    "s_cmp_eq_u32 %[rf], 0\n\t" \
    "s_cbranch_scc1 24b\n\t" \
    "s_add_u32 %[rf], %[rf], %[tz]\n\t" \
    "s_branch 22f\n\t" \
    "21:\n\t" \
    "s_sleep 3\n\t" \
    "22:\n\t" \
    "s_memrealtime vcc\n\t" \
    "s_waitcnt lgkmcnt(0)\n\t" \
    "s_sub_i32 vcc_lo, vcc_lo, %[rf]\n\t" \
    "s_cmp_lt_i32 vcc_lo, 0\n\t" \
    "s_cbranch_scc1 21b\n\t" \
    "23:\n\t" \
    "s_mov_b32 vcc_hi, 0x400000\n\t" \
    "0:\n\t" \
    "ds_read_b32 v49, v48\n\t" \
    "s_sub_u32 vcc_hi, vcc_hi, 1\n\t" \
    "s_cbranch_scc1 10f\n\t" \
    "s_waitcnt lgkmcnt(0)\n\t" \
    "v_readfirstlane_b32 %[rf], v49\n\t" \
    "s_cmp_eq_u32 %[rf], 0\n\t" \
    "s_cbranch_scc1 0b\n\t" \
    "4:\n\t" \
    "s_memrealtime vcc\n\t" \
    "s_waitcnt lgkmcnt(0)\n\t" \
    "s_sub_i32 vcc_lo, vcc_lo, %[rf]\n\t" \
    "s_cmp_lt_i32 vcc_lo, 0\n\t" \
    "s_cbranch_scc1 4b\n\t" \
    "10:\n\t" \
    "global_load_dwordx4 v[48:51], %[gs], %[ws] sc1\n\t" \
    "s_waitcnt vmcnt(0)\n\t" \
    "v_mfma_f32_16x16x32_bf16 v[16:19], v[16:19], v[48:51], 0\n\t" \
    "v_mfma_f32_16x16x32_bf16 v[20:23], v[20:23], v[48:51], 0\n\t" \
    "v_mov_b32_dpp v52, v48 row_shl:4 row_mask:0xf bank_mask:0xf\n\t" \
    "v_mov_b32_dpp v53, v49 row_shl:4 row_mask:0xf bank_mask:0xf\n\t" \
    "v_mov_b32_dpp v54, v50 row_shl:4 row_mask:0xf bank_mask:0xf\n\t" \
    "v_mov_b32_dpp v55, v51 row_shl:4 row_mask:0xf bank_mask:0xf\n\t" \
    "v_mov_b32_dpp v56, v48 row_shl:8 row_mask:0xf bank_mask:0xf\n\t" \
    "v_mov_b32_dpp v57, v49 row_shl:8 row_mask:0xf bank_mask:0xf\n\t" \
    "v_mfma_f32_16x16x32_bf16 v[16:19], v[24:27], v[52:55], v[16:19]\n\t" \
    "v_mov_b32_dpp v58, v50 row_shl:8 row_mask:0xf bank_mask:0xf\n\t" \
    "v_mov_b32_dpp v59, v51 row_shl:8 row_mask:0xf bank_mask:0xf\n\t" \
    "v_mfma_f32_16x16x32_bf16 v[20:23], v[28:31], v[52:55], v[20:23]\n\t" \
    "v_mov_b32_dpp v52, v48 row_shl:12 row_mask:0xf bank_mask:0xf\n\t" \
    "v_mov_b32_dpp v53, v49 row_shl:12 row_mask:0xf bank_mask:0xf\n\t" \
    "v_mfma_f32_16x16x32_bf16 v[16:19], v[32:35], v[56:59], v[16:19]\n\t" \
    "v_mov_b32_dpp v54, v50 row_shl:12 row_mask:0xf bank_mask:0xf\n\t" \
    "v_mov_b32_dpp v55, v51 row_shl:12 row_mask:0xf bank_mask:0xf\n\t" \
    "v_mfma_f32_16x16x32_bf16 v[20:23], v[36:39], v[56:59], v[20:23]\n\t" \
    "v_max3_u32 v24, v48, v49, v50\n\t" \
    "v_max_u32 v24, v24, v51\n\t" \
    "v_mfma_f32_16x16x32_bf16 v[16:19], v[40:43], v[52:55], v[16:19]\n\t" \
    "v_cmp_eq_u32 vcc, -1, v24\n\t" \
    "v_mfma_f32_16x16x32_bf16 v[20:23], v[44:47], v[52:55], v[20:23]\n\t" \
    "s_mov_b32 exec_lo, 0x00ff00ff\n\t" \
    "s_mov_b32 exec_hi, 0x00ff00ff\n\t" \
    "s_cbranch_vccnz 6f\n\t" \
    "s_nop 4\n\t" \
    "v_mov_b32_dpp v16, v20 row_shr:4 row_mask:0xf bank_mask:0x2\n\t" \
    "v_mov_b32_dpp v17, v21 row_shr:4 row_mask:0xf bank_mask:0x2\n\t" \
    "v_mov_b32_dpp v18, v22 row_shr:4 row_mask:0xf bank_mask:0x2\n\t" \
    "v_mov_b32_dpp v19, v23 row_shr:4 row_mask:0xf bank_mask:0x2\n\t" \
    "v_min_u32 v16, -2, v16\n\t" \
    "v_min_u32 v17, -2, v17\n\t" \
    "v_min_u32 v18, -2, v18\n\t" \
    "v_min_u32 v19, -2, v19\n\t" \
    "global_store_dwordx4 %[so], v[16:19], %[ws] sc1\n\t" \
    "s_mov_b64 exec, -1\n\t" \
    "s_branch 2f\n\t" \
    "6:\n\t" \
    "s_mov_b64 exec, -1\n\t" \
    "s_sub_u32 %[st], %[st], 1\n\t" \
    "s_cbranch_scc1 2f\n\t" \
    RETRY_ASM \
    "s_sleep 6\n\t" \
    "s_branch 5b\n\t" \
    "2:\n\t"
#define ASM_ATT_P \
    "s_setprio 2\n\t" \
    "v_mov_b32 v16, 0\n\t" \
    "v_mov_b32 v17, 0\n\t" \
    "v_mov_b32 v18, 0\n\t" \
    "v_mov_b32 v19, 0\n\t" \
    "v_mov_b32 v20, %[ma]\n\t" \
    "s_mov_b32 vcc_hi, 0x400000\n\t" \
    "0:\n\t" \
    "ds_read_b32 v21, v20 offset:340\n\t" \
    "s_sub_u32 vcc_hi, vcc_hi, 1\n\t" \
    "s_cbranch_scc1 4f\n\t" \
    "s_waitcnt lgkmcnt(0)\n\t" \
    "v_readfirstlane_b32 %[rf], v21\n\t" \
    "s_cmp_eq_u32 %[rf], 0\n\t" \
    "s_cbranch_scc1 0b\n\t" \
    "s_add_u32 %[st], %[rf], %[tg]\n\t" \
    "4:\n\t" \
    "s_memrealtime vcc\n\t" \
    "s_waitcnt lgkmcnt(0)\n\t" \
    "s_sub_i32 vcc_lo, vcc_lo, %[st]\n\t" \
    "s_cmp_lt_i32 vcc_lo, -4000\n\t" \
    "s_cbranch_scc1 10f\n\t" \
    "s_cmp_lt_i32 vcc_lo, 0\n\t" \
    "s_cbranch_scc1 4b\n\t" \
    "10:\n\t" \
    "s_mov_b32 exec_hi, 0xff\n\t" \
    "global_load_dwordx4 v[16:19], %[go], %[ws] sc1\n\t" \
    "s_mov_b32 exec_hi, -1\n\t" \
    "s_barrier\n\t" \
    "ds_read_b64 v[22:23], v20 offset:352\n\t" \
    "s_mov_b32 %[st], 0x10000\n\t" \
    "s_waitcnt lgkmcnt(0)\n\t" \
    "v_mul_f32 v22, v22, %[cs]\n\t" \
    "v_mul_f32_e64 v22, -v22, v23\n\t" \
    "5:\n\t" \
    "s_waitcnt vmcnt(0)\n\t" \
    "v_max3_u32 v24, v16, v17, v18\n\t" \
    "v_max_u32 v24, v24, v19\n\t" \
    "v_cmp_eq_u32 vcc, -1, v24\n\t" \
    "v_permlane32_swap_b32 v16, v18\n\t" \
    "v_permlane32_swap_b32 v17, v19\n\t" \
    "v_pk_add_f32 v[16:17], v[16:17], v[18:19]\n\t" \
    "s_nop 0\n\t" \
    "s_cbranch_vccnz 6f\n\t" \
    "7:\n\t" \
    "v_permlane16_swap_b32 v16, v17\n\t" \
    "v_add_f32 v16, v16, v17\n\t" \
    "s_nop 1\n\t" \
    "v_add_f32_dpp v16, v16, v16 row_ror:8 row_mask:0xf bank_mask:0xf\n\t" \
    "v_fmac_f32 v22, v16, v23\n\t" \
    "v_cvt_pk_bf16_f32 v22, v22, v22\n\t" \
    "ds_write_b16 %[qo], v22\n\t" \
    "s_waitcnt lgkmcnt(0)\n\t" \
    "s_barrier\n\t" \
    "ds_read_b128 v[16:19], %[qa] offset:0\n\t" \
    "ds_read_b128 v[20:23], %[qa] offset:64\n\t" \
    "ds_read_b128 v[24:27], %[qa] offset:128\n\t" \
    "ds_read_b128 v[28:31], %[qa] offset:192\n\t" \
    "ds_read_b128 v[32:35], %[qa] offset:256\n\t" \
    "ds_read_b128 v[36:39], %[qa] offset:320\n\t" \
    "s_waitcnt lgkmcnt(5)\n\t" \
    "v_mfma_f32_16x16x32_bf16 v[40:43], v[16:19], v[64:67], 0\n\t" \
    "v_mfma_f32_16x16x32_bf16 v[44:47], v[16:19], v[88:91], 0\n\t" \
    "v_mfma_f32_16x16x32_bf16 v[48:51], v[16:19], v[112:115], 0\n\t" \
    "v_mfma_f32_16x16x32_bf16 v[52:55], v[16:19], v[136:139], 0\n\t" \
    "s_waitcnt lgkmcnt(4)\n\t" \
    "v_mfma_f32_16x16x32_bf16 v[40:43], v[20:23], v[76:79], v[40:43]\n\t" \
    "v_mfma_f32_16x16x32_bf16 v[44:47], v[20:23], v[100:103], v[44:47]\n\t" \
    "v_mfma_f32_16x16x32_bf16 v[48:51], v[20:23], v[124:127], v[48:51]\n\t" \
    "v_mfma_f32_16x16x32_bf16 v[52:55], v[20:23], v[148:151], v[52:55]\n\t" \
    "s_waitcnt lgkmcnt(3)\n\t" \
    "v_mfma_f32_16x16x32_bf16 v[40:43], v[24:27], v[68:71], v[40:43]\n\t" \
    "v_mfma_f32_16x16x32_bf16 v[44:47], v[24:27], v[92:95], v[44:47]\n\t" \
    "v_mfma_f32_16x16x32_bf16 v[48:51], v[24:27], v[116:119], v[48:51]\n\t" \
    "v_mfma_f32_16x16x32_bf16 v[52:55], v[24:27], v[140:143], v[52:55]\n\t" \
    "s_waitcnt lgkmcnt(2)\n\t" \
    "v_mfma_f32_16x16x32_bf16 v[40:43], v[28:31], v[80:83], v[40:43]\n\t" \
    "v_mfma_f32_16x16x32_bf16 v[44:47], v[28:31], v[104:107], v[44:47]\n\t" \
    "v_mfma_f32_16x16x32_bf16 v[48:51], v[28:31], v[128:131], v[48:51]\n\t" \
    "v_mfma_f32_16x16x32_bf16 v[52:55], v[28:31], v[152:155], v[52:55]\n\t" \
    "s_waitcnt lgkmcnt(1)\n\t" \
    "v_mfma_f32_16x16x32_bf16 v[40:43], v[32:35], v[72:75], v[40:43]\n\t" \
    "v_mfma_f32_16x16x32_bf16 v[44:47], v[32:35], v[96:99], v[44:47]\n\t" \
    "v_mfma_f32_16x16x32_bf16 v[48:51], v[32:35], v[120:123], v[48:51]\n\t" \
    "v_mfma_f32_16x16x32_bf16 v[52:55], v[32:35], v[144:147], v[52:55]\n\t" \
    "s_waitcnt lgkmcnt(0)\n\t" \
    "v_mfma_f32_16x16x32_bf16 v[40:43], v[36:39], v[84:87], v[40:43]\n\t" \
    "v_mfma_f32_16x16x32_bf16 v[44:47], v[36:39], v[108:111], v[44:47]\n\t" \
    "v_mfma_f32_16x16x32_bf16 v[48:51], v[36:39], v[132:135], v[48:51]\n\t" \
    "v_mfma_f32_16x16x32_bf16 v[52:55], v[36:39], v[156:159], v[52:55]\n\t" \
    "s_nop 5\n\t" \
    "v_cndmask_b32_e64 v60, v40, v44, %[mo_]\n\t" \
    "s_nop 0\n\t" \
    "v_cndmask_b32_e64 v61, v48, v52, %[mo_]\n\t" \
    "v_cndmask_b32_e64 v60, v60, v61, %[mh_]\n\t" \
    "v_mul_f32 v60, 0x3dd53b94, v60\n\t" \
    "v_mov_b32 v63, %[mlw]\n\t" \
    "s_nop 0\n\t" \
    "v_max_f32_dpp v61, v60, v60 quad_perm:[1,0,3,2] row_mask:0xf bank_mask:0xf\n\t" \
    "s_nop 1\n\t" \
    "v_max_f32_dpp v61, v61, v61 quad_perm:[2,3,0,1] row_mask:0xf bank_mask:0xf\n\t" \
    "s_nop 1\n\t" \
    "v_max_f32_dpp v61, v61, v61 row_half_mirror row_mask:0xf bank_mask:0xf\n\t" \
    "s_nop 1\n\t" \
    "v_max_f32_dpp v61, v61, v61 row_mirror row_mask:0xf bank_mask:0xf\n\t" \
    "s_nop 1\n\t" \
    "v_max_f32_dpp v61, v61, v61 row_bcast:15 row_mask:0xa bank_mask:0xf\n\t" \
    "s_nop 1\n\t" \
    "v_max_f32_dpp v61, v61, v61 row_bcast:31 row_mask:0xc bank_mask:0xf\n\t" \
    "s_nop 0\n\t" \
    "v_readlane_b32 %[st], v61, 63\n\t" \
    "s_mov_b32 exec_lo, 0\n\t" \
    "s_brev_b32 exec_hi, 1\n\t" \
    "ds_write_b32 v63, v61\n\t" \
    "s_mov_b64 exec, -1\n\t" \
    "v_subrev_f32 v62, %[st], v60\n\t" \
    "v_exp_f32 v62, v62\n\t" \
    "s_nop 0\n\t" \
    "v_cvt_pk_bf16_f32 v60, v62, v62\n\t" \
    "ds_write_b16 %[pw], v60\n\t" \
    "ds_read_b128 v[16:19], %[pr] offset:0\n\t" \
    "ds_read_b128 v[20:23], %[pr] offset:192\n\t" \
    "ds_read_b128 v[24:27], %[pr] offset:384\n\t" \
    "ds_read_b128 v[28:31], %[pr] offset:576\n\t" \
    "ds_read_b128 v[32:35], %[pr] offset:768\n\t" \
    "ds_read_b128 v[36:39], %[pr] offset:960\n\t" \
    "ds_read_b128 v[40:43], %[pr] offset:1152\n\t" \
    "ds_read_b128 v[44:47], %[pr] offset:1344\n\t" \
    "v_mov_b32 v60, %[ma]\n\t" \
    "s_setprio 0\n\t" \
    "s_waitcnt lgkmcnt(7)\n\t" \
    "v_mfma_f32_16x16x32_bf16 v[48:51], v[160:163], v[16:19], 0\n\t" \
    "v_mfma_f32_16x16x32_bf16 v[52:55], v[192:195], v[16:19], 0\n\t" \
    "v_mfma_f32_16x16x32_bf16 v[56:59], v[224:227], v[16:19], 0\n\t" \
    "v_add_f32_dpp v62, v62, v62 quad_perm:[1,0,3,2] row_mask:0xf bank_mask:0xf\n\t" \
    "s_waitcnt lgkmcnt(6)\n\t" \
    "v_mfma_f32_16x16x32_bf16 v[48:51], v[164:167], v[20:23], v[48:51]\n\t" \
    "v_mfma_f32_16x16x32_bf16 v[52:55], v[196:199], v[20:23], v[52:55]\n\t" \
    "v_mfma_f32_16x16x32_bf16 v[56:59], v[228:231], v[20:23], v[56:59]\n\t" \
    "v_add_f32_dpp v62, v62, v62 quad_perm:[2,3,0,1] row_mask:0xf bank_mask:0xf\n\t" \
    "s_waitcnt lgkmcnt(5)\n\t" \
    "v_mfma_f32_16x16x32_bf16 v[48:51], v[168:171], v[24:27], v[48:51]\n\t" \
    "v_mfma_f32_16x16x32_bf16 v[52:55], v[200:203], v[24:27], v[52:55]\n\t" \
    "v_mfma_f32_16x16x32_bf16 v[56:59], v[232:235], v[24:27], v[56:59]\n\t" \
    "v_add_f32_dpp v62, v62, v62 row_half_mirror row_mask:0xf bank_mask:0xf\n\t" \
    "s_waitcnt lgkmcnt(4)\n\t" \
    "v_mfma_f32_16x16x32_bf16 v[48:51], v[172:175], v[28:31], v[48:51]\n\t" \
    "v_mfma_f32_16x16x32_bf16 v[52:55], v[204:207], v[28:31], v[52:55]\n\t" \
    "v_mfma_f32_16x16x32_bf16 v[56:59], v[236:239], v[28:31], v[56:59]\n\t" \
    "v_add_f32_dpp v62, v62, v62 row_mirror row_mask:0xf bank_mask:0xf\n\t" \
    "s_waitcnt lgkmcnt(3)\n\t" \
    "v_mfma_f32_16x16x32_bf16 v[48:51], v[176:179], v[32:35], v[48:51]\n\t" \
    "v_mfma_f32_16x16x32_bf16 v[52:55], v[208:211], v[32:35], v[52:55]\n\t" \
    "v_mfma_f32_16x16x32_bf16 v[56:59], v[240:243], v[32:35], v[56:59]\n\t" \
    "v_add_f32_dpp v62, v62, v62 row_bcast:15 row_mask:0xa bank_mask:0xf\n\t" \
    "s_waitcnt lgkmcnt(2)\n\t" \
    "v_mfma_f32_16x16x32_bf16 v[48:51], v[180:183], v[36:39], v[48:51]\n\t" \
    "v_mfma_f32_16x16x32_bf16 v[52:55], v[212:215], v[36:39], v[52:55]\n\t" \
    "v_mfma_f32_16x16x32_bf16 v[56:59], v[244:247], v[36:39], v[56:59]\n\t" \
    "v_add_f32_dpp v62, v62, v62 row_bcast:31 row_mask:0xc bank_mask:0xf\n\t" \
    "s_waitcnt lgkmcnt(1)\n\t" \
    "v_mfma_f32_16x16x32_bf16 v[48:51], v[184:187], v[40:43], v[48:51]\n\t" \
    "v_mfma_f32_16x16x32_bf16 v[52:55], v[216:219], v[40:43], v[52:55]\n\t" \
    "v_mfma_f32_16x16x32_bf16 v[56:59], v[248:251], v[40:43], v[56:59]\n\t" \
    "s_waitcnt lgkmcnt(0)\n\t" \
    "v_mfma_f32_16x16x32_bf16 v[48:51], v[188:191], v[44:47], v[48:51]\n\t" \
    "v_mfma_f32_16x16x32_bf16 v[52:55], v[220:223], v[44:47], v[52:55]\n\t" \
    "v_mfma_f32_16x16x32_bf16 v[56:59], v[252:255], v[44:47], v[56:59]\n\t" \
    "s_barrier\n\t" \
    "ds_read_b128 v[16:19], v60\n\t" \
    "ds_read_b128 v[20:23], v60 offset:16\n\t" \
    "ds_read_b32 v24, %[mo]\n\t" \
    "s_mov_b32 exec_lo, 0\n\t" \
    "s_brev_b32 exec_hi, 1\n\t" \
    "ds_write_b32 v63, v62 offset:32\n\t" \
    "s_mov_b64 exec, -1\n\t" \
    "v_mov_b32_dpp v48, v52 quad_perm:[0,1,2,3] row_mask:0xf bank_mask:0xc\n\t" \
    "v_mov_b32_dpp v49, v53 quad_perm:[0,1,2,3] row_mask:0xf bank_mask:0xc\n\t" \
    "v_mov_b32_dpp v50, v54 quad_perm:[0,1,2,3] row_mask:0xf bank_mask:0xc\n\t" \
    "v_mov_b32_dpp v51, v55 quad_perm:[0,1,2,3] row_mask:0xf bank_mask:0xc\n\t" \
    "ds_write_b128 %[ow], v[48:51] offset:0\n\t" \
    "s_mov_b32 exec_lo, 0x00ff00ff\n\t" \
    "s_mov_b32 exec_hi, 0xff00ff00\n\t" \
    "ds_write_b128 %[ow], v[56:59] offset:2048\n\t" \
    "s_mov_b64 exec, -1\n\t" \
    "s_waitcnt lgkmcnt(3)\n\t" \
    "v_max3_f32 v16, v16, v17, v18\n\t" \
    "v_max3_f32 v19, v19, v20, v21\n\t" \
    "v_max3_f32 v16, v16, v22, v23\n\t" \
    "v_max_f32 v26, v16, v19\n\t" \
    "v_sub_f32 v60, v24, v26\n\t" \
    "v_exp_f32 v60, v60\n\t" \
    "s_nop 0\n\t" \
    "ds_write2_b32 %[mo], v60, v26 offset0:96 offset1:104\n\t" \
    "ds_read_b64_tr_b16 v[28:29], %[a0] offset:0\n\t" \
    "ds_read_b64_tr_b16 v[30:31], %[a0] offset:1024\n\t" \
    "ds_read_b64_tr_b16 v[32:33], %[a1] offset:8192\n\t" \
    "ds_read_b64_tr_b16 v[34:35], %[a1] offset:9216\n\t" \
    "ds_read_b64_tr_b16 v[36:37], %[a0] offset:8192\n\t" \
    "ds_read_b64_tr_b16 v[38:39], %[a0] offset:9216\n\t" \
    "ds_read_b64_tr_b16 v[40:41], %[a1] offset:0\n\t" \
    "ds_read_b64_tr_b16 v[42:43], %[a1] offset:1024\n\t" \
    "ds_read_b64_tr_b16 v[44:45], %[a0] offset:16384\n\t" \
    "ds_read_b64_tr_b16 v[46:47], %[a0] offset:17408\n\t" \
    "ds_read_b64_tr_b16 v[48:49], %[a1] offset:24576\n\t" \
    "ds_read_b64_tr_b16 v[50:51], %[a1] offset:25600\n\t" \
    "ds_read_b64_tr_b16 v[52:53], %[a0] offset:24576\n\t" \
    "ds_read_b64_tr_b16 v[54:55], %[a0] offset:25600\n\t" \
    "ds_read_b64_tr_b16 v[56:57], %[a1] offset:16384\n\t" \
    "ds_read_b64_tr_b16 v[58:59], %[a1] offset:17408\n\t" \
    "s_waitcnt lgkmcnt(0)\n\t" \
    "s_barrier\n\t" \
    "ds_read_b128 v[16:19], %[r0]\n\t" \
    "ds_read_b128 v[20:23], %[r0] offset:128\n\t" \
    "s_waitcnt lgkmcnt(0)\n\t" \
    "v_pk_add_f32 v[16:17], v[16:17], v[20:21]\n\t" \
    "v_pk_add_f32 v[18:19], v[18:19], v[22:23]\n\t" \
    "v_pk_mul_f32 v[16:17], v[16:17], v[60:61] op_sel_hi:[1,0]\n\t" \
    "v_pk_mul_f32 v[18:19], v[18:19], v[60:61] op_sel_hi:[1,0]\n\t" \
    "s_nop 1\n\t" \
    "v_permlane32_swap_b32 v16, v18\n\t" \
    "v_permlane32_swap_b32 v17, v19\n\t" \
    "v_pk_add_f32 v[16:17], v[16:17], v[18:19]\n\t" \
    "s_nop 1\n\t" \
    "v_permlane16_swap_b32 v16, v17\n\t" \
    "v_add_f32 v16, v16, v17\n\t" \
    "s_nop 1\n\t" \
    "v_add_f32_dpp v17, v16, v16 row_ror:8 row_mask:0xf bank_mask:0xf\n\t" \
    "v_add_f32_dpp v16, v16, v16 row_ror:8 row_mask:0xf bank_mask:0xf\n\t" \
    "s_nop 1\n\t" \
    "v_permlane16_swap_b32 v16, v17\n\t" \
    "s_mov_b32 exec_lo, 0xff\n\t" \
    "v_cvt_pk_bf16_f32 v16, v16, v17\n\t" \
    "s_mov_b32 exec_hi, 0xff\n\t" \
    "v_min_u32 v16, -2, v16\n\t" \
    "global_store_dword %[so], v16, %[ws] sc1\n\t" \
    "s_mov_b64 exec, -1\n\t" \
    "ds_read_b64_tr_b16 v[60:61], %[a0] offset:32768\n\t" \
    "ds_read_b64_tr_b16 v[62:63], %[a0] offset:33792\n\t" \
    "s_add_u32 %[rf], %[rf], %[t2]\n\t" \
    "s_mov_b32 %[st], 0x10000\n\t" \
    "14:\n\t" \
    "s_memrealtime vcc\n\t" \
    "s_waitcnt lgkmcnt(0)\n\t" \
    "s_sub_i32 vcc_lo, vcc_lo, %[rf]\n\t" \
    "s_cmp_lt_i32 vcc_lo, -4000\n\t" \
    "s_cbranch_scc1 15f\n\t" \
    "s_cmp_lt_i32 vcc_lo, 0\n\t" \
    "s_cbranch_scc1 14b\n\t" \
    "15:\n\t" \
    "global_load_dwordx4 v[8:11], %[gs], %[ws] offset:0 sc1\n\t" \
    "global_load_dwordx4 v[12:15], %[gs], %[ws] offset:1024 sc1\n\t" \
    "global_load_dwordx4 v[16:19], %[gs], %[ws] offset:2048 sc1\n\t" \
    "global_load_dwordx4 v[24:27], %[gm], %[ws] sc1\n\t" \
    "s_waitcnt vmcnt(3)\n\t" \
    "v_mfma_f32_16x16x32_bf16 v[28:31], v[28:31], v[8:11], 0\n\t" \
    "v_mov_b32_dpp v20, v8 row_half_mirror row_mask:0xf bank_mask:0xf\n\t" \
    "v_mov_b32_dpp v21, v9 row_half_mirror row_mask:0xf bank_mask:0xf\n\t" \
    "v_mfma_f32_16x16x32_bf16 v[32:35], v[32:35], v[8:11], 0\n\t" \
    "v_mov_b32_dpp v22, v10 row_half_mirror row_mask:0xf bank_mask:0xf\n\t" \
    "v_mov_b32_dpp v23, v11 row_half_mirror row_mask:0xf bank_mask:0xf\n\t" \
    "v_max3_u32 %[t0], v8, v9, v10\n\t" \
    "s_waitcnt vmcnt(2)\n\t" \
    "v_mfma_f32_16x16x32_bf16 v[28:31], v[36:39], v[20:23], v[28:31]\n\t" \
    "v_max3_u32 %[t0], %[t0], v11, v12\n\t" \
    "ds_read_b64_tr_b16 v[36:37], %[a1] offset:40960\n\t" \
    "v_mfma_f32_16x16x32_bf16 v[32:35], v[40:43], v[20:23], v[32:35]\n\t" \
    "ds_read_b64_tr_b16 v[38:39], %[a1] offset:41984\n\t" \
    "ds_read_b64_tr_b16 v[40:41], %[a0] offset:40960\n\t" \
    "v_mfma_f32_16x16x32_bf16 v[28:31], v[44:47], v[12:15], v[28:31]\n\t" \
    "v_mov_b32_dpp v20, v12 row_half_mirror row_mask:0xf bank_mask:0xf\n\t" \
    "v_mov_b32_dpp v21, v13 row_half_mirror row_mask:0xf bank_mask:0xf\n\t" \
    "v_mfma_f32_16x16x32_bf16 v[32:35], v[48:51], v[12:15], v[32:35]\n\t" \
    "v_mov_b32_dpp v22, v14 row_half_mirror row_mask:0xf bank_mask:0xf\n\t" \
    "v_mov_b32_dpp v23, v15 row_half_mirror row_mask:0xf bank_mask:0xf\n\t" \
    "ds_read_b64_tr_b16 v[42:43], %[a0] offset:41984\n\t" \
    "ds_read_b64_tr_b16 v[44:45], %[a1] offset:32768\n\t" \
    "v_mfma_f32_16x16x32_bf16 v[28:31], v[52:55], v[20:23], v[28:31]\n\t" \
    "ds_read_b64_tr_b16 v[46:47], %[a1] offset:33792\n\t" \
    "s_waitcnt vmcnt(0)\n\t" \
    "v_max3_u32 %[t0], %[t0], v13, v14\n\t" \
    "v_mfma_f32_16x16x32_bf16 v[32:35], v[56:59], v[20:23], v[32:35]\n\t" \
    "v_max3_u32 v8, v24, v25, v26\n\t" \
    "v_sub_f32 v24, v26, v24\n\t" \
    "v_mfma_f32_16x16x32_bf16 v[28:31], v[60:63], v[16:19], v[28:31]\n\t" \
    "v_mov_b32_dpp v20, v16 row_half_mirror row_mask:0xf bank_mask:0xf\n\t" \
    "v_mov_b32_dpp v21, v17 row_half_mirror row_mask:0xf bank_mask:0xf\n\t" \
    "s_waitcnt lgkmcnt(4)\n\t" \
    "v_mfma_f32_16x16x32_bf16 v[32:35], v[36:39], v[16:19], v[32:35]\n\t" \
    "v_mov_b32_dpp v22, v18 row_half_mirror row_mask:0xf bank_mask:0xf\n\t" \
    "v_mov_b32_dpp v23, v19 row_half_mirror row_mask:0xf bank_mask:0xf\n\t" \
    "v_exp_f32 v24, v24\n\t" \
    "v_max3_u32 %[t0], %[t0], v15, v16\n\t" \
    "s_waitcnt lgkmcnt(0)\n\t" \
    "v_mfma_f32_16x16x32_bf16 v[28:31], v[40:43], v[20:23], v[28:31]\n\t" \
    "v_fmac_f32 v25, v24, v27\n\t" \
    "v_max3_u32 v8, v8, v27, v17\n\t" \
    "v_mfma_f32_16x16x32_bf16 v[32:35], v[44:47], v[20:23], v[32:35]\n\t" \
    "v_rcp_f32 v24, v25\n\t" \
    "v_max3_u32 %[t0], %[t0], v18, v19\n\t" \
    "v_max_u32 v8, v8, %[t0]\n\t" \
    "v_cmp_eq_u32 vcc, -1, v8\n\t" \
    "v_mov_b32 v9, 0xff\n\t" \
    "s_nop 1\n\t" \
    "s_cbranch_vccnz 16f\n\t" \
    "v_mov_b32_dpp v28, v32 quad_perm:[0,1,2,3] row_mask:0xf bank_mask:0xa\n\t" \
    "v_mov_b32_dpp v29, v33 quad_perm:[0,1,2,3] row_mask:0xf bank_mask:0xa\n\t" \
    "v_mov_b32_dpp v30, v34 quad_perm:[0,1,2,3] row_mask:0xf bank_mask:0xa\n\t" \
    "v_mov_b32_dpp v31, v35 quad_perm:[0,1,2,3] row_mask:0xf bank_mask:0xa\n\t" \
    "v_pk_mul_f32 v[28:29], v[28:29], v[24:25] op_sel_hi:[1,0]\n\t" \
    "v_pk_mul_f32 v[30:31], v[30:31], v[24:25] op_sel_hi:[1,0]\n\t" \
    "s_nop 0\n\t" \
    "v_add_f32_dpp v28, v28, v28 row_shl:8 row_mask:0xf bank_mask:0xf\n\t" \
    "v_add_f32_dpp v29, v29, v29 row_shl:8 row_mask:0xf bank_mask:0xf\n\t" \
    "v_add_f32_dpp v30, v30, v30 row_shl:8 row_mask:0xf bank_mask:0xf\n\t" \
    "v_add_f32_dpp v31, v31, v31 row_shl:8 row_mask:0xf bank_mask:0xf\n\t" \
    "s_mov_b32 exec_lo, 0x00ff00ff\n\t" \
    "s_mov_b32 exec_hi, 0x00ff00ff\n\t" \
    "v_bfi_b32 v28, v9, %[tag], v28\n\t" \
    "v_bfi_b32 v29, v9, %[tag], v29\n\t" \
    "v_bfi_b32 v30, v9, %[tag], v30\n\t" \
    "v_bfi_b32 v31, v9, %[tag], v31\n\t" \
    "global_store_dwordx4 %[s2], v[28:31], %[ws] sc1\n\t" \
    "s_mov_b64 exec, -1\n\t" \
    "s_branch 2f\n\t" \
    "6:\n\t" \
    "s_sub_u32 %[st], %[st], 1\n\t" \
    "s_cbranch_scc1 7b\n\t" \
    RETRY_ASM \
    "v_mov_b32 v16, 0\n\t" \
    "v_mov_b32 v17, 0\n\t" \
    "v_mov_b32 v18, 0\n\t" \
    "v_mov_b32 v19, 0\n\t" \
    "s_mov_b32 exec_hi, 0xff\n\t" \
    "global_load_dwordx4 v[16:19], %[go], %[ws] sc1\n\t" \
    "s_mov_b32 exec_hi, -1\n\t" \
    "s_branch 5b\n\t" \
    "16:\n\t" \
    "s_mov_b64 exec, -1\n\t" \
    "s_sub_u32 %[st], %[st], 1\n\t" \
    "s_cbranch_scc1 2f\n\t" \
    RETRY_ASM3 \
    "s_sleep 3\n\t" \
    "ds_read_b64_tr_b16 v[28:29], %[a0] offset:0\n\t" \
    "ds_read_b64_tr_b16 v[30:31], %[a0] offset:1024\n\t" \
    "ds_read_b64_tr_b16 v[32:33], %[a1] offset:8192\n\t" \
    "ds_read_b64_tr_b16 v[34:35], %[a1] offset:9216\n\t" \
    "ds_read_b64_tr_b16 v[36:37], %[a0] offset:8192\n\t" \
    "ds_read_b64_tr_b16 v[38:39], %[a0] offset:9216\n\t" \
    "ds_read_b64_tr_b16 v[40:41], %[a1] offset:0\n\t" \
    "ds_read_b64_tr_b16 v[42:43], %[a1] offset:1024\n\t" \
    "ds_read_b64_tr_b16 v[44:45], %[a0] offset:16384\n\t" \
    "ds_read_b64_tr_b16 v[46:47], %[a0] offset:17408\n\t" \
    "ds_read_b64_tr_b16 v[48:49], %[a1] offset:24576\n\t" \
    "ds_read_b64_tr_b16 v[50:51], %[a1] offset:25600\n\t" \
    "ds_read_b64_tr_b16 v[52:53], %[a0] offset:24576\n\t" \
    "ds_read_b64_tr_b16 v[54:55], %[a0] offset:25600\n\t" \
    "ds_read_b64_tr_b16 v[56:57], %[a1] offset:16384\n\t" \
    "ds_read_b64_tr_b16 v[58:59], %[a1] offset:17408\n\t" \
    "ds_read_b64_tr_b16 v[60:61], %[a0] offset:32768\n\t" \
    "ds_read_b64_tr_b16 v[62:63], %[a0] offset:33792\n\t" \
    "s_waitcnt lgkmcnt(0)\n\t" \
    "s_branch 15b\n\t" \
    "2:\n\t"
#define ASM_OP \
    "s_mov_b32 %[st], 0x10000\n\t" \
    "5:\n\t" \
    "v_mov_b32 v8, %[fl]\n\t" \
    "s_mov_b32 %[ct], 0x400000\n\t" \
    "0:\n\t" \
    "ds_read_b32 v9, v8\n\t" \
    "s_memrealtime vcc\n\t" \
    "s_sub_u32 %[ct], %[ct], 1\n\t" \
    "s_waitcnt lgkmcnt(0)\n\t" \
    "s_cbranch_scc1 10f\n\t" \
    "v_readfirstlane_b32 %[rf], v9\n\t" \
    "s_cmp_eq_u32 %[rf], 0\n\t" \
    "s_cbranch_scc1 0b\n\t" \
    "s_sub_i32 vcc_lo, vcc_lo, %[rf]\n\t" \
    "s_cmp_ge_i32 vcc_lo, -4\n\t" \
    "s_cbranch_scc1 10f\n\t" \
    "4:\n\t" \
    "s_memrealtime vcc\n\t" \
    "s_waitcnt lgkmcnt(0)\n\t" \
    "s_sub_i32 vcc_lo, vcc_lo, %[rf]\n\t" \
    "s_cmp_lt_i32 vcc_lo, 0\n\t" \
    "s_cbranch_scc1 4b\n\t" \
    "10:\n\t" \
    "global_load_dwordx4 v[8:11], %[gs], %[ws] offset:0 sc1\n\t" \
    "global_load_dwordx4 v[12:15], %[gs], %[ws] offset:1024 sc1\n\t" \
    "global_load_dwordx4 v[16:19], %[gs], %[ws] offset:2048 sc1\n\t" \
    "global_load_dwordx4 v[24:27], %[gm], %[ws] sc1\n\t" \
    "v_mov_b32 v28, %[fl]\n\t" \
    "v_add_u32 v28, -8, v28\n\t" \
    "s_mov_b32 %[ct], 0x400000\n\t" \
    "11:\n\t" \
    "ds_read_b32 v29, v28\n\t" \
    "s_sub_u32 %[ct], %[ct], 1\n\t" \
    "s_waitcnt lgkmcnt(0)\n\t" \
    "s_cbranch_scc1 12f\n\t" \
    "v_cmp_eq_u32 vcc, 0, v29\n\t" \
    "s_cbranch_vccnz 11b\n\t" \
    "12:\n\t" \
    "ds_read_b64_tr_b16 v[28:29], %[a0] offset:0\n\t" \
    "ds_read_b64_tr_b16 v[30:31], %[a0] offset:1024\n\t" \
    "ds_read_b64_tr_b16 v[32:33], %[a1] offset:8192\n\t" \
    "ds_read_b64_tr_b16 v[34:35], %[a1] offset:9216\n\t" \
    "ds_read_b64_tr_b16 v[36:37], %[a0] offset:8192\n\t" \
    "ds_read_b64_tr_b16 v[38:39], %[a0] offset:9216\n\t" \
    "ds_read_b64_tr_b16 v[40:41], %[a1] offset:0\n\t" \
    "ds_read_b64_tr_b16 v[42:43], %[a1] offset:1024\n\t" \
    "ds_read_b64_tr_b16 v[44:45], %[a0] offset:16384\n\t" \
    "ds_read_b64_tr_b16 v[46:47], %[a0] offset:17408\n\t" \
    "ds_read_b64_tr_b16 v[48:49], %[a1] offset:24576\n\t" \
    "ds_read_b64_tr_b16 v[50:51], %[a1] offset:25600\n\t" \
    "ds_read_b64_tr_b16 v[52:53], %[a0] offset:24576\n\t" \
    "ds_read_b64_tr_b16 v[54:55], %[a0] offset:25600\n\t" \
    "ds_read_b64_tr_b16 v[56:57], %[a1] offset:16384\n\t" \
    "ds_read_b64_tr_b16 v[58:59], %[a1] offset:17408\n\t" \
    "ds_read_b64_tr_b16 v[60:61], %[a0] offset:32768\n\t" \
    "ds_read_b64_tr_b16 v[62:63], %[a0] offset:33792\n\t" \
    "s_waitcnt vmcnt(3) lgkmcnt(0)\n\t" \
    "v_mfma_f32_16x16x32_bf16 v[28:31], v[28:31], v[8:11], 0\n\t" \
    "v_mov_b32_dpp v20, v8 row_half_mirror row_mask:0xf bank_mask:0xf\n\t" \
    "v_mov_b32_dpp v21, v9 row_half_mirror row_mask:0xf bank_mask:0xf\n\t" \
    "v_mfma_f32_16x16x32_bf16 v[32:35], v[32:35], v[8:11], 0\n\t" \
    "v_mov_b32_dpp v22, v10 row_half_mirror row_mask:0xf bank_mask:0xf\n\t" \
    "v_mov_b32_dpp v23, v11 row_half_mirror row_mask:0xf bank_mask:0xf\n\t" \
    "v_max3_u32 %[t0], v8, v9, v10\n\t" \
    "s_waitcnt vmcnt(2)\n\t" \
    "v_mfma_f32_16x16x32_bf16 v[28:31], v[36:39], v[20:23], v[28:31]\n\t" \
    "v_max3_u32 %[t0], %[t0], v11, v12\n\t" \
    "ds_read_b64_tr_b16 v[36:37], %[a1] offset:40960\n\t" \
    "v_mfma_f32_16x16x32_bf16 v[32:35], v[40:43], v[20:23], v[32:35]\n\t" \
    "ds_read_b64_tr_b16 v[38:39], %[a1] offset:41984\n\t" \
    "ds_read_b64_tr_b16 v[40:41], %[a0] offset:40960\n\t" \
    "v_mfma_f32_16x16x32_bf16 v[28:31], v[44:47], v[12:15], v[28:31]\n\t" \
    "v_mov_b32_dpp v20, v12 row_half_mirror row_mask:0xf bank_mask:0xf\n\t" \
    "v_mov_b32_dpp v21, v13 row_half_mirror row_mask:0xf bank_mask:0xf\n\t" \
    "v_mfma_f32_16x16x32_bf16 v[32:35], v[48:51], v[12:15], v[32:35]\n\t" \
    "v_mov_b32_dpp v22, v14 row_half_mirror row_mask:0xf bank_mask:0xf\n\t" \
    "v_mov_b32_dpp v23, v15 row_half_mirror row_mask:0xf bank_mask:0xf\n\t" \
    "ds_read_b64_tr_b16 v[42:43], %[a0] offset:41984\n\t" \
    "ds_read_b64_tr_b16 v[44:45], %[a1] offset:32768\n\t" \
    "v_mfma_f32_16x16x32_bf16 v[28:31], v[52:55], v[20:23], v[28:31]\n\t" \
    "ds_read_b64_tr_b16 v[46:47], %[a1] offset:33792\n\t" \
    "s_waitcnt vmcnt(0)\n\t" \
    "v_max3_u32 %[t0], %[t0], v13, v14\n\t" \
    "v_mfma_f32_16x16x32_bf16 v[32:35], v[56:59], v[20:23], v[32:35]\n\t" \
    "v_max3_u32 v8, v24, v25, v26\n\t" \
    "v_sub_f32 v24, v26, v24\n\t" \
    "v_mfma_f32_16x16x32_bf16 v[28:31], v[60:63], v[16:19], v[28:31]\n\t" \
    "v_mov_b32_dpp v20, v16 row_half_mirror row_mask:0xf bank_mask:0xf\n\t" \
    "v_mov_b32_dpp v21, v17 row_half_mirror row_mask:0xf bank_mask:0xf\n\t" \
    "s_waitcnt lgkmcnt(4)\n\t" \
    "v_mfma_f32_16x16x32_bf16 v[32:35], v[36:39], v[16:19], v[32:35]\n\t" \
    "v_mov_b32_dpp v22, v18 row_half_mirror row_mask:0xf bank_mask:0xf\n\t" \
    "v_mov_b32_dpp v23, v19 row_half_mirror row_mask:0xf bank_mask:0xf\n\t" \
    "v_exp_f32 v24, v24\n\t" \
    "v_max3_u32 %[t0], %[t0], v15, v16\n\t" \
    "s_waitcnt lgkmcnt(0)\n\t" \
    "v_mfma_f32_16x16x32_bf16 v[28:31], v[40:43], v[20:23], v[28:31]\n\t" \
    "v_fmac_f32 v25, v24, v27\n\t" \
    "v_max3_u32 v8, v8, v27, v17\n\t" \
    "v_mfma_f32_16x16x32_bf16 v[32:35], v[44:47], v[20:23], v[32:35]\n\t" \
    "v_rcp_f32 v24, v25\n\t" \
    "v_max3_u32 %[t0], %[t0], v18, v19\n\t" \
    "v_max_u32 v8, v8, %[t0]\n\t" \
    "v_cmp_eq_u32 vcc, -1, v8\n\t" \
    "v_mov_b32 v9, 0xff\n\t" \
    "s_nop 1\n\t" \
    "s_cbranch_vccnz 6f\n\t" \
    "v_mov_b32_dpp v28, v32 quad_perm:[0,1,2,3] row_mask:0xf bank_mask:0xa\n\t" \
    "v_mov_b32_dpp v29, v33 quad_perm:[0,1,2,3] row_mask:0xf bank_mask:0xa\n\t" \
    "v_mov_b32_dpp v30, v34 quad_perm:[0,1,2,3] row_mask:0xf bank_mask:0xa\n\t" \
    "v_mov_b32_dpp v31, v35 quad_perm:[0,1,2,3] row_mask:0xf bank_mask:0xa\n\t" \
    "v_pk_mul_f32 v[28:29], v[28:29], v[24:25] op_sel_hi:[1,0]\n\t" \
    "v_pk_mul_f32 v[30:31], v[30:31], v[24:25] op_sel_hi:[1,0]\n\t" \
    "s_nop 0\n\t" \
    "v_add_f32_dpp v28, v28, v28 row_shl:8 row_mask:0xf bank_mask:0xf\n\t" \
    "v_add_f32_dpp v29, v29, v29 row_shl:8 row_mask:0xf bank_mask:0xf\n\t" \
    "v_add_f32_dpp v30, v30, v30 row_shl:8 row_mask:0xf bank_mask:0xf\n\t" \
    "v_add_f32_dpp v31, v31, v31 row_shl:8 row_mask:0xf bank_mask:0xf\n\t" \
    "s_mov_b32 exec_lo, 0x00ff00ff\n\t" \
    "s_mov_b32 exec_hi, 0x00ff00ff\n\t" \
    "v_bfi_b32 v28, v9, %[tag], v28\n\t" \
    "v_bfi_b32 v29, v9, %[tag], v29\n\t" \
    "v_bfi_b32 v30, v9, %[tag], v30\n\t" \
    "v_bfi_b32 v31, v9, %[tag], v31\n\t" \
    "global_store_dwordx4 %[so], v[28:31], %[ws] sc1\n\t" \
    "s_mov_b64 exec, -1\n\t" \
    "s_branch 2f\n\t" \
    "6:\n\t" \
    "s_mov_b64 exec, -1\n\t" \
    "s_sub_u32 %[st], %[st], 1\n\t" \
    "s_cbranch_scc1 2f\n\t" \
    RETRY_ASM \
    "s_sleep 6\n\t" \
    "s_branch 5b\n\t" \
    "2:\n\t"
#define ASM_GU \
    "s_mov_b32 %[st], 0x10000\n\t" \
    "5:\n\t" \
    "v_xor_b32 v11, 0x80, %[a0]\n\t" \
    "ds_read_b64_tr_b16 v[16:17], %[a0] offset:0\n\t" \
    "ds_read_b64_tr_b16 v[18:19], v11 offset:0\n\t" \
    "ds_read_b64_tr_b16 v[36:37], %[a0] offset:20480\n\t" \
    "ds_read_b64_tr_b16 v[38:39], v11 offset:20480\n\t" \
    "ds_read_b64_tr_b16 v[20:21], %[a0] offset:1024\n\t" \
    "ds_read_b64_tr_b16 v[22:23], v11 offset:1024\n\t" \
    "ds_read_b64_tr_b16 v[40:41], %[a0] offset:21504\n\t" \
    "ds_read_b64_tr_b16 v[42:43], v11 offset:21504\n\t" \
    "ds_read_b64_tr_b16 v[24:25], %[a0] offset:2048\n\t" \
    "ds_read_b64_tr_b16 v[26:27], v11 offset:2048\n\t" \
    "ds_read_b64_tr_b16 v[44:45], %[a0] offset:22528\n\t" \
    "ds_read_b64_tr_b16 v[46:47], v11 offset:22528\n\t" \
    "ds_read_b64_tr_b16 v[28:29], %[a0] offset:3072\n\t" \
    "ds_read_b64_tr_b16 v[30:31], v11 offset:3072\n\t" \
    "ds_read_b64_tr_b16 v[48:49], %[a0] offset:23552\n\t" \
    "ds_read_b64_tr_b16 v[50:51], v11 offset:23552\n\t" \
    "ds_read_b64_tr_b16 v[32:33], %[a0] offset:4096\n\t" \
    "ds_read_b64_tr_b16 v[34:35], v11 offset:4096\n\t" \
    "ds_read_b64_tr_b16 v[52:53], %[a0] offset:24576\n\t" \
    "ds_read_b64_tr_b16 v[54:55], v11 offset:24576\n\t" \
    "v_mov_b32 v12, 0x3f803f80\n\t" \
    "v_mov_b32 v13, 0x3f803f80\n\t" \
    "v_mov_b32 v14, 0x3f803f80\n\t" \
    "v_mov_b32 v15, 0x3f803f80\n\t" \
    "v_mov_b32 v56, 0x3f803f80\n\t" \
    "v_mov_b32 v57, 0x3f803f80\n\t" \
    "v_mov_b32 v58, 0x3f803f80\n\t" \
    "v_mov_b32 v59, 0x3f803f80\n\t" \
    "4:\n\t" \
    "s_memrealtime vcc\n\t" \
    "s_waitcnt lgkmcnt(0)\n\t" \
    "s_sub_i32 vcc_lo, vcc_lo, %[tg]\n\t" \
    "s_cmp_lt_i32 vcc_lo, 0\n\t" \
    "s_cbranch_scc1 4b\n\t" \
    "global_load_dwordx4 v[6:9], %[gs], %[ws] sc1\n\t" \
    "global_load_dword v10, %[g2], %[ws] sc1\n\t" \
    "s_waitcnt vmcnt(1)\n\t" \
    "v_mov_b32_dpp v12, v6 quad_perm:[0,1,2,3] row_mask:0xf bank_mask:0x1\n\t" \
    "v_mov_b32_dpp v13, v7 quad_perm:[0,1,2,3] row_mask:0xf bank_mask:0x1\n\t" \
    "v_mov_b32_dpp v14, v8 quad_perm:[0,1,2,3] row_mask:0xf bank_mask:0x1\n\t" \
    "v_mov_b32_dpp v15, v9 quad_perm:[0,1,2,3] row_mask:0xf bank_mask:0x1\n\t" \
    "v_mov_b32_dpp v56, v6 row_shl:4 row_mask:0xf bank_mask:0x1\n\t" \
    "v_mov_b32_dpp v57, v7 row_shl:4 row_mask:0xf bank_mask:0x1\n\t" \
    "v_mfma_f32_16x16x32_bf16 v[16:19], v[16:19], v[12:15], 0\n\t" \
    "v_mov_b32_dpp v58, v8 row_shl:4 row_mask:0xf bank_mask:0x1\n\t" \
    "v_mov_b32_dpp v59, v9 row_shl:4 row_mask:0xf bank_mask:0x1\n\t" \
    "v_mfma_f32_16x16x32_bf16 v[36:39], v[36:39], v[12:15], 0\n\t" \
    "v_max3_u32 v11, v6, v7, v8\n\t" \
    "v_mfma_f32_16x16x32_bf16 v[60:63], v[12:15], v[12:15], 0\n\t" \
    "v_mfma_f32_16x16x32_bf16 v[16:19], v[20:23], v[56:59], v[16:19]\n\t" \
    "v_mov_b32_dpp v12, v6 row_shl:8 row_mask:0xf bank_mask:0x1\n\t" \
    "v_mov_b32_dpp v13, v7 row_shl:8 row_mask:0xf bank_mask:0x1\n\t" \
    "v_mfma_f32_16x16x32_bf16 v[36:39], v[40:43], v[56:59], v[36:39]\n\t" \
    "v_mov_b32_dpp v14, v8 row_shl:8 row_mask:0xf bank_mask:0x1\n\t" \
    "v_mov_b32_dpp v15, v9 row_shl:8 row_mask:0xf bank_mask:0x1\n\t" \
    "v_mfma_f32_16x16x32_bf16 v[60:63], v[56:59], v[56:59], v[60:63]\n\t" \
    "s_nop 0\n\t" \
    "v_mfma_f32_16x16x32_bf16 v[16:19], v[24:27], v[12:15], v[16:19]\n\t" \
    "v_mov_b32_dpp v56, v6 row_shl:12 row_mask:0xf bank_mask:0x1\n\t" \
    "v_mov_b32_dpp v57, v7 row_shl:12 row_mask:0xf bank_mask:0x1\n\t" \
    "v_mfma_f32_16x16x32_bf16 v[36:39], v[44:47], v[12:15], v[36:39]\n\t" \
    "v_mov_b32_dpp v58, v8 row_shl:12 row_mask:0xf bank_mask:0x1\n\t" \
    "v_mov_b32_dpp v59, v9 row_shl:12 row_mask:0xf bank_mask:0x1\n\t" \
    "v_mfma_f32_16x16x32_bf16 v[60:63], v[12:15], v[12:15], v[60:63]\n\t" \
    "s_nop 0\n\t" \
    "v_mfma_f32_16x16x32_bf16 v[16:19], v[28:31], v[56:59], v[16:19]\n\t" \
    "s_waitcnt vmcnt(0)\n\t" \
    "v_mov_b32_dpp v12, v10 quad_perm:[0,1,2,3] row_mask:0xf bank_mask:0x1\n\t" \
    "v_mov_b32_dpp v13, v10 row_shl:4 row_mask:0xf bank_mask:0x1\n\t" \
    "v_mfma_f32_16x16x32_bf16 v[36:39], v[48:51], v[56:59], v[36:39]\n\t" \
    "v_mov_b32_dpp v14, v10 row_shl:8 row_mask:0xf bank_mask:0x1\n\t" \
    "v_mov_b32_dpp v15, v10 row_shl:12 row_mask:0xf bank_mask:0x1\n\t" \
    "v_mfma_f32_16x16x32_bf16 v[60:63], v[56:59], v[56:59], v[60:63]\n\t" \
    "v_max3_u32 v11, v11, v9, v10\n\t" \
    "v_mfma_f32_16x16x32_bf16 v[16:19], v[32:35], v[12:15], v[16:19]\n\t" \
    "v_cmp_eq_u32 vcc, -1, v11\n\t" \
    "v_mfma_f32_16x16x32_bf16 v[36:39], v[52:55], v[12:15], v[36:39]\n\t" \
    "v_mfma_f32_16x16x32_bf16 v[60:63], v[12:15], v[12:15], v[60:63]\n\t" \
    "s_mov_b32 exec_lo, 0x001f001f\n\t" \
    "s_mov_b32 exec_hi, 0x001f001f\n\t" \
    "s_cbranch_vccnz 6f\n\t" \
    "7:\n\t" \
    "s_nop 3\n\t" \
    "ds_write2_b32 %[pw], v16, v36 offset0:0 offset1:1\n\t" \
    "ds_write2_b32 %[pw], v17, v37 offset0:12 offset1:13\n\t" \
    "ds_write2_b32 %[pw], v18, v38 offset0:24 offset1:25\n\t" \
    "ds_write2_b32 %[pw], v19, v39 offset0:36 offset1:37\n\t" \
    "ds_write2_b32 %[gw], v60, v61 offset1:4\n\t" \
    "ds_write2_b32 %[gw], v62, v63 offset0:8 offset1:12\n\t" \
    "s_mov_b64 exec, -1\n\t" \
    "s_waitcnt lgkmcnt(0)\n\t" \
    "s_barrier\n\t" \
    "s_branch 2f\n\t" \
    "6:\n\t" \
    "s_sub_u32 %[st], %[st], 1\n\t" \
    "s_cbranch_scc1 7b\n\t" \
    "s_mov_b64 exec, -1\n\t" \
    RETRY_ASM \
    "s_sleep 1\n\t" \
    "s_branch 5b\n\t" \
    "2:\n\t"
#define ASM_HS \
    "ds_read_b64_tr_b16 v[16:17], %[w0] offset:0\n\t" \
    "ds_read_b64_tr_b16 v[18:19], %[w1] offset:32\n\t" \
    "ds_read_b64_tr_b16 v[20:21], %[w2] offset:64\n\t" \
    "ds_read_b64_tr_b16 v[22:23], %[w3] offset:96\n\t" \
    "ds_read_b64_tr_b16 v[24:25], %[w0] offset:128\n\t" \
    "ds_read_b64_tr_b16 v[26:27], %[w1] offset:160\n\t" \
    "ds_read_b64_tr_b16 v[28:29], %[w2] offset:192\n\t" \
    "ds_read_b64_tr_b16 v[30:31], %[w3] offset:224\n\t" \
    "ds_read_b64_tr_b16 v[32:33], %[w0] offset:256\n\t" \
    "ds_read_b64_tr_b16 v[34:35], %[w1] offset:288\n\t" \
    "v_mov_b32 v36, 0\n\t" \
    "v_mov_b32 v37, 0\n\t" \
    "v_mov_b32 v38, 0\n\t" \
    "v_mov_b32 v39, 0\n\t" \
    "v_mov_b32 v40, 0\n\t" \
    "v_mov_b32 v41, 0\n\t" \
    "v_mov_b32 v42, 0\n\t" \
    "v_mov_b32 v43, 0\n\t" \
    "v_add_u32 v12, 0x40000, %[so]\n\t" \
    "v_add_u32 v13, 0x80000, %[so]\n\t" \
    "s_waitcnt lgkmcnt(0)\n\t" \
    "s_barrier\n\t" \
    "ds_read_b128 v[44:47], %[sx]\n\t" \
    "ds_read_b128 v[48:51], %[sq]\n\t" \
    "ds_read_b128 v[52:55], %[cp]\n\t" \
    "ds_read_b128 v[56:59], %[cp] offset:16\n\t" \
    "ds_read_b128 v[60:63], %[rp]\n\t" \
    "ds_read_b128 v[8:11], %[rp] offset:16\n\t" \
    "s_waitcnt lgkmcnt(5)\n\t" \
    "v_pk_add_f32 v[44:45], v[44:45], v[46:47]\n\t" \
    "s_waitcnt lgkmcnt(4)\n\t" \
    "v_pk_add_f32 v[48:49], v[48:49], v[50:51]\n\t" \
    "v_add_f32 v44, v44, v45\n\t" \
    "v_add_f32 v48, v48, v49\n\t" \
    "v_mul_f32 v44, 0x3acccccd, v44\n\t" \
    "v_mul_f32 v48, 0x3acccccd, v48\n\t" \
    "v_fma_f32 v48, -v44, v44, v48\n\t" \
    "v_max_f32 v48, 0, v48\n\t" \
    "v_cvt_pk_bf16_f32 v44, v44, v48\n\t" \
    "v_and_b32 v48, 0xffff0000, v44\n\t" \
    "v_add_f32 v48, 0x3727c5ac, v48\n\t" \
    "v_cvt_pk_bf16_f32 v48, v48, v48\n\t" \
    "v_lshlrev_b32 v48, 16, v48\n\t" \
    "v_sqrt_f32 v48, v48\n\t" \
    "s_waitcnt lgkmcnt(2)\n\t" \
    "v_pk_add_f32 v[52:53], v[52:53], v[54:55]\n\t" \
    "v_cvt_pk_bf16_f32 v48, v48, v48\n\t" \
    "v_lshlrev_b32 v48, 16, v48\n\t" \
    "v_rcp_f32 v48, v48\n\t" \
    "v_pk_add_f32 v[56:57], v[56:57], v[58:59]\n\t" \
    "v_lshlrev_b32 v44, 16, v44\n\t" \
    "s_waitcnt lgkmcnt(0)\n\t" \
    "v_pk_add_f32 v[60:61], v[60:61], v[62:63]\n\t" \
    "v_pk_add_f32 v[8:9], v[8:9], v[10:11]\n\t" \
    "v_pk_add_f32 v[52:53], v[52:53], v[56:57]\n\t" \
    "v_pk_add_f32 v[60:61], v[60:61], v[8:9]\n\t" \
    "v_pk_fma_f32 v[60:61], v[52:53], v[44:45], v[60:61] op_sel_hi:[1,0,1] neg_lo:[1,0,0] neg_hi:[1,0,0]\n\t" \
    "v_pk_mul_f32 v[60:61], v[60:61], v[48:49] op_sel_hi:[1,0]\n\t" \
    "v_cvt_pk_bf16_f32 v60, v60, v61\n\t" \
    "v_lshlrev_b32 v61, 16, v60\n\t" \
    "v_mul_f32 v62, 0xbfb8aa3b, v61\n\t" \
    "v_exp_f32 v62, v62\n\t" \
    "v_and_b32 v60, 0xffff0000, v60\n\t" \
    "v_add_f32 v62, 1.0, v62\n\t" \
    "v_rcp_f32 v62, v62\n\t" \
    "s_nop 0\n\t" \
    "v_mul_f32 v61, v62, v61\n\t" \
    "v_cvt_pk_bf16_f32 v61, v61, v61\n\t" \
    "v_lshlrev_b32 v61, 16, v61\n\t" \
    "v_mul_f32 v60, v60, v61\n\t" \
    "s_nop 1\n\t" \
    "v_mov_b32_dpp v61, v60 row_ror:12 row_mask:0xf bank_mask:0xf\n\t" \
    "v_cvt_pk_bf16_f32 v60, v60, v61\n\t" \
    "s_nop 1\n\t" \
    "v_mov_b32_dpp v36, v60 quad_perm:[0,1,2,3] row_mask:0xf bank_mask:0x1\n\t" \
    "v_mov_b32_dpp v37, v60 row_ror:8 row_mask:0xf bank_mask:0x1\n\t" \
    "v_mov_b32_dpp v38, v60 quad_perm:[0,1,2,3] row_mask:0xf bank_mask:0x2\n\t" \
    "v_mov_b32_dpp v39, v60 row_ror:8 row_mask:0xf bank_mask:0x2\n\t" \
    "v_mov_b32_dpp v40, v60 quad_perm:[0,1,2,3] row_mask:0xf bank_mask:0x4\n\t" \
    "v_mov_b32_dpp v41, v60 row_ror:8 row_mask:0xf bank_mask:0x4\n\t" \
    "v_mfma_f32_16x16x32_bf16 v[44:47], v[16:19], v[36:39], 0\n\t" \
    "v_mov_b32_dpp v42, v60 quad_perm:[0,1,2,3] row_mask:0xf bank_mask:0x8\n\t" \
    "v_mov_b32_dpp v43, v60 row_ror:8 row_mask:0xf bank_mask:0x8\n\t" \
    "v_mfma_f32_16x16x32_bf16 v[48:51], v[24:27], v[36:39], 0\n\t" \
    "v_mfma_f32_16x16x32_bf16 v[52:55], v[32:35], v[36:39], 0\n\t" \
    "v_mfma_f32_16x16x32_bf16 v[44:47], v[20:23], v[40:43], v[44:47]\n\t" \
    "v_mfma_f32_16x16x32_bf16 v[48:51], v[28:31], v[40:43], v[48:51]\n\t" \
    "s_nop 5\n\t" \
    "v_min_u32 v52, -2, v52\n\t" \
    "v_min_u32 v53, -2, v53\n\t" \
    "v_min_u32 v54, -2, v54\n\t" \
    "v_min_u32 v55, -2, v55\n\t" \
    "v_min_u32 v44, -2, v44\n\t" \
    "v_min_u32 v45, -2, v45\n\t" \
    "v_min_u32 v46, -2, v46\n\t" \
    "v_min_u32 v47, -2, v47\n\t" \
    "global_store_dwordx4 %[so], v[44:47], %[ws] sc1\n\t" \
    "v_min_u32 v48, -2, v48\n\t" \
    "v_min_u32 v49, -2, v49\n\t" \
    "v_min_u32 v50, -2, v50\n\t" \
    "v_min_u32 v51, -2, v51\n\t" \
    "global_store_dwordx4 v12, v[48:51], %[ws] sc1\n\t" \
    "s_mov_b32 exec_lo, 0x00ff00ff\n\t" \
    "s_mov_b32 exec_hi, 0x00ff00ff\n\t" \
    "global_store_dwordx4 v13, v[52:55], %[ws] sc1\n\t" \
    "s_mov_b64 exec, -1\n\t"
#define CLOB_8_15 "v8", "v9", "v10", "v11", "v12", "v13", "v14", "v15"
#define CLOB_16_59 "v16", "v17", "v18", "v19", "v20", "v21", "v22", "v23", "v24", "v25", "v26", "v27", "v28", "v29", "v30", "v31", "v32", "v33", "v34", "v35", "v36", "v37", "v38", "v39", "v40", "v41", "v42", "v43", "v44", "v45", "v46", "v47", "v48", "v49", "v50", "v51", "v52", "v53", "v54", "v55", "v56", "v57", "v58", "v59"
#define CLOB_16_63 "v16", "v17", "v18", "v19", "v20", "v21", "v22", "v23", "v24", "v25", "v26", "v27", "v28", "v29", "v30", "v31", "v32", "v33", "v34", "v35", "v36", "v37", "v38", "v39", "v40", "v41", "v42", "v43", "v44", "v45", "v46", "v47", "v48", "v49", "v50", "v51", "v52", "v53", "v54", "v55", "v56", "v57", "v58", "v59", "v60", "v61", "v62", "v63"
#define TRANS(name) do { unsigned t0_, t1_; asm volatile(name : "=&v"(t0_), "=&v"(t1_) : "s"(0x05040100u), "s"(0x07060302u)); } while (0)
template <int R, int IMM> __device__ __forceinline__ void ldkv(const void* sbase, unsigned voff) {
    if constexpr ((R < 160 && (POL & 1)) || (R >= 160 && (POL & 2)))
        asm volatile("global_load_dwordx4 v[%c2:%c3], %0, %1 offset:%c4" :: "v"(voff), "s"(sbase), "i"(R), "i"(R + 3), "i"(IMM) : "memory");
    else
        asm volatile("global_load_dwordx4 v[%c2:%c3], %0, %1 offset:%c4 nt" :: "v"(voff), "s"(sbase), "i"(R), "i"(R + 3), "i"(IMM) : "memory");
}
template <int A> __device__ __forceinline__ void fix_k() {
    unsigned t0, t1, t2, t3;
    asm volatile("v_mov_b32 %0, v%c4\n\tv_mov_b32 %1, v%c5\n\tv_mov_b32 %2, v%c6\n\tv_mov_b32 %3, v%c7\n\t"
                 "v_mov_b32_dpp v%c4, v%c8 row_ror:8 row_mask:0xf bank_mask:0xc\n\tv_mov_b32_dpp v%c5, v%c9 row_ror:8 row_mask:0xf bank_mask:0xc\n\t"
                 "v_mov_b32_dpp v%c6, v%c10 row_ror:8 row_mask:0xf bank_mask:0xc\n\tv_mov_b32_dpp v%c7, v%c11 row_ror:8 row_mask:0xf bank_mask:0xc\n\t"
                 "v_mov_b32_dpp v%c8, %0 row_ror:8 row_mask:0xf bank_mask:0x3\n\tv_mov_b32_dpp v%c9, %1 row_ror:8 row_mask:0xf bank_mask:0x3\n\t"
                 "v_mov_b32_dpp v%c10, %2 row_ror:8 row_mask:0xf bank_mask:0x3\n\tv_mov_b32_dpp v%c11, %3 row_ror:8 row_mask:0xf bank_mask:0x3"
                 : "=&v"(t0), "=&v"(t1), "=&v"(t2), "=&v"(t3) : "i"(A), "i"(A + 1), "i"(A + 2), "i"(A + 3), "i"(A + 12), "i"(A + 13), "i"(A + 14), "i"(A + 15));
}
#define PACE 1
#define D7 16
template <int D = DHI> __device__ __forceinline__ void pace(unsigned& due, unsigned step) {
    asm volatile("s_waitcnt vmcnt(%2)\n\ts_add_u32 %0, %0, %1\n1:\n\ts_memrealtime vcc\n\ts_waitcnt lgkmcnt(0)\n\ts_lshl_b32 vcc_lo, vcc_lo, 4\n\ts_sub_i32 vcc_lo, vcc_lo, %0\n\ts_cmp_ge_i32 vcc_lo, 0\n\ts_cbranch_scc0 1b"
                 : "+s"(due) : "s"(step), "i"(D) : "memory", "scc", "vcc");
}
#define MID 0
#define MIDN 7
#define JE 0
#define PW 0
#define TSX 0
#define CPF 0
#define KF 0
#define W7F 0
#define EPF_PULL 0
#define FIRSTD 0
#define EPM 248
#define EPN 16
#define PULLM 0
#define KEEPX " sc1"
#define LKEEP 0
#define LPN 4
#define TPN 0
#define OPCL
#define STG 2
#define AB(x) 0
#define GPOL 1
#define QZ 0
#define PRI 3
#define P1ONLY 0
#undef NET
#define NET 0
#define NPS 0
#define NP0 0
#define MP0 24
#define MPS 3
static_assert(!WOMODE || (NET <= 3 && MP0 >= 11 && MP0 + (6 - NET) * MPS < 36), "extras");
template <int I, int HI, bool THR, class F> __device__ __forceinline__ void kv_stream_x(const void* kbase, const void* vbase, unsigned ko, unsigned v0, const unsigned (&to)[4], unsigned& due, unsigned step, F&& extra) {
    if constexpr (I < HI) {
        if constexpr (NPS > 0 && I >= NP0 && (I - NP0) % (NPS > 0 ? NPS : 1) == 0 && (I - NP0) / (NPS > 0 ? NPS : 1) < 6) extra(100 + (I - NP0) / NPS);
        if constexpr (WOMODE && I >= MP0 && (I - MP0) % MPS == 0 && (I - MP0) / MPS < 7 - NET) { extra((I - MP0) / MPS); pace(due, step); }
        if constexpr (I < 24) ldkv<64 + 4 * I, (I % 3) * 128 + ((I / 3) & 1) * 3072>(kbase, ko + to[I / 6]);
        else ldkv<160 + 4 * (I - 24), ((I - 24) & 3) * 384>(vbase, v0 + to[(I - 24) >> 2]);
        if constexpr (I >= 14 && I < 38 && (I - 14) % 6 < 3) fix_k<64 + 4 * (I - 14)>();
        pace(due, step);
        kv_stream_x<I + 1, HI, THR>(kbase, vbase, ko, v0, to, due, step, extra);
    }
}
template <int I, int HI, int THR> __device__ __forceinline__ void kv_stream(const void* kbase, const void* vbase, unsigned ko, unsigned v0, const unsigned (&to)[4], unsigned& due, unsigned step) {
    if constexpr (I < HI) {
        if constexpr (I < 24) ldkv<64 + 4 * I, (I % 3) * 128 + ((I / 3) & 1) * 3072>(kbase, ko + to[I / 6]);
        else ldkv<160 + 4 * (I - 24), ((I - 24) & 3) * 384>(vbase, v0 + to[(I - 24) >> 2]);
        if constexpr (THR == 1 && I >= P_BA) pace(due, step);
        if constexpr (THR == 2) pace<D7>(due, step);
        if constexpr (THR == 3) pace(due, step);
        kv_stream<I + 1, HI, THR>(kbase, vbase, ko, v0, to, due, step);
    }
}
template <int I> struct IC { static constexpr int v = I; };
template <int I, int HI, class F> __device__ __forceinline__ void kv_stream7(const void* kbase, const void* vbase, unsigned ko, unsigned v0, unsigned v1, unsigned v2, const unsigned (&to)[4], unsigned& due, unsigned step, F&& nar) {
    if constexpr (I < HI) {
        if constexpr (I < 24) ldkv<64 + 4 * I, (I % 3) * 128 + ((I / 3) & 1) * 3072>(kbase, ko + to[I / 6]);
        else if constexpr (I < 40) ldkv<160 + 4 * (I - 24), ((I - 24) & 3) * 384>(vbase, v0 + to[(I - 24) >> 2]);
        else ldkv<160 + 4 * (I - 24), (I & 3) * 384>(vbase, I < 44 ? v1 : v2);
        if constexpr (((I - F7) * NN7) % (48 - F7) < NN7) nar(IC<((I - F7) * NN7) / (48 - F7)>{});
        if constexpr (I < 47) pace<D7>(due, step);
        kv_stream7<I + 1, HI>(kbase, vbase, ko, v0, v1, v2, to, due, step, nar);
    }
}
template <int I> __device__ __forceinline__ void fix_all() { if constexpr (I < 12) { fix_k<64 + 4 * ((I / 3) * 6 + I % 3)>(); fix_all<I + 1>(); } }
#define LANES() int lane; asm volatile("v_mbcnt_lo_u32_b32 %0, -1, 0\n\tv_mbcnt_hi_u32_b32 %0, -1, %0" : "=v"(lane)); __builtin_assume(lane >= 0 && lane < 64); \
    const int n = lane & 15, kq = lane >> 4, q4 = n >> 2, p4 = lane & 3; (void)n; (void)kq; (void)q4; (void)p4
template <unsigned C> __device__ __forceinline__ u32x4 splat() { unsigned a, b, c, d; asm volatile("v_mov_b32 %0, %4\n\tv_mov_b32 %1, %4\n\tv_mov_b32 %2, %4\n\tv_mov_b32 %3, %4" : "=v"(a), "=v"(b), "=v"(c), "=v"(d) : "i"(C)); return (u32x4){a, b, c, d}; }
static_assert(TDEPTH <= 8 && DHI <= 8, "fix_k schedule");
__device__ __forceinline__ u32x4 ld16_a(const void* sbase, unsigned voff) {
    u32x4 r; asm volatile("global_load_dwordx4 %0, %1, %2" : "=v"(r) : "v"(voff), "s"(sbase) : "memory"); return r;
}

template <bool KEEP = false> __device__ __forceinline__ void narrow(const u16* wg, const u16* wu, unsigned va, unsigned vb, unsigned lb, int s, int rot) {
    const int s10 = (s * 26) >> 8, r = s - 10 * s10, m = r >= 5, c = r - 5 * m, qt = (s10 + rot) & 3;
    if (KEEP) dma16p((m ? wu : wg) + (qt * 160 + (c & 3) * 8) * 1536, c == 4 ? vb : va, lb + L_GU + (unsigned)(m * 20480 + (qt * 5 + c) * 1024)); else
    dma16((m ? wu : wg) + (qt * 160 + (c & 3) * 8) * 1536, c == 4 ? vb : va, lb + L_GU + (unsigned)(m * 20480 + (qt * 5 + c) * 1024));
}
#define NROT 0
#define ROTS(s) (NROT ? ((s) + 7 * (mi & 3)) % (int)SCH(S_N1) : (s))
#define OSX ""
#define LATED 0
#define LDSL 2
template <int P> __device__ __forceinline__ void wo_piece(const u16* wob, unsigned oa, unsigned lb) {
    constexpr int j = P / 6, i = P % 6;
    dma16(wob + (((j >> 1) & 1) * 32 + (j >> 2) * 16 + (j & 1) * 2 + (i & ~1) * 32 + (i & 1) * 4) * DM, (j & 2) ? oa ^ 128u : oa, lb + L_WO + (j + 8 * i) * 1024);
}
template <int P> __device__ __forceinline__ void wo_rest(const u16* wob, unsigned oa, unsigned lb) { if constexpr (P < 48) { wo_piece<P>(wob, oa, lb); wo_rest<P + 1>(wob, oa, lb); } }
#define WO_ADDR() const u16* wob = wo + h * DH * DM + sub * 128; const unsigned woa = (unsigned)(((kq & 1) * 8 + (kq >> 1)) * DM * 2 + ((n >> 1) ^ kq) * 32 + (n & 1) * 16)
#define NARROW_ADDR() const int r5_ = swz(lane >> 1); const unsigned nva = (unsigned)(((r5_ & 7) + ((r5_ >> 3) << 5)) * 3072 + mi * 32 + (lane & 1) * 16), \
        nvb = (unsigned)((128 + r5_) * 3072 + mi * 32 + (lane & 1) * 16); const int nrot = 0

#define SLOWL(j) (((((unsigned)(size_t)wg >> 7) + (unsigned)(j)) & 7u) == 3u)
#define N1W ((int)(SLOWL(mi >> 2) ? SCH(S_N0) : SCH(S_N1)))
#define GUPF(rot) do { const int nd = (int)SCH(S_ND); int sh = (rr >> 3) * 5 + sub + (int)(rot); if (sh >= 20) sh -= 20; \
        _Pragma("unroll 1") for (int k_ = 0; k_ < nd; k_ += 21) { const int rw = k_ + lane / 3; \
        const unsigned o = (unsigned)((640 - 20 * nd + sh * nd + (rw < nd ? rw : nd - 1)) * 3072 + xg * 384 + (lane % 3) * 128); \
        if (GPOL) { pull(wg, o, lb + L_PF); pull(wu, o, lb + L_PF); } else { dma4(wg, o, lb + L_PF); dma4(wu, o, lb + L_PF); } } } while (0)
#define EXTRA(k) do { LANES(); \
        if (!is_mlp) { \
            const int j = (k) < 6 ? wave : 7, i = (k) < 6 ? (k) : wave < 5 ? wave : 5, pp = j * 4 + kq, f = (pp & 3) | (((pp >> 3) & 1) << 2); \
            const int dim = ((pp >> 4) & 1) * 16 + (pp & 1) * 8 + ((pp >> 1) & 7); \
            dma16(wo + (h * DH + i * 32) * DM + sub * 128, (unsigned)(dim * DM * 2 + ((n >> 1) ^ f) * 32 + (n & 1) * 16), lb + L_WO + (j + 8 * i) * 1024); \
        } else if ((k) < 4) { \
            const int s = (int)SCH(S_N0) + wave + 7 * (k); \
            if (s < (int)SCH(S_N1)) { NARROW_ADDR(); narrow(wg, wu, nva, nvb, lb, s, nrot); } \
        } else { \
            const int ii = wave + 7 * ((k) - 4), c = ii * 64 + lane; int row = c / 82, cc = c - 82 * row; if (cc > 79) cc = 79; if (row > 15) row = 15; \
            dma16(wd + mi * 16 * DM, (unsigned)((row * DM + cc * 8) * 2), lb + L_WD + ii * 1024); \
        } } while (0)

__global__ void __launch_bounds__(512, 2) __attribute__((amdgpu_num_vgpr(32), aligned(KAL)))
mega_kernel(const u16* __restrict__ kc, const u16* __restrict__ vc, unsigned char* __restrict__ ws, const u16* __restrict__ x0, const u16* __restrict__ wq,
            const u16* __restrict__ wg, const u16* __restrict__ wu, const u16* __restrict__ wo, const u16* __restrict__ wr,
            const u16* __restrict__ wd, u16* __restrict__ out
            ) {
    extern __shared__ __attribute__((aligned(16))) unsigned char smem[];
    constexpr unsigned sched_c[S_N] = SCHED; (void)sched_c;
    const unsigned lb = (unsigned)(size_t)smem;
#define STAMP0() do {} while (0)
    unsigned long long st64 = 0; (void)st64;
    const int wave0 = __builtin_amdgcn_readfirstlane(threadIdx.x >> 6);
    {
        unsigned long long kp, pc; unsigned s0, s1, s2, a0, a1;
        asm volatile("s_load_dwordx2 %0, %7, 0x0\n\ts_getpc_b64 %1\nmega_pf_here:\n\ts_lshr_b32 %2, %8, 3\n\tv_lshl_add_u32 %5, %9, 5, %2\n\tv_lshlrev_b32 %5, 7, %5\n\t"
                     "v_min_u32 %5, mega_pf_end - mega_pf_here - 4, %5\n\tglobal_load_dword v255, %5, %1\n\t"
                     "s_and_b32 %3, %8, 48\n\ts_and_b32 %4, %8, 7\n\ts_lshl1_add_u32 %3, %4, %3\n\ts_and_b32 %4, %2, 25\n\ts_lshl4_add_u32 %3, %3, %4\n\ts_add_u32 %3, %3, 6\n\ts_lshl_b32 %3, %3, 5\n\t"
                     "v_lshrrev_b32 %5, 6, %9\n\tv_add_u32 %5, %2, %5\n\tv_and_or_b32 %5, %5, 31, %3\n\tv_and_b32 %6, 7, %9\n\tv_lshl_add_u32 %5, %5, 4, %6\n\tv_mul_u32_u24 %5, 0x180, %5\n\t"
                     "v_and_or_b32 %5, %9, 48, %5\n\tv_lshlrev_b32 %6, 3, %9\n\tv_and_or_b32 %5, %6, 64, %5\n\ts_waitcnt lgkmcnt(0)\n\t"
                     "global_load_dwordx4 v[64:67], %5, %0 nt\n\tglobal_load_dwordx4 v[68:71], %5, %0 offset:128 nt\n\tglobal_load_dwordx4 v[72:75], %5, %0 offset:256 nt\n\tglobal_load_dwordx4 v[76:79], %5, %0 offset:3072 nt\n\t"
                     "s_memrealtime %1\n\ts_waitcnt lgkmcnt(0)"
                     : "=&s"(kp), "=&s"(pc), "=&s"(s0), "=&s"(s1), "=&s"(s2), "=&v"(a0), "=&v"(a1) : "s"(__builtin_amdgcn_kernarg_segment_ptr()), "s"(blockIdx.x), "v"(threadIdx.x) : "memory", "scc");
        st64 = pc;
    }
    const u32x4 ones = {~0u, ~0u, ~0u, ~0u};
    const f32x4 z4 = {0.f, 0.f, 0.f, 0.f};
    asm volatile("" ::: "v255");
#define WSOFF 0

#define DMARK(k) do {} while (0)
    unsigned to[4];
#pragma unroll
    for (int t = 0; t < 4; ++t) to[t] = ((t * 8 + wave0 + (GID >> 3)) & 31u) * 6144u;
    unsigned step = wave0 == 7 ? SCH(S_P7) : ((GID >> 3) & 7) >= 5 ? SCH(S_PM) : SCH(S_PD);
    unsigned t_start, due, t0, pol = 0u, tag = 0u;
#define START(off) do { t0 = t_start = (unsigned)st64; due = ((t_start + (off)) << 4) + ((((wave0 + STG) & 7) * step) >> 3); STAMPV(10, t_start); STAMP0(); } while (0)
    {
        const int wave = wave0, lane = threadIdx.x & 63;
        const int g = GID, xg = g & 7, rr = g >> 3;
        const int h = xg * 4 + (rr >> 3), sub = rr & 7, b = sub >> 1, half = sub & 1;
        const bool is_mlp = sub >= 5; const int mi = h * 3 + sub - 5, oi = h * 5 + sub;
        const int n = lane & 15, kq = lane >> 4, q4 = n >> 2, p4 = lane & 3;
        const u16* kbase = kc + ((size_t)(b * HQ + h) * SK + (SK - WIN) + half * 512) * DH;
        const u16* vbase = vc + ((size_t)(b * HQ + h) * SK + (SK - WIN) + half * 512) * DH;
        const unsigned ko = (unsigned)((lane & 7) * 384 + (((lane >> 3) & 1) * 4 + (lane >> 4)) * 16);
        const unsigned v0 = (unsigned)(4 * kq * 384 + n * 16);
        const unsigned v1 = (unsigned)(4 * kq * 384 + 256 + (n & 7) * 16) + (n >= 8 ? to[2] : to[0]), v2 = (unsigned)(4 * kq * 384 + 256 + (n & 7) * 16) + (n >= 8 ? to[3] : to[1]);
        auto lds_zero_nb = [&]() {
            if (threadIdx.x < 16) *reinterpret_cast<unsigned*>(smem + L_CNT + threadIdx.x * 4) = 0u;
            *reinterpret_cast<f32x4*>(smem + L_P + threadIdx.x * 16) = z4;
            if (threadIdx.x < 71 * 12 - 512) *reinterpret_cast<f32x4*>(smem + L_P + 8192 + threadIdx.x * 16) = z4;
        };
        auto lds_zero = [&]() { lds_zero_nb(); BAR(); };
        auto lds_inc = [&](int idx) {
            asm volatile("s_waitcnt lgkmcnt(0)" ::: "memory");
            if (lane == 0) __hip_atomic_fetch_add(reinterpret_cast<unsigned*>(smem + L_CNT) + idx, 1u, __ATOMIC_RELAXED, __HIP_MEMORY_SCOPE_WORKGROUP);
        };
        const unsigned po = WS_PR + (unsigned)((mi & 1) * LB + ((mi >> 1) & 1) * 8192 + (mi >> 1) * 128);
        auto clean = [&]() {
            if (is_mlp) {
#pragma unroll
                for (int i = 0; i < 3; ++i) {
                    const int c = mi * 3 + i;
                    if (c < 64 && (c & 7) < 6) { if (!AB(2)) st16_sc1(ws, WS_XA + (unsigned)(c * 1024 + lane * 16), ones); } else if (!AB(4)) st16_e(ws, WS_XA + (unsigned)(c * 1024 + lane * 16), ones);
                }
                if (lane < 16 && !AB(8)) st16_sc1(ws, (po ^ (unsigned)((lane & 8) << 10)) + (unsigned)((lane & 7) * 16), ones);
            }
        };
        auto first7 = [&]() {
            if (CA == 0) clean();
            if (EPF != 3) kv_stream<0, F7, 0>(kbase, vbase, ko, v0, to, due, step);
            START(SCH(S_O7));
            lds_zero_nb();
            __builtin_amdgcn_s_setprio(3);
            {
                unsigned d;
                asm volatile("s_waitcnt lgkmcnt(0)\n\ts_load_dword %0, %2, %3\n\ts_load_dword %1, %2, %4\n\ts_barrier\n\ts_waitcnt lgkmcnt(0)"
                             : "=&s"(tag), "=&s"(d) : "s"(ws), "s"(WS_EP), "s"(is_mlp ? (po | (2 * GP)) & ~16383u : WS_EP) : "memory");
                tag += 0x01010101u;
            }
            STAMP(24);
        };
        int w7 = wave;
        { if (wave == 7) first7(); asm volatile("" : "+s"(w7)); }
        if (w7 != 7) {
            const unsigned o2 = (unsigned)((wave & 3) * 1280 + lane * 16);
            u32x4 xa_ = ones, xc_ = ones;
            START(is_mlp ? SCH(S_MO) : SCH(S_PO));
            if (FIRSTD) { VMW(3); STAMP(36); VMW(0); STAMP(37); }
            lds_zero();
            if (!is_mlp) {
                xa_ = ld16_a(x0, o2); xc_ = ld16_a(x0, lane < 16 ? o2 + 1024 : o2);
#pragma unroll
                for (int i = 0; i < 7; ++i) {
                    int ii = wave + 7 * i; if (ii > 47) ii = 47;
                    const int c = ii * 64 + lane, row = c / 24, u = c - 24 * row;
                    const int f = ((row >> 1) & 1) | (((row >> 3) & 1) << 1);
                    unsigned o = (unsigned)(((sub * 128 + kpos(row)) * QD + h * DH) * 2 + ((u >> 1) ^ f) * 32 + (u & 1) * 16);
                    asm volatile("" : "+v"(o));
                    if (i < 2) pace<DHI + 2>(due, step); else pace(due, step);
                    dma16(wq, o, lb + L_WQ + ii * 1024);
                }
            }
            pace(due, step);
            kv_stream<4, P_LN, 3>(kbase, vbase, ko, v0, to, due, step);
            if (!is_mlp) {
                asm volatile("s_waitcnt vmcnt(%2)" : "+v"(xa_), "+v"(xc_) : "i"(P_LN - (EPF == 3 ? 4 : 0)) : "memory");
                if (wave >= 3) {
                    float mb, rden; row_stats(xa_, xc_, lane, mb, rden);
                    const f32x2 mb2 = {mb, mb}, rd2 = {rden, rden};
#pragma unroll
                    for (int j = 0; j < 4; ++j) { xa_[j] = pk2v(unpk(pk2v(unpk(xa_[j]) - mb2)) * rd2); xc_[j] = pk2v(unpk(pk2v(unpk(xc_[j]) - mb2)) * rd2); }
                    *reinterpret_cast<u32x4*>(smem + L_OP + (wave & 3) * 1344 + lane * 16) = xa_;
                    if (lane < 16) *reinterpret_cast<u32x4*>(smem + L_OP + (wave & 3) * 1344 + 1024 + lane * 16) = xc_;
                }
            }
            lds_inc(1);
            kv_stream<P_LN, P_QP, 1>(kbase, vbase, ko, v0, to, due, step);
            if (!is_mlp && wave < 6) {
                lds_wait_ge(lb + L_CNT + 4, 7u);
                STAMP(1);
                const unsigned a0 = lb + L_WQ + (unsigned)((8 * kq + q4) * 384 + ((2 * wave) ^ ((q4 >> 1) | ((kq & 1) << 1))) * 32 + p4 * 8), a1 = a0 ^ 32u;
                const unsigned char* xp = smem + L_OP + ((n >> 1) & 3) * 1344 + sub * 256 + kq * 64;
                f32x4 c0 = z4, c1 = z4;
#pragma unroll
                for (int s = 0; s < 4; ++s) {
                    const u32x4 bf = n >= 8 ? (u32x4){0x3f803f80u, 0x3f803f80u, 0x3f803f80u, 0x3f803f80u} : *reinterpret_cast<const u32x4*>(xp + s * 16);
                    c0 = mfma32w(cat8(tr64(a0 + s * 12288), tr64(a0 + s * 12288 + 1536)), bf, c0);
                    c1 = mfma32w(cat8(tr64(a1 + s * 12288), tr64(a1 + s * 12288 + 1536)), bf, c1);
                }
                if (n & 1) c0 = c1;
                if (n < 10) *reinterpret_cast<f32x4*>(smem + L_OP + 6144 + (n >> 1) * 768 + wave * 128 + kq * 32 + (n & 1) * 16) = c0;
                lds_inc(3);
            }
            kv_stream_x<P_QP, 36, true>(kbase, vbase, ko, v0, to, due, step, [&](int k) {
                (void)k; });
        } else {
            const unsigned t8 = tag & 0xffu;
            WO_ADDR();
            const int ml_ = mi - 12 * xg, l3_ = lane / 3, ne_ = (int)SCH(S_N0), r0e_ = 640 - 6 * LPN * (int)SCH(S_ND);
            const unsigned pe_ = (unsigned)((l3_ < ne_ ? l3_ : 0) * 3072 + xg * 384 + (lane - 3 * l3_) * 128);
            auto nar = [&](auto ic) {
                if (is_mlp) { const int row = (decltype(ic)::v * 6 + (ml_ >> 1)) * ne_; if (row < r0e_) pull(((ml_ & 1) ? wu : wg) + (row < 640 - ne_ ? row : 640 - ne_) * 1536, pe_, lb + L_PF); }
                else wo_piece<decltype(ic)::v>(wob, woa, lb); };
            auto qstore = [&]() {
                if (!is_mlp) {
                    lds_wait_ge(lb + L_CNT + 12, 6u);
#pragma unroll
                    for (int i = 0; i < 4; ++i) {
                        const int c = i * 64 + lane, ln = c >> 3;
                        if (c < 240 && !AB(16)) st16_x(ws, WS_H1A + (unsigned)((h * 30 + ln) * 640 + sub * 128 + (c & 7) * 16), tag4(*reinterpret_cast<const u32x4*>(smem + L_OP + 6144 + c * 16), t8));
                    }
                }
                STAMP(17);
            };
            if (EPF == 3) pace<D7>(due, step);
            kv_stream7<F7, (CA > F7 ? CA : F7)>(kbase, vbase, ko, v0, v1, v2, to, due, step, nar);
            if (CA) clean();
            kv_stream7<(CA > F7 ? CA : F7), A7>(kbase, vbase, ko, v0, v1, v2, to, due, step, nar);
            if (A7 >= Q7) qstore();
            if (is_mlp) due -= 4 * step;
            kv_stream7<A7, A7 + 4>(kbase, vbase, ko, v0, v1, v2, to, due, step, nar);
            if (is_mlp) {
                unsigned long long c0, c1, c2; unsigned d;
                due += 4 * step;
                VMW(A7 + 4 - CA);
                asm volatile(
                             "s_memtime %0\n\ts_load_dword %3, %4, %5\n\ts_waitcnt lgkmcnt(0)\n\t"
                             "s_memtime %1\n\ts_load_dword %3, %4, %6\n\ts_waitcnt lgkmcnt(0)\n\t"
                             "s_memtime %2\n\ts_waitcnt lgkmcnt(0)"
                             : "=&s"(c0), "=&s"(c1), "=&s"(c2), "=&s"(d) : "s"(ws), "s"(AB(8) ? WS_EP : po), "s"(AB(8) ? WS_EP : po ^ 8192u) : "memory");
                RETRY(48, (unsigned)c1 - (unsigned)c0); RETRY(49, (unsigned)c2 - (unsigned)c1);
                const unsigned arv = (t_start & ~1u) | ((((int)((unsigned)c2 + (unsigned)c0 - 2u * (unsigned)c1) > 0 ? 0u : 1u) ^ (unsigned)((mi >> 1) ^ (xg >> 2))) & 1u);
                if (lane == 0) st8_sc1(ws, WS_AR + (unsigned)((mi >> 2) * 128 + (mi & 3) * 8), (u32x2){tag, arv});
            }
            STAMP(16);
            if (A7 < Q7) { kv_stream7<A7 + 4, Q7>(kbase, vbase, ko, v0, v1, v2, to, due, step, nar); qstore(); }
            kv_stream7<(A7 < Q7 ? Q7 : A7 + 4), L7>(kbase, vbase, ko, v0, v1, v2, to, due, step, nar);
            u32x4 t = ones;
            const unsigned oa = WS_AR + (unsigned)((lane < 48 ? lane >> 1 : 0) * 128 + (lane & 1) * 16);
            if (g == LEADER) t = ld16_sc1(ws, oa);
            kv_stream7<L7, L7 + 4>(kbase, vbase, ko, v0, v1, v2, to, due, step, nar);
            if (g == LEADER) {
                asm volatile("s_waitcnt vmcnt(4)" : "+v"(t) :: "memory");
#pragma unroll 1
                for (unsigned spins = 0; spins < (kSpinCap << 4); ++spins) {
                    if (__builtin_amdgcn_ballot_w64(t[0] != tag || t[2] != tag) == 0ull) break;
                    RETRY(3, spins + 1);
                    __builtin_amdgcn_s_sleep(8);
                    t = ld16_sc1(ws, oa);
                    asm volatile("s_waitcnt vmcnt(0)" : "+v"(t) :: "memory");
                }
                t0 = t_start + (unsigned)wave_imax(imax((int)(t[1] - t_start), (int)(t[3] - t_start)));
                const int v0_ = __builtin_popcountll(__builtin_amdgcn_ballot_w64((t[1] & 1u) != 0u) & 0xffffffffffffull), v1_ = __builtin_popcountll(__builtin_amdgcn_ballot_w64((t[3] & 1u) != 0u) & 0xffffffffffffull);
                RETRY(50, v0_ + 100); RETRY(51, v1_ + 100);
                t0 = (t0 & ~7u) | (v0_ > 24 ? 2u : 0u) | (v1_ > 24 ? 4u : 0u);
                if (SCH(S_FL) & 256) { RETRY(5, (t0 ^ (SCH(S_FL) >> 8)) & 2u); RETRY(6, (t0 ^ (SCH(S_FL) >> 8)) & 4u); t0 = (t0 & ~7u) | ((SCH(S_FL) >> 8) & 6u); }
                if (lane < 8) st8_sc1(ws, WS_GO + (unsigned)(lane * 128), (u32x2){tag, t0});
                STAMP(29);
            }
#define LDQ5() do { a[0] = ld16_xo<0>(ws, o); a[1] = ld16_xo<128>(ws, o); a[2] = ld16_xo<256>(ws, o); a[3] = ld16_xo<384>(ws, o); a[4] = ld16_xo<512>(ws, o); } while (0)
#define WTQ5(n) asm volatile("s_waitcnt vmcnt(%5)" : "+v"(a[0]), "+v"(a[1]), "+v"(a[2]), "+v"(a[3]), "+v"(a[4]) : "i"(n) : "memory")
#define GATH5(row, lo, hi) \
                const unsigned o = WS_H1A + (unsigned)((h * 30 + (row) * 6 + (lane < 48 ? lane >> 3 : 0)) * 640 + (lane & 7) * 16); \
                u32x4 a[5]; \
                LDQ5(); \
                kv_stream7<lo, hi>(kbase, vbase, ko, v0, v1, v2, to, due, step, nar); \
                WTQ5((hi) - (lo)); \
                _Pragma("unroll 1") for (unsigned spins = 0; spins < (kSpinCap << 4); ++spins) { \
                    _Pragma("unroll") for (int i = 0; i < 5; ++i) a[i] ^= t8; \
                    if (tagok(or4(a[0] | a[1] | a[2] | a[3] | a[4])) || AB(16)) break; \
                    RETRY(1, spins + 1); \
                    __builtin_amdgcn_s_sleep(8); \
                    LDQ5(); \
                    WTQ5(0); \
                } \
                const f32x4 q = ((asf(a[0]) + asf(a[1])) + (asf(a[2]) + asf(a[3]))) + asf(a[4])
            kv_stream7<L7 + 4, QG>(kbase, vbase, ko, v0, v1, v2, to, due, step, nar);
            {
                u32x2 r = ld8_sc1o<0>(ws, WS_GO + (unsigned)(xg * 128));
                GATH5(b, QG, QG + QW);
                if (lane < 48) *reinterpret_cast<u32x2*>(smem + L_Q + (lane >> 3) * 64 + (lane & 1) * 32 + ((lane >> 1) & 3) * 8) = (u32x2){pk2(q[0], q[1]), pk2(q[2], q[3])};
                STAMP(30);
                asm volatile("" : "+v"(r));
#pragma unroll 1
                for (unsigned spins = 0; spins < (kSpinCap << 4); ++spins) {
                    t0 = __builtin_amdgcn_readfirstlane(r[1]);
                    if (__builtin_amdgcn_readfirstlane(r[0]) == tag) break;
                    RETRY(2, spins + 1);
                    __builtin_amdgcn_s_sleep(8);
                    r = ld8_sc1o<0>(ws, WS_GO + (unsigned)(xg * 128));
                    asm volatile("s_waitcnt vmcnt(0)" : "+v"(r) :: "memory");
                }
            }
            pol = t0;
            asm volatile("ds_write2_b32 %0, %1, %2 offset1:3\n\tds_write_b32 %0, %3 offset:4" :: "v"(lb + L_POL), "v"(pol), "v"(0u), "v"(tag) : "memory");
            if (t0 - t_start + 8u > 500u || (SCH(S_FL) & 4096)) t0 = t_start;
            asm volatile("ds_write_b32 %0, %1" :: "v"(lb + L_CNT + 36), "v"(t0 | 1u) : "memory");
            lds_inc(0);
            STAMP(2);
            if (!AB(512)) {
                GATH5(4, QG + QW, QG + 2 * QW);
                if (lane < 48) *reinterpret_cast<f32x4*>(smem + L_CS + (lane >> 3) * 128 + (lane & 1) * 64 + ((lane >> 1) & 3) * 16) = q;
            } else kv_stream7<QG + QW, QG + 2 * QW>(kbase, vbase, ko, v0, v1, v2, to, due, step, nar);
            kv_stream7<QG + 2 * QW, 48>(kbase, vbase, ko, v0, v1, v2, to, due, step, nar);
            if (AB(1024)) {
                unsigned long long pc; unsigned len;
                asm volatile("s_getpc_b64 %0\nmega_pf_two:\n\ts_mov_b32 %1, mega_pf_end - mega_pf_two - 4" : "=s"(pc), "=s"(len));
                const unsigned o = (unsigned)((lane * 16 + (rr & 15)) * 128);
                dma4((const void*)pc, o < len ? o : len, lb + L_PF);
            }
            VMW(16);
            fix_all<0>();
            TRANS(ASM_TR00); TRANS(ASM_TR01); TRANS(ASM_TR02); TRANS(ASM_TR03);
            VMW(8);
            TRANS(ASM_TR10); TRANS(ASM_TR11); TRANS(ASM_TR12); TRANS(ASM_TR13);
            VMW(0);
            TRANS(ASM_TR20); TRANS(ASM_TR21); TRANS(ASM_TR22); TRANS(ASM_TR23);
            STAMP(19);
            __builtin_amdgcn_s_setprio(0);
        }
    }

#define IDS() int g = GID; asm volatile("" : "+s"(g)); int wave = wave0; asm volatile("" : "+s"(wave)); \
        const int xg = g & 7, rr = g >> 3, h = xg * 4 + (rr >> 3), sub = rr & 7, b = sub >> 1; \
        const bool is_mlp = sub >= 5; const int mi = h * 3 + sub - 5, oi = h * 5 + sub; (void)mi; (void)oi; (void)b
#define ATT_ADDR(layer) \
        unsigned qa = lb + (unsigned)((QZ && p4 ? L_P + 256 : L_Q) + kq * 16); \
        unsigned pw = lb + L_P + (unsigned)((wave * 8 + 7) * 192 + q4 * 32 + (kq >> 1) * 16 + (kq & 1) * 8 + p4 * 2); \
        unsigned pr = lb + L_P + (unsigned)((wave * 8 + 7 - (n & 7)) * 192 + kq * 32 + (n >> 3) * 16); \
        unsigned ow = lb + L_OP + (unsigned)(wave * OPS + lane * 16); \
        unsigned mlw = lb + L_ML + (unsigned)(wave * 4), mo = lb + L_ML + (unsigned)((lane >> 3) * 4), ma = lb + L_ML; \
        unsigned r0 = lb + L_OP + (unsigned)((lane >> 3) * OPS + (wave < 4 ? wave * 256 : 2048 + (wave & 1) * 256) + (lane & 7) * 16), r1 = r0 + (wave < 4 ? 1152u : 640u); \
        unsigned so = (a_h2(h) ^ (layer ? CX(1) : 0u)) + (unsigned)(layer * LB + (wave < 6 ? ((wave >> 1) * 8 + (lane >> 5) * 4 + (wave & 1) * 2 + (sub & 1)) * 128 + b * 32 + (lane & 7) * 4 \
                                                                                  : 3072 + (((sub & 1) ^ (lane & 1)) * 4 + b) * 128 + (lane & 1) * 8)); \
        (void)mlw; (void)ma; (void)r1

    if (PADN) asm volatile("s_branch 1f\n\t.fill %0, 4, 0xbf800000\n1:" :: "i"(PADN) : "memory");
    {
        IDS(); LANES();
        ATT_ADDR(0);
        asm volatile("" : "+v"(qa), "+v"(pw), "+v"(pr), "+v"(ow), "+v"(mlw), "+v"(mo), "+v"(ma), "+v"(r0), "+v"(r1), "+v"(so));
        const u16* vbase = vc + ((size_t)(b * HQ + h) * SK + (SK - WIN) + (sub & 1) * 512) * DH;
        auto tail = [&]() {
            LANES();
            const unsigned ta = to[0], tb = to[1], tc = to[2], td = to[3];
            const unsigned v1 = (unsigned)(4 * kq * 384 + 256 + (n & 7) * 16) + (n >= 8 ? tc : ta), v2 = (unsigned)(4 * kq * 384 + 256 + (n & 7) * 16) + (n >= 8 ? td : tb);
            ldkv<224, 0>(vbase, v1); pace(due, step); ldkv<228, 384>(vbase, v1); pace(due, step); ldkv<232, 768>(vbase, v1); pace(due, step); ldkv<236, 1152>(vbase, v1); pace(due, step);
            ldkv<240, 0>(vbase, v2); if (TPN >= 1) pace(due, step); ldkv<244, 384>(vbase, v2); if (TPN >= 2) pace(due, step); ldkv<248, 768>(vbase, v2); if (TPN >= 3) pace(due, step); ldkv<252, 1152>(vbase, v2);
            asm volatile("s_mov_b64 exec, 1\n\tds_add_u32 %0, %1 offset:336\n\ts_mov_b64 exec, -1" :: "v"(ma), "v"(1u) : "memory");
            STAMP(3);
        };
        if (wave != 7) {
            const unsigned t = lds_ld(lb + L_CNT + 36);
            if (t) due += (t - t_start) << 4;
            step = (step * SCH(S_X3)) >> 8;
        }
        const bool early = wave == 7 || lds_ld(lb + L_CNT) != 0u;
        if (!early) {
            const unsigned v0 = (unsigned)(4 * kq * 384 + n * 16) + to[3];
            ldkv<208, 0>(vbase, v0); pace(due, step); ldkv<212, 384>(vbase, v0); pace(due, step); ldkv<216, 768>(vbase, v0); pace(due, step); ldkv<220, 1152>(vbase, v0); pace(due, step);
            tail();
            lds_wait_ge(lb + L_CNT, 1u);
        }
        pol = lds_ld(lb + L_POL); tag = lds_ld(lb + L_POL + 4);
        so ^= CX(0);
        STAMP(4);
        f32x4 acc[4];
        {
            u32x4 qf[6];
            asm volatile(ASM_QK : "=&v"(acc[0]), "=&v"(acc[1]), "=&v"(acc[2]), "=&v"(acc[3]), "=&v"(qf[0]), "=&v"(qf[1]), "=&v"(qf[2]), "=&v"(qf[3]), "=&v"(qf[4]), "=&v"(qf[5]) : "v"(qa) : "memory");
        }
        const float sa = (kq & 1) ? acc[1][0] : acc[0][0], sb = (kq & 1) ? acc[3][0] : acc[2][0];
        const float s = bfr(bfr(kq & 2 ? sb : sa) * SCALE) * L2E;
        float mt, p; int ms;
        asm volatile("s_nop 1\n\tv_max_f32_dpp %0, %3, %3 quad_perm:[1,0,3,2] row_mask:0xf bank_mask:0xf\n\ts_nop 1\n\t"
                     "v_max_f32_dpp %0, %0, %0 quad_perm:[2,3,0,1] row_mask:0xf bank_mask:0xf\n\ts_nop 1\n\t"
                     "v_max_f32_dpp %0, %0, %0 row_half_mirror row_mask:0xf bank_mask:0xf\n\ts_nop 1\n\t"
                     "v_max_f32_dpp %0, %0, %0 row_mirror row_mask:0xf bank_mask:0xf\n\ts_nop 1\n\t"
                     "v_max_f32_dpp %0, %0, %0 row_bcast:15 row_mask:0xa bank_mask:0xf\n\ts_nop 1\n\t"
                     "v_max_f32_dpp %0, %0, %0 row_bcast:31 row_mask:0xc bank_mask:0xf\n\ts_nop 0\n\t"
                     "v_readlane_b32 %2, %0, 63\n\ts_nop 1\n\t"
                     "v_subrev_f32 %1, %2, %3\n\tv_exp_f32 %1, %1\n\ts_nop 0"
                     : "=&v"(mt), "=&v"(p), "=&s"(ms) : "v"(s));
        u32x4 bm[8];
        {
            unsigned pb;
            asm volatile("v_cvt_pk_bf16_f32 %9, %8, %8\n\tds_write_b16 %10, %9\n\t"
                     "ds_read_b128 %0, %11\n\tds_read_b128 %1, %11 offset:192\n\tds_read_b128 %2, %11 offset:384\n\tds_read_b128 %3, %11 offset:576\n\t"
                     "ds_read_b128 %4, %11 offset:768\n\tds_read_b128 %5, %11 offset:960\n\tds_read_b128 %6, %11 offset:1152\n\tds_read_b128 %7, %11 offset:1344\n\t"
                     "v_add_f32_dpp %8, %8, %8 quad_perm:[1,0,3,2] row_mask:0xf bank_mask:0xf\n\ts_nop 1\n\t"
                     "v_add_f32_dpp %8, %8, %8 quad_perm:[2,3,0,1] row_mask:0xf bank_mask:0xf\n\ts_nop 1\n\t"
                     "v_add_f32_dpp %8, %8, %8 row_half_mirror row_mask:0xf bank_mask:0xf\n\ts_nop 1\n\t"
                     "v_add_f32_dpp %8, %8, %8 row_mirror row_mask:0xf bank_mask:0xf\n\ts_nop 1\n\t"
                     "v_add_f32_dpp %8, %8, %8 row_bcast:15 row_mask:0xa bank_mask:0xf\n\ts_nop 1\n\t"
                     "v_add_f32_dpp %8, %8, %8 row_bcast:31 row_mask:0xc bank_mask:0xf\n\t"
                     "s_mov_b32 exec_lo, 0\n\ts_brev_b32 exec_hi, 1\n\tds_write2_b32 %13, %12, %8 offset1:8\n\ts_mov_b64 exec, -1\n\ts_waitcnt lgkmcnt(0)"
                     : "=&v"(bm[0]), "=&v"(bm[1]), "=&v"(bm[2]), "=&v"(bm[3]), "=&v"(bm[4]), "=&v"(bm[5]), "=&v"(bm[6]), "=&v"(bm[7]), "+v"(p), "=&v"(pb)
                     : "v"(pw), "v"(pr), "v"(mt), "v"(mlw) : "memory");
        }
        f32x4 da;
#define PVB(name) asm volatile(name : "=&v"(da) : "v"(bm[0]), "v"(bm[1]), "v"(bm[2]), "v"(bm[3]), "v"(bm[4]), "v"(bm[5]), "v"(bm[6]), "v"(bm[7]), "v"(ow) : "memory")
        if (wave != 7) {
            {
                const unsigned v0 = (unsigned)(4 * kq * 384 + n * 16) + to[3];
                if (early) { ldkv<208, 0>(vbase, v0); pace(due, step); ldkv<212, 384>(vbase, v0); VMW(6); }
                TRANS(ASM_TR00); TRANS(ASM_TR01); TRANS(ASM_TR02); TRANS(ASM_TR03);
                if (early) { pace(due, step); ldkv<216, 768>(vbase, v0); }
                PVB(ASM_PVB0);
                if (early) { pace(due, step); ldkv<220, 1152>(vbase, v0); pace(due, step); tail(); }
            }
            VMW(8 + NET);
            TRANS(ASM_TR10); TRANS(ASM_TR11); TRANS(ASM_TR12); TRANS(ASM_TR13);
            PVB(ASM_PVB1);
#define MEET(off) do { unsigned t_; int c_; \
            asm volatile("s_waitcnt lgkmcnt(0)\n\ts_mov_b64 exec, 1\n\tds_add_u32 %2, %3 offset:" #off "\n\ts_mov_b64 exec, -1\n\ts_mov_b32 %1, 0x400000\n1:\n\tds_read_b32 %0, %2 offset:" #off "\n\ts_sub_u32 %1, %1, 1\n\ts_cbranch_scc1 2f\n\t" \
                         "s_waitcnt lgkmcnt(0)\n\tv_cmp_gt_u32 vcc, 8, %0\n\ts_cbranch_vccnz 1b\n2:" : "=&v"(t_), "=&s"(c_) : "v"(ma), "v"(1u) : "memory", "scc", "vcc"); } while (0)
            MEET(372);
            const f32x4 m0 = *(const LAS f32x4*)(size_t)ma, m1 = *(const LAS f32x4*)(size_t)(ma + 16);
            const float M = vmax3(vmax3(m0[0], m0[1], m0[2]), vmax3(m0[3], m1[0], m1[1]), vmax3(m1[2], m1[3], m1[3]));
            float fj = __builtin_amdgcn_exp2f(*(const LAS float*)(size_t)mo - M);
            auto combine = [&]() {
                const f32x4 a = *(const LAS f32x4*)(size_t)r0, c = *(const LAS f32x4*)(size_t)r1;
                const float z = red3((a + c) * fj);
                auto sw = __builtin_amdgcn_permlane16_swap(__float_as_uint(z), __float_as_uint(z), false, false);
                st4_m<0x000000ffu, 0x000000ffu>(ws, so, enc(pk2(__uint_as_float(sw[0]), __uint_as_float(sw[1]))));
            };
            if (wave < 4) combine();
            else if (wave == 6) {
                float L = *(const LAS float*)(size_t)(mo + 32) * fj;
                L += dpp<0x128>(L); L = sw16_add(L, L); L = sw32_add(L, L);
                if (lane < 2) st8_sc1(ws, so, (u32x2){enc(__float_as_uint(M)), enc(__float_as_uint(L))});
            } else st4_m<1u, 0u>(ws, WS_SINK + (unsigned)(g * 128), 0u);
            unsigned hfa = (a_hf(h) ^ CX(0)) + (unsigned)(lane * 128 + ((sub - lane) & 7) * 4);
            unsigned arr_ = 0;
            asm volatile("" : "+v"(fj), "+v"(hfa), "+v"(arr_));
            STAMP(6); VMW(7 + NET); STAMP(21); TRANS(ASM_TR20); VMW(5 + NET); STAMP(22); TRANS(ASM_TR21); VMW(3 + NET); STAMP(25); TRANS(ASM_TR22); VMW(1 + NET);
            STAMP(20); DMARK(46);
            {
                unsigned t0_, t1_, arr;
                asm volatile("s_mov_b64 exec, 1\n\tds_add_rtn_u32 %4, %5, %6 offset:376\n\ts_mov_b64 exec, -1\n\ts_memrealtime vcc\n\t" ASM_TR23
                             "s_waitcnt lgkmcnt(0)\n\tv_readfirstlane_b32 vcc_hi, %4\n\ts_bitset0_b32 vcc_lo, 0\n\tv_mov_b32 %0, vcc_lo\n\ts_cmp_lg_u32 vcc_hi, 7\n\ts_cbranch_scc1 1f\n\t"
                             "s_mov_b64 exec, 31\n\tglobal_store_dword %7, %0, %8 sc1\n\tv_or_b32 %1, 1, %0\n\tds_write_b32 %5, %1 offset:%9\n\ts_mov_b64 exec, -1\n1:"
                             : "=&v"(t0_), "=&v"(t1_) : "s"(0x05040100u), "s"(0x07060302u), "v"(arr_), "v"(ma), "v"(1u), "v"(hfa), "s"(ws), "i"(L_POL + 12 - L_ML) : "memory", "scc", "vcc");
                (void)arr;
            }
            PVB(ASM_PV2);
            if ((wave & 6) == 4) { MEET(348); combine(); STAMP(5); asm volatile("s_mov_b64 exec, 1\n\tds_add_u32 %0, %1 offset:344\n\ts_mov_b64 exec, -1" :: "v"(ma), "v"(1u) : "memory"); }
            else asm volatile("s_waitcnt lgkmcnt(0)\n\ts_mov_b64 exec, 1\n\tds_add_u32 %0, %1 offset:348\n\ts_mov_b64 exec, -1" :: "v"(ma), "v"(1u) : "memory");
            if (is_mlp && wave != 6) {
                lds_wait_ge(lb + L_CNT + 24, 2u);
                NARROW_ADDR();
                lds_wait_ge(lb + L_CNT + 40, 1u);
                STAMP(18);
#pragma unroll 1
                for (int s = wave; s < 40; s += 6) narrow<LKEEP>(wg, wu, nva, nvb, lb, s, nrot);
                if (P1ONLY) { VMW(0); STAMP(27); }
            }
        } else {
            PVB(ASM_PVB0); PVB(ASM_PVB1); PVB(ASM_PV2);
            asm volatile("s_waitcnt lgkmcnt(0)\n\ts_mov_b64 exec, 1\n\tds_add_u32 %0, %1 offset:372\n\tds_add_u32 %0, %1 offset:348\n\ts_mov_b64 exec, -1" :: "v"(ma), "v"(1u) : "memory");
        }
    }
    {
        IDS();
        if (wave == 7) {
            STAMP(15);
            auto arrive = [&]() {
                unsigned arr = 0;
                asm volatile("s_mov_b64 exec, 1\n\tds_add_rtn_u32 %0, %1, %2\n\ts_mov_b64 exec, -1\n\ts_waitcnt lgkmcnt(0)" : "+v"(arr) : "v"(lb + L_CNT + 56), "v"(1u) : "memory");
                if (__builtin_amdgcn_readfirstlane(arr) == 7u) {
                    const unsigned t = (now32() - SCH(S_G3)) & ~1u;
                    LANES();
                    st4_m<31u, 0u>(ws, (a_hf(h) ^ CX(0)) + (unsigned)(lane * 128 + ((sub - lane) & 7) * 4), t);
                    asm volatile("ds_write_b32 %0, %1" :: "v"(lb + L_POL + 12), "v"(t | 1u) : "memory");
                    return true;
                }
                return false;
            };
            LANES();
            {
                __builtin_amdgcn_s_setprio(3);
                if (!is_mlp) {
                    dma16p(wr, (unsigned)(lane * 16), lb + L_WR); dma4(wr, (unsigned)(1024 + lane * 4), lb + L_WR + 1024);
                    dma4(x0, (unsigned)((((lane >> 1) & 3) * DM + oi * 4) * 2 + (lane & 1) * 4), lb + L_WR + 1280);
                    dma4n(out, (unsigned)(xg * 128 + (lane & 1) * 4096), lb + L_PF);
                    WO_ADDR();
                    lds_wait_ge(lb + L_CNT + 16, SCH(S_WT));
                    if (SCH(S_WD)) wait_until(now32() + SCH(S_WD));
                    STAMP(35);
                    wo_rest<NN7>(wob, woa, lb);
                    STAMP(36);
                    VMW(WK);
                    if (WK) { if (arrive()) VMW(1); else VMW(0); }
                    STAMP(33); DMARK(44);
                    asm volatile("ds_write_b32 %0, %1" :: "v"(lb + L_CNT + 40), "v"(1u) : "memory");
                    if (!WK) arrive();
                } else {
                    arrive();
                    const int ml = mi - 12 * xg, nl = (int)SCH(S_ND), l3 = lane / 3;
                    unsigned pa = (unsigned)((l3 < nl ? l3 : 0) * 3072 + xg * 384 + (lane - 3 * l3) * 128);
                    const u16* pb = ((ml & 1) ? wu : wg) + (640 - (6 - (ml >> 1)) * LPN * nl) * 1536;
                    auto down = [&]() {
                        int cc = lane, ro = 0;
#pragma unroll
                        for (int ii = 0; ii < 21; ++ii) {
                            dma16(wd + mi * 16 * DM, (unsigned)((ii == 20 && ro > 15 * 1280 ? 15 * 1280 : ro) + (cc > 79 ? 79 : cc) * 16), lb + L_WD + ii * 1024);
                            cc += 64; if (cc >= 82) { cc -= 82; ro += 1280; }
                        }
                    };
                    asm volatile("" : "+v"(pa));
                    if (SCH(S_PA) > 7u) lds_wait_ge(lb + L_CNT + 56, SCH(S_PA) - 7u); else lds_wait_ge(lb + L_CNT + 16, SCH(S_PA));
                    if (SCH(S_NT)) wait_until(t0 + SCH(S_NT));
                    if (nl) {
#pragma unroll
                        for (int k = 0; k < LPN; ++k) { if (SCH(S_X7) & 1) dma4n(pb + k * nl * 1536, pa, lb + L_PF); else pull(pb + k * nl * 1536, pa, lb + L_PF); }
                    }
                    if (SCH(S_X7) & 2) {
                        VMW(0);
                        asm volatile("ds_write_b32 %0, %1" :: "v"(lb + L_CNT + 40), "v"(1u) : "memory");
                        lds_wait_ge(lb + L_CNT + 24, 2u);
                        wait_until(now32() + SCH(S_X6));
                        down();
                        VMW(0);
                    } else {
                        down();
                        VMW(DNW);
                        asm volatile("ds_write_b32 %0, %1" :: "v"(lb + L_CNT + 40), "v"(1u) : "memory");
                        VMW(0);
                    }
                    STAMP(34);
                    asm volatile("s_mov_b64 exec, 1\n\tds_add_u32 %0, %1\n\ts_mov_b64 exec, -1" :: "v"(lb + L_CNT + 44), "v"(1u) : "memory");
                }
                __builtin_amdgcn_s_setprio(0);
            }
        }
        if (wave == 6) {
            LANES();
            if (P1ONLY) {} else {
            if (sub < 5) {
                lds_wait_ge(lb + L_CNT + 56, SCH(S_G1));
                int d = 0; int left;
                const unsigned o = (a_hf(h) ^ CX(0)) + (unsigned)(sub * 128);
                const int tk = sub * 4 + ((lane >> 3) & 3);
                unsigned ta = (a_ts(lane & 7) ^ CX(0)) + (unsigned)((lane < 32 ? tk + (tk / 5) * 3 : (5 + tk + (tk / 3) * 5) & 31) * 128 + h * 4), fa = lb + L_CNT + 48, t;
                asm volatile("" : "+v"(ta), "+v"(fa));
                {
                    u32x8 e; unsigned x;
                    asm volatile("s_movk_i32 %1, 4000\n1:\n\ts_load_dwordx8 s[4:11], %4, %5 glc\n\tds_read_b32 %3, %6 offset:%7\n\ts_memrealtime vcc\n\ts_sub_u32 %1, %1, 1\n\ts_waitcnt lgkmcnt(0)\n\ts_cbranch_scc1 2f\n\t"
                                 "v_readfirstlane_b32 s4, %3\n\ts_or_b32 vcc_hi, s5, s6\n\ts_or_b32 %2, s7, s8\n\ts_or_b32 vcc_hi, vcc_hi, s9\n\ts_or_b32 %2, %2, s10\n\ts_or_b32 vcc_hi, vcc_hi, s11\n\ts_orn2_b32 %2, %2, s4\n\t"
                                 "s_or_b32 vcc_hi, vcc_hi, %2\n\ts_bitcmp1_b32 vcc_hi, 0\n\ts_cbranch_scc1 1b\n2:\n\ts_mov_b32 %2, vcc_lo"
                                 : "=&{s[4:11]}"(e), "=&s"(left), "=&s"(t), "=&v"(x) : "s"(ws), "s"(o), "v"(fa), "i"(L_POL + 12 - L_CNT - 48) : "memory", "scc", "vcc");
                    asm volatile("s_sub_i32 s4, s4, %2\n\ts_sub_i32 s5, s5, %2\n\ts_sub_i32 s6, s6, %2\n\ts_sub_i32 s7, s7, %2\n\ts_max_i32 s4, s4, s5\n\ts_sub_i32 s8, s8, %2\n\ts_max_i32 s6, s6, s7\n\ts_sub_i32 s9, s9, %2\n\t"
                                 "s_max_i32 s4, s4, s6\n\ts_sub_i32 s10, s10, %2\n\ts_max_i32 s8, s8, s9\n\ts_sub_i32 s11, s11, %2\n\ts_max_i32 s4, s4, s8\n\ts_max_i32 s10, s10, s11\n\ts_max_i32 %0, s4, s10"
                                 : "=s"(d), "+{s[4:11]}"(e) : "s"(t) : "scc");
                }
                if (__builtin_expect(left < 0, 0)) {
#pragma unroll 1
                    for (unsigned spins = 0; spins < kSpinCap; ++spins) {
                        if (allok(poll4(ws, o + (unsigned)((lane & 7) * 4)))) break;
                    }
                    t = now32(); d = 0;
                    RETRY(21, 1);
                }
                d += (int)SCH(S_H20);
                if (TSX && d < (int)SCH(S_G2) - TSX) d = (int)SCH(S_G2) - TSX;
                DMARK(45);
                const unsigned tg = (t + (unsigned)(d > (int)SCH(S_G2) && d < 1000 ? d : (int)SCH(S_G2))) | 1u;
                asm volatile("ds_write_b32 %0, %1" :: "v"(fa), "v"(tg) : "memory");
                st4_m<~0u, 0u>(ws, ta, enc(t + (unsigned)d));
                if (sub < 3) { wait_until(tg + 4u); st4_m<0u, ~0u>(ws, ta, enc(t + (unsigned)d)); }
                STAMPV(28, tg);
            } else lds_wait_ge(lb + L_CNT + 28, 8u);
            unsigned t; int d;
            {
                const unsigned o = (a_ts(xg) ^ CX(0)) + (unsigned)(rr * 128);
                unsigned r, lo; int left;
                asm volatile("s_movk_i32 %2, 4000\n1:\n\ts_load_dwordx16 s[68:83], %4, %5 glc\n\ts_load_dwordx16 s[84:99], %4, %6 glc\n\ts_memrealtime vcc\n\ts_sub_u32 %2, %2, 1\n\ts_waitcnt lgkmcnt(0)\n\ts_cbranch_scc1 2f\n\t"
                             "s_max_u32 s68, s68, s84\n\ts_max_u32 s69, s69, s85\n\ts_max_u32 s70, s70, s86\n\ts_max_u32 s71, s71, s87\n\ts_max_u32 s72, s72, s88\n\ts_max_u32 s73, s73, s89\n\ts_max_u32 s74, s74, s90\n\ts_max_u32 s75, s75, s91\n\ts_max_u32 s76, s76, s92\n\ts_max_u32 s77, s77, s93\n\ts_max_u32 s78, s78, s94\n\ts_max_u32 s79, s79, s95\n\ts_max_u32 s80, s80, s96\n\ts_max_u32 s81, s81, s97\n\ts_max_u32 s82, s82, s98\n\ts_max_u32 s83, s83, s99\n\ts_max_u32 s68, s68, s76\n\ts_max_u32 s69, s69, s77\n\ts_max_u32 s70, s70, s78\n\ts_max_u32 s71, s71, s79\n\ts_max_u32 s72, s72, s80\n\ts_max_u32 s73, s73, s81\n\ts_max_u32 s74, s74, s82\n\ts_max_u32 s75, s75, s83\n\ts_max_u32 s84, s68, s72\n\ts_max_u32 s85, s69, s73\n\ts_max_u32 s86, s70, s74\n\ts_max_u32 s87, s71, s75\n\ts_max_u32 s84, s84, s86\n\ts_max_u32 s85, s85, s87\n\ts_max_u32 %0, s84, s85\n\t"
                             "s_cmp_eq_u32 %0, -1\n\ts_cbranch_scc1 1b\n2:\n\ts_mov_b32 %1, vcc_lo\n\t"
                             "s_min_u32 s68, s68, s69\n\ts_min_u32 s70, s70, s71\n\ts_min_u32 s72, s72, s73\n\ts_min_u32 s74, s74, s75\n\ts_min_u32 s68, s68, s70\n\ts_min_u32 s72, s72, s74\n\ts_min_u32 %[lo], s68, s72"
                             : "=&s"(r), "=&s"(t), "=&s"(left), [lo] "=&s"(lo) : "s"(ws), "s"(o), "s"(o + 64u) : "memory", "scc", "vcc", "s68", "s69", "s70", "s71", "s72", "s73", "s74", "s75", "s76", "s77", "s78", "s79", "s80", "s81", "s82", "s83", "s84", "s85", "s86", "s87", "s88", "s89", "s90", "s91", "s92", "s93", "s94", "s95", "s96", "s97", "s98", "s99");
                if (__builtin_expect(left < 0, 0)) {
#pragma unroll 1
                    for (unsigned spins = 0; spins < kSpinCap; ++spins) {
                        if (allok(poll4(ws, o + (unsigned)((lane & 31) * 4)))) break;
                    }
                    lo = r = t = now32();
                    RETRY(22, 1);
                }
                const unsigned sp = r - lo, ex = sp < SCH(S_Y0) ? ((SCH(S_Y0) - sp) * 5u) >> 3 : 0u;
                d = (int)(r + (ex < SCH(S_Y1) ? ex : SCH(S_Y1)) - t);
            }
            asm volatile("ds_write_b32 %0, %1" :: "v"(lb + L_CNT + 8), "v"((t + (unsigned)(d < 150 ? d : 0)) | 1u) : "memory");
            STAMP(23); if (!is_mlp) DMARK(33);
            STAMPV(31, t + (unsigned)(d < 150 ? d : 0));
            }
        }
    }

    unsigned ref = 0;
#pragma unroll 1
    for (int layer_ = 0; layer_ < (P1ONLY ? 0 : 2); ++layer_) {
        IDS();
#define LAYER() int layer = layer_; asm volatile("" : "+s"(layer)); const unsigned cx = CX(layer), xa = WS_XA + (unsigned)(layer * LB), xm = xa + (a_rep(xg) ^ cx); (void)xa; (void)xm
        auto gather_img = [&](unsigned o, u32x4 (&a)[5], int sid, unsigned off) {
#pragma unroll 1
            for (unsigned spins = 0; spins < kSpinCap; ++spins) {
                a[0] = ld16_sc1o<-2048>(ws, o); a[1] = ld16_sc1o<-1024>(ws, o); a[2] = ld16_sc1o<0>(ws, o); a[3] = ld16_sc1o<1024>(ws, o); a[4] = ld16_sc1o<2048>(ws, o);
                asm volatile("s_waitcnt vmcnt(0)" : "+v"(a[0]), "+v"(a[1]), "+v"(a[2]), "+v"(a[3]), "+v"(a[4]) :: "memory");
                if (allok(umax(mx4(a[0], mx4(a[1])), mx4(a[2], mx4(a[3], mx4(a[4])))))) break;
                RETRY(sid, spins + 1);
                __builtin_amdgcn_s_sleep(10);
            }
        };
        auto gather_tab = [&](unsigned o, u32x4 (&a)[5], u32x4& r, int sid, unsigned t) {
            wait_until(t);
            a[0] = ld16_sc1o<-2048>(ws, o); a[1] = ld16_sc1o<-1024>(ws, o); a[2] = ld16_sc1o<0>(ws, o); a[3] = ld16_sc1o<1024>(ws, o); a[4] = ld16_sc1o<2048>(ws, o);
            wait_until(t + SCH(S_H4));
            r = ld16_sc1o<3072>(ws, o);
            asm volatile("s_waitcnt vmcnt(1)" : "+v"(a[0]), "+v"(a[1]), "+v"(a[2]), "+v"(a[3]), "+v"(a[4]) :: "memory");
            if (__builtin_expect(!allok(umax(mx4(a[0], mx4(a[1])), mx4(a[2], mx4(a[3], mx4(a[4]))))), 0)) {
                RETRY(sid, 1);
                asm volatile("s_waitcnt vmcnt(0)" : "+v"(r) :: "memory");
                __builtin_amdgcn_s_sleep(4);
                gather_img(o, a, sid, 0);
                r = ld16_sc1o<3072>(ws, o);
            }
        };
        auto late = [&](u32x4 v, unsigned base) {
            int d = -1000;
#pragma unroll
            for (int i = 0; i < 4; ++i) d = imax(d, v[i] == ~0u ? -1000 : (int)(v[i] - base));
            d = wave_imax(d);
            return d < 1000 ? d : 1000;
        };
        auto img_stats = [&](const u32x4 (&a)[5], float& mb, float& rden) {
            float s0 = 0.f, s1 = 0.f, q0 = 0.f, q1 = 0.f;
#pragma unroll
            for (int i = 0; i < 10; ++i) {
                const unsigned u = a[i >> 1][(i & 1) * 2], v = a[i >> 1][(i & 1) * 2 + 1];
                s0 = dot2(u, 0x3f803f80u, s0); q0 = dot2(u, u, q0); s1 = dot2(v, 0x3f803f80u, s1); q1 = dot2(v, v, q1);
            }
            const float mean = sum_x4(s0 + s1) * (1.f / DM);
            ln_consts(mean, sum_x4(q0 + q1) * (1.f / DM) - mean * mean, mb, rden);
        };
        if (!is_mlp) {
            if (wave < 4) { if (layer_ == 0) {
                LANES(); const int layer = 0;
                const unsigned h2 = a_h2(h) ^ CX(0); const int ot = (8 * sub + 2 * wave + (q4 & 1)) * 4 + kq, rw = (q4 & 1) ? 3 - p4 : p4;
                const unsigned a0 = lb + L_WO + (unsigned)((8 * kq + q4) * 256 + ((2 * wave) ^ (q4 | ((kq & 1) << 2))) * 32 + p4 * 8);
                const unsigned gm = h2 + (unsigned)(3072 + ((n >> 3) * 4 + rw) * 128);
                int st; unsigned t0;
                int rf, ct;
                asm volatile(ASM_OP : [st] "=&s"(st), [rf] "=&s"(rf), [ct] "=&s"(ct), [t0] "=&{v7}"(t0) : [a0] "{v0}"(a0), [a1] "{v1}"(a0 ^ 32u), [gs] "{v2}"(h2 + (unsigned)((lane >> 3) * 128 + rw * 32 + (q4 & 1) * 16)), [gm] "{v3}"(gm),
                             [so] "{v4}"((a_own(ot) + (unsigned)(6144 + h * 64 + rw * 16)) ^ ((unsigned)((ot & 15) << 7) | CX(0))),
                             [ws] "s"(ws), [fl] "s"(lb + L_CNT + 48), [tag] "s"(tag), [dbg] "s"(lb + L_DBG + wave * 512 + 256 + (7 + 32 * layer) * 4) : "memory", "scc", "vcc", CLOB_8_15, CLOB_16_63 OPCL);
                STAMP(7 + 32 * layer); DMARK(30);
            } } else if (wave == 5) {
                LANES(); LAYER();
                const unsigned po = (a_rep((lane >> 2) & 7) ^ cx) + (unsigned)((oi >> 1) * 64 + p4 * 16 + (oi & 1) * 8 + (lane >> 5) * 4);
                if (layer == 0) lds_wait_ge(lb + L_CNT + 40, 1u);
                u32x4 w[5];
#pragma unroll
                for (int j = 0; j < 5; ++j) w[j] = *reinterpret_cast<const u32x4*>(smem + L_WR + j * 256 + (lane >> 2) * 16);
                float xr = layer ? *reinterpret_cast<const float*>(smem + L_XR + lane * 4) : __uint_as_float((unsigned)*reinterpret_cast<const u16*>(smem + L_WR + 1280 + p4 * 8 + kq * 2) << 16);
                float cw = 0.f;
#pragma unroll
                for (int j = 0; j < 20; ++j) cw = dot2(w[j >> 2][j & 3], 0x3f803f80u, cw);
                cw = sum_x4(cw);
                const unsigned tw = (a_rep(lane & 7) ^ cx) + (unsigned)(5120 + xg * 128 + (oi - 20 * xg) * 4);
                {
                    unsigned o = (a_own(oi) ^ cx) + (unsigned)(layer * LB + 6144 + lane * 16), so = xa + po;
                    unsigned t8 = tag & 0xffu;
                    asm volatile("" : "+v"(xr), "+v"(o), "+v"(so), "+v"(cw), "+s"(t8));
                    if (layer == 0) {
                        if (!(SCH(S_FL) & 96)) lds_wait_ge(lb + L_CNT + 44, 1u);
#pragma unroll 1
                        for (unsigned spins = 0; spins < (kSpinCap << 6) && ref == 0u; ++spins) ref = lds_ld(lb + L_CNT + 8);
                    }
                    if (PRI & 1) __builtin_amdgcn_s_setprio(2);
                    if (!layer) DMARK(35);
                    if (layer == 0 && (SCH(S_FL) & 96)) lds_wait_ge(lb + L_CNT + 44, 1u);
                    wait_until(ref + (layer ? SCH(S_H3) : SCH(S_H30)));
                    if (!layer) DMARK(34);
                    CY(c_a);
                    u32x4 a0, a1;
                    GATHER(8 + 32 * layer, { a0 = ld16_sc1o<0>(ws, o); a1 = ld16_sc1o<1024>(ws, o);
                                 asm volatile("s_waitcnt vmcnt(0)" : "+v"(a0), "+v"(a1) :: "memory"); a0 ^= t8; a1 ^= t8; }, tagok(or4(a0) | or4(a1)));
                    CY(c_b);
                    xr += bfr(red4(asf(a0) + asf(a1)));
                    auto sw = __builtin_amdgcn_permlane16_swap(__float_as_uint(xr), __float_as_uint(xr), false, false);
                    st4_sc1(ws, so, enc(pk2(__uint_as_float(sw[0]), __uint_as_float(sw[1]))));
                    xr = bfr(xr);
                    const unsigned t = now32() + SCH(S_GAP);
                    if ((int)(t - ref - (layer ? SCH(S_XA1) : SCH(S_XA0))) > 0) st4_m<0xffu, 0u>(ws, xa + tw, enc(t + SCH(S_EX)));
                    CY(c_c); CYW(55, c_a, c_b); CYW(56, c_b, c_c);
                    STAMP(8 + 32 * layer); if (layer) DMARK(25); else DMARK(31);
                }
                float rs, xn;
                {
                    unsigned o = xm + (unsigned)(2048 + lane * 16);
                    asm volatile("" : "+v"(o), "+v"(cw));
                    u32x4 a[5], lt; gather_tab(o, a, lt, 9 + 32 * layer, ref + (layer ? SCH(S_XA1) : SCH(S_XA0)) + 8u);
                    float mb, rden; img_stats(a, mb, rden);
                    float r0 = 0.f, r1 = 0.f;
#pragma unroll
                    for (int j = 0; j < 10; ++j) { r0 = dot2(a[j >> 1][(j & 1) * 2], w[j >> 1][(j & 1) * 2], r0); r1 = dot2(a[j >> 1][(j & 1) * 2 + 1], w[j >> 1][(j & 1) * 2 + 1], r1); }
                    const float r = bfr((sum_x4(r0 + r1) - mb * cw) * rden);
                    rs = bfr(frcp(1.f + fexp(-r)));
                    xn = bfr(bfr(xr - mb) * rden);
                    asm volatile("s_waitcnt vmcnt(0)" : "+v"(lt), "+v"(rs), "+v"(xn) :: "memory");
                    ref += (unsigned)imax(late(lt, ref + (layer ? SCH(S_XA1) : SCH(S_XA0))), 0);
                    if (!layer) DMARK(38);
                    if (layer == 0) {
                        if (g == 0) st4_m<1u, 0u>(ws, WS_EP, tag);
                        lds_wait_ge(lb + L_CNT + 44, 2u);
                    }
                    STAMP(9 + 32 * layer); if (!layer) DMARK(37);
                }
                {
                    unsigned o = (a_own(oi) ^ cx) + (unsigned)(layer * LB + lane * 16 + 2048);
                    unsigned so = layer ? (unsigned)((p4 * DM + oi * 4 + (lane >> 5) * 2) * 2) : WS_XB + po;
                    asm volatile("" : "+v"(rs), "+v"(xn), "+v"(o), "+v"(so));
                    wait_until(ref + (layer ? SCH(S_H51) : SCH(S_H50)));
                    CY(c_a);
                    u32x4 a[6];
                    GATHER(13 + 32 * layer, { a[0] = ld16_sc1o<-2048>(ws, o); a[1] = ld16_sc1o<-1024>(ws, o); a[2] = ld16_sc1o<0>(ws, o);
                                 a[3] = ld16_sc1o<1024>(ws, o); a[4] = ld16_sc1o<2048>(ws, o); a[5] = ld16_sc1o<3072>(ws, o);
                                 asm volatile("s_waitcnt vmcnt(0)" : "+v"(a[0]), "+v"(a[1]), "+v"(a[2]), "+v"(a[3]), "+v"(a[4]), "+v"(a[5]) :: "memory"); },
                           allok(umax(umax(mx4(a[0], mx4(a[1])), mx4(a[2], mx4(a[3]))), mx4(a[4], mx4(a[5])))));
                    CY(c_b);
                    const float y = bfr(red4(((asf(a[0]) + asf(a[1])) + (asf(a[2]) + asf(a[3]))) + (asf(a[4]) + asf(a[5]))));
                    xr += bfr(xn + bfr(y * rs));
                    auto sw = __builtin_amdgcn_permlane16_swap(__float_as_uint(xr), __float_as_uint(xr), false, false);
                    const unsigned w = pk2(__uint_as_float(sw[0]), __uint_as_float(sw[1]));
                    if (layer == 0) {
                        st4_sc1(ws, so, enc(w));
                        const unsigned t = now32() + SCH(S_GAP), t2 = ref + SCH(S_XB), tx = ((int)(t - t2) > 0 ? t + SCH(S_EXB) : t2) | 1u;
                        asm volatile("ds_write_b32 %0, %1" :: "v"(lb + L_CNT + 60), "v"(tx) : "memory");
                        if ((sub == 0 && (h & 3) == 0) || (int)(t - t2) > 0) st4_m<0xffu, 0u>(ws, WS_XB + tw, enc(tx));
                    }
                    else {
                        asm volatile("s_mov_b32 exec_lo, 15\n\ts_mov_b32 exec_hi, 15\n\tglobal_store_dword %0, %1, %2 " OSX "\n\ts_mov_b64 exec, -1" :: "v"(so), "v"(w), "s"(out) : "memory");
                    }
                    *reinterpret_cast<float*>(smem + L_XR + lane * 4) = bfr(xr);
                    CY(c_c); CYW(57, c_a, c_b); CYW(58, c_b, c_c);
                    STAMP(13 + 32 * layer); if (!layer) DMARK(36);
                }
            } else if (wave == 4 && layer_ == 0) {
                LANES();
                const u32x4 ones = splat<~0u>();
                if (SCH(S_FL) & 32) lds_wait_ge(lb + L_CNT + 8, 1u);
                if (SCH(S_FL) & 64) { lds_wait_ge(lb + L_CNT + 48, 1u); wait_until(lds_ld(lb + L_CNT + 48) + 30u); }
#pragma unroll
                for (int i = 0; i < 6; ++i) st16_l(ws, (a_own(oi) ^ CX(0)) + (unsigned)(i * 1024 + lane * 16), ones);
                if (oi < 96 && lane < 32) st16_l(ws, WS_XB + (unsigned)((oi / 12) * 8192 + (oi % 12) * 512 + lane * 16), ones);
                asm volatile("s_waitcnt vmcnt(0)\n\tds_write_b32 %0, %1" :: "v"(lb + L_CNT + 44), "v"(1u) : "memory");
                lds_wait_ge(lb + L_CNT + 8, 1u); wait_until(lds_ld(lb + L_CNT + 8) + SCH(S_CL) - 100u);
                if (SCH(S_FL) & 2) {
#pragma unroll
                    for (int i = 0; i < 2; ++i) st16_l(ws, a_own(oi) + (unsigned)(LB + 6144 + i * 1024 + lane * 16), (u32x4){tag + 1u, tag + 1u, tag + 1u, tag + 1u});
                }
                if (oi < 128) { st16_l(ws, WS_XA + LB + (unsigned)(oi * 1536 + lane * 16), ones); if (lane < 32) st16_l(ws, WS_XA + LB + (unsigned)(oi * 1536 + 1024 + lane * 16), ones); }
#pragma unroll
                for (int i = 0; i < 3; ++i) st16_l(ws, a_h1(h, lane >> 4) + (unsigned)(((2 * i + ((lane >> 3) & 1)) * 5 + sub) * 128 + (lane & 7) * 16), ones);
                asm volatile("s_waitcnt vmcnt(0)\n\tds_write_b32 %0, %1" :: "v"(lb + L_CNT + 44), "v"(2u) : "memory");
                STAMP(26);
            }
        } else {
            LAYER();
#define LANDED() do { asm volatile("s_waitcnt vmcnt(0)\n\ts_mov_b64 exec, 1\n\tds_add_u32 %0, %1\n\ts_mov_b64 exec, -1" :: "v"(lb + L_CNT + 44), "v"(1u) : "memory"); STAMP(27); } while (0)
            if (wave < 4) {
                LANES();
                if (layer == 0) { LANDED(); lds_wait_ge(lb + L_CNT + 8, 1u); ref = lds_ld(lb + L_CNT + 8); }
                if (layer == 0) lds_wait_ge(lb + L_CNT + 44, WOMODE || LATED ? 7u : 8u);
                const unsigned gs = xm + (unsigned)(wave * 1280);
                int st;
                asm volatile(ASM_GU : [st] "=&s"(st) : [a0] "v"(lb + L_GU + (unsigned)((wave * 160 + 8 * kq + q4 + 4 * (kq & 1)) * 32 + p4 * 8)), [gs] "v"(gs + lane * 16),
                             [g2] "v"(gs + (unsigned)(1024 + kq * 64 + p4 * 16 + q4 * 4)), [pw] "v"(lb + L_OP + (unsigned)(n * 784 + kq * 192 + wave * 8)), [gw] "v"(lb + L_OP + (unsigned)(4096 + n * 272 + kq * 64 + wave * 4)),
                             [ws] "s"(ws), [tg] "s"(ref + (layer ? SCH(S_XA1) : SCH(S_XA0))), [dbg] "s"(lb + L_DBG + wave * 512 + 256 + (10 + 32 * layer) * 4) : "memory", "scc", "vcc", "v6", "v7", CLOB_8_15, CLOB_16_63);
                STAMP(11 + 32 * layer);
                if (wave == 1 && layer == 0) {
                    u32x4 lt = ld16_sc1(ws, xm + (unsigned)(5120 + lane * 16));
                    asm volatile("s_waitcnt vmcnt(0)" : "+v"(lt) :: "memory");
                    asm volatile("ds_write_b32 %0, %1" :: "v"(lb + L_CNT + 60), "v"((ref + SCH(S_XB) + (unsigned)imax(late(lt, ref + SCH(S_XA0)), 0)) | 1u) : "memory");
                }
            } else {
                LANES();
                if (layer == 0) {
                    if (wave != 7) LANDED();
                    else if (LATED) asm volatile("s_waitcnt vmcnt(0)\n\tds_write_b32 %0, %1" :: "v"(lb + L_CNT + 40), "v"(1u) : "memory");
                    if (LATED) lds_wait_ge(lb + L_CNT + 40, 1u); else lds_wait_ge(lb + L_CNT + 44, WOMODE ? 7u : 8u);
                    if (wave == 7) DMARK(39);
                }
                const unsigned wdo = lb + L_WD + (unsigned)(4 * kq * 1312 + (160 * (wave & 3) + 4 * p4) * 2);
                unsigned w0 = wdo + q4 * 1312, w1 = wdo + ((q4 + 1) & 3) * 1312, w2 = wdo + ((q4 + 2) & 3) * 1312, w3 = wdo + ((q4 + 3) & 3) * 1312;
                asm volatile(ASM_HS : [w0] "+{v8}"(w0), [w1] "+{v9}"(w1), [w2] "+{v10}"(w2), [w3] "+{v11}"(w3) :
                             [rp] "v"(lb + L_OP + (unsigned)(p4 * 784 + (4 * kq + q4) * 48)), [cp] "v"(lb + L_OP + (unsigned)(4 * 784 + (4 * kq + q4) * 48)),
                             [sq] "v"(lb + L_OP + (unsigned)(4096 + p4 * 288)), [sx] "v"(lb + L_OP + (unsigned)(4096 + p4 * 272 + 64)),
                             [so] "v"(((unsigned)(layer * LB + ((10 * (wave & 1) + q4) * 4 + kq) * GP + (wave & 2) * 4096 + mi * 64 + p4 * 16)) ^ ((unsigned)((q4 * 4 + kq) << 7) | cx)), [ws] "s"(ws)
                             : "memory", "v12", "v13", CLOB_16_63);
                STAMP(12 + 32 * layer); if (!layer) DMARK(32);
            }
        }
        if (layer_ != 0) break;
        unsigned tz = 0u;
        if (wave == 7) lds_wait_ge(lb + L_CNT + 8, 1u);
        if (is_mlp || wave == 7) tz = (lds_ld(lb + L_CNT + 8) + SCH(S_H50) + SCH(S_Y2)) | 1u;
        {
            const unsigned xb = WS_XB + (a_rep(xg) ^ CX(0));
#define QPROJ() do { const int tp = wave == 6 ? 5 : wave; int st_, rf_; \
                    const unsigned a0_ = lb + L_WQ + (unsigned)((8 * kq + q4) * 384 + ((2 * tp) ^ ((q4 >> 1) | ((kq & 1) << 1))) * 32 + p4 * 8); \
                    asm volatile(ASM_QP : [st] "=&s"(st_), [rf] "=&s"(rf_) : [a0] "{v60}"(a0_), [a1] "{v61}"(a0_ ^ 32u), [gs] "{v62}"(xb + (unsigned)(sub * 1024 + lane * 16)), \
                                 [so] "{v63}"((a_h1(h, p4) ^ CX(1)) + (unsigned)((tp * 5 + sub) * 128 + kq * 32 + (q4 & 1) * 16)), \
                                 [ws] "s"(ws), [fl] "s"(lb + L_CNT + 60), [tz] "s"(SCH(S_H50) + SCH(S_Y2)), [dbg] "s"(lb + L_DBG + wave * 512 + 256 + 14 * 4) : "memory", "scc", "vcc", CLOB_16_59); } while (0)
#define ATT_IN() LANES(); \
            ATT_ADDR(1); \
            const unsigned go = (wave < 6 ? (a_h1(h, b) ^ CX(1)) + (unsigned)(wave * 640) : (a_own(oi) ^ CX(1)) + (unsigned)(LB + (wave & 1) * 3072)) + (unsigned)(lane * 16); \
            const unsigned qo = lb + L_Q + (unsigned)(wave * 64 + (lane & 1) * 32 + ((lane >> 1) & 3) * 8 + kq * 2); \
            const float cs = *reinterpret_cast<const float*>(smem + L_CS + wave * 128 + (lane & 1) * 64 + ((lane >> 1) & 3) * 16 + kq * 4); \
            int st
#define ATT_OPS : [st] "=&s"(st) : [qa] "{v8}"(qa), [pw] "{v9}"(pw), [pr] "{v10}"(pr), [ow] "{v11}"(ow), [mo] "{v12}"(mo), [r0] "{v5}"(r0), [so] "{v6}"(so), [go] "{v13}"(go), [qo] "{v14}"(qo), [cs] "{v15}"(cs), \
                [mlw] "s"(lb + L_ML + wave * 4), [ma] "s"(lb + L_ML), [ws] "s"(ws), [mo_] "s"(0xffff0000ffff0000ull), [mh_] "s"(0xffffffff00000000ull), [wv] "s"(wave), \
                [cl] "s"((wave + 2) & (sub - 5) & 8), [tg] "s"(SCH(S_H1)), [tz] "s"(is_mlp && tz ? tz + SCH(S_Y3) : 0u), [dbg] "s"(lb + L_DBG + wave * 512 + 256 + 35 * 4) : "memory", "scc", "vcc", CLOB_16_63
            if (wave >= 4) {
                ATT_IN();
                unsigned go_ = go, qo_ = qo; float cs_ = cs;
                asm volatile("" : "+{v8}"(qa), "+{v9}"(pw), "+{v10}"(pr), "+{v11}"(ow), "+{v12}"(mo), "+{v5}"(r0), "+{v6}"(so), "+{v13}"(go_), "+{v14}"(qo_), "+{v15}"(cs_));
                if (!(wave & 1) && !is_mlp) { QPROJ(); DMARK(21); }
                if (wave == 7) {
                unsigned o = xb + (unsigned)(2048 + lane * 16);
                asm volatile("" : "+v"(o));
                unsigned gx = 0u;
                if (PRI & 2) __builtin_amdgcn_s_setprio(3);
                if (tz) while ((int)(now32() - tz) < 0) __builtin_amdgcn_s_sleep(3);
#pragma unroll 1
                for (unsigned spins = 0; spins < (kSpinCap << 6) && gx == 0u; ++spins) gx = lds_ld(lb + L_CNT + 60);
                u32x4 a[5], r; gather_tab(o, a, r, 14, gx + 4u);
                unsigned k0 = lane & 7 ? 1u : 2u;
                asm volatile("s_waitcnt vmcnt(0)" : "+v"(r), "+v"(k0) :: "memory");
                auto dif = [&](unsigned w, unsigned k) { const unsigned x = w ^ gx, y = w + k; return x < y ? x : y; };
                if (__builtin_expect(__builtin_amdgcn_ballot_w64((dif(r[0], k0) | dif(r[1], 1u) | dif(r[2], 1u) | dif(r[3], 1u)) != 0u) != 0ull, 0)) {
#pragma unroll 1
                    for (unsigned spins = 0; spins < kSpinCap && !allok(lane & 7 ? 0u : r[0]); ++spins) {
                        RETRY(15, spins + 1);
                        __builtin_amdgcn_s_sleep(4);
                        r = ld16_sc1o<3072>(ws, o);
                        asm volatile("s_waitcnt vmcnt(0)" : "+v"(r) :: "memory");
                    }
                    gx = (gx + (unsigned)late(r, gx)) | 1u;
                }
                asm volatile("ds_write_b32 %0, %1" :: "v"(lb + L_CNT + 20), "v"(gx) : "memory");
                STAMP(38);
                float mb, rden; img_stats(a, mb, rden);
                STAMP(14); DMARK(24);
                if (lane == b) *reinterpret_cast<u32x2*>(smem + L_CNT + 32) = (u32x2){__float_as_uint(mb), __float_as_uint(rden)};
                BAR();
                }
                { const unsigned go = go_, qo = qo_; const float cs = cs_; asm volatile(ASM_ATT_B ATT_OPS); }
            }
            else if (is_mlp) { ATT_IN(); asm volatile(ASM_ATT_A ATT_OPS); }
            else {
                ATT_IN();
                const unsigned a0 = lb + L_WO + (unsigned)((8 * kq + q4) * 256 + ((2 * wave) ^ (q4 | ((kq & 1) << 2))) * 32 + p4 * 8), h2 = (a_h2(h) ^ CX(1)) + LB; const int ot = (8 * sub + 2 * wave + (q4 & 1)) * 4 + kq, rw = (q4 & 1) ? 3 - p4 : p4;
                unsigned i0 = qa, i1 = pw, i2 = pr, i3 = ow, i4 = mo, i5 = go, i6 = qo, t0; float i7 = cs; int rf;
                unsigned j0 = a0, j1 = a0 ^ 32u, j2 = h2 + (unsigned)((lane >> 3) * 128 + rw * 32 + (q4 & 1) * 16), j3 = h2 + (unsigned)(3072 + ((n >> 3) * 4 + rw) * 128);
                unsigned j4 = (a_own(ot) + (unsigned)(LB + 6144 + h * 64 + rw * 16)) ^ ((unsigned)((ot & 15) << 7) | CX(1));
                asm volatile("" : "+{v8}"(i0), "+{v9}"(i1), "+{v10}"(i2), "+{v11}"(i3), "+{v12}"(i4), "+{v13}"(i5), "+{v14}"(i6), "+{v15}"(i7), "+{v0}"(j0), "+{v1}"(j1), "+{v2}"(j2), "+{v3}"(j3), "+{v4}"(j4), "+{v5}"(r0), "+{v6}"(so));
                QPROJ();
                asm volatile(ASM_ATT_P : [st] "=&s"(st), [rf] "=&s"(rf), [t0] "=&{v7}"(t0), [qa] "+{v8}"(i0), [pw] "+{v9}"(i1), [pr] "+{v10}"(i2), [ow] "+{v11}"(i3), [mo] "+{v12}"(i4), [go] "+{v13}"(i5), [qo] "+{v14}"(i6), [cs] "+{v15}"(i7)
                             : [a0] "{v0}"(j0), [a1] "{v1}"(j1), [gs] "{v2}"(j2), [gm] "{v3}"(j3), [s2] "{v4}"(j4), [r0] "{v5}"(r0), [so] "{v6}"(so),
                               [mlw] "s"(lb + L_ML + wave * 4), [ma] "s"(lb + L_ML), [ws] "s"(ws), [mo_] "s"(0xffff0000ffff0000ull), [mh_] "s"(0xffffffff00000000ull),
                               [tg] "s"(SCH(S_H1)), [t2] "s"(SCH(S_H2)), [wv] "s"(wave), [tag] "s"(tag), [dbg] "s"(lb + L_DBG + wave * 512 + 256 + 35 * 4) : "memory", "scc", "vcc", CLOB_16_63);
                STAMP(39); DMARK(23);
                break;
            }
            if (wave != 7) STAMP(38);
            DMARK(22); if (is_mlp) DMARK(26);
            ref = lds_ld(lb + L_CNT + 20);
        }
    }
    asm volatile("s_endpgm\n\t.fill 128, 4, 0xbf800000\nmega_pf_end:" ::: "memory");
}

extern "C" void kernel_launch(void* const* d_in, const int* in_sizes, int n_in, void* d_out, int out_size, void* d_ws,
                              size_t ws_size, hipStream_t stream) {
    const u16* x0 = (const u16*)d_in[0];
    const u16* kc = (const u16*)d_in[1];
    const u16* vc = (const u16*)d_in[2];
    const u16* wq = (const u16*)d_in[3];
    const u16* wo = (const u16*)d_in[4];
    const u16* wr = (const u16*)d_in[6];
    const u16* wg = (const u16*)d_in[7];
    const u16* wu = (const u16*)d_in[8];
    const u16* wd = (const u16*)d_in[9];
    (void)hipFuncSetAttribute((const void*)mega_kernel, hipFuncAttributeMaxDynamicSharedMemorySize, L_END);
    mega_kernel<<<256, 512, L_END, stream>>>(kc, vc, (unsigned char*)(((size_t)d_ws + LB - 1) & ~(size_t)(LB - 1)), x0, wq, wg, wu, wo, wr, wd, (u16*)d_out
                                             );
}
```

```cpp
#define SCHED {134,236,304,56,200,276,328,424,10,0,495,3,0,260,14,0,256,16,18,0,78,65,572,60,4,16,0,9,70,40,16,40,0,4,170,230,460,30,70,0,120,45,44,46}
#define WOMODE 0
#define KAL 4096
#define P_LN 7
#define P_QP 9
#define F7 4
#define DHI 4
#define WQP 4
#define EPF 3
#define LDR 38
#define CA 8
#define A7 13
#define Q7 22
#define L7 25
#define QG 35
#define QW 6
#define ECX "sc1"
#define NN7 18
#define WK -4
#define DNW 21
#define NM7 0
#define TSX 1
#define WSP -1
#define WPRE 0
#include <hip/hip_runtime.h>
#include <hip/hip_bf16.h>
#include <stdint.h>

typedef unsigned short u16;
typedef unsigned u32x4 __attribute__((ext_vector_type(4)));
typedef unsigned u32x2 __attribute__((ext_vector_type(2)));
typedef unsigned u32x8 __attribute__((ext_vector_type(8)));
typedef float f32x4 __attribute__((ext_vector_type(4)));
typedef short s16x4 __attribute__((ext_vector_type(4)));
typedef __attribute__((ext_vector_type(4))) __bf16 bf16x4;
typedef __attribute__((ext_vector_type(8))) __bf16 bf16x8;
#define LAS __attribute__((address_space(3)))

constexpr int DM = 640, HQ = 32, SK = 4096, DH = 192, WIN = 1024;
constexpr int QD = HQ * DH;
constexpr float SCALE = 0.07216878364870322f;
constexpr float L2E = 1.44269504088896341f;
constexpr unsigned kSpinCap = 1u << 16;
constexpr int NMLP = 96;
#define TDEPTH 4

#define POL 0
#define WPOL " nt"
enum { S_XA0, S_H50, S_XB, S_H1, S_H2, S_H3, S_XA1, S_H51, S_GAP, S_PA, S_PD, S_WT, S_WD, S_CL, S_H4, S_GS, S_TS, S_TW, S_TR, S_C5, S_H30, S_H20, S_PM, S_EX, S_N0, S_N1, S_NT, S_ND, S_NR, S_PT, S_FL, S_BT, S_G1, S_G2, S_G3, S_X3, S_X4, S_X5, S_X6, S_X7, S_Y0, S_Y1, S_Y2, S_Y3, S_N };
#define S_LT S_GS
#define S_EXB S_NR
#define S_PO S_BT
#define S_MO S_PT
#define S_P7 S_X4
#define S_O7 S_X5

constexpr unsigned LB = 2097152, GP = 16384;
constexpr unsigned WS_XA = 80 * GP;
constexpr unsigned WS_H2 = 84 * GP;
constexpr unsigned WS_XB = 92 * GP;
constexpr unsigned WS_TS = 96 * GP;
constexpr unsigned WS_H1B = LB + 92 * GP;
constexpr unsigned WS_PR = 124 * GP;
constexpr unsigned WS_EP = 2 * LB;
constexpr unsigned WS_AR = WS_EP + 1024;
constexpr unsigned WS_GO = WS_EP + 4096;
constexpr unsigned WS_H1A = WS_EP + 8192;
constexpr unsigned WS_SINK = WS_H1A + 32 * 5 * 3840;
__device__ __forceinline__ unsigned a_rep(int r) { return (unsigned)((r & 3) * GP + (r >> 2) * 8192); }
__device__ __forceinline__ unsigned a_own(int o) { return (unsigned)(o * GP - (o >= 80 ? 80 * GP - 8192 : 0)); }
__device__ __forceinline__ unsigned a_h2(int h) { return WS_H2 + (unsigned)(((h & 15) >> 1) * GP + (h >> 4) * 8192 + (h & 1) * 4096); }
__device__ __forceinline__ unsigned a_hf(int h) { return WS_XA + a_rep(h >> 2) + (unsigned)((h & 2) * 6 * GP + 6144 + (h & 1) * 1024); }
__device__ __forceinline__ unsigned a_ts(int r) { return WS_TS + (unsigned)(((r & 3) >> 1) * GP + (r >> 2) * 8192 + (r & 1) * 4096); }
__device__ __forceinline__ unsigned a_h1(int h, int row) { return WS_H1B + (unsigned)(((h & 15) * 2 + (row >> 1)) * GP + (h >> 4) * 8192 + (row & 1) * 3840); }
#define CX(layer) ((pol << (12 - (layer))) & 8192u)
constexpr int LEADER = LDR;
#define RST 128
constexpr unsigned WS_DBG = 6680576;
static_assert(WS_SINK + 256 * 128 <= WS_DBG, "ws map");

constexpr int L_WQ = 0;
constexpr int L_WO = 49152;
constexpr int L_GU = 0;
constexpr int L_WD = 40960;
constexpr int OPS = 3200;
constexpr int L_OP = 98304;
constexpr int L_P = L_OP + 8 * OPS;
constexpr int L_Q = L_P + 8 * 1088;
constexpr int L_CS = L_Q + 512;
constexpr int L_ML = L_CS + 1536;
constexpr int L_XR = L_ML + 64;
constexpr int L_CNT = L_XR + 256;
constexpr int L_FW = L_CNT + 64;
constexpr int L_PF = L_FW + 64;
constexpr int L_POL = L_PF + 256;
constexpr int L_WR = L_POL + 32;
constexpr int L_DBG = L_WR + 1536;
constexpr int L_END = L_DBG + 4096;
static_assert(L_CNT + 32 - L_ML == 352, "stats offset");
static_assert(L_END <= 163840 && L_END > 81920 && L_Q % 16 == 0, "LDS");

typedef __attribute__((ext_vector_type(2))) __bf16 bf16x2;
typedef float f32x2 __attribute__((ext_vector_type(2)));
__device__ __forceinline__ unsigned pk2(float a, float b) { return __builtin_bit_cast(unsigned, __builtin_convertvector((f32x2){a, b}, bf16x2)); }
__device__ __forceinline__ float lo16(unsigned w) { return __uint_as_float(w << 16); }
__device__ __forceinline__ float hi16(unsigned w) { return __uint_as_float(w & 0xffff0000u); }
__device__ __forceinline__ float bfr(float f) { return lo16(pk2(f, f)); }
template <int CTRL> __device__ __forceinline__ float dpp(float x) {
    return __builtin_bit_cast(float, __builtin_amdgcn_mov_dpp(__builtin_bit_cast(int, x), CTRL, 0xf, 0xf, true));
}
template <int CTRL> __device__ __forceinline__ unsigned dppu(unsigned x) { return (unsigned)__builtin_amdgcn_mov_dpp((int)x, CTRL, 0xf, 0xf, true); }
__device__ __forceinline__ float row16_sum(float v) {
    v += dpp<0xB1>(v); v += dpp<0x4E>(v); v += dpp<0x141>(v); v += dpp<0x140>(v); return v;
}
__device__ __forceinline__ float sw16_add(float a, float b) { auto s = __builtin_amdgcn_permlane16_swap(__float_as_uint(a), __float_as_uint(b), false, false); return __uint_as_float(s[0]) + __uint_as_float(s[1]); }
__device__ __forceinline__ float sw32_add(float a, float b) { auto s = __builtin_amdgcn_permlane32_swap(__float_as_uint(a), __float_as_uint(b), false, false); return __uint_as_float(s[0]) + __uint_as_float(s[1]); }
__device__ __forceinline__ float wave_sum(float v) { v = row16_sum(v); v = sw16_add(v, v); return sw32_add(v, v); }
__device__ __forceinline__ int imax(int a, int b) { return a > b ? a : b; }
__device__ __forceinline__ unsigned umax(unsigned a, unsigned b) { return a > b ? a : b; }
__device__ __forceinline__ int wave_imax(int v) {
    v = imax(v, __builtin_amdgcn_mov_dpp(v, 0xB1, 0xf, 0xf, true)); v = imax(v, __builtin_amdgcn_mov_dpp(v, 0x4E, 0xf, 0xf, true));
    v = imax(v, __builtin_amdgcn_mov_dpp(v, 0x141, 0xf, 0xf, true)); v = imax(v, __builtin_amdgcn_mov_dpp(v, 0x140, 0xf, 0xf, true));
    return imax(imax(__builtin_amdgcn_readlane(v, 0), __builtin_amdgcn_readlane(v, 16)), imax(__builtin_amdgcn_readlane(v, 32), __builtin_amdgcn_readlane(v, 48)));
}
__device__ __forceinline__ int fkey(int bits) { return bits ^ ((bits >> 31) & 0x7fffffff); }

#define BAR() asm volatile("s_waitcnt lgkmcnt(0)\n\ts_barrier" ::: "memory")
#define VMW(n) asm volatile("s_waitcnt vmcnt(%0)" :: "i"(n) : "memory")

__device__ __forceinline__ u32x4 ld16_sc1(const void* sbase, unsigned voff) {
    u32x4 r; asm volatile("global_load_dwordx4 %0, %1, %2 sc1" : "=v"(r) : "v"(voff), "s"(sbase) : "memory"); return r;
}
template <int IMM> __device__ __forceinline__ u32x4 ld16_sc1o(const void* sbase, unsigned voff) {
    u32x4 r; asm volatile("global_load_dwordx4 %0, %1, %2 offset:%3 sc1" : "=v"(r) : "v"(voff), "s"(sbase), "i"(IMM) : "memory"); return r;
}
template <int IMM> __device__ __forceinline__ u32x2 ld8_sc1o(const void* sbase, unsigned voff) {
    u32x2 r; asm volatile("global_load_dwordx2 %0, %1, %2 offset:%3 sc1" : "=v"(r) : "v"(voff), "s"(sbase), "i"(IMM) : "memory"); return r;
}
__device__ __forceinline__ unsigned ld4_sc1(const void* sbase, unsigned voff) {
    unsigned r; asm volatile("global_load_dword %0, %1, %2 sc1" : "=v"(r) : "v"(voff), "s"(sbase) : "memory"); return r;
}
__device__ __forceinline__ void st16_sc1(void* sbase, unsigned voff, u32x4 v) {
    asm volatile("global_store_dwordx4 %0, %1, %2 sc1\n\ts_nop 1" :: "v"(voff), "v"(v), "s"(sbase) : "memory");
}
#define XSX "sc1 nt"
#define XLX "sc1 nt"
#define LCX "sc1"
__device__ __forceinline__ void st16_e(void* sbase, unsigned voff, u32x4 v) {
    asm volatile("global_store_dwordx4 %0, %1, %2 " ECX "\n\ts_nop 1" :: "v"(voff), "v"(v), "s"(sbase) : "memory");
}
__device__ __forceinline__ void st16_l(void* sbase, unsigned voff, u32x4 v) {
    asm volatile("global_store_dwordx4 %0, %1, %2 " LCX "\n\ts_nop 1" :: "v"(voff), "v"(v), "s"(sbase) : "memory");
}
__device__ __forceinline__ void st16_x(void* sbase, unsigned voff, u32x4 v) {
    asm volatile("global_store_dwordx4 %0, %1, %2 " XSX "\n\ts_nop 1" :: "v"(voff), "v"(v), "s"(sbase) : "memory");
}
template <int IMM> __device__ __forceinline__ u32x4 ld16_xo(const void* sbase, unsigned voff) {
    u32x4 r; asm volatile("global_load_dwordx4 %0, %1, %2 offset:%3 " XLX : "=v"(r) : "v"(voff), "s"(sbase), "i"(IMM) : "memory"); return r;
}
__device__ __forceinline__ void st8_sc1(void* sbase, unsigned voff, u32x2 v) {
    asm volatile("global_store_dwordx2 %0, %1, %2 sc1" :: "v"(voff), "v"(v), "s"(sbase) : "memory");
}
__device__ __forceinline__ void st4_sc1(void* sbase, unsigned voff, unsigned v) {
    asm volatile("global_store_dword %0, %1, %2 sc1" :: "v"(voff), "v"(v), "s"(sbase) : "memory");
}
__device__ __forceinline__ void st8(void* sbase, unsigned voff, u32x2 v) {
    asm volatile("global_store_dwordx2 %0, %1, %2" :: "v"(voff), "v"(v), "s"(sbase) : "memory");
}
template <int IMM> __device__ __forceinline__ u32x4 ld16_nt(const void* sbase, unsigned voff) {
    return __builtin_nontemporal_load(reinterpret_cast<const u32x4*>(reinterpret_cast<const unsigned char*>(sbase) + voff + IMM));
}
__device__ __forceinline__ u32x4 ld16_v(const void* sbase, unsigned voff) {
    return *reinterpret_cast<const u32x4*>(reinterpret_cast<const unsigned char*>(sbase) + voff);
}
__device__ __forceinline__ void dma16(const void* sbase, unsigned voff, unsigned lds_dst) {
    unsigned keep;
    asm volatile("s_mov_b32 %0, m0\n\ts_mov_b32 m0, %3\n\ts_nop 0\n\tglobal_load_lds_dwordx4 %1, %2" WPOL "\n\ts_mov_b32 m0, %0"
                 : "=&s"(keep) : "v"(voff), "s"(sbase), "s"(lds_dst) : "memory");
}
__device__ __forceinline__ void dma16p(const void* sbase, unsigned voff, unsigned lds_dst) {
    unsigned keep;
    asm volatile("s_mov_b32 %0, m0\n\ts_mov_b32 m0, %3\n\ts_nop 0\n\tglobal_load_lds_dwordx4 %1, %2\n\ts_mov_b32 m0, %0"
                 : "=&s"(keep) : "v"(voff), "s"(sbase), "s"(lds_dst) : "memory");
}
__device__ __forceinline__ void dma4(const void* sbase, unsigned voff, unsigned lds_dst) {
    unsigned keep;
    asm volatile("s_mov_b32 %0, m0\n\ts_mov_b32 m0, %3\n\ts_nop 0\n\tglobal_load_lds_dword %1, %2\n\ts_mov_b32 m0, %0"
                 : "=&s"(keep) : "v"(voff), "s"(sbase), "s"(lds_dst) : "memory");
}
__device__ __forceinline__ void dma4n(const void* sbase, unsigned voff, unsigned lds_dst) {
    unsigned keep;
    asm volatile("s_mov_b32 %0, m0\n\ts_mov_b32 m0, %3\n\ts_nop 0\n\tglobal_load_lds_dword %1, %2 nt\n\ts_mov_b32 m0, %0"
                 : "=&s"(keep) : "v"(voff), "s"(sbase), "s"(lds_dst) : "memory");
}
#define KEEPY " sc1"
__device__ __forceinline__ void pull(const void* sbase, unsigned voff, unsigned lds_dst) {
    unsigned keep;
    asm volatile("s_mov_b32 %0, m0\n\ts_mov_b32 m0, %3\n\ts_nop 0\n\tglobal_load_lds_dword %1, %2 nt\n\tglobal_load_lds_dword %1, %2" KEEPY "\n\ts_mov_b32 m0, %0"
                 : "=&s"(keep) : "v"(voff), "s"(sbase), "s"(lds_dst) : "memory");
}
__device__ __forceinline__ unsigned poll4(void* sbase, unsigned voff) {
    unsigned r;
    asm volatile("global_load_dword %0, %1, %2 sc1\n\ts_waitcnt vmcnt(0)" : "=v"(r) : "v"(voff), "s"(sbase) : "memory");
    return r;
}
__device__ __forceinline__ unsigned enc(unsigned w) { return w < 0xfffffffeu ? w : 0xfffffffeu; }
__device__ __forceinline__ u32x4 enc4(f32x4 c) { return (u32x4){enc(__float_as_uint(c[0])), enc(__float_as_uint(c[1])), enc(__float_as_uint(c[2])), enc(__float_as_uint(c[3]))}; }
__device__ __forceinline__ unsigned mx4(u32x4 a) { return umax(umax(umax(a[0], a[1]), a[2]), a[3]); }
__device__ __forceinline__ unsigned mx4(u32x4 a, unsigned m) { return umax(umax(umax(umax(m, a[0]), a[1]), a[2]), a[3]); }
__device__ __forceinline__ bool allok(unsigned m) { return __builtin_amdgcn_ballot_w64(m == 0xffffffffu) == 0ull; }
__device__ __forceinline__ u32x4 tag4(u32x4 a, unsigned t8) { return (a & 0xffffff00u) | t8; }
__device__ __forceinline__ unsigned or4(u32x4 a) { return a[0] | a[1] | a[2] | a[3]; }
__device__ __forceinline__ bool tagok(unsigned m) { return __builtin_amdgcn_ballot_w64((m & 0xffu) != 0u) == 0ull; }
__device__ __forceinline__ f32x4 asf(u32x4 a) { return __builtin_bit_cast(f32x4, a); }

__device__ __forceinline__ bf16x4 tr64(unsigned lds_addr) { return __builtin_amdgcn_ds_read_tr16_b64_v4bf16((LAS bf16x4*)(size_t)lds_addr); }
__device__ __forceinline__ bf16x8 cat8(bf16x4 a, bf16x4 b) { return __builtin_shufflevector(a, b, 0, 1, 2, 3, 4, 5, 6, 7); }
__device__ __forceinline__ f32x4 mfma32(bf16x8 a, bf16x8 b, f32x4 c) { return __builtin_amdgcn_mfma_f32_16x16x32_bf16(a, b, c, 0, 0, 0); }
__device__ __forceinline__ f32x4 mfma32u(u32x4 a, u32x4 b, f32x4 c) { return mfma32(__builtin_bit_cast(bf16x8, a), __builtin_bit_cast(bf16x8, b), c); }
__device__ __forceinline__ f32x4 mfma32w(bf16x8 a, u32x4 b, f32x4 c) { return mfma32(a, __builtin_bit_cast(bf16x8, b), c); }
__device__ __forceinline__ f32x4 mfma16(bf16x4 a, u32x2 b, f32x4 c) {
    return __builtin_amdgcn_mfma_f32_16x16x16bf16_1k(__builtin_bit_cast(s16x4, a), __builtin_bit_cast(s16x4, b), c, 0, 0, 0);
}
__device__ __forceinline__ unsigned now32() { return (unsigned)__builtin_amdgcn_s_memrealtime(); }
#define WSL 1
__device__ __forceinline__ void wait_until(unsigned t) {
    asm volatile("s_branch 2f\n1:\n\ts_sleep 2\n2:\n\ts_memrealtime vcc\n\ts_waitcnt lgkmcnt(0)\n\ts_sub_i32 vcc_lo, vcc_lo, %0\n\ts_cmp_lt_i32 vcc_lo, -14\n\ts_cbranch_scc1 1b\n\ts_cmp_lt_i32 vcc_lo, 0\n\ts_cbranch_scc0 4f\n"
                 "3:\n\ts_memrealtime vcc\n\ts_waitcnt lgkmcnt(0)\n\ts_sub_i32 vcc_lo, vcc_lo, %0\n\ts_cmp_lt_i32 vcc_lo, 0\n\ts_cbranch_scc1 3b\n4:" :: "s"(t) : "memory", "scc", "vcc");
}
__device__ __forceinline__ unsigned lds_ld(unsigned addr) { unsigned t; asm volatile("ds_read_b32 %0, %1\n\ts_waitcnt lgkmcnt(0)\n\ts_nop 0" : "=v"(t) : "v"(addr) : "memory"); return __builtin_amdgcn_readfirstlane(t); }
__device__ __forceinline__ void lds_wait_ge(unsigned addr, unsigned v) {
#pragma unroll 1
    for (unsigned spins = 0; spins < (kSpinCap << 6); ++spins) { if (lds_ld(addr) >= v) break; __builtin_amdgcn_s_sleep(1); }
}

#define STAMPS 0
#define STAMP(i) do {} while (0)
#define STAMPV(i, t) do {} while (0)
#define RETRY(i, n) do {} while (0)
#define RETRY_ASM ""
#define RETRY_ASM3 ""
#define RETRY_ASM2 ""

#define CY(v) do {} while (0)
#define CYW(slot, a, b) do {} while (0)
#define CYS(slot, a, b) do {} while (0)
__device__ __forceinline__ int swz(int r) { return r ^ (((r >> 3) & 1) << 2); }
__device__ __forceinline__ f32x2 unpk(unsigned w) { return (f32x2){lo16(w), hi16(w)}; }
__device__ __forceinline__ unsigned pk2v(f32x2 v) { return __builtin_bit_cast(unsigned, __builtin_convertvector(v, bf16x2)); }
__device__ __forceinline__ float frcp(float x) { return __builtin_amdgcn_rcpf(x); }
__device__ __forceinline__ float fexp(float x) { return __builtin_amdgcn_exp2f(x * L2E); }

__device__ __forceinline__ void ln_consts(float mean, float var, float& mb, float& rden) {
    const unsigned mv = pk2(mean, __builtin_amdgcn_fmed3f(var, 0.f, 3.0e38f));
    mb = lo16(mv);
    rden = frcp(bfr(__builtin_amdgcn_sqrtf(bfr(hi16(mv) + 1e-5f))));
}
__device__ __forceinline__ void row_stats(u32x4 a, u32x4 c, int lane, float& mb, float& rden) {
    const float c0 = __uint_as_float((unsigned)__builtin_amdgcn_readfirstlane((int)a[0]) << 16);
    const f32x2 c2 = {c0, c0};
    f32x2 s2 = {0.f, 0.f}, q2 = s2, t2 = s2, u2 = s2;
#pragma unroll
    for (int i = 0; i < 4; ++i) { const f32x2 d = unpk(a[i]) - c2, e = unpk(c[i]) - c2; s2 += d; q2 += d * d; t2 += e; u2 += e * e; }
    float s = s2[0] + s2[1], q = q2[0] + q2[1];
    if (lane < 16) { s += t2[0] + t2[1]; q += u2[0] + u2[1]; }
    s = wave_sum(s); q = wave_sum(q);
    const float ms = s * (1.f / DM);
    ln_consts(c0 + ms, (q - s * ms) * (1.f / DM), mb, rden);
}
__device__ __forceinline__ float red3(f32x4 v) { float z = sw16_add(sw32_add(v[0], v[2]), sw32_add(v[1], v[3])); return z + dpp<0x128>(z); }
__device__ __forceinline__ float red4(f32x4 v) { float z = red3(v); return z + dpp<0x124>(z); }
__device__ __forceinline__ float sum_x4(float v) { v += dpp<0x124>(v); v += dpp<0x128>(v); v = sw16_add(v, v); return sw32_add(v, v); }
__device__ __forceinline__ float dot2(unsigned a, unsigned b, float c) { return __builtin_amdgcn_fdot2_f32_bf16(__builtin_bit_cast(bf16x2, a), __builtin_bit_cast(bf16x2, b), c, false); }
__device__ __forceinline__ int kpos(int r) { return (r & 7) | (((r >> 5) & 3) << 3) | (((r >> 3) & 3) << 5); }
__device__ __forceinline__ int gpos(int r) { const int qt = r / 160, i = r - 160 * qt; return 160 * qt + (i < 128 ? kpos(i) : i); }
__device__ __forceinline__ float vmax3(float a, float b, float c) { float r; asm("v_max3_f32 %0, %1, %2, %3" : "=v"(r) : "v"(a), "v"(b), "v"(c)); return r; }
template <unsigned LO, unsigned HI> __device__ __forceinline__ void st16_m(void* sbase, unsigned voff, u32x4 v) {
    asm volatile("s_mov_b32 exec_lo, %3\n\ts_mov_b32 exec_hi, %4\n\tglobal_store_dwordx4 %0, %1, %2 sc1\n\ts_mov_b64 exec, -1\n\ts_nop 0" :: "v"(voff), "v"(v), "s"(sbase), "i"(LO), "i"(HI) : "memory");
}
template <unsigned LO, unsigned HI> __device__ __forceinline__ void st4_m(void* sbase, unsigned voff, unsigned v) {
    asm volatile("s_mov_b32 exec_lo, %3\n\ts_mov_b32 exec_hi, %4\n\tglobal_store_dword %0, %1, %2 sc1\n\ts_mov_b64 exec, -1" :: "v"(voff), "v"(v), "s"(sbase), "i"(LO), "i"(HI) : "memory");
}

#define GATHER(sid, LOADS, OK) do { LOADS; if (__builtin_expect(!(OK), 0)) { for (unsigned spins_ = 1; spins_ < kSpinCap; ++spins_) { RETRY(sid, spins_); __builtin_amdgcn_s_sleep(1); LOADS; if (OK) break; } } } while (0)
#define SCH(i) (sched_c[i])
#define PRIO_MODE 0
#define TAILN 8
#define ABL 0
#define DIRECT 0
#define GMODE 0
#define GP_ 0
#define GID blockIdx.x
#define H1ROW 1
#define P_BA 3
#define WO_AT 7

#define ASM_QK \
    "ds_read_b128 v[48:51], %1 offset:0\n\t" \
    "ds_read_b128 v[52:55], %1 offset:128\n\t" \
    "ds_read_b128 v[56:59], %1 offset:256\n\t" \
    "v_mov_b32 v32, 0\n\t" \
    "v_mov_b32 v33, 0\n\t" \
    "v_mov_b32 v36, 0\n\t" \
    "v_mov_b32 v37, 0\n\t" \
    "v_mov_b32 v40, 0\n\t" \
    "v_mov_b32 v41, 0\n\t" \
    "v_mov_b32 v44, 0\n\t" \
    "v_mov_b32 v45, 0\n\t" \
    "s_waitcnt lgkmcnt(2)\n\t" \
    "v_smfmac_f32_16x16x64_bf16 v[32:35], v[48:51], v[64:71], %2\n\t" \
    "v_smfmac_f32_16x16x64_bf16 v[36:39], v[48:51], v[88:95], %2\n\t" \
    "v_smfmac_f32_16x16x64_bf16 v[40:43], v[48:51], v[112:119], %2\n\t" \
    "v_smfmac_f32_16x16x64_bf16 v[44:47], v[48:51], v[136:143], %2\n\t" \
    "s_waitcnt lgkmcnt(1)\n\t" \
    "v_smfmac_f32_16x16x64_bf16 v[32:35], v[52:55], v[72:79], %2\n\t" \
    "v_smfmac_f32_16x16x64_bf16 v[36:39], v[52:55], v[96:103], %2\n\t" \
    "v_smfmac_f32_16x16x64_bf16 v[40:43], v[52:55], v[120:127], %2\n\t" \
    "v_smfmac_f32_16x16x64_bf16 v[44:47], v[52:55], v[144:151], %2\n\t" \
    "s_waitcnt lgkmcnt(0)\n\t" \
    "v_smfmac_f32_16x16x64_bf16 v[32:35], v[56:59], v[80:87], %2\n\t" \
    "v_smfmac_f32_16x16x64_bf16 v[36:39], v[56:59], v[104:111], %2\n\t" \
    "v_smfmac_f32_16x16x64_bf16 v[40:43], v[56:59], v[128:135], %2\n\t" \
    "v_smfmac_f32_16x16x64_bf16 v[44:47], v[56:59], v[152:159], %2\n\t" \
    "s_nop 4\n\t" \
    "v_add_f32_dpp %0, v32, v33 quad_perm:[0,1,2,3] row_mask:0x1 bank_mask:0xf\n\t" \
    "v_add_f32_dpp %0, v36, v37 quad_perm:[0,1,2,3] row_mask:0x2 bank_mask:0xf\n\t" \
    "v_add_f32_dpp %0, v40, v41 quad_perm:[0,1,2,3] row_mask:0x4 bank_mask:0xf\n\t" \
    "v_add_f32_dpp %0, v44, v45 quad_perm:[0,1,2,3] row_mask:0x8 bank_mask:0xf\n\t"
#define ASM_PVB0 \
    "s_nop 1\n\t" \
    "v_smfmac_f32_16x16x64_bf16 %0, %1, v[160:167], %6 abid:1\n\t" \
    "v_smfmac_f32_16x16x64_bf16 %0, %2, v[168:175], %6 abid:1\n\t" \
    "v_smfmac_f32_16x16x64_bf16 %0, %3, v[176:183], %6 abid:1\n\t" \
    "v_smfmac_f32_16x16x64_bf16 %0, %4, v[184:191], %6 abid:1\n\t" \
    "s_nop 7\n\t" \
    "ds_write_b128 %5, %0 offset:0\n\t"
#define ASM_PVB1 \
    "s_nop 1\n\t" \
    "v_smfmac_f32_16x16x64_bf16 %0, %1, v[192:199], %6 abid:1\n\t" \
    "v_smfmac_f32_16x16x64_bf16 %0, %2, v[200:207], %6 abid:1\n\t" \
    "v_smfmac_f32_16x16x64_bf16 %0, %3, v[208:215], %6 abid:1\n\t" \
    "v_smfmac_f32_16x16x64_bf16 %0, %4, v[216:223], %6 abid:1\n\t" \
    "s_nop 7\n\t" \
    "ds_write_b128 %5, %0 offset:1024\n\t"
#define ASM_PVB2 \
    "s_nop 1\n\t" \
    "v_smfmac_f32_16x16x64_bf16 %0, %1, v[224:231], %6 abid:1\n\t" \
    "v_smfmac_f32_16x16x64_bf16 %0, %2, v[232:239], %6 abid:1\n\t" \
    "v_smfmac_f32_16x16x64_bf16 %0, %3, v[240:247], %6 abid:1\n\t" \
    "v_smfmac_f32_16x16x64_bf16 %0, %4, v[248:255], %6 abid:1\n\t" \
    "s_nop 7\n\t" \
    "ds_write_b128 %5, %0 offset:2048\n\t"
#define ASM_PV20 \
    "v_smfmac_f32_16x16x64_bf16 %0, %1, v[224:231], %6 abid:1\n\t"
#define ASM_PV21 \
    "v_smfmac_f32_16x16x64_bf16 %0, %2, v[232:239], %6 abid:1\n\t"
#define ASM_PV22 \
    "v_smfmac_f32_16x16x64_bf16 %0, %3, v[240:247], %6 abid:1\n\t"
#define ASM_PV23 \
    "v_smfmac_f32_16x16x64_bf16 %0, %4, v[248:255], %6 abid:1\n\t"
#define ASM_ATT_A \
    "s_setprio 2\n\t" \
    "v_mov_b32 v28, 0\n\t" \
    "v_mov_b32 v29, 0\n\t" \
    "v_mov_b32 v32, 0\n\t" \
    "v_mov_b32 v33, 0\n\t" \
    "v_mov_b32 v36, 0\n\t" \
    "v_mov_b32 v37, 0\n\t" \
    "v_mov_b32 v40, 0\n\t" \
    "v_mov_b32 v41, 0\n\t" \
    "v_mov_b32 v48, 0\n\t" \
    "v_mov_b32 v49, 0\n\t" \
    "v_mov_b32 v50, 0\n\t" \
    "v_mov_b32 v51, 0\n\t" \
    "v_mov_b32 v52, 0\n\t" \
    "v_mov_b32 v53, 0\n\t" \
    "v_mov_b32 v54, 0\n\t" \
    "v_mov_b32 v55, 0\n\t" \
    "v_mov_b32 v56, 0\n\t" \
    "v_mov_b32 v57, 0\n\t" \
    "v_mov_b32 v58, 0\n\t" \
    "v_mov_b32 v59, 0\n\t" \
    "v_mov_b32 v16, 0\n\t" \
    "v_mov_b32 v17, 0\n\t" \
    "v_mov_b32 v18, 0\n\t" \
    "v_mov_b32 v19, 0\n\t" \
    "v_mov_b32 v20, %[ma]\n\t" \
    "s_cmp_eq_u32 %[tz], 0\n\t" \
    "s_cbranch_scc1 23f\n\t" \
    "s_branch 22f\n\t" \
    "21:\n\t" \
    "s_sleep 3\n\t" \
    "22:\n\t" \
    "s_memrealtime vcc\n\t" \
    "s_waitcnt lgkmcnt(0)\n\t" \
    "s_sub_i32 vcc_lo, vcc_lo, %[tz]\n\t" \
    "s_cmp_lt_i32 vcc_lo, 0\n\t" \
    "s_cbranch_scc1 21b\n\t" \
    "23:\n\t" \
    "s_mov_b32 vcc_hi, 0x400000\n\t" \
    "0:\n\t" \
    "ds_read_b32 v21, v20 offset:340\n\t" \
    "s_sub_u32 vcc_hi, vcc_hi, 1\n\t" \
    "s_cbranch_scc1 4f\n\t" \
    "s_waitcnt lgkmcnt(0)\n\t" \
    "v_readfirstlane_b32 %[st], v21\n\t" \
    "s_cmp_eq_u32 %[st], 0\n\t" \
    "s_cbranch_scc1 0b\n\t" \
    "s_add_u32 %[st], %[st], %[tg]\n\t" \
    "4:\n\t" \
    "s_memrealtime vcc\n\t" \
    "s_waitcnt lgkmcnt(0)\n\t" \
    "s_sub_i32 vcc_lo, vcc_lo, %[st]\n\t" \
    "s_cmp_lt_i32 vcc_lo, -4000\n\t" \
    "s_cbranch_scc1 10f\n\t" \
    "s_cmp_lt_i32 vcc_lo, 0\n\t" \
    "s_cbranch_scc1 4b\n\t" \
    "10:\n\t" \
    "s_mov_b32 exec_hi, 0xff\n\t" \
    "global_load_dwordx4 v[16:19], %[go], %[ws] sc1\n\t" \
    "s_mov_b32 exec_hi, -1\n\t" \
    "s_barrier\n\t" \
    "ds_read_b64 v[22:23], v20 offset:352\n\t" \
    "s_mov_b32 %[st], 0x10000\n\t" \
    "s_waitcnt lgkmcnt(0)\n\t" \
    "v_mul_f32 v22, v22, %[cs]\n\t" \
    "v_mul_f32_e64 v22, -v22, v23\n\t" \
    "5:\n\t" \
    "s_waitcnt vmcnt(0)\n\t" \
    "v_max3_u32 v24, v16, v17, v18\n\t" \
    "v_max_u32 v24, v24, v19\n\t" \
    "v_cmp_eq_u32 vcc, -1, v24\n\t" \
    "v_permlane32_swap_b32 v16, v18\n\t" \
    "v_permlane32_swap_b32 v17, v19\n\t" \
    "v_pk_add_f32 v[16:17], v[16:17], v[18:19]\n\t" \
    "s_nop 0\n\t" \
    "s_cbranch_vccnz 6f\n\t" \
    "7:\n\t" \
    "v_permlane16_swap_b32 v16, v17\n\t" \
    "v_add_f32 v16, v16, v17\n\t" \
    "s_nop 1\n\t" \
    "v_add_f32_dpp v16, v16, v16 row_ror:8 row_mask:0xf bank_mask:0xf\n\t" \
    "v_fmac_f32 v22, v16, v23\n\t" \
    "v_cvt_pk_bf16_f32 v22, v22, v22\n\t" \
    "ds_write_b16 %[qo], v22\n\t" \
    "s_waitcnt lgkmcnt(0)\n\t" \
    "s_barrier\n\t" \
    "ds_read_b128 v[16:19], %[qa] offset:0\n\t" \
    "ds_read_b128 v[20:23], %[qa] offset:128\n\t" \
    "ds_read_b128 v[24:27], %[qa] offset:256\n\t" \
    "s_waitcnt lgkmcnt(2)\n\t" \
    "v_smfmac_f32_16x16x64_bf16 v[28:31], v[16:19], v[64:71], v7\n\t" \
    "v_smfmac_f32_16x16x64_bf16 v[32:35], v[16:19], v[88:95], v7\n\t" \
    "v_smfmac_f32_16x16x64_bf16 v[36:39], v[16:19], v[112:119], v7\n\t" \
    "v_smfmac_f32_16x16x64_bf16 v[40:43], v[16:19], v[136:143], v7\n\t" \
    "s_waitcnt lgkmcnt(1)\n\t" \
    "v_smfmac_f32_16x16x64_bf16 v[28:31], v[20:23], v[72:79], v7\n\t" \
    "v_smfmac_f32_16x16x64_bf16 v[32:35], v[20:23], v[96:103], v7\n\t" \
    "v_smfmac_f32_16x16x64_bf16 v[36:39], v[20:23], v[120:127], v7\n\t" \
    "v_smfmac_f32_16x16x64_bf16 v[40:43], v[20:23], v[144:151], v7\n\t" \
    "s_waitcnt lgkmcnt(0)\n\t" \
    "v_smfmac_f32_16x16x64_bf16 v[28:31], v[24:27], v[80:87], v7\n\t" \
    "v_smfmac_f32_16x16x64_bf16 v[32:35], v[24:27], v[104:111], v7\n\t" \
    "v_smfmac_f32_16x16x64_bf16 v[36:39], v[24:27], v[128:135], v7\n\t" \
    "v_smfmac_f32_16x16x64_bf16 v[40:43], v[24:27], v[152:159], v7\n\t" \
    "v_mov_b32 v63, %[mlw]\n\t" \
    "s_nop 3\n\t" \
    "v_add_f32_dpp v60, v28, v29 quad_perm:[0,1,2,3] row_mask:0x1 bank_mask:0xf\n\t" \
    "v_add_f32_dpp v60, v32, v33 quad_perm:[0,1,2,3] row_mask:0x2 bank_mask:0xf\n\t" \
    "v_add_f32_dpp v60, v36, v37 quad_perm:[0,1,2,3] row_mask:0x4 bank_mask:0xf\n\t" \
    "v_add_f32_dpp v60, v40, v41 quad_perm:[0,1,2,3] row_mask:0x8 bank_mask:0xf\n\t" \
    "v_mul_f32 v60, 0x3dd53b94, v60\n\t" \
    "s_nop 1\n\t" \
    "v_max_f32_dpp v61, v60, v60 quad_perm:[1,0,3,2] row_mask:0xf bank_mask:0xf\n\t" \
    "s_nop 1\n\t" \
    "v_max_f32_dpp v61, v61, v61 quad_perm:[2,3,0,1] row_mask:0xf bank_mask:0xf\n\t" \
    "s_nop 1\n\t" \
    "v_max_f32_dpp v61, v61, v61 row_half_mirror row_mask:0xf bank_mask:0xf\n\t" \
    "s_nop 1\n\t" \
    "v_max_f32_dpp v61, v61, v61 row_mirror row_mask:0xf bank_mask:0xf\n\t" \
    "s_nop 1\n\t" \
    "v_max_f32_dpp v61, v61, v61 row_bcast:15 row_mask:0xa bank_mask:0xf\n\t" \
    "s_nop 1\n\t" \
    "v_max_f32_dpp v61, v61, v61 row_bcast:31 row_mask:0xc bank_mask:0xf\n\t" \
    "s_nop 0\n\t" \
    "v_readlane_b32 %[st], v61, 63\n\t" \
    "s_mov_b32 exec_lo, 0\n\t" \
    "s_brev_b32 exec_hi, 1\n\t" \
    "ds_write_b32 v63, v61\n\t" \
    "s_mov_b64 exec, -1\n\t" \
    "v_subrev_f32 v62, %[st], v60\n\t" \
    "v_exp_f32 v62, v62\n\t" \
    "s_nop 0\n\t" \
    "v_cvt_pk_bf16_f32 v60, v62, v62\n\t" \
    "ds_write_b16 %[pw], v60\n\t" \
    "ds_write_b16 %[pw], v60 offset:20\n\t" \
    "ds_read_b128 v[16:19], %[pr] offset:0\n\t" \
    "ds_read_b128 v[20:23], %[pr] offset:272\n\t" \
    "ds_read_b128 v[24:27], %[pr] offset:544\n\t" \
    "ds_read_b128 v[28:31], %[pr] offset:816\n\t" \
    "v_mov_b32 v60, %[ma]\n\t" \
    "s_setprio 0\n\t" \
    "v_add_f32_dpp v62, v62, v62 quad_perm:[1,0,3,2] row_mask:0xf bank_mask:0xf\n\t" \
    "s_nop 1\n\t" \
    "v_add_f32_dpp v62, v62, v62 quad_perm:[2,3,0,1] row_mask:0xf bank_mask:0xf\n\t" \
    "s_nop 1\n\t" \
    "v_add_f32_dpp v62, v62, v62 row_half_mirror row_mask:0xf bank_mask:0xf\n\t" \
    "s_nop 1\n\t" \
    "v_add_f32_dpp v62, v62, v62 row_mirror row_mask:0xf bank_mask:0xf\n\t" \
    "s_waitcnt lgkmcnt(3)\n\t" \
    "v_smfmac_f32_16x16x64_bf16 v[48:51], v[16:19], v[160:167], v7 abid:1\n\t" \
    "v_smfmac_f32_16x16x64_bf16 v[52:55], v[16:19], v[192:199], v7 abid:1\n\t" \
    "v_smfmac_f32_16x16x64_bf16 v[56:59], v[16:19], v[224:231], v7 abid:1\n\t" \
    "v_add_f32_dpp v62, v62, v62 row_bcast:15 row_mask:0xa bank_mask:0xf\n\t" \
    "s_waitcnt lgkmcnt(2)\n\t" \
    "v_smfmac_f32_16x16x64_bf16 v[48:51], v[20:23], v[168:175], v7 abid:1\n\t" \
    "v_smfmac_f32_16x16x64_bf16 v[52:55], v[20:23], v[200:207], v7 abid:1\n\t" \
    "v_smfmac_f32_16x16x64_bf16 v[56:59], v[20:23], v[232:239], v7 abid:1\n\t" \
    "v_add_f32_dpp v62, v62, v62 row_bcast:31 row_mask:0xc bank_mask:0xf\n\t" \
    "s_waitcnt lgkmcnt(1)\n\t" \
    "v_smfmac_f32_16x16x64_bf16 v[48:51], v[24:27], v[176:183], v7 abid:1\n\t" \
    "v_smfmac_f32_16x16x64_bf16 v[52:55], v[24:27], v[208:215], v7 abid:1\n\t" \
    "v_smfmac_f32_16x16x64_bf16 v[56:59], v[24:27], v[240:247], v7 abid:1\n\t" \
    "s_waitcnt lgkmcnt(0)\n\t" \
    "v_smfmac_f32_16x16x64_bf16 v[48:51], v[28:31], v[184:191], v7 abid:1\n\t" \
    "v_smfmac_f32_16x16x64_bf16 v[52:55], v[28:31], v[216:223], v7 abid:1\n\t" \
    "v_smfmac_f32_16x16x64_bf16 v[56:59], v[28:31], v[248:255], v7 abid:1\n\t" \
    "s_barrier\n\t" \
    "ds_read_b128 v[16:19], v60\n\t" \
    "ds_read_b128 v[20:23], v60 offset:16\n\t" \
    "ds_read_b32 v24, %[mo]\n\t" \
    "s_mov_b32 exec_lo, 0\n\t" \
    "s_brev_b32 exec_hi, 1\n\t" \
    "ds_write_b32 v63, v62 offset:32\n\t" \
    "s_mov_b64 exec, -1\n\t" \
    "v_mov_b32_dpp v48, v52 quad_perm:[0,1,2,3] row_mask:0xc bank_mask:0xf\n\t" \
    "v_mov_b32_dpp v49, v53 quad_perm:[0,1,2,3] row_mask:0xc bank_mask:0xf\n\t" \
    "v_mov_b32_dpp v50, v54 quad_perm:[0,1,2,3] row_mask:0xc bank_mask:0xf\n\t" \
    "v_mov_b32_dpp v51, v55 quad_perm:[0,1,2,3] row_mask:0xc bank_mask:0xf\n\t" \
    "ds_write_b128 %[ow], v[48:51] offset:0\n\t" \
    "s_mov_b32 exec_lo, 0x00ff00ff\n\t" \
    "s_mov_b32 exec_hi, 0xff00ff00\n\t" \
    "ds_write_b128 %[ow], v[56:59] offset:2048\n\t" \
    "s_mov_b64 exec, -1\n\t" \
    "s_waitcnt lgkmcnt(3)\n\t" \
    "v_max3_f32 v16, v16, v17, v18\n\t" \
    "v_max3_f32 v19, v19, v20, v21\n\t" \
    "v_max3_f32 v16, v16, v22, v23\n\t" \
    "v_max_f32 v26, v16, v19\n\t" \
    "v_sub_f32 v60, v24, v26\n\t" \
    "v_exp_f32 v60, v60\n\t" \
    "s_nop 0\n\t" \
    "ds_write2_b32 %[mo], v60, v26 offset0:96 offset1:104\n\t" \
    "s_waitcnt lgkmcnt(0)\n\t" \
    "s_barrier\n\t" \
    "ds_read_b128 v[16:19], %[r0]\n\t" \
    "ds_read_b128 v[20:23], %[r0] offset:512\n\t" \
    "s_waitcnt lgkmcnt(0)\n\t" \
    "v_pk_add_f32 v[16:17], v[16:17], v[20:21]\n\t" \
    "v_pk_add_f32 v[18:19], v[18:19], v[22:23]\n\t" \
    "v_pk_mul_f32 v[16:17], v[16:17], v[60:61] op_sel_hi:[1,0]\n\t" \
    "v_pk_mul_f32 v[18:19], v[18:19], v[60:61] op_sel_hi:[1,0]\n\t" \
    "s_nop 1\n\t" \
    "v_permlane32_swap_b32 v16, v18\n\t" \
    "v_permlane32_swap_b32 v17, v19\n\t" \
    "v_pk_add_f32 v[16:17], v[16:17], v[18:19]\n\t" \
    "s_nop 1\n\t" \
    "v_permlane16_swap_b32 v16, v17\n\t" \
    "v_add_f32 v16, v16, v17\n\t" \
    "s_nop 1\n\t" \
    "v_add_f32_dpp v17, v16, v16 row_ror:8 row_mask:0xf bank_mask:0xf\n\t" \
    "v_add_f32_dpp v16, v16, v16 row_ror:8 row_mask:0xf bank_mask:0xf\n\t" \
    "s_nop 1\n\t" \
    "v_permlane16_swap_b32 v16, v17\n\t" \
    "s_mov_b32 exec_lo, 0xff\n\t" \
    "v_cvt_pk_bf16_f32 v16, v16, v17\n\t" \
    "s_mov_b32 exec_hi, 0xff\n\t" \
    "v_min_u32 v16, -2, v16\n\t" \
    "global_store_dword %[so], v16, %[ws] sc1\n\t" \
    "s_mov_b64 exec, -1\n\t" \
    "s_branch 2f\n\t" \
    "6:\n\t" \
    "s_sub_u32 %[st], %[st], 1\n\t" \
    "s_cbranch_scc1 7b\n\t" \
    RETRY_ASM \
    "v_mov_b32 v16, 0\n\t" \
    "v_mov_b32 v17, 0\n\t" \
    "v_mov_b32 v18, 0\n\t" \
    "v_mov_b32 v19, 0\n\t" \
    "s_mov_b32 exec_hi, 0xff\n\t" \
    "global_load_dwordx4 v[16:19], %[go], %[ws] sc1\n\t" \
    "s_mov_b32 exec_hi, -1\n\t" \
    "s_branch 5b\n\t" \
    "2:\n\t"
#define ASM_ATT_B \
    "s_setprio 1\n\t" \
    "v_mov_b32 v28, 0\n\t" \
    "v_mov_b32 v29, 0\n\t" \
    "v_mov_b32 v32, 0\n\t" \
    "v_mov_b32 v33, 0\n\t" \
    "v_mov_b32 v36, 0\n\t" \
    "v_mov_b32 v37, 0\n\t" \
    "v_mov_b32 v40, 0\n\t" \
    "v_mov_b32 v41, 0\n\t" \
    "v_mov_b32 v48, 0\n\t" \
    "v_mov_b32 v49, 0\n\t" \
    "v_mov_b32 v50, 0\n\t" \
    "v_mov_b32 v51, 0\n\t" \
    "v_mov_b32 v52, 0\n\t" \
    "v_mov_b32 v53, 0\n\t" \
    "v_mov_b32 v54, 0\n\t" \
    "v_mov_b32 v55, 0\n\t" \
    "v_mov_b32 v56, 0\n\t" \
    "v_mov_b32 v57, 0\n\t" \
    "v_mov_b32 v58, 0\n\t" \
    "v_mov_b32 v59, 0\n\t" \
    "v_mov_b32 v44, -1\n\t" \
    "v_mov_b32 v45, -1\n\t" \
    "v_mov_b32 v46, -1\n\t" \
    "v_mov_b32 v47, -1\n\t" \
    "s_cmp_gt_u32 %[wv], 5\n\t" \
    "s_cbranch_scc0 8f\n\t" \
    "s_cmp_eq_u32 %[wv], 7\n\t" \
    "s_cbranch_scc1 9f\n\t" \
    "s_barrier\n\t" \
    "s_branch 9f\n\t" \
    "8:\n\t" \
    "v_mov_b32 v16, 0\n\t" \
    "v_mov_b32 v17, 0\n\t" \
    "v_mov_b32 v18, 0\n\t" \
    "v_mov_b32 v19, 0\n\t" \
    "v_mov_b32 v20, %[ma]\n\t" \
    "s_cmp_eq_u32 %[tz], 0\n\t" \
    "s_cbranch_scc1 23f\n\t" \
    "s_branch 22f\n\t" \
    "21:\n\t" \
    "s_sleep 3\n\t" \
    "22:\n\t" \
    "s_memrealtime vcc\n\t" \
    "s_waitcnt lgkmcnt(0)\n\t" \
    "s_sub_i32 vcc_lo, vcc_lo, %[tz]\n\t" \
    "s_cmp_lt_i32 vcc_lo, 0\n\t" \
    "s_cbranch_scc1 21b\n\t" \
    "23:\n\t" \
    "s_mov_b32 vcc_hi, 0x400000\n\t" \
    "0:\n\t" \
    "ds_read_b32 v21, v20 offset:340\n\t" \
    "s_sub_u32 vcc_hi, vcc_hi, 1\n\t" \
    "s_cbranch_scc1 4f\n\t" \
    "s_waitcnt lgkmcnt(0)\n\t" \
    "v_readfirstlane_b32 %[st], v21\n\t" \
    "s_cmp_eq_u32 %[st], 0\n\t" \
    "s_cbranch_scc1 0b\n\t" \
    "s_add_u32 %[st], %[st], %[tg]\n\t" \
    "4:\n\t" \
    "s_memrealtime vcc\n\t" \
    "s_waitcnt lgkmcnt(0)\n\t" \
    "s_sub_i32 vcc_lo, vcc_lo, %[st]\n\t" \
    "s_cmp_lt_i32 vcc_lo, -4000\n\t" \
    "s_cbranch_scc1 10f\n\t" \
    "s_cmp_lt_i32 vcc_lo, 0\n\t" \
    "s_cbranch_scc1 4b\n\t" \
    "10:\n\t" \
    "s_mov_b32 exec_hi, 0xff\n\t" \
    "global_load_dwordx4 v[16:19], %[go], %[ws] sc1\n\t" \
    "s_mov_b32 exec_hi, -1\n\t" \
    "s_barrier\n\t" \
    "ds_read_b64 v[22:23], v20 offset:352\n\t" \
    "s_mov_b32 %[st], 0x10000\n\t" \
    "s_waitcnt lgkmcnt(0)\n\t" \
    "v_mul_f32 v22, v22, %[cs]\n\t" \
    "v_mul_f32_e64 v22, -v22, v23\n\t" \
    "5:\n\t" \
    "s_waitcnt vmcnt(0)\n\t" \
    "v_max3_u32 v24, v16, v17, v18\n\t" \
    "v_max_u32 v24, v24, v19\n\t" \
    "v_cmp_eq_u32 vcc, -1, v24\n\t" \
    "v_permlane32_swap_b32 v16, v18\n\t" \
    "v_permlane32_swap_b32 v17, v19\n\t" \
    "v_pk_add_f32 v[16:17], v[16:17], v[18:19]\n\t" \
    "s_nop 0\n\t" \
    "s_cbranch_vccnz 6f\n\t" \
    "7:\n\t" \
    "v_permlane16_swap_b32 v16, v17\n\t" \
    "v_add_f32 v16, v16, v17\n\t" \
    "s_nop 1\n\t" \
    "v_add_f32_dpp v16, v16, v16 row_ror:8 row_mask:0xf bank_mask:0xf\n\t" \
    "v_fmac_f32 v22, v16, v23\n\t" \
    "v_cvt_pk_bf16_f32 v22, v22, v22\n\t" \
    "ds_write_b16 %[qo], v22\n\t" \
    "9:\n\t" \
    "s_waitcnt lgkmcnt(0)\n\t" \
    "s_barrier\n\t" \
    "ds_read_b128 v[16:19], %[qa] offset:0\n\t" \
    "ds_read_b128 v[20:23], %[qa] offset:128\n\t" \
    "ds_read_b128 v[24:27], %[qa] offset:256\n\t" \
    "s_cmp_eq_u32 %[cl], 0\n\t" \
    "s_cbranch_scc1 3f\n\t" \
    "global_store_dwordx4 %[go], v[44:47], %[ws] offset:0 sc1\n\t" \
    "global_store_dwordx4 %[go], v[44:47], %[ws] offset:1024 sc1\n\t" \
    "global_store_dwordx4 %[go], v[44:47], %[ws] offset:2048 sc1\n\t" \
    "3:\n\t" \
    "s_waitcnt lgkmcnt(2)\n\t" \
    "v_smfmac_f32_16x16x64_bf16 v[28:31], v[16:19], v[64:71], v7\n\t" \
    "v_smfmac_f32_16x16x64_bf16 v[32:35], v[16:19], v[88:95], v7\n\t" \
    "v_smfmac_f32_16x16x64_bf16 v[36:39], v[16:19], v[112:119], v7\n\t" \
    "v_smfmac_f32_16x16x64_bf16 v[40:43], v[16:19], v[136:143], v7\n\t" \
    "s_waitcnt lgkmcnt(1)\n\t" \
    "v_smfmac_f32_16x16x64_bf16 v[28:31], v[20:23], v[72:79], v7\n\t" \
    "v_smfmac_f32_16x16x64_bf16 v[32:35], v[20:23], v[96:103], v7\n\t" \
    "v_smfmac_f32_16x16x64_bf16 v[36:39], v[20:23], v[120:127], v7\n\t" \
    "v_smfmac_f32_16x16x64_bf16 v[40:43], v[20:23], v[144:151], v7\n\t" \
    "s_waitcnt lgkmcnt(0)\n\t" \
    "v_smfmac_f32_16x16x64_bf16 v[28:31], v[24:27], v[80:87], v7\n\t" \
    "v_smfmac_f32_16x16x64_bf16 v[32:35], v[24:27], v[104:111], v7\n\t" \
    "v_smfmac_f32_16x16x64_bf16 v[36:39], v[24:27], v[128:135], v7\n\t" \
    "v_smfmac_f32_16x16x64_bf16 v[40:43], v[24:27], v[152:159], v7\n\t" \
    "v_mov_b32 v63, %[mlw]\n\t" \
    "s_nop 3\n\t" \
    "v_add_f32_dpp v60, v28, v29 quad_perm:[0,1,2,3] row_mask:0x1 bank_mask:0xf\n\t" \
    "v_add_f32_dpp v60, v32, v33 quad_perm:[0,1,2,3] row_mask:0x2 bank_mask:0xf\n\t" \
    "v_add_f32_dpp v60, v36, v37 quad_perm:[0,1,2,3] row_mask:0x4 bank_mask:0xf\n\t" \
    "v_add_f32_dpp v60, v40, v41 quad_perm:[0,1,2,3] row_mask:0x8 bank_mask:0xf\n\t" \
    "v_mul_f32 v60, 0x3dd53b94, v60\n\t" \
    "s_nop 1\n\t" \
    "v_max_f32_dpp v61, v60, v60 quad_perm:[1,0,3,2] row_mask:0xf bank_mask:0xf\n\t" \
    "s_nop 1\n\t" \
    "v_max_f32_dpp v61, v61, v61 quad_perm:[2,3,0,1] row_mask:0xf bank_mask:0xf\n\t" \
    "s_nop 1\n\t" \
    "v_max_f32_dpp v61, v61, v61 row_half_mirror row_mask:0xf bank_mask:0xf\n\t" \
    "s_nop 1\n\t" \
    "v_max_f32_dpp v61, v61, v61 row_mirror row_mask:0xf bank_mask:0xf\n\t" \
    "s_nop 1\n\t" \
    "v_max_f32_dpp v61, v61, v61 row_bcast:15 row_mask:0xa bank_mask:0xf\n\t" \
    "s_nop 1\n\t" \
    "v_max_f32_dpp v61, v61, v61 row_bcast:31 row_mask:0xc bank_mask:0xf\n\t" \
    "s_nop 0\n\t" \
    "v_readlane_b32 %[st], v61, 63\n\t" \
    "s_mov_b32 exec_lo, 0\n\t" \
    "s_brev_b32 exec_hi, 1\n\t" \
    "ds_write_b32 v63, v61\n\t" \
    "s_mov_b64 exec, -1\n\t" \
    "v_subrev_f32 v62, %[st], v60\n\t" \
    "v_exp_f32 v62, v62\n\t" \
    "s_nop 0\n\t" \
    "v_cvt_pk_bf16_f32 v60, v62, v62\n\t" \
    "ds_write_b16 %[pw], v60\n\t" \
    "ds_write_b16 %[pw], v60 offset:20\n\t" \
    "ds_read_b128 v[16:19], %[pr] offset:0\n\t" \
    "ds_read_b128 v[20:23], %[pr] offset:272\n\t" \
    "ds_read_b128 v[24:27], %[pr] offset:544\n\t" \
    "ds_read_b128 v[28:31], %[pr] offset:816\n\t" \
    "v_mov_b32 v60, %[ma]\n\t" \
    "s_setprio 0\n\t" \
    "v_add_f32_dpp v62, v62, v62 quad_perm:[1,0,3,2] row_mask:0xf bank_mask:0xf\n\t" \
    "s_nop 1\n\t" \
    "v_add_f32_dpp v62, v62, v62 quad_perm:[2,3,0,1] row_mask:0xf bank_mask:0xf\n\t" \
    "s_nop 1\n\t" \
    "v_add_f32_dpp v62, v62, v62 row_half_mirror row_mask:0xf bank_mask:0xf\n\t" \
    "s_nop 1\n\t" \
    "v_add_f32_dpp v62, v62, v62 row_mirror row_mask:0xf bank_mask:0xf\n\t" \
    "s_waitcnt lgkmcnt(3)\n\t" \
    "s_barrier\n\t" \
    "v_smfmac_f32_16x16x64_bf16 v[48:51], v[16:19], v[160:167], v7 abid:1\n\t" \
    "v_smfmac_f32_16x16x64_bf16 v[52:55], v[16:19], v[192:199], v7 abid:1\n\t" \
    "v_smfmac_f32_16x16x64_bf16 v[56:59], v[16:19], v[224:231], v7 abid:1\n\t" \
    "v_add_f32_dpp v62, v62, v62 row_bcast:15 row_mask:0xa bank_mask:0xf\n\t" \
    "s_waitcnt lgkmcnt(2)\n\t" \
    "v_smfmac_f32_16x16x64_bf16 v[48:51], v[20:23], v[168:175], v7 abid:1\n\t" \
    "v_smfmac_f32_16x16x64_bf16 v[52:55], v[20:23], v[200:207], v7 abid:1\n\t" \
    "v_smfmac_f32_16x16x64_bf16 v[56:59], v[20:23], v[232:239], v7 abid:1\n\t" \
    "v_add_f32_dpp v62, v62, v62 row_bcast:31 row_mask:0xc bank_mask:0xf\n\t" \
    "s_waitcnt lgkmcnt(1)\n\t" \
    "v_smfmac_f32_16x16x64_bf16 v[48:51], v[24:27], v[176:183], v7 abid:1\n\t" \
    "v_smfmac_f32_16x16x64_bf16 v[52:55], v[24:27], v[208:215], v7 abid:1\n\t" \
    "v_smfmac_f32_16x16x64_bf16 v[56:59], v[24:27], v[240:247], v7 abid:1\n\t" \
    "s_waitcnt lgkmcnt(0)\n\t" \
    "v_smfmac_f32_16x16x64_bf16 v[48:51], v[28:31], v[184:191], v7 abid:1\n\t" \
    "v_smfmac_f32_16x16x64_bf16 v[52:55], v[28:31], v[216:223], v7 abid:1\n\t" \
    "v_smfmac_f32_16x16x64_bf16 v[56:59], v[28:31], v[248:255], v7 abid:1\n\t" \
    "s_mov_b32 exec_lo, 0\n\t" \
    "s_brev_b32 exec_hi, 1\n\t" \
    "ds_write_b32 v63, v62 offset:32\n\t" \
    "s_mov_b64 exec, -1\n\t" \
    "s_cmp_gt_u32 %[wv], 5\n\t" \
    "s_nop 1\n\t" \
    "v_mov_b32_dpp v48, v52 quad_perm:[0,1,2,3] row_mask:0xc bank_mask:0xf\n\t" \
    "v_mov_b32_dpp v49, v53 quad_perm:[0,1,2,3] row_mask:0xc bank_mask:0xf\n\t" \
    "v_mov_b32_dpp v50, v54 quad_perm:[0,1,2,3] row_mask:0xc bank_mask:0xf\n\t" \
    "v_mov_b32_dpp v51, v55 quad_perm:[0,1,2,3] row_mask:0xc bank_mask:0xf\n\t" \
    "ds_write_b128 %[ow], v[48:51] offset:0\n\t" \
    "s_mov_b32 exec_lo, 0x00ff00ff\n\t" \
    "s_mov_b32 exec_hi, 0xff00ff00\n\t" \
    "ds_write_b128 %[ow], v[56:59] offset:2048\n\t" \
    "s_mov_b64 exec, -1\n\t" \
    "s_cbranch_scc1 1f\n\t" \
    "s_waitcnt lgkmcnt(0)\n\t" \
    "s_barrier\n\t" \
    "ds_read_b128 v[16:19], %[r0]\n\t" \
    "ds_read_b128 v[20:23], %[r0] offset:640\n\t" \
    "ds_read_b32 v60, %[mo] offset:384\n\t" \
    "s_waitcnt lgkmcnt(1)\n\t" \
    "v_pk_add_f32 v[16:17], v[16:17], v[20:21]\n\t" \
    "v_pk_add_f32 v[18:19], v[18:19], v[22:23]\n\t" \
    "s_waitcnt lgkmcnt(0)\n\t" \
    "v_pk_mul_f32 v[16:17], v[16:17], v[60:61] op_sel_hi:[1,0]\n\t" \
    "v_pk_mul_f32 v[18:19], v[18:19], v[60:61] op_sel_hi:[1,0]\n\t" \
    "s_nop 1\n\t" \
    "v_permlane32_swap_b32 v16, v18\n\t" \
    "v_permlane32_swap_b32 v17, v19\n\t" \
    "v_pk_add_f32 v[16:17], v[16:17], v[18:19]\n\t" \
    "s_nop 1\n\t" \
    "v_permlane16_swap_b32 v16, v17\n\t" \
    "v_add_f32 v16, v16, v17\n\t" \
    "s_nop 1\n\t" \
    "v_add_f32_dpp v17, v16, v16 row_ror:8 row_mask:0xf bank_mask:0xf\n\t" \
    "v_add_f32_dpp v16, v16, v16 row_ror:8 row_mask:0xf bank_mask:0xf\n\t" \
    "s_nop 1\n\t" \
    "v_permlane16_swap_b32 v16, v17\n\t" \
    "s_mov_b32 exec_lo, 0xff\n\t" \
    "v_cvt_pk_bf16_f32 v16, v16, v17\n\t" \
    "s_mov_b32 exec_hi, 0xff\n\t" \
    "v_min_u32 v16, -2, v16\n\t" \
    "global_store_dword %[so], v16, %[ws] sc1\n\t" \
    "s_mov_b64 exec, -1\n\t" \
    "s_branch 2f\n\t" \
    "6:\n\t" \
    "s_sub_u32 %[st], %[st], 1\n\t" \
    "s_cbranch_scc1 7b\n\t" \
    RETRY_ASM \
    "v_mov_b32 v16, 0\n\t" \
    "v_mov_b32 v17, 0\n\t" \
    "v_mov_b32 v18, 0\n\t" \
    "v_mov_b32 v19, 0\n\t" \
    "s_mov_b32 exec_hi, 0xff\n\t" \
    "global_load_dwordx4 v[16:19], %[go], %[ws] sc1\n\t" \
    "s_mov_b32 exec_hi, -1\n\t" \
    "s_branch 5b\n\t" \
    "1:\n\t" \
    "s_cmp_eq_u32 %[wv], 7\n\t" \
    "s_waitcnt vmcnt(0) lgkmcnt(0)\n\t" \
    "s_barrier\n\t" \
    "s_cbranch_scc1 2f\n\t" \
    "ds_read_b32 v16, %[mo] offset:32\n\t" \
    "ds_read_b32 v60, %[mo] offset:384\n\t" \
    "ds_read_b32 v26, %[mo] offset:416\n\t" \
    "s_waitcnt lgkmcnt(0)\n\t" \
    "v_min_u32 v26, -2, v26\n\t" \
    "v_mul_f32 v16, v16, v60\n\t" \
    "s_nop 1\n\t" \
    "v_add_f32_dpp v17, v16, v16 row_ror:8 row_mask:0xf bank_mask:0xf\n\t" \
    "v_add_f32_dpp v16, v16, v16 row_ror:8 row_mask:0xf bank_mask:0xf\n\t" \
    "s_nop 1\n\t" \
    "v_permlane16_swap_b32 v16, v17\n\t" \
    "v_add_f32 v18, v16, v17\n\t" \
    "v_add_f32 v16, v16, v17\n\t" \
    "s_nop 1\n\t" \
    "v_permlane32_swap_b32 v16, v18\n\t" \
    "s_mov_b64 exec, 3\n\t" \
    "v_add_f32 v27, v16, v18\n\t" \
    "v_min_u32 v27, -2, v27\n\t" \
    "global_store_dwordx2 %[so], v[26:27], %[ws] sc1\n\t" \
    "s_mov_b64 exec, -1\n\t" \
    "2:\n\t"
#define ASM_QP \
    "s_mov_b32 %[st], 0x10000\n\t" \
    "5:\n\t" \
    "ds_read_b64_tr_b16 v[16:17], %[a0] offset:0\n\t" \
    "ds_read_b64_tr_b16 v[18:19], %[a0] offset:1536\n\t" \
    "ds_read_b64_tr_b16 v[20:21], %[a1] offset:0\n\t" \
    "ds_read_b64_tr_b16 v[22:23], %[a1] offset:1536\n\t" \
    "ds_read_b64_tr_b16 v[24:25], %[a0] offset:12288\n\t" \
    "ds_read_b64_tr_b16 v[26:27], %[a0] offset:13824\n\t" \
    "ds_read_b64_tr_b16 v[28:29], %[a1] offset:12288\n\t" \
    "ds_read_b64_tr_b16 v[30:31], %[a1] offset:13824\n\t" \
    "ds_read_b64_tr_b16 v[32:33], %[a0] offset:24576\n\t" \
    "ds_read_b64_tr_b16 v[34:35], %[a0] offset:26112\n\t" \
    "ds_read_b64_tr_b16 v[36:37], %[a1] offset:24576\n\t" \
    "ds_read_b64_tr_b16 v[38:39], %[a1] offset:26112\n\t" \
    "ds_read_b64_tr_b16 v[40:41], %[a0] offset:36864\n\t" \
    "ds_read_b64_tr_b16 v[42:43], %[a0] offset:38400\n\t" \
    "ds_read_b64_tr_b16 v[44:45], %[a1] offset:36864\n\t" \
    "ds_read_b64_tr_b16 v[46:47], %[a1] offset:38400\n\t" \
    "v_mov_b32 v48, %[fl]\n\t" \
    "v_add_u32 v50, -52, v48\n\t" \
    "s_mov_b32 vcc_hi, 0x100000\n\t" \
    "s_branch 25f\n\t" \
    "24:\n\t" \
    "s_sleep 4\n\t" \
    "25:\n\t" \
    "ds_read_b32 v49, v50\n\t" \
    "s_sub_u32 vcc_hi, vcc_hi, 1\n\t" \
    "s_cbranch_scc1 23f\n\t" \
    "s_waitcnt lgkmcnt(0)\n\t" \
    "v_readfirstlane_b32 %[rf], v49\n\t" \
    "s_cmp_eq_u32 %[rf], 0\n\t" \
    "s_cbranch_scc1 24b\n\t" \
    "s_add_u32 %[rf], %[rf], %[tz]\n\t" \
    "s_branch 22f\n\t" \
    "21:\n\t" \
    "s_sleep 3\n\t" \
    "22:\n\t" \
    "s_memrealtime vcc\n\t" \
    "s_waitcnt lgkmcnt(0)\n\t" \
    "s_sub_i32 vcc_lo, vcc_lo, %[rf]\n\t" \
    "s_cmp_lt_i32 vcc_lo, 0\n\t" \
    "s_cbranch_scc1 21b\n\t" \
    "23:\n\t" \
    "s_mov_b32 vcc_hi, 0x400000\n\t" \
    "0:\n\t" \
    "ds_read_b32 v49, v48\n\t" \
    "s_sub_u32 vcc_hi, vcc_hi, 1\n\t" \
    "s_cbranch_scc1 10f\n\t" \
    "s_waitcnt lgkmcnt(0)\n\t" \
    "v_readfirstlane_b32 %[rf], v49\n\t" \
    "s_cmp_eq_u32 %[rf], 0\n\t" \
    "s_cbranch_scc1 0b\n\t" \
    "4:\n\t" \
    "s_memrealtime vcc\n\t" \
    "s_waitcnt lgkmcnt(0)\n\t" \
    "s_sub_i32 vcc_lo, vcc_lo, %[rf]\n\t" \
    "s_cmp_lt_i32 vcc_lo, 0\n\t" \
    "s_cbranch_scc1 4b\n\t" \
    "10:\n\t" \
    "global_load_dwordx4 v[48:51], %[gs], %[ws] sc1\n\t" \
    "s_waitcnt vmcnt(0)\n\t" \
    "v_mfma_f32_16x16x32_bf16 v[16:19], v[16:19], v[48:51], 0\n\t" \
    "v_mfma_f32_16x16x32_bf16 v[20:23], v[20:23], v[48:51], 0\n\t" \
    "v_mov_b32_dpp v52, v48 row_shl:4 row_mask:0xf bank_mask:0xf\n\t" \
    "v_mov_b32_dpp v53, v49 row_shl:4 row_mask:0xf bank_mask:0xf\n\t" \
    "v_mov_b32_dpp v54, v50 row_shl:4 row_mask:0xf bank_mask:0xf\n\t" \
    "v_mov_b32_dpp v55, v51 row_shl:4 row_mask:0xf bank_mask:0xf\n\t" \
    "v_mov_b32_dpp v56, v48 row_shl:8 row_mask:0xf bank_mask:0xf\n\t" \
    "v_mov_b32_dpp v57, v49 row_shl:8 row_mask:0xf bank_mask:0xf\n\t" \
    "v_mfma_f32_16x16x32_bf16 v[16:19], v[24:27], v[52:55], v[16:19]\n\t" \
    "v_mov_b32_dpp v58, v50 row_shl:8 row_mask:0xf bank_mask:0xf\n\t" \
    "v_mov_b32_dpp v59, v51 row_shl:8 row_mask:0xf bank_mask:0xf\n\t" \
    "v_mfma_f32_16x16x32_bf16 v[20:23], v[28:31], v[52:55], v[20:23]\n\t" \
    "v_mov_b32_dpp v52, v48 row_shl:12 row_mask:0xf bank_mask:0xf\n\t" \
    "v_mov_b32_dpp v53, v49 row_shl:12 row_mask:0xf bank_mask:0xf\n\t" \
    "v_mfma_f32_16x16x32_bf16 v[16:19], v[32:35], v[56:59], v[16:19]\n\t" \
    "v_mov_b32_dpp v54, v50 row_shl:12 row_mask:0xf bank_mask:0xf\n\t" \
    "v_mov_b32_dpp v55, v51 row_shl:12 row_mask:0xf bank_mask:0xf\n\t" \
    "v_mfma_f32_16x16x32_bf16 v[20:23], v[36:39], v[56:59], v[20:23]\n\t" \
    "v_max3_u32 v24, v48, v49, v50\n\t" \
    "v_max_u32 v24, v24, v51\n\t" \
    "v_mfma_f32_16x16x32_bf16 v[16:19], v[40:43], v[52:55], v[16:19]\n\t" \
    "v_cmp_eq_u32 vcc, -1, v24\n\t" \
    "v_mfma_f32_16x16x32_bf16 v[20:23], v[44:47], v[52:55], v[20:23]\n\t" \
    "s_mov_b32 exec_lo, 0x00ff00ff\n\t" \
    "s_mov_b32 exec_hi, 0x00ff00ff\n\t" \
    "s_cbranch_vccnz 6f\n\t" \
    "s_nop 4\n\t" \
    "v_mov_b32_dpp v16, v20 row_shr:4 row_mask:0xf bank_mask:0x2\n\t" \
    "v_mov_b32_dpp v17, v21 row_shr:4 row_mask:0xf bank_mask:0x2\n\t" \
    "v_mov_b32_dpp v18, v22 row_shr:4 row_mask:0xf bank_mask:0x2\n\t" \
    "v_mov_b32_dpp v19, v23 row_shr:4 row_mask:0xf bank_mask:0x2\n\t" \
    "v_min_u32 v16, -2, v16\n\t" \
    "v_min_u32 v17, -2, v17\n\t" \
    "v_min_u32 v18, -2, v18\n\t" \
    "v_min_u32 v19, -2, v19\n\t" \
    "global_store_dwordx4 %[so], v[16:19], %[ws] sc1\n\t" \
    "s_mov_b64 exec, -1\n\t" \
    "s_branch 2f\n\t" \
    "6:\n\t" \
    "s_mov_b64 exec, -1\n\t" \
    "s_sub_u32 %[st], %[st], 1\n\t" \
    "s_cbranch_scc1 2f\n\t" \
    RETRY_ASM \
    "s_sleep 6\n\t" \
    "s_branch 5b\n\t" \
    "2:\n\t"
#define ASM_ATT_P \
    "s_setprio 2\n\t" \
    "v_mov_b32 v28, 0\n\t" \
    "v_mov_b32 v29, 0\n\t" \
    "v_mov_b32 v32, 0\n\t" \
    "v_mov_b32 v33, 0\n\t" \
    "v_mov_b32 v36, 0\n\t" \
    "v_mov_b32 v37, 0\n\t" \
    "v_mov_b32 v40, 0\n\t" \
    "v_mov_b32 v41, 0\n\t" \
    "v_mov_b32 v48, 0\n\t" \
    "v_mov_b32 v49, 0\n\t" \
    "v_mov_b32 v50, 0\n\t" \
    "v_mov_b32 v51, 0\n\t" \
    "v_mov_b32 v52, 0\n\t" \
    "v_mov_b32 v53, 0\n\t" \
    "v_mov_b32 v54, 0\n\t" \
    "v_mov_b32 v55, 0\n\t" \
    "v_mov_b32 v56, 0\n\t" \
    "v_mov_b32 v57, 0\n\t" \
    "v_mov_b32 v58, 0\n\t" \
    "v_mov_b32 v59, 0\n\t" \
    "v_mov_b32 v16, 0\n\t" \
    "v_mov_b32 v17, 0\n\t" \
    "v_mov_b32 v18, 0\n\t" \
    "v_mov_b32 v19, 0\n\t" \
    "v_mov_b32 v20, %[ma]\n\t" \
    "s_mov_b32 vcc_hi, 0x400000\n\t" \
    "0:\n\t" \
    "ds_read_b32 v21, v20 offset:340\n\t" \
    "s_sub_u32 vcc_hi, vcc_hi, 1\n\t" \
    "s_cbranch_scc1 4f\n\t" \
    "s_waitcnt lgkmcnt(0)\n\t" \
    "v_readfirstlane_b32 %[rf], v21\n\t" \
    "s_cmp_eq_u32 %[rf], 0\n\t" \
    "s_cbranch_scc1 0b\n\t" \
    "s_add_u32 %[st], %[rf], %[tg]\n\t" \
    "4:\n\t" \
    "s_memrealtime vcc\n\t" \
    "s_waitcnt lgkmcnt(0)\n\t" \
    "s_sub_i32 vcc_lo, vcc_lo, %[st]\n\t" \
    "s_cmp_lt_i32 vcc_lo, -4000\n\t" \
    "s_cbranch_scc1 10f\n\t" \
    "s_cmp_lt_i32 vcc_lo, 0\n\t" \
    "s_cbranch_scc1 4b\n\t" \
    "10:\n\t" \
    "s_mov_b32 exec_hi, 0xff\n\t" \
    "global_load_dwordx4 v[16:19], %[go], %[ws] sc1\n\t" \
    "s_mov_b32 exec_hi, -1\n\t" \
    "s_barrier\n\t" \
    "ds_read_b64 v[22:23], v20 offset:352\n\t" \
    "s_mov_b32 %[st], 0x10000\n\t" \
    "s_waitcnt lgkmcnt(0)\n\t" \
    "v_mul_f32 v22, v22, %[cs]\n\t" \
    "v_mul_f32_e64 v22, -v22, v23\n\t" \
    "5:\n\t" \
    "s_waitcnt vmcnt(0)\n\t" \
    "v_max3_u32 v24, v16, v17, v18\n\t" \
    "v_max_u32 v24, v24, v19\n\t" \
    "v_cmp_eq_u32 vcc, -1, v24\n\t" \
    "v_permlane32_swap_b32 v16, v18\n\t" \
    "v_permlane32_swap_b32 v17, v19\n\t" \
    "v_pk_add_f32 v[16:17], v[16:17], v[18:19]\n\t" \
    "s_nop 0\n\t" \
    "s_cbranch_vccnz 6f\n\t" \
    "7:\n\t" \
    "v_permlane16_swap_b32 v16, v17\n\t" \
    "v_add_f32 v16, v16, v17\n\t" \
    "s_nop 1\n\t" \
    "v_add_f32_dpp v16, v16, v16 row_ror:8 row_mask:0xf bank_mask:0xf\n\t" \
    "v_fmac_f32 v22, v16, v23\n\t" \
    "v_cvt_pk_bf16_f32 v22, v22, v22\n\t" \
    "ds_write_b16 %[qo], v22\n\t" \
    "s_waitcnt lgkmcnt(0)\n\t" \
    "s_barrier\n\t" \
    "ds_read_b128 v[16:19], %[qa] offset:0\n\t" \
    "ds_read_b128 v[20:23], %[qa] offset:128\n\t" \
    "ds_read_b128 v[24:27], %[qa] offset:256\n\t" \
    "s_waitcnt lgkmcnt(2)\n\t" \
    "v_smfmac_f32_16x16x64_bf16 v[28:31], v[16:19], v[64:71], v7\n\t" \
    "v_smfmac_f32_16x16x64_bf16 v[32:35], v[16:19], v[88:95], v7\n\t" \
    "v_smfmac_f32_16x16x64_bf16 v[36:39], v[16:19], v[112:119], v7\n\t" \
    "v_smfmac_f32_16x16x64_bf16 v[40:43], v[16:19], v[136:143], v7\n\t" \
    "s_waitcnt lgkmcnt(1)\n\t" \
    "v_smfmac_f32_16x16x64_bf16 v[28:31], v[20:23], v[72:79], v7\n\t" \
    "v_smfmac_f32_16x16x64_bf16 v[32:35], v[20:23], v[96:103], v7\n\t" \
    "v_smfmac_f32_16x16x64_bf16 v[36:39], v[20:23], v[120:127], v7\n\t" \
    "v_smfmac_f32_16x16x64_bf16 v[40:43], v[20:23], v[144:151], v7\n\t" \
    "s_waitcnt lgkmcnt(0)\n\t" \
    "v_smfmac_f32_16x16x64_bf16 v[28:31], v[24:27], v[80:87], v7\n\t" \
    "v_smfmac_f32_16x16x64_bf16 v[32:35], v[24:27], v[104:111], v7\n\t" \
    "v_smfmac_f32_16x16x64_bf16 v[36:39], v[24:27], v[128:135], v7\n\t" \
    "v_smfmac_f32_16x16x64_bf16 v[40:43], v[24:27], v[152:159], v7\n\t" \
    "v_mov_b32 v63, %[mlw]\n\t" \
    "s_nop 3\n\t" \
    "v_add_f32_dpp v60, v28, v29 quad_perm:[0,1,2,3] row_mask:0x1 bank_mask:0xf\n\t" \
    "v_add_f32_dpp v60, v32, v33 quad_perm:[0,1,2,3] row_mask:0x2 bank_mask:0xf\n\t" \
    "v_add_f32_dpp v60, v36, v37 quad_perm:[0,1,2,3] row_mask:0x4 bank_mask:0xf\n\t" \
    "v_add_f32_dpp v60, v40, v41 quad_perm:[0,1,2,3] row_mask:0x8 bank_mask:0xf\n\t" \
    "v_mul_f32 v60, 0x3dd53b94, v60\n\t" \
    "s_nop 1\n\t" \
    "v_max_f32_dpp v61, v60, v60 quad_perm:[1,0,3,2] row_mask:0xf bank_mask:0xf\n\t" \
    "s_nop 1\n\t" \
    "v_max_f32_dpp v61, v61, v61 quad_perm:[2,3,0,1] row_mask:0xf bank_mask:0xf\n\t" \
    "s_nop 1\n\t" \
    "v_max_f32_dpp v61, v61, v61 row_half_mirror row_mask:0xf bank_mask:0xf\n\t" \
    "s_nop 1\n\t" \
    "v_max_f32_dpp v61, v61, v61 row_mirror row_mask:0xf bank_mask:0xf\n\t" \
    "s_nop 1\n\t" \
    "v_max_f32_dpp v61, v61, v61 row_bcast:15 row_mask:0xa bank_mask:0xf\n\t" \
    "s_nop 1\n\t" \
    "v_max_f32_dpp v61, v61, v61 row_bcast:31 row_mask:0xc bank_mask:0xf\n\t" \
    "s_nop 0\n\t" \
    "v_readlane_b32 %[st], v61, 63\n\t" \
    "s_mov_b32 exec_lo, 0\n\t" \
    "s_brev_b32 exec_hi, 1\n\t" \
    "ds_write_b32 v63, v61\n\t" \
    "s_mov_b64 exec, -1\n\t" \
    "v_subrev_f32 v62, %[st], v60\n\t" \
    "v_exp_f32 v62, v62\n\t" \
    "s_nop 0\n\t" \
    "v_cvt_pk_bf16_f32 v60, v62, v62\n\t" \
    "ds_write_b16 %[pw], v60\n\t" \
    "ds_write_b16 %[pw], v60 offset:20\n\t" \
    "ds_read_b128 v[16:19], %[pr] offset:0\n\t" \
    "ds_read_b128 v[20:23], %[pr] offset:272\n\t" \
    "ds_read_b128 v[24:27], %[pr] offset:544\n\t" \
    "ds_read_b128 v[28:31], %[pr] offset:816\n\t" \
    "v_mov_b32 v60, %[ma]\n\t" \
    "s_setprio 0\n\t" \
    "v_add_f32_dpp v62, v62, v62 quad_perm:[1,0,3,2] row_mask:0xf bank_mask:0xf\n\t" \
    "s_nop 1\n\t" \
    "v_add_f32_dpp v62, v62, v62 quad_perm:[2,3,0,1] row_mask:0xf bank_mask:0xf\n\t" \
    "s_nop 1\n\t" \
    "v_add_f32_dpp v62, v62, v62 row_half_mirror row_mask:0xf bank_mask:0xf\n\t" \
    "s_nop 1\n\t" \
    "v_add_f32_dpp v62, v62, v62 row_mirror row_mask:0xf bank_mask:0xf\n\t" \
    "s_waitcnt lgkmcnt(3)\n\t" \
    "v_smfmac_f32_16x16x64_bf16 v[48:51], v[16:19], v[160:167], v7 abid:1\n\t" \
    "v_smfmac_f32_16x16x64_bf16 v[52:55], v[16:19], v[192:199], v7 abid:1\n\t" \
    "v_smfmac_f32_16x16x64_bf16 v[56:59], v[16:19], v[224:231], v7 abid:1\n\t" \
    "v_add_f32_dpp v62, v62, v62 row_bcast:15 row_mask:0xa bank_mask:0xf\n\t" \
    "s_waitcnt lgkmcnt(2)\n\t" \
    "v_smfmac_f32_16x16x64_bf16 v[48:51], v[20:23], v[168:175], v7 abid:1\n\t" \
    "v_smfmac_f32_16x16x64_bf16 v[52:55], v[20:23], v[200:207], v7 abid:1\n\t" \
    "v_smfmac_f32_16x16x64_bf16 v[56:59], v[20:23], v[232:239], v7 abid:1\n\t" \
    "v_add_f32_dpp v62, v62, v62 row_bcast:31 row_mask:0xc bank_mask:0xf\n\t" \
    "s_waitcnt lgkmcnt(1)\n\t" \
    "v_smfmac_f32_16x16x64_bf16 v[48:51], v[24:27], v[176:183], v7 abid:1\n\t" \
    "v_smfmac_f32_16x16x64_bf16 v[52:55], v[24:27], v[208:215], v7 abid:1\n\t" \
    "v_smfmac_f32_16x16x64_bf16 v[56:59], v[24:27], v[240:247], v7 abid:1\n\t" \
    "s_waitcnt lgkmcnt(0)\n\t" \
    "v_smfmac_f32_16x16x64_bf16 v[48:51], v[28:31], v[184:191], v7 abid:1\n\t" \
    "v_smfmac_f32_16x16x64_bf16 v[52:55], v[28:31], v[216:223], v7 abid:1\n\t" \
    "v_smfmac_f32_16x16x64_bf16 v[56:59], v[28:31], v[248:255], v7 abid:1\n\t" \
    "s_barrier\n\t" \
    "ds_read_b128 v[16:19], v60\n\t" \
    "ds_read_b128 v[20:23], v60 offset:16\n\t" \
    "ds_read_b32 v24, %[mo]\n\t" \
    "s_mov_b32 exec_lo, 0\n\t" \
    "s_brev_b32 exec_hi, 1\n\t" \
    "ds_write_b32 v63, v62 offset:32\n\t" \
    "s_mov_b64 exec, -1\n\t" \
    "v_mov_b32_dpp v48, v52 quad_perm:[0,1,2,3] row_mask:0xc bank_mask:0xf\n\t" \
    "v_mov_b32_dpp v49, v53 quad_perm:[0,1,2,3] row_mask:0xc bank_mask:0xf\n\t" \
    "v_mov_b32_dpp v50, v54 quad_perm:[0,1,2,3] row_mask:0xc bank_mask:0xf\n\t" \
    "v_mov_b32_dpp v51, v55 quad_perm:[0,1,2,3] row_mask:0xc bank_mask:0xf\n\t" \
    "ds_write_b128 %[ow], v[48:51] offset:0\n\t" \
    "s_mov_b32 exec_lo, 0x00ff00ff\n\t" \
    "s_mov_b32 exec_hi, 0xff00ff00\n\t" \
    "ds_write_b128 %[ow], v[56:59] offset:2048\n\t" \
    "s_mov_b64 exec, -1\n\t" \
    "s_waitcnt lgkmcnt(3)\n\t" \
    "v_max3_f32 v16, v16, v17, v18\n\t" \
    "v_max3_f32 v19, v19, v20, v21\n\t" \
    "v_max3_f32 v16, v16, v22, v23\n\t" \
    "v_max_f32 v26, v16, v19\n\t" \
    "v_sub_f32 v60, v24, v26\n\t" \
    "v_exp_f32 v60, v60\n\t" \
    "s_nop 0\n\t" \
    "ds_write2_b32 %[mo], v60, v26 offset0:96 offset1:104\n\t" \
    "ds_read_b64_tr_b16 v[28:29], %[a0] offset:0\n\t" \
    "ds_read_b64_tr_b16 v[30:31], %[a0] offset:1024\n\t" \
    "ds_read_b64_tr_b16 v[32:33], %[a1] offset:8192\n\t" \
    "ds_read_b64_tr_b16 v[34:35], %[a1] offset:9216\n\t" \
    "ds_read_b64_tr_b16 v[36:37], %[a0] offset:8192\n\t" \
    "ds_read_b64_tr_b16 v[38:39], %[a0] offset:9216\n\t" \
    "ds_read_b64_tr_b16 v[40:41], %[a1] offset:0\n\t" \
    "ds_read_b64_tr_b16 v[42:43], %[a1] offset:1024\n\t" \
    "ds_read_b64_tr_b16 v[44:45], %[a0] offset:16384\n\t" \
    "ds_read_b64_tr_b16 v[46:47], %[a0] offset:17408\n\t" \
    "ds_read_b64_tr_b16 v[48:49], %[a1] offset:24576\n\t" \
    "ds_read_b64_tr_b16 v[50:51], %[a1] offset:25600\n\t" \
    "ds_read_b64_tr_b16 v[52:53], %[a0] offset:24576\n\t" \
    "ds_read_b64_tr_b16 v[54:55], %[a0] offset:25600\n\t" \
    "ds_read_b64_tr_b16 v[56:57], %[a1] offset:16384\n\t" \
    "ds_read_b64_tr_b16 v[58:59], %[a1] offset:17408\n\t" \
    "s_waitcnt lgkmcnt(0)\n\t" \
    "s_barrier\n\t" \
    "ds_read_b128 v[16:19], %[r0]\n\t" \
    "ds_read_b128 v[20:23], %[r0] offset:512\n\t" \
    "s_waitcnt lgkmcnt(0)\n\t" \
    "v_pk_add_f32 v[16:17], v[16:17], v[20:21]\n\t" \
    "v_pk_add_f32 v[18:19], v[18:19], v[22:23]\n\t" \
    "v_pk_mul_f32 v[16:17], v[16:17], v[60:61] op_sel_hi:[1,0]\n\t" \
    "v_pk_mul_f32 v[18:19], v[18:19], v[60:61] op_sel_hi:[1,0]\n\t" \
    "s_nop 1\n\t" \
    "v_permlane32_swap_b32 v16, v18\n\t" \
    "v_permlane32_swap_b32 v17, v19\n\t" \
    "v_pk_add_f32 v[16:17], v[16:17], v[18:19]\n\t" \
    "s_nop 1\n\t" \
    "v_permlane16_swap_b32 v16, v17\n\t" \
    "v_add_f32 v16, v16, v17\n\t" \
    "s_nop 1\n\t" \
    "v_add_f32_dpp v17, v16, v16 row_ror:8 row_mask:0xf bank_mask:0xf\n\t" \
    "v_add_f32_dpp v16, v16, v16 row_ror:8 row_mask:0xf bank_mask:0xf\n\t" \
    "s_nop 1\n\t" \
    "v_permlane16_swap_b32 v16, v17\n\t" \
    "s_mov_b32 exec_lo, 0xff\n\t" \
    "v_cvt_pk_bf16_f32 v16, v16, v17\n\t" \
    "s_mov_b32 exec_hi, 0xff\n\t" \
    "v_min_u32 v16, -2, v16\n\t" \
    "global_store_dword %[so], v16, %[ws] sc1\n\t" \
    "s_mov_b64 exec, -1\n\t" \
    "ds_read_b64_tr_b16 v[60:61], %[a0] offset:32768\n\t" \
    "ds_read_b64_tr_b16 v[62:63], %[a0] offset:33792\n\t" \
    "s_add_u32 %[rf], %[rf], %[t2]\n\t" \
    "s_mov_b32 %[st], 0x10000\n\t" \
    "14:\n\t" \
    "s_memrealtime vcc\n\t" \
    "s_waitcnt lgkmcnt(0)\n\t" \
    "s_sub_i32 vcc_lo, vcc_lo, %[rf]\n\t" \
    "s_cmp_lt_i32 vcc_lo, -4000\n\t" \
    "s_cbranch_scc1 15f\n\t" \
    "s_cmp_lt_i32 vcc_lo, 0\n\t" \
    "s_cbranch_scc1 14b\n\t" \
    "15:\n\t" \
    "global_load_dwordx4 v[8:11], %[gs], %[ws] offset:0 sc1\n\t" \
    "global_load_dwordx4 v[12:15], %[gs], %[ws] offset:1024 sc1\n\t" \
    "global_load_dwordx4 v[16:19], %[gs], %[ws] offset:2048 sc1\n\t" \
    "global_load_dwordx4 v[24:27], %[gm], %[ws] sc1\n\t" \
    "s_waitcnt vmcnt(3)\n\t" \
    "v_mfma_f32_16x16x32_bf16 v[28:31], v[28:31], v[8:11], 0\n\t" \
    "v_mov_b32_dpp v20, v8 row_half_mirror row_mask:0xf bank_mask:0xf\n\t" \
    "v_mov_b32_dpp v21, v9 row_half_mirror row_mask:0xf bank_mask:0xf\n\t" \
    "v_mfma_f32_16x16x32_bf16 v[32:35], v[32:35], v[8:11], 0\n\t" \
    "v_mov_b32_dpp v22, v10 row_half_mirror row_mask:0xf bank_mask:0xf\n\t" \
    "v_mov_b32_dpp v23, v11 row_half_mirror row_mask:0xf bank_mask:0xf\n\t" \
    "v_max3_u32 %[t0], v8, v9, v10\n\t" \
    "s_waitcnt vmcnt(2)\n\t" \
    "v_mfma_f32_16x16x32_bf16 v[28:31], v[36:39], v[20:23], v[28:31]\n\t" \
    "v_max3_u32 %[t0], %[t0], v11, v12\n\t" \
    "ds_read_b64_tr_b16 v[36:37], %[a1] offset:40960\n\t" \
    "v_mfma_f32_16x16x32_bf16 v[32:35], v[40:43], v[20:23], v[32:35]\n\t" \
    "ds_read_b64_tr_b16 v[38:39], %[a1] offset:41984\n\t" \
    "ds_read_b64_tr_b16 v[40:41], %[a0] offset:40960\n\t" \
    "v_mfma_f32_16x16x32_bf16 v[28:31], v[44:47], v[12:15], v[28:31]\n\t" \
    "v_mov_b32_dpp v20, v12 row_half_mirror row_mask:0xf bank_mask:0xf\n\t" \
    "v_mov_b32_dpp v21, v13 row_half_mirror row_mask:0xf bank_mask:0xf\n\t" \
    "v_mfma_f32_16x16x32_bf16 v[32:35], v[48:51], v[12:15], v[32:35]\n\t" \
    "v_mov_b32_dpp v22, v14 row_half_mirror row_mask:0xf bank_mask:0xf\n\t" \
    "v_mov_b32_dpp v23, v15 row_half_mirror row_mask:0xf bank_mask:0xf\n\t" \
    "ds_read_b64_tr_b16 v[42:43], %[a0] offset:41984\n\t" \
    "ds_read_b64_tr_b16 v[44:45], %[a1] offset:32768\n\t" \
    "v_mfma_f32_16x16x32_bf16 v[28:31], v[52:55], v[20:23], v[28:31]\n\t" \
    "ds_read_b64_tr_b16 v[46:47], %[a1] offset:33792\n\t" \
    "s_waitcnt vmcnt(0)\n\t" \
    "v_max3_u32 %[t0], %[t0], v13, v14\n\t" \
    "v_mfma_f32_16x16x32_bf16 v[32:35], v[56:59], v[20:23], v[32:35]\n\t" \
    "v_max3_u32 v8, v24, v25, v26\n\t" \
    "v_sub_f32 v24, v26, v24\n\t" \
    "v_mfma_f32_16x16x32_bf16 v[28:31], v[60:63], v[16:19], v[28:31]\n\t" \
    "v_mov_b32_dpp v20, v16 row_half_mirror row_mask:0xf bank_mask:0xf\n\t" \
    "v_mov_b32_dpp v21, v17 row_half_mirror row_mask:0xf bank_mask:0xf\n\t" \
    "s_waitcnt lgkmcnt(4)\n\t" \
    "v_mfma_f32_16x16x32_bf16 v[32:35], v[36:39], v[16:19], v[32:35]\n\t" \
    "v_mov_b32_dpp v22, v18 row_half_mirror row_mask:0xf bank_mask:0xf\n\t" \
    "v_mov_b32_dpp v23, v19 row_half_mirror row_mask:0xf bank_mask:0xf\n\t" \
    "v_exp_f32 v24, v24\n\t" \
    "v_max3_u32 %[t0], %[t0], v15, v16\n\t" \
    "s_waitcnt lgkmcnt(0)\n\t" \
    "v_mfma_f32_16x16x32_bf16 v[28:31], v[40:43], v[20:23], v[28:31]\n\t" \
    "v_fmac_f32 v25, v24, v27\n\t" \
    "v_max3_u32 v8, v8, v27, v17\n\t" \
    "v_mfma_f32_16x16x32_bf16 v[32:35], v[44:47], v[20:23], v[32:35]\n\t" \
    "v_rcp_f32 v24, v25\n\t" \
    "v_max3_u32 %[t0], %[t0], v18, v19\n\t" \
    "v_max_u32 v8, v8, %[t0]\n\t" \
    "v_cmp_eq_u32 vcc, -1, v8\n\t" \
    "v_mov_b32 v9, 0xff\n\t" \
    "s_nop 1\n\t" \
    "s_cbranch_vccnz 16f\n\t" \
    "v_mov_b32_dpp v28, v32 quad_perm:[0,1,2,3] row_mask:0xf bank_mask:0xa\n\t" \
    "v_mov_b32_dpp v29, v33 quad_perm:[0,1,2,3] row_mask:0xf bank_mask:0xa\n\t" \
    "v_mov_b32_dpp v30, v34 quad_perm:[0,1,2,3] row_mask:0xf bank_mask:0xa\n\t" \
    "v_mov_b32_dpp v31, v35 quad_perm:[0,1,2,3] row_mask:0xf bank_mask:0xa\n\t" \
    "v_pk_mul_f32 v[28:29], v[28:29], v[24:25] op_sel_hi:[1,0]\n\t" \
    "v_pk_mul_f32 v[30:31], v[30:31], v[24:25] op_sel_hi:[1,0]\n\t" \
    "s_nop 0\n\t" \
    "v_add_f32_dpp v28, v28, v28 row_shl:8 row_mask:0xf bank_mask:0xf\n\t" \
    "v_add_f32_dpp v29, v29, v29 row_shl:8 row_mask:0xf bank_mask:0xf\n\t" \
    "v_add_f32_dpp v30, v30, v30 row_shl:8 row_mask:0xf bank_mask:0xf\n\t" \
    "v_add_f32_dpp v31, v31, v31 row_shl:8 row_mask:0xf bank_mask:0xf\n\t" \
    "s_mov_b32 exec_lo, 0x00ff00ff\n\t" \
    "s_mov_b32 exec_hi, 0x00ff00ff\n\t" \
    "v_bfi_b32 v28, v9, %[tag], v28\n\t" \
    "v_bfi_b32 v29, v9, %[tag], v29\n\t" \
    "v_bfi_b32 v30, v9, %[tag], v30\n\t" \
    "v_bfi_b32 v31, v9, %[tag], v31\n\t" \
    "global_store_dwordx4 %[s2], v[28:31], %[ws] sc1\n\t" \
    "s_mov_b64 exec, -1\n\t" \
    "s_branch 2f\n\t" \
    "6:\n\t" \
    "s_sub_u32 %[st], %[st], 1\n\t" \
    "s_cbranch_scc1 7b\n\t" \
    RETRY_ASM \
    "v_mov_b32 v16, 0\n\t" \
    "v_mov_b32 v17, 0\n\t" \
    "v_mov_b32 v18, 0\n\t" \
    "v_mov_b32 v19, 0\n\t" \
    "s_mov_b32 exec_hi, 0xff\n\t" \
    "global_load_dwordx4 v[16:19], %[go], %[ws] sc1\n\t" \
    "s_mov_b32 exec_hi, -1\n\t" \
    "s_branch 5b\n\t" \
    "16:\n\t" \
    "s_mov_b64 exec, -1\n\t" \
    "s_sub_u32 %[st], %[st], 1\n\t" \
    "s_cbranch_scc1 2f\n\t" \
    RETRY_ASM3 \
    "s_sleep 3\n\t" \
    "ds_read_b64_tr_b16 v[28:29], %[a0] offset:0\n\t" \
    "ds_read_b64_tr_b16 v[30:31], %[a0] offset:1024\n\t" \
    "ds_read_b64_tr_b16 v[32:33], %[a1] offset:8192\n\t" \
    "ds_read_b64_tr_b16 v[34:35], %[a1] offset:9216\n\t" \
    "ds_read_b64_tr_b16 v[36:37], %[a0] offset:8192\n\t" \
    "ds_read_b64_tr_b16 v[38:39], %[a0] offset:9216\n\t" \
    "ds_read_b64_tr_b16 v[40:41], %[a1] offset:0\n\t" \
    "ds_read_b64_tr_b16 v[42:43], %[a1] offset:1024\n\t" \
    "ds_read_b64_tr_b16 v[44:45], %[a0] offset:16384\n\t" \
    "ds_read_b64_tr_b16 v[46:47], %[a0] offset:17408\n\t" \
    "ds_read_b64_tr_b16 v[48:49], %[a1] offset:24576\n\t" \
    "ds_read_b64_tr_b16 v[50:51], %[a1] offset:25600\n\t" \
    "ds_read_b64_tr_b16 v[52:53], %[a0] offset:24576\n\t" \
    "ds_read_b64_tr_b16 v[54:55], %[a0] offset:25600\n\t" \
    "ds_read_b64_tr_b16 v[56:57], %[a1] offset:16384\n\t" \
    "ds_read_b64_tr_b16 v[58:59], %[a1] offset:17408\n\t" \
    "ds_read_b64_tr_b16 v[60:61], %[a0] offset:32768\n\t" \
    "ds_read_b64_tr_b16 v[62:63], %[a0] offset:33792\n\t" \
    "s_waitcnt lgkmcnt(0)\n\t" \
    "s_branch 15b\n\t" \
    "2:\n\t"
#define ASM_OP \
    "s_mov_b32 %[st], 0x10000\n\t" \
    "5:\n\t" \
    "v_mov_b32 v8, %[fl]\n\t" \
    "s_mov_b32 %[ct], 0x400000\n\t" \
    "0:\n\t" \
    "ds_read_b32 v9, v8\n\t" \
    "s_memrealtime vcc\n\t" \
    "s_sub_u32 %[ct], %[ct], 1\n\t" \
    "s_waitcnt lgkmcnt(0)\n\t" \
    "s_cbranch_scc1 10f\n\t" \
    "v_readfirstlane_b32 %[rf], v9\n\t" \
    "s_cmp_eq_u32 %[rf], 0\n\t" \
    "s_cbranch_scc1 0b\n\t" \
    "s_sub_i32 vcc_lo, vcc_lo, %[rf]\n\t" \
    "s_cmp_ge_i32 vcc_lo, -4\n\t" \
    "s_cbranch_scc1 10f\n\t" \
    "4:\n\t" \
    "s_memrealtime vcc\n\t" \
    "s_waitcnt lgkmcnt(0)\n\t" \
    "s_sub_i32 vcc_lo, vcc_lo, %[rf]\n\t" \
    "s_cmp_lt_i32 vcc_lo, 0\n\t" \
    "s_cbranch_scc1 4b\n\t" \
    "10:\n\t" \
    "global_load_dwordx4 v[8:11], %[gs], %[ws] offset:0 sc1\n\t" \
    "global_load_dwordx4 v[12:15], %[gs], %[ws] offset:1024 sc1\n\t" \
    "global_load_dwordx4 v[16:19], %[gs], %[ws] offset:2048 sc1\n\t" \
    "global_load_dwordx4 v[24:27], %[gm], %[ws] sc1\n\t" \
    "v_mov_b32 v28, %[fl]\n\t" \
    "v_add_u32 v28, -8, v28\n\t" \
    "s_mov_b32 %[ct], 0x400000\n\t" \
    "11:\n\t" \
    "ds_read_b32 v29, v28\n\t" \
    "s_sub_u32 %[ct], %[ct], 1\n\t" \
    "s_waitcnt lgkmcnt(0)\n\t" \
    "s_cbranch_scc1 12f\n\t" \
    "v_cmp_eq_u32 vcc, 0, v29\n\t" \
    "s_cbranch_vccnz 11b\n\t" \
    "12:\n\t" \
    "ds_read_b64_tr_b16 v[28:29], %[a0] offset:0\n\t" \
    "ds_read_b64_tr_b16 v[30:31], %[a0] offset:1024\n\t" \
    "ds_read_b64_tr_b16 v[32:33], %[a1] offset:8192\n\t" \
    "ds_read_b64_tr_b16 v[34:35], %[a1] offset:9216\n\t" \
    "ds_read_b64_tr_b16 v[36:37], %[a0] offset:8192\n\t" \
    "ds_read_b64_tr_b16 v[38:39], %[a0] offset:9216\n\t" \
    "ds_read_b64_tr_b16 v[40:41], %[a1] offset:0\n\t" \
    "ds_read_b64_tr_b16 v[42:43], %[a1] offset:1024\n\t" \
    "ds_read_b64_tr_b16 v[44:45], %[a0] offset:16384\n\t" \
    "ds_read_b64_tr_b16 v[46:47], %[a0] offset:17408\n\t" \
    "ds_read_b64_tr_b16 v[48:49], %[a1] offset:24576\n\t" \
    "ds_read_b64_tr_b16 v[50:51], %[a1] offset:25600\n\t" \
    "ds_read_b64_tr_b16 v[52:53], %[a0] offset:24576\n\t" \
    "ds_read_b64_tr_b16 v[54:55], %[a0] offset:25600\n\t" \
    "ds_read_b64_tr_b16 v[56:57], %[a1] offset:16384\n\t" \
    "ds_read_b64_tr_b16 v[58:59], %[a1] offset:17408\n\t" \
    "ds_read_b64_tr_b16 v[60:61], %[a0] offset:32768\n\t" \
    "ds_read_b64_tr_b16 v[62:63], %[a0] offset:33792\n\t" \
    "s_waitcnt vmcnt(3) lgkmcnt(0)\n\t" \
    "v_mfma_f32_16x16x32_bf16 v[28:31], v[28:31], v[8:11], 0\n\t" \
    "v_mov_b32_dpp v20, v8 row_half_mirror row_mask:0xf bank_mask:0xf\n\t" \
    "v_mov_b32_dpp v21, v9 row_half_mirror row_mask:0xf bank_mask:0xf\n\t" \
    "v_mfma_f32_16x16x32_bf16 v[32:35], v[32:35], v[8:11], 0\n\t" \
    "v_mov_b32_dpp v22, v10 row_half_mirror row_mask:0xf bank_mask:0xf\n\t" \
    "v_mov_b32_dpp v23, v11 row_half_mirror row_mask:0xf bank_mask:0xf\n\t" \
    "v_max3_u32 %[t0], v8, v9, v10\n\t" \
    "s_waitcnt vmcnt(2)\n\t" \
    "v_mfma_f32_16x16x32_bf16 v[28:31], v[36:39], v[20:23], v[28:31]\n\t" \
    "v_max3_u32 %[t0], %[t0], v11, v12\n\t" \
    "ds_read_b64_tr_b16 v[36:37], %[a1] offset:40960\n\t" \
    "v_mfma_f32_16x16x32_bf16 v[32:35], v[40:43], v[20:23], v[32:35]\n\t" \
    "ds_read_b64_tr_b16 v[38:39], %[a1] offset:41984\n\t" \
    "ds_read_b64_tr_b16 v[40:41], %[a0] offset:40960\n\t" \
    "v_mfma_f32_16x16x32_bf16 v[28:31], v[44:47], v[12:15], v[28:31]\n\t" \
    "v_mov_b32_dpp v20, v12 row_half_mirror row_mask:0xf bank_mask:0xf\n\t" \
    "v_mov_b32_dpp v21, v13 row_half_mirror row_mask:0xf bank_mask:0xf\n\t" \
    "v_mfma_f32_16x16x32_bf16 v[32:35], v[48:51], v[12:15], v[32:35]\n\t" \
    "v_mov_b32_dpp v22, v14 row_half_mirror row_mask:0xf bank_mask:0xf\n\t" \
    "v_mov_b32_dpp v23, v15 row_half_mirror row_mask:0xf bank_mask:0xf\n\t" \
    "ds_read_b64_tr_b16 v[42:43], %[a0] offset:41984\n\t" \
    "ds_read_b64_tr_b16 v[44:45], %[a1] offset:32768\n\t" \
    "v_mfma_f32_16x16x32_bf16 v[28:31], v[52:55], v[20:23], v[28:31]\n\t" \
    "ds_read_b64_tr_b16 v[46:47], %[a1] offset:33792\n\t" \
    "s_waitcnt vmcnt(0)\n\t" \
    "v_max3_u32 %[t0], %[t0], v13, v14\n\t" \
    "v_mfma_f32_16x16x32_bf16 v[32:35], v[56:59], v[20:23], v[32:35]\n\t" \
    "v_max3_u32 v8, v24, v25, v26\n\t" \
    "v_sub_f32 v24, v26, v24\n\t" \
    "v_mfma_f32_16x16x32_bf16 v[28:31], v[60:63], v[16:19], v[28:31]\n\t" \
    "v_mov_b32_dpp v20, v16 row_half_mirror row_mask:0xf bank_mask:0xf\n\t" \
    "v_mov_b32_dpp v21, v17 row_half_mirror row_mask:0xf bank_mask:0xf\n\t" \
    "s_waitcnt lgkmcnt(4)\n\t" \
    "v_mfma_f32_16x16x32_bf16 v[32:35], v[36:39], v[16:19], v[32:35]\n\t" \
    "v_mov_b32_dpp v22, v18 row_half_mirror row_mask:0xf bank_mask:0xf\n\t" \
    "v_mov_b32_dpp v23, v19 row_half_mirror row_mask:0xf bank_mask:0xf\n\t" \
    "v_exp_f32 v24, v24\n\t" \
    "v_max3_u32 %[t0], %[t0], v15, v16\n\t" \
    "s_waitcnt lgkmcnt(0)\n\t" \
    "v_mfma_f32_16x16x32_bf16 v[28:31], v[40:43], v[20:23], v[28:31]\n\t" \
    "v_fmac_f32 v25, v24, v27\n\t" \
    "v_max3_u32 v8, v8, v27, v17\n\t" \
    "v_mfma_f32_16x16x32_bf16 v[32:35], v[44:47], v[20:23], v[32:35]\n\t" \
    "v_rcp_f32 v24, v25\n\t" \
    "v_max3_u32 %[t0], %[t0], v18, v19\n\t" \
    "v_max_u32 v8, v8, %[t0]\n\t" \
    "v_cmp_eq_u32 vcc, -1, v8\n\t" \
    "v_mov_b32 v9, 0xff\n\t" \
    "s_nop 1\n\t" \
    "s_cbranch_vccnz 6f\n\t" \
    "v_mov_b32_dpp v28, v32 quad_perm:[0,1,2,3] row_mask:0xf bank_mask:0xa\n\t" \
    "v_mov_b32_dpp v29, v33 quad_perm:[0,1,2,3] row_mask:0xf bank_mask:0xa\n\t" \
    "v_mov_b32_dpp v30, v34 quad_perm:[0,1,2,3] row_mask:0xf bank_mask:0xa\n\t" \
    "v_mov_b32_dpp v31, v35 quad_perm:[0,1,2,3] row_mask:0xf bank_mask:0xa\n\t" \
    "v_pk_mul_f32 v[28:29], v[28:29], v[24:25] op_sel_hi:[1,0]\n\t" \
    "v_pk_mul_f32 v[30:31], v[30:31], v[24:25] op_sel_hi:[1,0]\n\t" \
    "s_nop 0\n\t" \
    "v_add_f32_dpp v28, v28, v28 row_shl:8 row_mask:0xf bank_mask:0xf\n\t" \
    "v_add_f32_dpp v29, v29, v29 row_shl:8 row_mask:0xf bank_mask:0xf\n\t" \
    "v_add_f32_dpp v30, v30, v30 row_shl:8 row_mask:0xf bank_mask:0xf\n\t" \
    "v_add_f32_dpp v31, v31, v31 row_shl:8 row_mask:0xf bank_mask:0xf\n\t" \
    "s_mov_b32 exec_lo, 0x00ff00ff\n\t" \
    "s_mov_b32 exec_hi, 0x00ff00ff\n\t" \
    "v_bfi_b32 v28, v9, %[tag], v28\n\t" \
    "v_bfi_b32 v29, v9, %[tag], v29\n\t" \
    "v_bfi_b32 v30, v9, %[tag], v30\n\t" \
    "v_bfi_b32 v31, v9, %[tag], v31\n\t" \
    "global_store_dwordx4 %[so], v[28:31], %[ws] sc1\n\t" \
    "s_mov_b64 exec, -1\n\t" \
    "s_branch 2f\n\t" \
    "6:\n\t" \
    "s_mov_b64 exec, -1\n\t" \
    "s_sub_u32 %[st], %[st], 1\n\t" \
    "s_cbranch_scc1 2f\n\t" \
    RETRY_ASM \
    "s_sleep 6\n\t" \
    "s_branch 5b\n\t" \
    "2:\n\t"
#define ASM_GU \
    "s_mov_b32 %[st], 0x10000\n\t" \
    "5:\n\t" \
    "v_xor_b32 v11, 0x80, %[a0]\n\t" \
    "ds_read_b64_tr_b16 v[16:17], %[a0] offset:0\n\t" \
    "ds_read_b64_tr_b16 v[18:19], v11 offset:0\n\t" \
    "ds_read_b64_tr_b16 v[36:37], %[a0] offset:20480\n\t" \
    "ds_read_b64_tr_b16 v[38:39], v11 offset:20480\n\t" \
    "ds_read_b64_tr_b16 v[20:21], %[a0] offset:1024\n\t" \
    "ds_read_b64_tr_b16 v[22:23], v11 offset:1024\n\t" \
    "ds_read_b64_tr_b16 v[40:41], %[a0] offset:21504\n\t" \
    "ds_read_b64_tr_b16 v[42:43], v11 offset:21504\n\t" \
    "ds_read_b64_tr_b16 v[24:25], %[a0] offset:2048\n\t" \
    "ds_read_b64_tr_b16 v[26:27], v11 offset:2048\n\t" \
    "ds_read_b64_tr_b16 v[44:45], %[a0] offset:22528\n\t" \
    "ds_read_b64_tr_b16 v[46:47], v11 offset:22528\n\t" \
    "ds_read_b64_tr_b16 v[28:29], %[a0] offset:3072\n\t" \
    "ds_read_b64_tr_b16 v[30:31], v11 offset:3072\n\t" \
    "ds_read_b64_tr_b16 v[48:49], %[a0] offset:23552\n\t" \
    "ds_read_b64_tr_b16 v[50:51], v11 offset:23552\n\t" \
    "ds_read_b64_tr_b16 v[32:33], %[a0] offset:4096\n\t" \
    "ds_read_b64_tr_b16 v[34:35], v11 offset:4096\n\t" \
    "ds_read_b64_tr_b16 v[52:53], %[a0] offset:24576\n\t" \
    "ds_read_b64_tr_b16 v[54:55], v11 offset:24576\n\t" \
    "v_mov_b32 v12, 0x3f803f80\n\t" \
    "v_mov_b32 v13, 0x3f803f80\n\t" \
    "v_mov_b32 v14, 0x3f803f80\n\t" \
    "v_mov_b32 v15, 0x3f803f80\n\t" \
    "v_mov_b32 v56, 0x3f803f80\n\t" \
    "v_mov_b32 v57, 0x3f803f80\n\t" \
    "v_mov_b32 v58, 0x3f803f80\n\t" \
    "v_mov_b32 v59, 0x3f803f80\n\t" \
    "4:\n\t" \
    "s_memrealtime vcc\n\t" \
    "s_waitcnt lgkmcnt(0)\n\t" \
    "s_sub_i32 vcc_lo, vcc_lo, %[tg]\n\t" \
    "s_cmp_lt_i32 vcc_lo, 0\n\t" \
    "s_cbranch_scc1 4b\n\t" \
    "global_load_dwordx4 v[6:9], %[gs], %[ws] sc1\n\t" \
    "global_load_dword v10, %[g2], %[ws] sc1\n\t" \
    "s_waitcnt vmcnt(1)\n\t" \
    "v_mov_b32_dpp v12, v6 quad_perm:[0,1,2,3] row_mask:0xf bank_mask:0x1\n\t" \
    "v_mov_b32_dpp v13, v7 quad_perm:[0,1,2,3] row_mask:0xf bank_mask:0x1\n\t" \
    "v_mov_b32_dpp v14, v8 quad_perm:[0,1,2,3] row_mask:0xf bank_mask:0x1\n\t" \
    "v_mov_b32_dpp v15, v9 quad_perm:[0,1,2,3] row_mask:0xf bank_mask:0x1\n\t" \
    "v_mov_b32_dpp v56, v6 row_shl:4 row_mask:0xf bank_mask:0x1\n\t" \
    "v_mov_b32_dpp v57, v7 row_shl:4 row_mask:0xf bank_mask:0x1\n\t" \
    "v_mfma_f32_16x16x32_bf16 v[16:19], v[16:19], v[12:15], 0\n\t" \
    "v_mov_b32_dpp v58, v8 row_shl:4 row_mask:0xf bank_mask:0x1\n\t" \
    "v_mov_b32_dpp v59, v9 row_shl:4 row_mask:0xf bank_mask:0x1\n\t" \
    "v_mfma_f32_16x16x32_bf16 v[36:39], v[36:39], v[12:15], 0\n\t" \
    "v_max3_u32 v11, v6, v7, v8\n\t" \
    "v_mfma_f32_16x16x32_bf16 v[60:63], v[12:15], v[12:15], 0\n\t" \
    "v_mfma_f32_16x16x32_bf16 v[16:19], v[20:23], v[56:59], v[16:19]\n\t" \
    "v_mov_b32_dpp v12, v6 row_shl:8 row_mask:0xf bank_mask:0x1\n\t" \
    "v_mov_b32_dpp v13, v7 row_shl:8 row_mask:0xf bank_mask:0x1\n\t" \
    "v_mfma_f32_16x16x32_bf16 v[36:39], v[40:43], v[56:59], v[36:39]\n\t" \
    "v_mov_b32_dpp v14, v8 row_shl:8 row_mask:0xf bank_mask:0x1\n\t" \
    "v_mov_b32_dpp v15, v9 row_shl:8 row_mask:0xf bank_mask:0x1\n\t" \
    "v_mfma_f32_16x16x32_bf16 v[60:63], v[56:59], v[56:59], v[60:63]\n\t" \
    "s_nop 0\n\t" \
    "v_mfma_f32_16x16x32_bf16 v[16:19], v[24:27], v[12:15], v[16:19]\n\t" \
    "v_mov_b32_dpp v56, v6 row_shl:12 row_mask:0xf bank_mask:0x1\n\t" \
    "v_mov_b32_dpp v57, v7 row_shl:12 row_mask:0xf bank_mask:0x1\n\t" \
    "v_mfma_f32_16x16x32_bf16 v[36:39], v[44:47], v[12:15], v[36:39]\n\t" \
    "v_mov_b32_dpp v58, v8 row_shl:12 row_mask:0xf bank_mask:0x1\n\t" \
    "v_mov_b32_dpp v59, v9 row_shl:12 row_mask:0xf bank_mask:0x1\n\t" \
    "v_mfma_f32_16x16x32_bf16 v[60:63], v[12:15], v[12:15], v[60:63]\n\t" \
    "s_nop 0\n\t" \
    "v_mfma_f32_16x16x32_bf16 v[16:19], v[28:31], v[56:59], v[16:19]\n\t" \
    "s_waitcnt vmcnt(0)\n\t" \
    "v_mov_b32_dpp v12, v10 quad_perm:[0,1,2,3] row_mask:0xf bank_mask:0x1\n\t" \
    "v_mov_b32_dpp v13, v10 row_shl:4 row_mask:0xf bank_mask:0x1\n\t" \
    "v_mfma_f32_16x16x32_bf16 v[36:39], v[48:51], v[56:59], v[36:39]\n\t" \
    "v_mov_b32_dpp v14, v10 row_shl:8 row_mask:0xf bank_mask:0x1\n\t" \
    "v_mov_b32_dpp v15, v10 row_shl:12 row_mask:0xf bank_mask:0x1\n\t" \
    "v_mfma_f32_16x16x32_bf16 v[60:63], v[56:59], v[56:59], v[60:63]\n\t" \
    "v_max3_u32 v11, v11, v9, v10\n\t" \
    "v_mfma_f32_16x16x32_bf16 v[16:19], v[32:35], v[12:15], v[16:19]\n\t" \
    "v_cmp_eq_u32 vcc, -1, v11\n\t" \
    "v_mfma_f32_16x16x32_bf16 v[36:39], v[52:55], v[12:15], v[36:39]\n\t" \
    "v_mfma_f32_16x16x32_bf16 v[60:63], v[12:15], v[12:15], v[60:63]\n\t" \
    "s_mov_b32 exec_lo, 0x001f001f\n\t" \
    "s_mov_b32 exec_hi, 0x001f001f\n\t" \
    "s_cbranch_vccnz 6f\n\t" \
    "7:\n\t" \
    "s_nop 3\n\t" \
    "ds_write2_b32 %[pw], v16, v36 offset0:0 offset1:1\n\t" \
    "ds_write2_b32 %[pw], v17, v37 offset0:12 offset1:13\n\t" \
    "ds_write2_b32 %[pw], v18, v38 offset0:24 offset1:25\n\t" \
    "ds_write2_b32 %[pw], v19, v39 offset0:36 offset1:37\n\t" \
    "ds_write2_b32 %[gw], v60, v61 offset1:4\n\t" \
    "ds_write2_b32 %[gw], v62, v63 offset0:8 offset1:12\n\t" \
    "s_mov_b64 exec, -1\n\t" \
    "s_waitcnt lgkmcnt(0)\n\t" \
    "s_barrier\n\t" \
    "s_branch 2f\n\t" \
    "6:\n\t" \
    "s_sub_u32 %[st], %[st], 1\n\t" \
    "s_cbranch_scc1 7b\n\t" \
    "s_mov_b64 exec, -1\n\t" \
    RETRY_ASM \
    "s_sleep 1\n\t" \
    "s_branch 5b\n\t" \
    "2:\n\t"
#define ASM_HS \
    "ds_read_b64_tr_b16 v[16:17], %[w0] offset:0\n\t" \
    "ds_read_b64_tr_b16 v[18:19], %[w1] offset:32\n\t" \
    "ds_read_b64_tr_b16 v[20:21], %[w2] offset:64\n\t" \
    "ds_read_b64_tr_b16 v[22:23], %[w3] offset:96\n\t" \
    "ds_read_b64_tr_b16 v[24:25], %[w0] offset:128\n\t" \
    "ds_read_b64_tr_b16 v[26:27], %[w1] offset:160\n\t" \
    "ds_read_b64_tr_b16 v[28:29], %[w2] offset:192\n\t" \
    "ds_read_b64_tr_b16 v[30:31], %[w3] offset:224\n\t" \
    "ds_read_b64_tr_b16 v[32:33], %[w0] offset:256\n\t" \
    "ds_read_b64_tr_b16 v[34:35], %[w1] offset:288\n\t" \
    "v_mov_b32 v36, 0\n\t" \
    "v_mov_b32 v37, 0\n\t" \
    "v_mov_b32 v38, 0\n\t" \
    "v_mov_b32 v39, 0\n\t" \
    "v_mov_b32 v40, 0\n\t" \
    "v_mov_b32 v41, 0\n\t" \
    "v_mov_b32 v42, 0\n\t" \
    "v_mov_b32 v43, 0\n\t" \
    "v_add_u32 v12, 0x40000, %[so]\n\t" \
    "v_add_u32 v13, 0x80000, %[so]\n\t" \
    "s_waitcnt lgkmcnt(0)\n\t" \
    "s_barrier\n\t" \
    "ds_read_b128 v[44:47], %[sx]\n\t" \
    "ds_read_b128 v[48:51], %[sq]\n\t" \
    "ds_read_b128 v[52:55], %[cp]\n\t" \
    "ds_read_b128 v[56:59], %[cp] offset:16\n\t" \
    "ds_read_b128 v[60:63], %[rp]\n\t" \
    "ds_read_b128 v[8:11], %[rp] offset:16\n\t" \
    "s_waitcnt lgkmcnt(5)\n\t" \
    "v_pk_add_f32 v[44:45], v[44:45], v[46:47]\n\t" \
    "s_waitcnt lgkmcnt(4)\n\t" \
    "v_pk_add_f32 v[48:49], v[48:49], v[50:51]\n\t" \
    "v_add_f32 v44, v44, v45\n\t" \
    "v_add_f32 v48, v48, v49\n\t" \
    "v_mul_f32 v44, 0x3acccccd, v44\n\t" \
    "v_mul_f32 v48, 0x3acccccd, v48\n\t" \
    "v_fma_f32 v48, -v44, v44, v48\n\t" \
    "v_max_f32 v48, 0, v48\n\t" \
    "v_cvt_pk_bf16_f32 v44, v44, v48\n\t" \
    "v_and_b32 v48, 0xffff0000, v44\n\t" \
    "v_add_f32 v48, 0x3727c5ac, v48\n\t" \
    "v_cvt_pk_bf16_f32 v48, v48, v48\n\t" \
    "v_lshlrev_b32 v48, 16, v48\n\t" \
    "v_sqrt_f32 v48, v48\n\t" \
    "s_waitcnt lgkmcnt(2)\n\t" \
    "v_pk_add_f32 v[52:53], v[52:53], v[54:55]\n\t" \
    "v_cvt_pk_bf16_f32 v48, v48, v48\n\t" \
    "v_lshlrev_b32 v48, 16, v48\n\t" \
    "v_rcp_f32 v48, v48\n\t" \
    "v_pk_add_f32 v[56:57], v[56:57], v[58:59]\n\t" \
    "v_lshlrev_b32 v44, 16, v44\n\t" \
    "s_waitcnt lgkmcnt(0)\n\t" \
    "v_pk_add_f32 v[60:61], v[60:61], v[62:63]\n\t" \
    "v_pk_add_f32 v[8:9], v[8:9], v[10:11]\n\t" \
    "v_pk_add_f32 v[52:53], v[52:53], v[56:57]\n\t" \
    "v_pk_add_f32 v[60:61], v[60:61], v[8:9]\n\t" \
    "v_pk_fma_f32 v[60:61], v[52:53], v[44:45], v[60:61] op_sel_hi:[1,0,1] neg_lo:[1,0,0] neg_hi:[1,0,0]\n\t" \
    "v_pk_mul_f32 v[60:61], v[60:61], v[48:49] op_sel_hi:[1,0]\n\t" \
    "v_cvt_pk_bf16_f32 v60, v60, v61\n\t" \
    "v_lshlrev_b32 v61, 16, v60\n\t" \
    "v_mul_f32 v62, 0xbfb8aa3b, v61\n\t" \
    "v_exp_f32 v62, v62\n\t" \
    "v_and_b32 v60, 0xffff0000, v60\n\t" \
    "v_add_f32 v62, 1.0, v62\n\t" \
    "v_rcp_f32 v62, v62\n\t" \
    "s_nop 0\n\t" \
    "v_mul_f32 v61, v62, v61\n\t" \
    "v_cvt_pk_bf16_f32 v61, v61, v61\n\t" \
    "v_lshlrev_b32 v61, 16, v61\n\t" \
    "v_mul_f32 v60, v60, v61\n\t" \
    "s_nop 1\n\t" \
    "v_mov_b32_dpp v61, v60 row_ror:12 row_mask:0xf bank_mask:0xf\n\t" \
    "v_cvt_pk_bf16_f32 v60, v60, v61\n\t" \
    "s_nop 1\n\t" \
    "v_mov_b32_dpp v36, v60 quad_perm:[0,1,2,3] row_mask:0xf bank_mask:0x1\n\t" \
    "v_mov_b32_dpp v37, v60 row_ror:8 row_mask:0xf bank_mask:0x1\n\t" \
    "v_mov_b32_dpp v38, v60 quad_perm:[0,1,2,3] row_mask:0xf bank_mask:0x2\n\t" \
    "v_mov_b32_dpp v39, v60 row_ror:8 row_mask:0xf bank_mask:0x2\n\t" \
    "v_mov_b32_dpp v40, v60 quad_perm:[0,1,2,3] row_mask:0xf bank_mask:0x4\n\t" \
    "v_mov_b32_dpp v41, v60 row_ror:8 row_mask:0xf bank_mask:0x4\n\t" \
    "v_mfma_f32_16x16x32_bf16 v[44:47], v[16:19], v[36:39], 0\n\t" \
    "v_mov_b32_dpp v42, v60 quad_perm:[0,1,2,3] row_mask:0xf bank_mask:0x8\n\t" \
    "v_mov_b32_dpp v43, v60 row_ror:8 row_mask:0xf bank_mask:0x8\n\t" \
    "v_mfma_f32_16x16x32_bf16 v[48:51], v[24:27], v[36:39], 0\n\t" \
    "v_mfma_f32_16x16x32_bf16 v[52:55], v[32:35], v[36:39], 0\n\t" \
    "v_mfma_f32_16x16x32_bf16 v[44:47], v[20:23], v[40:43], v[44:47]\n\t" \
    "v_mfma_f32_16x16x32_bf16 v[48:51], v[28:31], v[40:43], v[48:51]\n\t" \
    "s_nop 5\n\t" \
    "v_min_u32 v52, -2, v52\n\t" \
    "v_min_u32 v53, -2, v53\n\t" \
    "v_min_u32 v54, -2, v54\n\t" \
    "v_min_u32 v55, -2, v55\n\t" \
    "v_min_u32 v44, -2, v44\n\t" \
    "v_min_u32 v45, -2, v45\n\t" \
    "v_min_u32 v46, -2, v46\n\t" \
    "v_min_u32 v47, -2, v47\n\t" \
    "global_store_dwordx4 %[so], v[44:47], %[ws] sc1\n\t" \
    "v_min_u32 v48, -2, v48\n\t" \
    "v_min_u32 v49, -2, v49\n\t" \
    "v_min_u32 v50, -2, v50\n\t" \
    "v_min_u32 v51, -2, v51\n\t" \
    "global_store_dwordx4 v12, v[48:51], %[ws] sc1\n\t" \
    "s_mov_b32 exec_lo, 0x00ff00ff\n\t" \
    "s_mov_b32 exec_hi, 0x00ff00ff\n\t" \
    "global_store_dwordx4 v13, v[52:55], %[ws] sc1\n\t" \
    "s_mov_b64 exec, -1\n\t"
#define CLOB_32_59 "v32", "v33", "v34", "v35", "v36", "v37", "v38", "v39", "v40", "v41", "v42", "v43", "v44", "v45", "v46", "v47", "v48", "v49", "v50", "v51", "v52", "v53", "v54", "v55", "v56", "v57", "v58", "v59"
#define CLOB_8_15 "v8", "v9", "v10", "v11", "v12", "v13", "v14", "v15"
#define CLOB_16_59 "v16", "v17", "v18", "v19", "v20", "v21", "v22", "v23", "v24", "v25", "v26", "v27", "v28", "v29", "v30", "v31", "v32", "v33", "v34", "v35", "v36", "v37", "v38", "v39", "v40", "v41", "v42", "v43", "v44", "v45", "v46", "v47", "v48", "v49", "v50", "v51", "v52", "v53", "v54", "v55", "v56", "v57", "v58", "v59"
#define CLOB_16_63 "v16", "v17", "v18", "v19", "v20", "v21", "v22", "v23", "v24", "v25", "v26", "v27", "v28", "v29", "v30", "v31", "v32", "v33", "v34", "v35", "v36", "v37", "v38", "v39", "v40", "v41", "v42", "v43", "v44", "v45", "v46", "v47", "v48", "v49", "v50", "v51", "v52", "v53", "v54", "v55", "v56", "v57", "v58", "v59", "v60", "v61", "v62", "v63"
template <int R, int IMM> __device__ __forceinline__ void ldkv(const void* sbase, unsigned voff) {
    if constexpr ((R < 160 && (POL & 1)) || (R >= 160 && (POL & 2)))
        asm volatile("global_load_dwordx4 v[%c2:%c3], %0, %1 offset:%c4" :: "v"(voff), "s"(sbase), "i"(R), "i"(R + 3), "i"(IMM) : "memory");
    else
        asm volatile("global_load_dwordx4 v[%c2:%c3], %0, %1 offset:%c4 nt" :: "v"(voff), "s"(sbase), "i"(R), "i"(R + 3), "i"(IMM) : "memory");
}
template <int A> __device__ __forceinline__ void fix_k() {
    unsigned t0, t1, t2, t3;
    asm volatile("v_mov_b32 %0, v%c4\n\tv_mov_b32 %1, v%c5\n\tv_mov_b32 %2, v%c6\n\tv_mov_b32 %3, v%c7\n\t"
                 "v_mov_b32_dpp v%c4, v%c8 row_ror:8 row_mask:0xf bank_mask:0xc\n\tv_mov_b32_dpp v%c5, v%c9 row_ror:8 row_mask:0xf bank_mask:0xc\n\t"
                 "v_mov_b32_dpp v%c6, v%c10 row_ror:8 row_mask:0xf bank_mask:0xc\n\tv_mov_b32_dpp v%c7, v%c11 row_ror:8 row_mask:0xf bank_mask:0xc\n\t"
                 "v_mov_b32_dpp v%c8, %0 row_ror:8 row_mask:0xf bank_mask:0x3\n\tv_mov_b32_dpp v%c9, %1 row_ror:8 row_mask:0xf bank_mask:0x3\n\t"
                 "v_mov_b32_dpp v%c10, %2 row_ror:8 row_mask:0xf bank_mask:0x3\n\tv_mov_b32_dpp v%c11, %3 row_ror:8 row_mask:0xf bank_mask:0x3"
                 : "=&v"(t0), "=&v"(t1), "=&v"(t2), "=&v"(t3) : "i"(A), "i"(A + 1), "i"(A + 2), "i"(A + 3), "i"(A + 12), "i"(A + 13), "i"(A + 14), "i"(A + 15));
}
#define PACE 1
#define D7 16
template <int D = DHI> __device__ __forceinline__ void pace(unsigned& due, unsigned step) {
    asm volatile("s_waitcnt vmcnt(%2)\n\ts_add_u32 %0, %0, %1\n1:\n\ts_memrealtime vcc\n\ts_waitcnt lgkmcnt(0)\n\ts_lshl_b32 vcc_lo, vcc_lo, 4\n\ts_sub_i32 vcc_lo, vcc_lo, %0\n\ts_cmp_ge_i32 vcc_lo, 0\n\ts_cbranch_scc0 1b"
                 : "+s"(due) : "s"(step), "i"(D) : "memory", "scc", "vcc");
}
__device__ __forceinline__ unsigned pace_t(unsigned& due, unsigned step) {
    unsigned t;
    asm volatile("s_waitcnt vmcnt(%3)\n\ts_add_u32 %0, %0, %2\n1:\n\ts_memrealtime vcc\n\ts_waitcnt lgkmcnt(0)\n\ts_lshl_b32 %1, vcc_lo, 4\n\ts_sub_i32 vcc_lo, %1, %0\n\ts_cmp_ge_i32 vcc_lo, 0\n\ts_cbranch_scc0 1b"
                 : "+s"(due), "=&s"(t) : "s"(step), "i"(DHI) : "memory", "scc", "vcc");
    return t;
}
#define MID 0
#define MIDN 7
#define JE 0
#define PW 0
#define CPF 0
#define PADN 0
#define KF 0
#define W7F 0
#define EPF_PULL 0
#define FIRSTD 0
#define EPM 248
#define EPN 16
#define PULLM 0
#define KEEPX " sc1"
#define LKEEP 0
#define LPN 4
#define TPN 0
#define OPCL
#define STG 2
#define AB(x) 0
#define OWN 0
#define OWNER(h, sub) ((OWN & 1) ? ((h) >> 4) * 80 + ((0x03142 >> (4 * (sub))) & 7) * 16 + ((h) & 15) : (h) * 5 + (sub))
#define PGM 255
#define PAGE(k) do { if (PGM & (1 << (k))) asm volatile("s_branch 1f\n\t.p2align 12\n1:" ::: "memory"); } while (0)
#define TRCL
#define QSP 0
#define XI 14
#define OPA 1
#define EHW 2
#define GPOL 1
#define QZ 0
#define PRI 3
#define P1ONLY 0
#undef NET
#define NET 0
#define NPS 0
#define NP0 0
#define MP0 24
#define MPS 3
static_assert(!WOMODE || (NET <= 3 && MP0 >= 11 && MP0 + (6 - NET) * MPS < 36), "extras");
template <int I, int HI, bool THR, class F> __device__ __forceinline__ void kv_stream_x(const void* kbase, const void* vbase, unsigned ko, unsigned v0, const unsigned (&to)[4], unsigned& due, unsigned& step, F&& extra) {
    if constexpr (I < HI) {
        if constexpr (NPS > 0 && I >= NP0 && (I - NP0) % (NPS > 0 ? NPS : 1) == 0 && (I - NP0) / (NPS > 0 ? NPS : 1) < 6) extra(100 + (I - NP0) / NPS);
        if constexpr (WOMODE && I >= MP0 && (I - MP0) % MPS == 0 && (I - MP0) / MPS < 7 - NET) { extra((I - MP0) / MPS); pace(due, step); }
        if constexpr (I < 24) ldkv<64 + 4 * I, (I % 3) * 128 + ((I / 3) & 1) * 3072>(kbase, ko + to[I / 6]);
        else ldkv<160 + 4 * (I - 24), ((I - 24) & 3) * 384>(vbase, v0 + to[(I - 24) >> 2]);
        if constexpr (I >= 14 && I < 38 && (I - 14) % 6 < 3) fix_k<64 + 4 * (I - 14)>();
        pace(due, step);
        if constexpr (I == XI || I == XI + 5) extra(300 + I - XI);
        if constexpr (I == P_QP || I == 14 || I == 18 || I == 26 || I == 34) extra(I == P_QP ? 216 : I == 14 ? 220 : I == 18 ? 224 : I == 26 ? 228 : 232);
        kv_stream_x<I + 1, HI, THR>(kbase, vbase, ko, v0, to, due, step, extra);
    }
}
template <int I, int HI, int THR> __device__ __forceinline__ void kv_stream(const void* kbase, const void* vbase, unsigned ko, unsigned v0, const unsigned (&to)[4], unsigned& due, unsigned step) {
    if constexpr (I < HI) {
        if constexpr (I < 24) ldkv<64 + 4 * I, (I % 3) * 128 + ((I / 3) & 1) * 3072>(kbase, ko + to[I / 6]);
        else ldkv<160 + 4 * (I - 24), ((I - 24) & 3) * 384>(vbase, v0 + to[(I - 24) >> 2]);
        if constexpr (THR == 1 && I >= P_BA) pace(due, step);
        if constexpr (THR == 2) pace<D7>(due, step);
        if constexpr (THR == 3) pace(due, step);
        kv_stream<I + 1, HI, THR>(kbase, vbase, ko, v0, to, due, step);
    }
}
#define NME 46
static_assert((NM7 == 0 || NN7 % 6 == 0) && NM7 <= NME - F7 && NN7 <= 48 - F7, "slots");
template <int I> struct IC { static constexpr int v = I; };
template <int I, int HI, class F> __device__ __forceinline__ void kv_stream7(const void* kbase, const void* vbase, unsigned ko, unsigned v0, unsigned v1, unsigned v2, const unsigned (&to)[4], unsigned& due, unsigned step, F&& nar) {
    if constexpr (I < HI) {
        if constexpr (I < 24) ldkv<64 + 4 * I, (I % 3) * 128 + ((I / 3) & 1) * 3072>(kbase, ko + to[I / 6]);
        else if constexpr (I < 40) ldkv<160 + 4 * (I - 24), ((I - 24) & 3) * 384>(vbase, v0 + to[(I - 24) >> 2]);
        else ldkv<160 + 4 * (I - 24), (I & 3) * 384>(vbase, I < 44 ? v1 : v2);
        if constexpr (((I - F7) * NN7) % (48 - F7) < NN7) nar(IC<((I - F7) * NN7) / (48 - F7)>{});
        if constexpr (I < NME && ((I - F7) * NM7) % (NME - F7) < NM7) nar(IC<100 + ((I - F7) * NM7) / (NME - F7)>{});
        if constexpr (I < 47) pace<D7>(due, step);
        kv_stream7<I + 1, HI>(kbase, vbase, ko, v0, v1, v2, to, due, step, nar);
    }
}
template <int I> __device__ __forceinline__ void fix_all() { if constexpr (I < 12) { fix_k<64 + 4 * ((I / 3) * 6 + I % 3)>(); fix_all<I + 1>(); } }
#define LANES() int lane; asm volatile("v_mbcnt_lo_u32_b32 %0, -1, 0\n\tv_mbcnt_hi_u32_b32 %0, -1, %0" : "=v"(lane)); __builtin_assume(lane >= 0 && lane < 64); \
    const int n = lane & 15, kq = lane >> 4, q4 = n >> 2, p4 = lane & 3; (void)n; (void)kq; (void)q4; (void)p4
template <unsigned C> __device__ __forceinline__ u32x4 splat() { unsigned a, b, c, d; asm volatile("v_mov_b32 %0, %4\n\tv_mov_b32 %1, %4\n\tv_mov_b32 %2, %4\n\tv_mov_b32 %3, %4" : "=v"(a), "=v"(b), "=v"(c), "=v"(d) : "i"(C)); return (u32x4){a, b, c, d}; }
static_assert(TDEPTH <= 8 && DHI <= 8, "fix_k schedule");
__device__ __forceinline__ u32x4 ld16_a(const void* sbase, unsigned voff) {
    u32x4 r; asm volatile("global_load_dwordx4 %0, %1, %2" : "=v"(r) : "v"(voff), "s"(sbase) : "memory"); return r;
}

template <bool KEEP = false> __device__ __forceinline__ void narrow(const u16* wg, const u16* wu, unsigned va, unsigned vb, unsigned lb, int s, int rot) {
    const int s10 = (s * 26) >> 8, r = s - 10 * s10, m = r >= 5, c = r - 5 * m, qt = (s10 + rot) & 3;
    if (KEEP) dma16p((m ? wu : wg) + (qt * 160 + (c & 3) * 8) * 1536, c == 4 ? vb : va, lb + L_GU + (unsigned)(m * 20480 + (qt * 5 + c) * 1024)); else
    dma16((m ? wu : wg) + (qt * 160 + (c & 3) * 8) * 1536, c == 4 ? vb : va, lb + L_GU + (unsigned)(m * 20480 + (qt * 5 + c) * 1024));
}
#define NROT 0
#define ROTS(s) (NROT ? ((s) + 7 * (mi & 3)) % (int)SCH(S_N1) : (s))
#define OSX ""
#define LATED 0
#define LDSL 2
template <int P> __device__ __forceinline__ void wo_piece(const u16* wob, unsigned oa, unsigned lb) {
    constexpr int j = P / 6, i = P % 6;
    dma16(wob + (i * 32 + (j & 1) * 16 + ((j >> 1) & 1) * 4 + (j >> 2) * 2) * DM, (j & 2) ? oa ^ 128u : oa, lb + L_WO + (j + 8 * i) * 1024);
}
template <int P, int HI, int SP = WSP> __device__ __forceinline__ void wo_rest(const u16* wob, unsigned oa, unsigned ob, unsigned lb) {
    if constexpr (P < HI) {
        constexpr int j = P / 6, i = P % 6; unsigned t;
        asm volatile("s_add_u32 m0, %3, %4\n\tv_add_u32 %0, %5, %1\n\tglobal_load_lds_dwordx4 %0, %2" WPOL
                     : "=&v"(t) : "v"((j & 2) ? ob : oa), "s"(wob), "s"(lb), "i"(L_WO + (j + 8 * i) * 1024), "i"((i * 32 + (j & 1) * 16 + ((j >> 1) & 1) * 4 + (j >> 2) * 2) * DM * 2) : "memory", "scc");
        if (SP > 0 && P < 47) __builtin_amdgcn_s_sleep(SP);
        if (SP < 0 && (P & 1) && P < 47) __builtin_amdgcn_s_sleep(-SP);
        wo_rest<P + 1, HI, SP>(wob, oa, ob, lb);
    }
}
#define WDP 8
template <int P, int HI> __device__ __forceinline__ void wo_paced(const u16* wob, unsigned oa, unsigned ob, unsigned lb, unsigned& due, unsigned step) {
    if constexpr (P < HI) { pace<WDP>(due, step); wo_rest<P, P + 1, 0>(wob, oa, ob, lb); wo_paced<P + 1, HI>(wob, oa, ob, lb, due, step); }
}
#define M0_KEEP() unsigned m0_; asm volatile("s_mov_b32 %0, m0" : "=s"(m0_) :: "memory")
#define M0_BACK() asm volatile("s_mov_b32 m0, %0" :: "s"(m0_) : "memory")
#define WO_ADDR() const u16* wob = wo + h * DH * DM + sub * 128; const unsigned woa = (unsigned)(((kq >> 1) * 8 + (kq & 1)) * DM * 2 + ((n >> 1) ^ kq) * 32 + (n & 1) * 16)
#define NARROW_ADDR() const int r5_ = swz(lane >> 1); const unsigned nva = (unsigned)(((r5_ & 7) + ((r5_ >> 3) << 5)) * 3072 + mi * 32 + (lane & 1) * 16), \
        nvb = (unsigned)((128 + r5_) * 3072 + mi * 32 + (lane & 1) * 16); const int nrot = 0

#define SLOWL(j) (((((unsigned)(size_t)wg >> 7) + (unsigned)(j)) & 7u) == 3u)
#define N1W ((int)(SLOWL(mi >> 2) ? SCH(S_N0) : SCH(S_N1)))
#define GUPF(rot) do { const int nd = (int)SCH(S_ND); int sh = (rr >> 3) * 5 + sub + (int)(rot); if (sh >= 20) sh -= 20; \
        _Pragma("unroll 1") for (int k_ = 0; k_ < nd; k_ += 21) { const int rw = k_ + lane / 3; \
        const unsigned o = (unsigned)((640 - 20 * nd + sh * nd + (rw < nd ? rw : nd - 1)) * 3072 + xg * 384 + (lane % 3) * 128); \
        if (GPOL) { pull(wg, o, lb + L_PF); pull(wu, o, lb + L_PF); } else { dma4(wg, o, lb + L_PF); dma4(wu, o, lb + L_PF); } } } while (0)
#define EXTRA(k) do { LANES(); \
        if (!is_mlp) { \
            const int j = (k) < 6 ? wave : 7, i = (k) < 6 ? (k) : wave < 5 ? wave : 5, pp = j * 4 + kq, f = (pp & 3) | (((pp >> 3) & 1) << 2); \
            const int dim = ((pp >> 4) & 1) * 16 + (pp & 1) * 8 + ((pp >> 1) & 7); \
            dma16(wo + (h * DH + i * 32) * DM + sub * 128, (unsigned)(dim * DM * 2 + ((n >> 1) ^ f) * 32 + (n & 1) * 16), lb + L_WO + (j + 8 * i) * 1024); \
        } else if ((k) < 4) { \
            const int s = (int)SCH(S_N0) + wave + 7 * (k); \
            if (s < (int)SCH(S_N1)) { NARROW_ADDR(); narrow(wg, wu, nva, nvb, lb, s, nrot); } \
        } else { \
            const int ii = wave + 7 * ((k) - 4), c = ii * 64 + lane; int row = c / 82, cc = c - 82 * row; if (cc > 79) cc = 79; if (row > 15) row = 15; \
            dma16(wd + mi * 16 * DM, (unsigned)((row * DM + cc * 8) * 2), lb + L_WD + ii * 1024); \
        } } while (0)

__global__ void __launch_bounds__(512, 2) __attribute__((amdgpu_num_vgpr(32), aligned(KAL)))
mega_kernel(const u16* __restrict__ kc, const u16* __restrict__ vc, unsigned char* __restrict__ ws, const u16* __restrict__ x0, const u16* __restrict__ wq,
            const u16* __restrict__ wg, const u16* __restrict__ wu, const u16* __restrict__ wo, const u16* __restrict__ wr,
            const u16* __restrict__ wd, u16* __restrict__ out
            ) {
    extern __shared__ __attribute__((aligned(16))) unsigned char smem[];
    constexpr unsigned sched_c[S_N] = SCHED; (void)sched_c;
    const unsigned lb = (unsigned)(size_t)smem;
#define STAMP0() do {} while (0)
    unsigned long long st64 = 0; (void)st64;
    const int wave0 = __builtin_amdgcn_readfirstlane(threadIdx.x >> 6);
    {
        unsigned long long kp, pc; unsigned s0, s1, s2, a0, a1;
        asm volatile("s_load_dwordx2 %0, %7, 0x0\n\ts_getpc_b64 %1\nmega_pf_here:\n\ts_lshr_b32 %2, %8, 3\n\tv_lshl_add_u32 %5, %9, 5, %2\n\tv_lshlrev_b32 %5, 7, %5\n\t"
                     "v_min_u32 %5, mega_pf_end - mega_pf_here - 4, %5\n\tglobal_load_dword v255, %5, %1\n\t"
                     "s_and_b32 %3, %8, 48\n\ts_and_b32 %4, %8, 7\n\ts_lshl1_add_u32 %3, %4, %3\n\ts_and_b32 %4, %2, 25\n\ts_lshl4_add_u32 %3, %3, %4\n\ts_add_u32 %3, %3, 6\n\ts_lshl_b32 %3, %3, 5\n\t"
                     "v_lshrrev_b32 %5, 6, %9\n\tv_add_u32 %5, %2, %5\n\tv_and_or_b32 %5, %5, 31, %3\n\tv_and_b32 %6, 7, %9\n\tv_lshl_add_u32 %5, %5, 4, %6\n\tv_mul_u32_u24 %5, 0x180, %5\n\t"
                     "v_and_or_b32 %5, %9, 48, %5\n\tv_lshlrev_b32 %6, 3, %9\n\tv_and_or_b32 %5, %6, 64, %5\n\ts_waitcnt lgkmcnt(0)\n\t"
                     "global_load_dwordx4 v[64:67], %5, %0 nt\n\tglobal_load_dwordx4 v[68:71], %5, %0 offset:128 nt\n\tglobal_load_dwordx4 v[72:75], %5, %0 offset:256 nt\n\tglobal_load_dwordx4 v[76:79], %5, %0 offset:3072 nt\n\t"
                     "s_memrealtime %1\n\ts_waitcnt lgkmcnt(0)"
                     : "=&s"(kp), "=&s"(pc), "=&s"(s0), "=&s"(s1), "=&s"(s2), "=&v"(a0), "=&v"(a1) : "s"(__builtin_amdgcn_kernarg_segment_ptr()), "s"(blockIdx.x), "v"(threadIdx.x) : "memory", "scc");
        st64 = pc;
    }
    const u32x4 ones = {~0u, ~0u, ~0u, ~0u};
    const f32x4 z4 = {0.f, 0.f, 0.f, 0.f};
    asm volatile("" ::: "v255");
#define WSOFF 0

#define DMARK(k) do {} while (0)
    unsigned to[4];
#pragma unroll
    for (int t = 0; t < 4; ++t) to[t] = ((t * 8 + wave0 + (GID >> 3)) & 31u) * 6144u;
    unsigned step = wave0 == 7 ? SCH(S_P7) : ((GID >> 3) & 7) >= 5 ? SCH(S_PM) : SCH(S_PD);
    unsigned t_start, due, t0, pol = 0u, tag = 0u;
#define START(off) do { t0 = t_start = (unsigned)st64; due = ((t_start + (off)) << 4) + ((((wave0 + STG) & 7) * step) >> 3); STAMPV(10, t_start); STAMP0(); } while (0)
    {
        const int wave = wave0, lane = threadIdx.x & 63;
        const int g = GID, xg = g & 7, rr = g >> 3;
        const int h = xg * 4 + (rr >> 3), sub = rr & 7, b = sub >> 1, half = sub & 1;
        const bool is_mlp = sub >= 5; const int mi = h * 3 + sub - 5, oi = OWNER(h, sub);
        const int n = lane & 15, kq = lane >> 4, q4 = n >> 2, p4 = lane & 3;
        const u16* kbase = kc + ((size_t)(b * HQ + h) * SK + (SK - WIN) + half * 512) * DH;
        const u16* vbase = vc + ((size_t)(b * HQ + h) * SK + (SK - WIN) + half * 512) * DH;
        const unsigned ko = (unsigned)((lane & 7) * 384 + (((lane >> 3) & 1) * 4 + (lane >> 4)) * 16);
        const unsigned v0 = (unsigned)(4 * kq * 384 + n * 16);
        const unsigned v1 = (unsigned)(4 * kq * 384 + 256 + (n & 7) * 16) + (n >= 8 ? to[2] : to[0]), v2 = (unsigned)(4 * kq * 384 + 256 + (n & 7) * 16) + (n >= 8 ? to[3] : to[1]);
        auto lds_zero_nb = [&]() {
            if (threadIdx.x < 16) *reinterpret_cast<unsigned*>(smem + L_CNT + threadIdx.x * 4) = 0u;
            *reinterpret_cast<f32x4*>(smem + L_P + threadIdx.x * 16) = z4;
            if (threadIdx.x < 32) *reinterpret_cast<f32x4*>(smem + L_P + 8192 + threadIdx.x * 16) = z4;
        };
        auto lds_zero = [&]() { lds_zero_nb(); BAR(); };
        auto lds_inc = [&](int idx) {
            asm volatile("s_waitcnt lgkmcnt(0)" ::: "memory");
            if (lane == 0) __hip_atomic_fetch_add(reinterpret_cast<unsigned*>(smem + L_CNT) + idx, 1u, __ATOMIC_RELAXED, __HIP_MEMORY_SCOPE_WORKGROUP);
        };
        const unsigned po = WS_PR + (unsigned)((mi & 1) * LB + ((mi >> 1) & 1) * 8192 + (mi >> 1) * 128);
        auto clean = [&]() {
            if (is_mlp) {
#pragma unroll
                for (int i = 0; i < 3; ++i) {
                    const int c = mi * 3 + i;
                    if (c < 64 && (c & 7) < 6) { if (!AB(2)) st16_sc1(ws, WS_XA + (unsigned)(c * 1024 + lane * 16), ones); } else if (!AB(4)) st16_e(ws, WS_XA + (unsigned)(c * 1024 + lane * 16), ones);
                }
                if (lane < 16 && !AB(8)) st16_sc1(ws, (po ^ (unsigned)((lane & 8) << 10)) + (unsigned)((lane & 7) * 16), ones);
            }
        };
        auto first7 = [&]() {
            if (CA == 0) clean();
            if (EPF != 3) kv_stream<0, F7, 0>(kbase, vbase, ko, v0, to, due, step);
            START(SCH(S_O7));
            lds_zero_nb();
            __builtin_amdgcn_s_setprio(3);
            {
                unsigned d;
                asm volatile("s_waitcnt lgkmcnt(0)\n\ts_load_dword %0, %2, %3\n\ts_load_dword %1, %2, %4\n\ts_barrier\n\ts_waitcnt lgkmcnt(0)"
                             : "=&s"(tag), "=&s"(d) : "s"(ws), "s"(WS_EP), "s"(is_mlp ? (po | (2 * GP)) & ~16383u : WS_EP) : "memory");
                tag += 0x01010101u;
            }
            STAMP(24);
        };
        int w7 = wave;
        { if (wave == 7) first7(); asm volatile("" : "+s"(w7)); }
        if (w7 != 7) {
            const unsigned o2 = (unsigned)((wave & 3) * 1280 + lane * 16);
            u32x4 xa_ = ones, xc_ = ones;
            START(is_mlp ? SCH(S_MO) : SCH(S_PO));
            if (FIRSTD) { VMW(3); STAMP(36); VMW(0); STAMP(37); }
            lds_zero();
            if (!is_mlp) {
                xa_ = ld16_a(x0, o2); xc_ = ld16_a(x0, lane < 16 ? o2 + 1024 : o2);
#pragma unroll
                for (int i = 0; i < 7; ++i) {
                    int ii = wave + 7 * i; if (ii > 47) ii = 47;
                    const int c = ii * 64 + lane, row = c / 24, u = c - 24 * row;
                    const int f = ((row >> 1) & 1) | (((row >> 3) & 1) << 1);
                    unsigned o = (unsigned)(((sub * 128 + kpos(row)) * QD + h * DH) * 2 + ((u >> 1) ^ f) * 32 + (u & 1) * 16);
                    asm volatile("" : "+v"(o));
                    if (i < 2) pace<DHI + 2>(due, step); else pace(due, step);
                    dma16(wq, o, lb + L_WQ + ii * 1024);
                }
            }
            pace(due, step);
            kv_stream<4, P_LN, 3>(kbase, vbase, ko, v0, to, due, step);
            if (!is_mlp) {
                asm volatile("s_waitcnt vmcnt(%2)" : "+v"(xa_), "+v"(xc_) : "i"(P_LN - (EPF == 3 ? 4 : 0)) : "memory");
                if (wave >= 3) {
                    float mb, rden; row_stats(xa_, xc_, lane, mb, rden);
                    const f32x2 mb2 = {mb, mb}, rd2 = {rden, rden};
#pragma unroll
                    for (int j = 0; j < 4; ++j) { xa_[j] = pk2v(unpk(pk2v(unpk(xa_[j]) - mb2)) * rd2); xc_[j] = pk2v(unpk(pk2v(unpk(xc_[j]) - mb2)) * rd2); }
                    *reinterpret_cast<u32x4*>(smem + L_OP + (wave & 3) * 1344 + lane * 16) = xa_;
                    if (lane < 16) *reinterpret_cast<u32x4*>(smem + L_OP + (wave & 3) * 1344 + 1024 + lane * 16) = xc_;
                }
            }
            lds_inc(1);
            kv_stream<P_LN, P_QP, 1>(kbase, vbase, ko, v0, to, due, step);
            if (!is_mlp && wave < 6) {
                lds_wait_ge(lb + L_CNT + 4, 7u);
                STAMP(1);
                const unsigned a0 = lb + L_WQ + (unsigned)((8 * kq + q4) * 384 + ((2 * wave) ^ ((q4 >> 1) | ((kq & 1) << 1))) * 32 + p4 * 8), a1 = a0 ^ 32u;
                const unsigned char* xp = smem + L_OP + ((n >> 1) & 3) * 1344 + sub * 256 + kq * 64;
                f32x4 c0 = z4, c1 = z4;
#pragma unroll
                for (int s = 0; s < 4; ++s) {
                    const u32x4 bf = n >= 8 ? (u32x4){0x3f803f80u, 0x3f803f80u, 0x3f803f80u, 0x3f803f80u} : *reinterpret_cast<const u32x4*>(xp + s * 16);
                    c0 = mfma32w(cat8(tr64(a0 + s * 12288), tr64(a0 + s * 12288 + 1536)), bf, c0);
                    c1 = mfma32w(cat8(tr64(a1 + s * 12288), tr64(a1 + s * 12288 + 1536)), bf, c1);
                }
                if (n & 1) c0 = c1;
                if (n < 10) *reinterpret_cast<f32x4*>(smem + L_OP + 6144 + (n >> 1) * 768 + wave * 128 + kq * 32 + (n & 1) * 16) = c0;
                lds_inc(3);
            }
            kv_stream_x<P_QP, 36, true>(kbase, vbase, ko, v0, to, due, step, [&](int k) {
                if (k >= 300) { if ((k == 305) == is_mlp) { step = (step * SCH(S_TS)) >> 8; due += SCH(S_WD) << 4; } } else
                if (k >= 200) STAMPV(56 + (k - 216) / 4, now32() - (due >> 4) + t_start + 100u - ((now32() - (due >> 4) + 0x8000000u) & 0xf0000000u)); });
        } else {
            const unsigned t8 = tag & 0xffu;
            WO_ADDR();
            const u16* wpb = wo + (h * DH + (is_mlp ? sub - 5 : 0) * 64) * DM; const unsigned wpa = (unsigned)((lane < 20 ? lane : 19) * 128);
            const int ml_ = mi - 12 * xg, l3_ = lane / 3, ne_ = (int)SCH(S_N0), r0e_ = 640 - 6 * LPN * (int)SCH(S_ND);
            const unsigned pe_ = (unsigned)((l3_ < ne_ ? l3_ : 0) * 3072 + xg * 384 + (lane - 3 * l3_) * 128);
            auto nar = [&](auto ic) {
                constexpr int k = decltype(ic)::v;
                if constexpr (k >= 100) {
                    constexpr int j = NN7 / 6 + (k - 100) / 4;
                    if (is_mlp) pull(wpb + (((k >> 1) & 1) * 32 + (j & 1) * 16 + (k & 1) * 8 + ((j >> 1) & 1) * 4 + (j >> 2) * 2) * DM, wpa, lb + L_PF);
                } else if (is_mlp) { const int row = (k * 6 + (ml_ >> 1)) * ne_; if (ne_ && row < r0e_) pull(((ml_ & 1) ? wu : wg) + (row < 640 - ne_ ? row : 640 - ne_) * 1536, pe_, lb + L_PF); }
                else wo_piece<k>(wob, woa, lb); };
            auto qstore = [&](int i0 = 0, int i1 = 4) {
                if (!is_mlp) {
                    if (i0 == 0) lds_wait_ge(lb + L_CNT + 12, 6u);
#pragma unroll
                    for (int i = i0; i < i1; ++i) {
                        const int c = i * 64 + lane, ln = c >> 3;
                        if (c < (AB(64) ? (int)SCH(S_TS) : 240) && !AB(16) && !(i == 3 && AB(32))) st16_x(ws, AB(4096) ? 16777216u + (unsigned)(((h * 5 + sub) / (int)SCH(S_TS)) * (int)SCH(S_WD) * 4096 + ((h * 5 + sub) % (int)SCH(S_TS)) * 4096 + c * 16) : WS_H1A + (AB(2048) ? (unsigned)((h * 5 + sub) * (int)SCH(S_TS) * 16 + c * 16) : (unsigned)((h * 30 + ln) * 640 + sub * 128 + (c & 7) * 16)), tag4(*reinterpret_cast<const u32x4*>(smem + L_OP + 6144 + c * 16), t8));
                    }
                }
                STAMP(17);
            };
            if (EPF == 3) pace<D7>(due, step);
            kv_stream7<F7, (CA > F7 ? CA : F7)>(kbase, vbase, ko, v0, v1, v2, to, due, step, nar);
            if (CA) clean();
            kv_stream7<(CA > F7 ? CA : F7), A7>(kbase, vbase, ko, v0, v1, v2, to, due, step, nar);
            if (A7 >= Q7) qstore();
            if (is_mlp) due -= 4 * step;
            kv_stream7<A7, A7 + 4>(kbase, vbase, ko, v0, v1, v2, to, due, step, nar);
            if (is_mlp) {
                unsigned long long c0, c1, c2; unsigned d;
                due += 4 * step;
                VMW(A7 + 4 - CA);
                asm volatile(
                             "s_memtime %0\n\ts_load_dword %3, %4, %5\n\ts_waitcnt lgkmcnt(0)\n\t"
                             "s_memtime %1\n\ts_load_dword %3, %4, %6\n\ts_waitcnt lgkmcnt(0)\n\t"
                             "s_memtime %2\n\ts_waitcnt lgkmcnt(0)"
                             : "=&s"(c0), "=&s"(c1), "=&s"(c2), "=&s"(d) : "s"(ws), "s"(AB(8) ? WS_EP : po), "s"(AB(8) ? WS_EP : po ^ 8192u) : "memory");
                RETRY(48, (unsigned)c1 - (unsigned)c0); RETRY(49, (unsigned)c2 - (unsigned)c1);
                const unsigned arv = (t_start & ~1u) | ((((int)((unsigned)c2 + (unsigned)c0 - 2u * (unsigned)c1) > 0 ? 0u : 1u) ^ (unsigned)((mi >> 1) ^ (xg >> 2))) & 1u);
                if (lane == 0) st8_sc1(ws, WS_AR + (unsigned)((mi >> 2) * 128 + (mi & 3) * 8), (u32x2){tag, arv});
            }
            STAMP(16);
            if (A7 < Q7) { kv_stream7<A7 + 4, Q7>(kbase, vbase, ko, v0, v1, v2, to, due, step, nar); qstore(); }
            kv_stream7<(A7 < Q7 ? Q7 : A7 + 4), L7>(kbase, vbase, ko, v0, v1, v2, to, due, step, nar);
            u32x4 t = ones;
            const unsigned oa = WS_AR + (unsigned)((lane < 48 ? lane >> 1 : 0) * 128 + (lane & 1) * 16);
            if (g == LEADER) t = ld16_sc1(ws, oa);
            kv_stream7<L7, L7 + 4>(kbase, vbase, ko, v0, v1, v2, to, due, step, nar);
            if (g == LEADER) {
                asm volatile("s_waitcnt vmcnt(4)" : "+v"(t) :: "memory");
#pragma unroll 1
                for (unsigned spins = 0; spins < (kSpinCap << 4); ++spins) {
                    if (__builtin_amdgcn_ballot_w64(t[0] != tag || t[2] != tag) == 0ull) break;
                    RETRY(3, spins + 1);
                    __builtin_amdgcn_s_sleep(8);
                    t = ld16_sc1(ws, oa);
                    asm volatile("s_waitcnt vmcnt(0)" : "+v"(t) :: "memory");
                }
                t0 = t_start + (unsigned)wave_imax(imax((int)(t[1] - t_start), (int)(t[3] - t_start)));
                const int v0_ = __builtin_popcountll(__builtin_amdgcn_ballot_w64((t[1] & 1u) != 0u) & 0xffffffffffffull), v1_ = __builtin_popcountll(__builtin_amdgcn_ballot_w64((t[3] & 1u) != 0u) & 0xffffffffffffull);
                RETRY(50, v0_ + 100); RETRY(51, v1_ + 100);
                t0 = (t0 & ~7u) | (v0_ > 24 ? 2u : 0u) | (v1_ > 24 ? 4u : 0u);
                if (SCH(S_FL) & 256) { RETRY(5, (t0 ^ (SCH(S_FL) >> 8)) & 2u); RETRY(6, (t0 ^ (SCH(S_FL) >> 8)) & 4u); t0 = (t0 & ~7u) | ((SCH(S_FL) >> 8) & 6u); }
                if (lane < 8) st8_sc1(ws, WS_GO + (unsigned)(lane * 128), (u32x2){tag, t0});
                STAMP(29);
            }
#define LDQ5() do { a[0] = ld16_xo<0>(ws, o); a[1] = ld16_xo<128>(ws, o); a[2] = ld16_xo<256>(ws, o); a[3] = ld16_xo<384>(ws, o); a[4] = ld16_xo<512>(ws, o); } while (0)
#define WTQ5(n) asm volatile("s_waitcnt vmcnt(%5)" : "+v"(a[0]), "+v"(a[1]), "+v"(a[2]), "+v"(a[3]), "+v"(a[4]) : "i"(n) : "memory")
#define GATH5(row, lo, hi) \
                const unsigned o = WS_H1A + (AB(128) ? 655360u : 0u) + (unsigned)((h * 30 + (row) * 6 + (lane < 48 ? lane >> 3 : 0)) * 640 + (lane & 7) * 16); \
                u32x4 a[5]; \
                LDQ5(); \
                kv_stream7<lo, hi>(kbase, vbase, ko, v0, v1, v2, to, due, step, nar); \
                WTQ5((hi) - (lo)); \
                _Pragma("unroll 1") for (unsigned spins = 0; spins < (kSpinCap << 4); ++spins) { \
                    _Pragma("unroll") for (int i = 0; i < 5; ++i) a[i] ^= t8; \
                    if (tagok(or4(a[0] | a[1] | a[2] | a[3] | a[4])) || AB(16 | 32 | 64 | 128 | 4096)) break; \
                    RETRY(1, spins + 1); \
                    __builtin_amdgcn_s_sleep(8); \
                    LDQ5(); \
                    WTQ5(0); \
                } \
                const f32x4 q = ((asf(a[0]) + asf(a[1])) + (asf(a[2]) + asf(a[3]))) + asf(a[4])
            kv_stream7<L7 + 4, QG>(kbase, vbase, ko, v0, v1, v2, to, due, step, nar);
            {
                u32x2 r = ld8_sc1o<0>(ws, WS_GO + (unsigned)(xg * 128));
                GATH5(b, QG, QG + QW);
                if (lane < 48) {
                    unsigned* qp = reinterpret_cast<unsigned*>(smem + L_Q + ((lane >> 3) & 1) * 192 + (lane >> 4) * 64 + (lane & 1) * 32 + ((lane >> 1) & 3) * 4);
                    qp[0] = pk2(q[0], q[1]); qp[4] = pk2(q[2], q[3]);
                }
                STAMP(30);
                asm volatile("" : "+v"(r));
#pragma unroll 1
                for (unsigned spins = 0; spins < (kSpinCap << 4); ++spins) {
                    t0 = __builtin_amdgcn_readfirstlane(r[1]);
                    if (__builtin_amdgcn_readfirstlane(r[0]) == tag) break;
                    RETRY(2, spins + 1);
                    __builtin_amdgcn_s_sleep(8);
                    r = ld8_sc1o<0>(ws, WS_GO + (unsigned)(xg * 128));
                    asm volatile("s_waitcnt vmcnt(0)" : "+v"(r) :: "memory");
                }
            }
            pol = t0;
            asm volatile("ds_write2_b32 %0, %1, %2 offset1:3\n\tds_write2_b32 %0, %3, %2 offset0:1 offset1:5" :: "v"(lb + L_POL), "v"(pol), "v"(0u), "v"(tag) : "memory");
            if (t0 - t_start + 8u > 500u || (SCH(S_FL) & 4096)) t0 = t_start;
            asm volatile("ds_write_b32 %0, %1" :: "v"(lb + L_CNT + 36), "v"(t0 | 1u) : "memory");
            lds_inc(0);
            STAMP(2);
            if (!AB(512)) {
                GATH5(4, QG + QW, QG + 2 * QW);
                if (lane < 48) *reinterpret_cast<f32x4*>(smem + L_CS + (lane >> 3) * 128 + (lane & 1) * 64 + ((lane >> 1) & 3) * 16) = q;
            } else kv_stream7<QG + QW, QG + 2 * QW>(kbase, vbase, ko, v0, v1, v2, to, due, step, nar);
            kv_stream7<QG + 2 * QW, 48>(kbase, vbase, ko, v0, v1, v2, to, due, step, nar);
            if (AB(1024)) {
                unsigned long long pc; unsigned len;
                asm volatile("s_getpc_b64 %0\nmega_pf_two:\n\ts_mov_b32 %1, mega_pf_end - mega_pf_two - 4" : "=s"(pc), "=s"(len));
                const unsigned o = (unsigned)((lane * 16 + (rr & 15)) * 128);
                dma4((const void*)pc, o < len ? o : len, lb + L_PF);
            }
            if (!is_mlp) {
                dma16p(wr, (unsigned)(lane * 16), lb + L_WR); dma4(wr, (unsigned)(1024 + lane * 4), lb + L_WR + 1024);
                dma4(x0, (unsigned)((((lane >> 1) & 3) * DM + oi * 4) * 2 + (lane & 1) * 4), lb + L_WR + 1280);
                dma4n(out, (unsigned)(xg * 128 + (lane & 1) * 4096), lb + L_PF);
            }
            VMW(16);
            fix_all<0>();
            VMW(0);
            STAMP(19);
            __builtin_amdgcn_s_setprio(0);
        }
    }

#define IDS() int g = GID; asm volatile("" : "+s"(g)); int wave = wave0; asm volatile("" : "+s"(wave)); \
        const int xg = g & 7, rr = g >> 3, h = xg * 4 + (rr >> 3), sub = rr & 7, b = sub >> 1; \
        const bool is_mlp = sub >= 5; const int mi = h * 3 + sub - 5, oi = OWNER(h, sub); (void)mi; (void)oi; (void)b
#define ATT_ADDR(layer) \
        unsigned qa = lb + (unsigned)(L_Q + kq * 32 + (lane & 1) * 16), ix = (unsigned)((p4 & 1) * 0xaaaa + p4 * 0x55550000) ^ 0x44444444u; \
        unsigned pw = lb + L_P + (unsigned)(wave * 1088 + ((kq & 1) * 2 + (p4 >> 1)) * 272 + (p4 & 1) * 128 + (q4 >> 1) * 64 + (kq >> 1) * 32 + (q4 & 1) * 8); \
        unsigned pr = lb + L_P + (unsigned)(wave * 1088 + (lane >> 2) * 16); \
        unsigned ow = lb + L_OP + (unsigned)(wave * OPS + lane * 16); \
        unsigned mlw = lb + L_ML + (unsigned)(wave * 4), mo = lb + L_ML + (unsigned)((lane >> 3) * 4), ma = lb + L_ML; \
        unsigned r0 = lb + L_OP + (unsigned)((lane >> 3) * OPS + (wave < 4 ? (wave >> 1) * 128 : 2048) + (wave & 1) * 256 + (lane & 7) * 16), r1 = r0 + (wave < 4 ? 1536u : 640u); \
        unsigned so = (a_h2(h) ^ (layer ? CX(1) : 0u)) + (unsigned)(layer * LB + (wave < 6 ? ((wave >> 1) * 8 + (lane >> 5) * 4 + (wave & 1) * 2 + (sub & 1)) * 128 + b * 32 + (lane & 7) * 4 \
                                                                                  : 3072 + (((sub & 1) ^ (lane & 1)) * 4 + b) * 128 + (lane & 1) * 8)); \
        (void)mlw; (void)ma; (void)r1

    if (PADN) asm volatile("s_branch 1f\n\t.fill %0, 4, 0xbf800000\n1:" :: "i"(PADN) : "memory");
    {
        IDS(); LANES();
        ATT_ADDR(0);
        asm volatile("" : "+v"(qa), "+v"(pw), "+v"(pr), "+v"(ow), "+v"(mlw), "+v"(mo), "+v"(ma), "+v"(r0), "+v"(r1), "+v"(so), "+v"(ix));
        const u16* vbase = vc + ((size_t)(b * HQ + h) * SK + (SK - WIN) + (sub & 1) * 512) * DH;
        unsigned v1 = (unsigned)(4 * kq * 384 + 256 + (n & 7) * 16), v2 = v1 + (n >= 8 ? to[3] : to[1]);
        v1 += n >= 8 ? to[2] : to[0];
        auto tail1 = [&]() { ldkv<224, 0>(vbase, v1); pace(due, step); ldkv<228, 384>(vbase, v1); pace(due, step); ldkv<232, 768>(vbase, v1); pace(due, step); ldkv<236, 1152>(vbase, v1); };
        unsigned dt = 0u;
        auto tail2 = [&]() { if (EHW == 3) dt = pace_t(due, step); else pace(due, step); ldkv<240, 0>(vbase, v2); };
        auto tail3 = [&]() {
            if (EHW == 2) dt = pace_t(due, step); else pace(due, step);
            ldkv<244, 384>(vbase, v2);
            if (EHW == 1) dt = pace_t(due, step); else pace(due, step);
            ldkv<248, 768>(vbase, v2); dt = pace_t(due, step) - dt; ldkv<252, 1152>(vbase, v2);
            asm volatile("s_mov_b64 exec, 1\n\tds_add_u32 %0, %1 offset:336\n\ts_mov_b64 exec, -1" :: "v"(ma), "v"(1u) : "memory");
            STAMP(3);
        };
        if (wave != 7) {
            const unsigned t = lds_ld(lb + L_CNT + 36);
            if (t) due += (t - t_start) << 4;
            step = (step * SCH(S_X3)) >> 8;
            if (wave == 6 && !is_mlp) step -= SCH(S_X6);
        }
        const bool early = wave == 7 || lds_ld(lb + L_CNT) != 0u;
        if (!early) {
            const unsigned v0 = (unsigned)(4 * kq * 384 + n * 16) + to[3];
            ldkv<208, 0>(vbase, v0); pace(due, step); ldkv<212, 384>(vbase, v0); pace(due, step); ldkv<216, 768>(vbase, v0); pace(due, step); ldkv<220, 1152>(vbase, v0); pace(due, step);
            tail1(); tail2(); tail3();
            lds_wait_ge(lb + L_CNT, 1u);
        }
        pol = lds_ld(lb + L_POL); tag = lds_ld(lb + L_POL + 4);
        so ^= CX(0);
        STAMP(4);
        PAGE(0);
        float sa;
        asm volatile(ASM_QK : "=&v"(sa) : "v"(qa), "v"(ix) : "memory", CLOB_32_59);
        const float s = bfr(bfr(sa) * SCALE) * L2E;
        float mt, p; int ms;
        asm volatile("s_nop 1\n\tv_max_f32_dpp %0, %3, %3 quad_perm:[1,0,3,2] row_mask:0xf bank_mask:0xf\n\ts_nop 1\n\t"
                     "v_max_f32_dpp %0, %0, %0 quad_perm:[2,3,0,1] row_mask:0xf bank_mask:0xf\n\ts_nop 1\n\t"
                     "v_max_f32_dpp %0, %0, %0 row_half_mirror row_mask:0xf bank_mask:0xf\n\ts_nop 1\n\t"
                     "v_max_f32_dpp %0, %0, %0 row_mirror row_mask:0xf bank_mask:0xf\n\ts_nop 1\n\t"
                     "v_max_f32_dpp %0, %0, %0 row_bcast:15 row_mask:0xa bank_mask:0xf\n\ts_nop 1\n\t"
                     "v_max_f32_dpp %0, %0, %0 row_bcast:31 row_mask:0xc bank_mask:0xf\n\ts_nop 0\n\t"
                     "v_readlane_b32 %2, %0, 63\n\ts_nop 1\n\t"
                     "v_subrev_f32 %1, %2, %3\n\tv_exp_f32 %1, %1\n\ts_nop 0"
                     : "=&v"(mt), "=&v"(p), "=&s"(ms) : "v"(s));
        u32x4 bm[4];
        {
            unsigned pb;
            asm volatile("v_cvt_pk_bf16_f32 %5, %4, %4\n\tds_write_b16 %6, %5\n\tds_write_b16 %6, %5 offset:20\n\t"
                     "ds_read_b128 %0, %7\n\tds_read_b128 %1, %7 offset:272\n\tds_read_b128 %2, %7 offset:544\n\tds_read_b128 %3, %7 offset:816\n\t"
                     "v_add_f32_dpp %4, %4, %4 quad_perm:[1,0,3,2] row_mask:0xf bank_mask:0xf\n\ts_nop 1\n\t"
                     "v_add_f32_dpp %4, %4, %4 quad_perm:[2,3,0,1] row_mask:0xf bank_mask:0xf\n\ts_nop 1\n\t"
                     "v_add_f32_dpp %4, %4, %4 row_half_mirror row_mask:0xf bank_mask:0xf\n\ts_nop 1\n\t"
                     "v_add_f32_dpp %4, %4, %4 row_mirror row_mask:0xf bank_mask:0xf\n\ts_nop 1\n\t"
                     "v_add_f32_dpp %4, %4, %4 row_bcast:15 row_mask:0xa bank_mask:0xf\n\ts_nop 1\n\t"
                     "v_add_f32_dpp %4, %4, %4 row_bcast:31 row_mask:0xc bank_mask:0xf\n\t"
                     "s_mov_b32 exec_lo, 0\n\ts_brev_b32 exec_hi, 1\n\tds_write2_b32 %9, %8, %4 offset1:8\n\ts_mov_b64 exec, -1\n\ts_waitcnt lgkmcnt(0)"
                     : "=&v"(bm[0]), "=&v"(bm[1]), "=&v"(bm[2]), "=&v"(bm[3]), "+v"(p), "=&v"(pb)
                     : "v"(pw), "v"(pr), "v"(mt), "v"(mlw) : "memory");
        }
#define PVP(name) asm volatile(name : "+v"(da) : "v"(bm[0]), "v"(bm[1]), "v"(bm[2]), "v"(bm[3]), "v"(ow), "v"(ix) : "memory")
#define PVB(name) do { f32x4 da = z4; PVP(name); } while (0)
        int w7 = wave;
        if (wave == 7) {
            PVB(ASM_PVB0); PVB(ASM_PVB1); PVB(ASM_PVB2);
            asm volatile("s_waitcnt lgkmcnt(0)\n\ts_mov_b64 exec, 1\n\tds_add_u32 %0, %1 offset:372\n\tds_add_u32 %0, %1 offset:348\n\ts_mov_b64 exec, -1" :: "v"(ma), "v"(1u) : "memory");
        }
        asm volatile("" : "+s"(w7));
        if (w7 != 7) {
            LANES();
            {
                const unsigned v0 = (unsigned)(4 * kq * 384 + n * 16) + to[3];
                if (early) { ldkv<208, 0>(vbase, v0); pace(due, step); ldkv<212, 384>(vbase, v0); VMW(6); }
                if (early) { pace(due, step); ldkv<216, 768>(vbase, v0); }
                PVB(ASM_PVB0);
                if (early) { pace(due, step); ldkv<220, 1152>(vbase, v0); pace(due, step); tail1(); }
            }
            VMW(4);
            PVB(ASM_PVB1);
#define MEET(off) do { unsigned t_; int c_; \
            asm volatile("s_waitcnt lgkmcnt(0)\n\ts_mov_b64 exec, 1\n\tds_add_u32 %2, %3 offset:" #off "\n\ts_mov_b64 exec, -1\n\ts_mov_b32 %1, 0x400000\n1:\n\tds_read_b32 %0, %2 offset:" #off "\n\ts_sub_u32 %1, %1, 1\n\ts_cbranch_scc1 2f\n\t" \
                         "s_waitcnt lgkmcnt(0)\n\tv_cmp_gt_u32 vcc, 8, %0\n\ts_cbranch_vccnz 1b\n2:" : "=&v"(t_), "=&s"(c_) : "v"(ma), "v"(1u) : "memory", "scc", "vcc"); } while (0)
            asm volatile("s_waitcnt lgkmcnt(0)\n\ts_mov_b64 exec, 1\n\tds_add_u32 %0, %1 offset:372\n\ts_mov_b64 exec, -1" :: "v"(ma), "v"(1u) : "memory");
            f32x4 da = z4; int la;
            {
                unsigned hfa = (a_hf(h) ^ CX(0)) + (unsigned)(lane * 128 + ((sub - lane) & 7) * 4), hb = lds_ld(lb + L_CNT + 36);
                const unsigned tw_ = SCH(S_TW);
                asm volatile("" : "+v"(da), "+v"(hfa), "+v"(hb));
                if (early) { tail2(); tail3(); } else dt = 0u;
                unsigned ar = 1u, mx, dv = ((dt * tw_) >> 8) - hb; int th;
                asm volatile("s_waitcnt vmcnt(%4)\n\ts_memrealtime vcc\n\ts_mov_b64 exec, 1\n\ts_waitcnt lgkmcnt(0)\n\tv_add_u32 %1, vcc_lo, %3\n\t"
                             "ds_max_u32 %2, %1 offset:%5\n\tds_add_rtn_u32 %0, %2, %0 offset:376\n\tds_read_b32 %1, %2 offset:%5\n\ts_mov_b64 exec, -1"
                             : "+v"(ar), "=&v"(mx) : "v"(ma), "v"(dv), "i"(EHW), "i"(L_POL + 20 - L_ML) : "memory", "scc", "vcc");
                PVP(ASM_PV20); PVP(ASM_PV21); if (EHW <= 2) PVP(ASM_PV22);
                asm volatile("s_waitcnt lgkmcnt(0)\n\tv_add_u32 %3, %3, %4\n\tv_readfirstlane_b32 %0, %2\n\tv_readfirstlane_b32 %1, %3\n\ts_cmp_lg_u32 %0, 7\n\ts_cbranch_scc1 2f\n\t"
                             "s_mov_b64 exec, 31\n\tv_and_b32 %3, -2, %1\n\tglobal_store_dword %5, %3, %7 sc1\n\tv_or_b32 %2, 1, %1\n\tds_write_b32 %6, %2 offset:%8\n\ts_mov_b64 exec, -1\n2:"
                             : "=&s"(la), "=&s"(th), "+v"(ar), "+v"(mx) : "v"(hb), "v"(hfa), "v"(ma), "s"(ws), "i"(L_POL + 12 - L_ML) : "memory", "scc");
            }
            lds_wait_ge(lb + L_CNT + 52, 8u);
            const f32x4 m0 = *(const LAS f32x4*)(size_t)ma, m1 = *(const LAS f32x4*)(size_t)(ma + 16);
            const float M = vmax3(vmax3(m0[0], m0[1], m0[2]), vmax3(m0[3], m1[0], m1[1]), vmax3(m1[2], m1[3], m1[3]));
            float fj = __builtin_amdgcn_exp2f(*(const LAS float*)(size_t)mo - M);
            auto combine = [&]() {
                const f32x4 a = *(const LAS f32x4*)(size_t)r0, c = *(const LAS f32x4*)(size_t)r1;
                const float z = red3((a + c) * fj);
                auto sw = __builtin_amdgcn_permlane16_swap(__float_as_uint(z), __float_as_uint(z), false, false);
                return enc(pk2(__uint_as_float(sw[0]), __uint_as_float(sw[1])));
            };
            if (wave < 4) st4_m<0x000000ffu, 0x000000ffu>(ws, so, combine());
            else if (wave == 6) {
                float L = *(const LAS float*)(size_t)(mo + 32) * fj;
                L += dpp<0x128>(L); L = sw16_add(L, L); L = sw32_add(L, L);
                asm volatile("s_mov_b64 exec, 3\n\tglobal_store_dwordx2 %0, %1, %2 sc1\n\ts_mov_b64 exec, -1" :: "v"(so), "v"((u32x2){enc(__float_as_uint(M)), enc(__float_as_uint(L))}), "s"(ws) : "memory");
            } else la += 8;
            const unsigned h2 = a_h2(h) ^ CX(0); const int ot = (8 * sub + 2 * wave + (q4 & 1)) * 4 + kq, rw = (q4 & 1) ? 3 - p4 : p4;
            unsigned j0 = lb + L_WO + (unsigned)((8 * kq + q4) * 256 + ((2 * wave) ^ (q4 | ((kq & 1) << 2))) * 32 + p4 * 8), j1 = j0 ^ 32u;
            unsigned j2 = h2 + (unsigned)((lane >> 3) * 128 + rw * 32 + (q4 & 1) * 16), j3 = h2 + (unsigned)(3072 + ((n >> 3) * 4 + rw) * 128);
            unsigned j4 = (a_own(ot) + (unsigned)(6144 + h * 64 + rw * 16)) ^ ((unsigned)((ot & 15) << 7) | CX(0));
            if (OPA) asm volatile("" : "+v"(fj), "+{v0}"(j0), "+{v1}"(j1), "+{v2}"(j2), "+{v3}"(j3), "+{v4}"(j4)); else asm volatile("" : "+v"(fj));
            STAMP(6);
            asm volatile("s_waitcnt vmcnt(2)\n\ts_cmp_eq_u32 %0, 7\n\ts_cbranch_scc1 1f\n\ts_waitcnt vmcnt(1)\n\ts_sub_u32 %0, %0, 8\n\ts_cmp_gt_u32 %0, 6\n\ts_cbranch_scc1 1f\n\ts_waitcnt vmcnt(0)\n1:" : "+s"(la) :: "memory", "scc");
            if (EHW > 2) PVP(ASM_PV22);
            STAMP(20); DMARK(46);
            asm volatile(ASM_PV23 "s_nop 7\n\tds_write_b128 %5, %0 offset:2048" : "+v"(da) : "v"(bm[0]), "v"(bm[1]), "v"(bm[2]), "v"(bm[3]), "v"(ow), "v"(ix) : "memory");
            if ((wave & 6) == 4) { MEET(348); st4_m<0x000000ffu, 0x000000ffu>(ws, so, combine()); STAMP(5); asm volatile("s_mov_b64 exec, 1\n\tds_add_u32 %0, %1 offset:344\n\ts_mov_b64 exec, -1" :: "v"(ma), "v"(1u) : "memory"); }
            else asm volatile("s_mov_b64 exec, 1\n\tds_add_u32 %0, %1 offset:348\n\ts_mov_b64 exec, -1" :: "v"(ma), "v"(1u) : "memory");
            if (is_mlp && wave < 4) {
                LANES();
                NARROW_ADDR();
                unsigned va = nva + (unsigned)(wave * 160 * 3072), vb = nvb + (unsigned)(wave * 160 * 3072), lw = lb + (unsigned)(L_GU + wave * 5120);
                asm volatile("" : "+v"(va), "+v"(vb), "+s"(lw));
                M0_KEEP();
                lds_wait_ge(lb + L_CNT + 40, 1u);
                lds_wait_ge(lb + L_CNT + 24, 2u);
                if (SCH(S_LT)) wait_until(now32() + SCH(S_LT));
                STAMP(18);
#pragma unroll
                for (int c = 0; c < 5; ++c) {
                    unsigned t;
                    asm volatile("s_add_u32 m0, %4, %5\n\tv_add_u32 %0, %6, %1\n\tglobal_load_lds_dwordx4 %0, %2" WPOL "\n\ts_add_u32 m0, %4, %7\n\ts_nop 0\n\tglobal_load_lds_dwordx4 %0, %3" WPOL
                                 : "=&v"(t) : "v"(c == 4 ? vb : va), "s"(wg), "s"(wu), "s"(lw), "i"(c * 1024), "i"((c & 3) * 8 * 3072), "i"(c * 1024 + 20480) : "memory", "scc");
                }
                M0_BACK();
                if (P1ONLY) { VMW(0); STAMP(27); }
            }
            if (!P1ONLY && !is_mlp && wave < 4) {
                int st, rf, ct; unsigned t0;
                asm volatile(ASM_OP : [st] "=&s"(st), [rf] "=&s"(rf), [ct] "=&s"(ct), [t0] "=&{v7}"(t0) : [a0] "{v0}"(j0), [a1] "{v1}"(j1), [gs] "{v2}"(j2), [gm] "{v3}"(j3), [so] "{v4}"(j4),
                             [ws] "s"(ws), [fl] "s"(lb + L_CNT + 48), [tag] "s"(tag), [dbg] "s"(lb + L_DBG + wave * 512 + 256 + 7 * 4) : "memory", "scc", "vcc", CLOB_8_15, CLOB_16_63 OPCL);
                STAMP(7); DMARK(30);
            }
        }
    }
    {
        IDS();
        if (wave == 7) {
            PAGE(1);
            STAMP(15);
            LANES();
            {
                __builtin_amdgcn_s_setprio(3);
                unsigned th = 0u;
                auto hint = [&]() { lds_wait_ge(lb + L_POL + 12, 1u); th = lds_ld(lb + L_POL + 12); };
                unsigned hfa = (a_hf(h) ^ CX(0)) + (unsigned)(lane * 128 + ((sub - lane) & 7) * 4), hb = lds_ld(lb + L_CNT + 36), ma = lb + L_ML;
                asm volatile("" : "+v"(hfa), "+v"(hb), "+v"(ma));
#define ARRIVE(off, W) do { \
                    unsigned ar = 1u, mx, dv = (off) - hb; int la, th_; \
                    asm volatile("s_memrealtime vcc\n\ts_mov_b64 exec, 1\n\ts_waitcnt lgkmcnt(0)\n\tv_add_u32 %1, vcc_lo, %3\n\t" \
                                 "ds_max_u32 %2, %1 offset:%4\n\tds_add_rtn_u32 %0, %2, %0 offset:376\n\tds_read_b32 %1, %2 offset:%4\n\ts_mov_b64 exec, -1" \
                                 : "+v"(ar), "=&v"(mx) : "v"(ma), "v"(dv), "i"(L_POL + 20 - L_ML) : "memory", "scc", "vcc"); \
                    asm volatile("s_waitcnt lgkmcnt(0)\n\tv_add_u32 %3, %3, %4\n\tv_readfirstlane_b32 %0, %2\n\tv_readfirstlane_b32 %1, %3\n\ts_cmp_eq_u32 %0, 7\n\ts_cbranch_scc1 1f\n\ts_waitcnt vmcnt(%9)\n\ts_branch 2f\n" \
                                 "1:\n\ts_mov_b64 exec, 31\n\tv_and_b32 %3, -2, %1\n\tglobal_store_dword %5, %3, %7 sc1\n\tv_or_b32 %2, 1, %1\n\tds_write_b32 %6, %2 offset:%8\n\ts_mov_b64 exec, -1\n\ts_waitcnt vmcnt(%10)\n2:" \
                                 : "=&s"(la), "=&s"(th_), "+v"(ar), "+v"(mx) : "v"(hb), "v"(hfa), "v"(ma), "s"(ws), "i"(L_POL + 12 - L_ML), "i"(W), "i"((W) | 1) : "memory", "scc"); \
                    if (STAMPS && __builtin_amdgcn_readfirstlane(la) == 7) { STAMPV(21, th_); RETRY(20, 1); } \
                } while (0)
                if (!is_mlp) {
                    WO_ADDR();
                    unsigned oa = woa, ob = woa ^ 128u;
                    asm volatile("" : "+v"(oa), "+v"(ob), "+s"(wob));
                    if (SCH(S_FL) & 128) {
                        const u32x4 ones = splat<~0u>();
#pragma unroll
                        for (int i = 0; i < 6; ++i) st16_l(ws, (a_own(oi) ^ CX(0)) + (unsigned)(i * 1024 + lane * 16), ones);
                        if (oi < 96 && lane < 32) st16_l(ws, WS_XB + (unsigned)((oi / 12) * 8192 + (oi % 12) * 512 + lane * 16), ones);
                    }
                    M0_KEEP();
                    if (WPRE) {
                        unsigned dw = (t0 + SCH(S_WD)) << 4;
                        wo_paced<NN7, NN7 + WPRE>(wob, oa, ob, lb, dw, SCH(S_TR));
                    }
                    if (NN7 + WPRE < 48) { if (SCH(S_WT) > 7u) lds_wait_ge(lb + L_CNT + 56, SCH(S_WT) - 7u); else lds_wait_ge(lb + L_CNT + 16, SCH(S_WT)); }
                    STAMP(35);
                    if (WK < 0) { wo_rest<NN7 + WPRE, NN7 + WPRE - WK>(wob, oa, ob, lb); ARRIVE(SCH(S_G3) - 100u, 63); }
                    wo_rest<NN7 + WPRE - (WK < 0 ? WK : 0), 48>(wob, oa, ob, lb);
                    M0_BACK();
                    STAMP(36);
                    if (WK < 0) VMW(0); else { VMW(WK < 0 ? 0 : WK); ARRIVE(SCH(S_G3) - 100u, 0); }
                    STAMP(33); DMARK(44);
                    asm volatile("ds_write_b32 %0, %1" :: "v"(lb + L_CNT + 40), "v"(1u) : "memory");
                } else {
                    ARRIVE(0u, 63);
                    const int ml = mi - 12 * xg, nl = (int)SCH(S_ND), l3 = lane / 3;
                    unsigned pa = (unsigned)((l3 < nl ? l3 : 0) * 3072 + xg * 384 + (lane - 3 * l3) * 128);
                    const u16* pb = ((ml & 1) ? wu : wg) + (640 - (6 - (ml >> 1)) * LPN * nl) * 1536;
                    auto late = [&]() {
                        unsigned t = now32();
                        if (nl) {
#pragma unroll
                            for (int k = 0; k < LPN; ++k) { pull(pb + k * nl * 1536, pa, lb + L_PF); if (SCH(S_PA) && k < LPN - 1) { t += SCH(S_PA); wait_until(t); } }
                        }
                    };
                    if (SCH(S_PA)) late();
                    unsigned da[21]; const u16* wdb = wd + mi * 16 * DM;
                    {
                        int cc = lane, ro = 0;
#pragma unroll
                        for (int ii = 0; ii < 21; ++ii) {
                            da[ii] = (unsigned)((ii == 20 && ro > 15 * 1280 ? 15 * 1280 : ro) + (cc > 79 ? 79 : cc) * 16);
                            asm volatile("" : "+v"(da[ii]));
                            cc += 64; if (cc >= 82) { cc -= 82; ro += 1280; }
                        }
                    }
                    auto down = [&]() {
                        M0_KEEP();
#pragma unroll
                        for (int ii = 0; ii < 21; ++ii)
                            asm volatile("s_add_u32 m0, %2, %3\n\ts_nop 0\n\tglobal_load_lds_dwordx4 %0, %1" WPOL :: "v"(da[ii]), "s"(wdb), "s"(lb), "i"(L_WD + ii * 1024) : "memory", "scc");
                        M0_BACK();
                    };
                    asm volatile("" : "+v"(pa), "+s"(wdb));
                    if (SCH(S_N1) > 15u) { lds_wait_ge(lb + L_CNT + 24, 2u); if (SCH(S_NT)) wait_until(now32() + SCH(S_NT)); }
                    else if (SCH(S_N1) > 14u) { hint(); wait_until(th + SCH(S_NT)); }
                    else if (SCH(S_N1) > 7u) lds_wait_ge(lb + L_CNT + 56, SCH(S_N1) - 6u); else lds_wait_ge(lb + L_CNT + 16, SCH(S_N1));
                    if (!SCH(S_PA)) late();
                    if (SCH(S_X7) & 2) {
                        VMW(0);
                        asm volatile("ds_write_b32 %0, %1" :: "v"(lb + L_CNT + 40), "v"(1u) : "memory");
                        lds_wait_ge(lb + L_CNT + 24, 2u);
                        wait_until(now32() + SCH(S_LT));
                        down();
                        VMW(0);
                    } else {
                        down();
                        VMW(DNW);
                        asm volatile("ds_write_b32 %0, %1" :: "v"(lb + L_CNT + 40), "v"(1u) : "memory");
                        VMW(0);
                    }
                    STAMP(34);
                    asm volatile("ds_write_b32 %0, %1" :: "v"(lb + L_CNT + 48), "v"(1u) : "memory");
                }
                __builtin_amdgcn_s_setprio(0);
            }
        }
        if (wave == 6) {
            PAGE(2);
            LANES();
            if (P1ONLY) {} else {
            if (sub < 5) {
                if (SCH(S_G1)) { lds_wait_ge(lb + L_POL + 12, 1u); wait_until(lds_ld(lb + L_POL + 12) + SCH(S_G1)); }
                int d = 0; int left;
                const unsigned o = (a_hf(h) ^ CX(0)) + (unsigned)(sub * 128);
                const int tk = sub * 4 + ((lane >> 3) & 3);
                unsigned ta = (a_ts(lane & 7) ^ CX(0)) + (unsigned)((lane < 32 ? tk + (tk / 5) * 3 : (5 + tk + (tk / 3) * 5) & 31) * 128 + h * 4), fa = lb + L_CNT + 48, t;
                asm volatile("" : "+v"(ta), "+v"(fa));
                {
                    u32x8 e; unsigned x;
                    asm volatile("s_movk_i32 %1, 4000\n1:\n\ts_load_dwordx8 s[4:11], %4, %5 glc\n\tds_read_b32 %3, %6 offset:%7\n\ts_memrealtime vcc\n\ts_sub_u32 %1, %1, 1\n\ts_waitcnt lgkmcnt(0)\n\ts_cbranch_scc1 2f\n\t"
                                 "v_readfirstlane_b32 s4, %3\n\ts_or_b32 vcc_hi, s5, s6\n\ts_or_b32 %2, s7, s8\n\ts_or_b32 vcc_hi, vcc_hi, s9\n\ts_or_b32 %2, %2, s10\n\ts_or_b32 vcc_hi, vcc_hi, s11\n\ts_orn2_b32 %2, %2, s4\n\t"
                                 "s_or_b32 vcc_hi, vcc_hi, %2\n\ts_bitcmp1_b32 vcc_hi, 0\n\ts_cbranch_scc1 1b\n2:\n\ts_mov_b32 %2, vcc_lo"
                                 : "=&{s[4:11]}"(e), "=&s"(left), "=&s"(t), "=&v"(x) : "s"(ws), "s"(o), "v"(fa), "i"(L_POL + 12 - L_CNT - 48) : "memory", "scc", "vcc");
                    asm volatile("s_sub_i32 s4, s4, %2\n\ts_sub_i32 s5, s5, %2\n\ts_sub_i32 s6, s6, %2\n\ts_sub_i32 s7, s7, %2\n\ts_max_i32 s4, s4, s5\n\ts_sub_i32 s8, s8, %2\n\ts_max_i32 s6, s6, s7\n\ts_sub_i32 s9, s9, %2\n\t"
                                 "s_max_i32 s4, s4, s6\n\ts_sub_i32 s10, s10, %2\n\ts_max_i32 s8, s8, s9\n\ts_sub_i32 s11, s11, %2\n\ts_max_i32 s4, s4, s8\n\ts_max_i32 s10, s10, s11\n\ts_max_i32 %0, s4, s10"
                                 : "=s"(d), "+{s[4:11]}"(e) : "s"(t) : "scc");
                }
                if (__builtin_expect(left < 0, 0)) {
#pragma unroll 1
                    for (unsigned spins = 0; spins < kSpinCap; ++spins) {
                        if (allok(poll4(ws, o + (unsigned)((lane & 7) * 4)))) break;
                    }
                    t = now32(); d = 0;
                    RETRY(21, 1);
                }
                d += (int)SCH(S_H20);
                if (TSX && d < (int)SCH(S_G2) - TSX) d = (int)SCH(S_G2) - TSX;
                DMARK(45);
                const unsigned tg = (t + (unsigned)(d > (int)SCH(S_G2) && d < 1000 ? d : (int)SCH(S_G2))) | 1u;
                asm volatile("ds_write_b32 %0, %1" :: "v"(fa), "v"(tg) : "memory");
                STAMP(25);
                if (SCH(S_X7) & 4) wait_until(tg + SCH(S_TS));
                st4_m<~0u, 0u>(ws, ta, enc(t + (unsigned)d));
                if (sub < 3) { wait_until(tg + 4u); st4_m<0u, ~0u>(ws, ta, enc(t + (unsigned)d)); }
                STAMPV(28, tg);
            } else lds_wait_ge(lb + L_CNT + 28, 8u);
            unsigned t; int d;
            {
                const unsigned o = (a_ts(xg) ^ CX(0)) + (unsigned)(rr * 128);
                unsigned r, lo; int left;
                asm volatile("s_movk_i32 %2, 4000\n1:\n\ts_load_dwordx16 s[68:83], %4, %5 glc\n\ts_load_dwordx16 s[84:99], %4, %6 glc\n\ts_memrealtime vcc\n\ts_sub_u32 %2, %2, 1\n\ts_waitcnt lgkmcnt(0)\n\ts_cbranch_scc1 2f\n\t"
                             "s_max_u32 s68, s68, s84\n\ts_max_u32 s69, s69, s85\n\ts_max_u32 s70, s70, s86\n\ts_max_u32 s71, s71, s87\n\ts_max_u32 s72, s72, s88\n\ts_max_u32 s73, s73, s89\n\ts_max_u32 s74, s74, s90\n\ts_max_u32 s75, s75, s91\n\ts_max_u32 s76, s76, s92\n\ts_max_u32 s77, s77, s93\n\ts_max_u32 s78, s78, s94\n\ts_max_u32 s79, s79, s95\n\ts_max_u32 s80, s80, s96\n\ts_max_u32 s81, s81, s97\n\ts_max_u32 s82, s82, s98\n\ts_max_u32 s83, s83, s99\n\ts_max_u32 s68, s68, s76\n\ts_max_u32 s69, s69, s77\n\ts_max_u32 s70, s70, s78\n\ts_max_u32 s71, s71, s79\n\ts_max_u32 s72, s72, s80\n\ts_max_u32 s73, s73, s81\n\ts_max_u32 s74, s74, s82\n\ts_max_u32 s75, s75, s83\n\ts_max_u32 s84, s68, s72\n\ts_max_u32 s85, s69, s73\n\ts_max_u32 s86, s70, s74\n\ts_max_u32 s87, s71, s75\n\ts_max_u32 s84, s84, s86\n\ts_max_u32 s85, s85, s87\n\ts_max_u32 %0, s84, s85\n\t"
                             "s_cmp_eq_u32 %0, -1\n\ts_cbranch_scc1 1b\n2:\n\ts_mov_b32 %1, vcc_lo\n\t"
                             "s_min_u32 s68, s68, s69\n\ts_min_u32 s70, s70, s71\n\ts_min_u32 s72, s72, s73\n\ts_min_u32 s74, s74, s75\n\ts_min_u32 s68, s68, s70\n\ts_min_u32 s72, s72, s74\n\ts_min_u32 %[lo], s68, s72"
                             : "=&s"(r), "=&s"(t), "=&s"(left), [lo] "=&s"(lo) : "s"(ws), "s"(o), "s"(o + 64u) : "memory", "scc", "vcc", "s68", "s69", "s70", "s71", "s72", "s73", "s74", "s75", "s76", "s77", "s78", "s79", "s80", "s81", "s82", "s83", "s84", "s85", "s86", "s87", "s88", "s89", "s90", "s91", "s92", "s93", "s94", "s95", "s96", "s97", "s98", "s99");
                if (__builtin_expect(left < 0, 0)) {
#pragma unroll 1
                    for (unsigned spins = 0; spins < kSpinCap; ++spins) {
                        if (allok(poll4(ws, o + (unsigned)((lane & 31) * 4)))) break;
                    }
                    lo = r = t = now32();
                    RETRY(22, 1);
                }
                const unsigned sp = r - lo, ex = sp < SCH(S_Y0) ? ((SCH(S_Y0) - sp) * 5u) >> 3 : 0u;
                d = (int)(r + (ex < SCH(S_Y1) ? ex : SCH(S_Y1)) - t);
            }
            asm volatile("ds_write_b32 %0, %1" :: "v"(lb + L_CNT + 8), "v"((t + (unsigned)(d < 150 ? d : 0)) | 1u) : "memory");
            STAMP(23); if (!is_mlp) DMARK(33);
            STAMPV(31, t + (unsigned)(d < 150 ? d : 0));
            }
        }
    }

    unsigned ref = 0;
#pragma unroll 1
    for (int layer_ = 0; layer_ < (P1ONLY ? 0 : 2); ++layer_) {
        IDS();
#define LAYER() int layer = layer_; asm volatile("" : "+s"(layer)); const unsigned cx = CX(layer), xa = WS_XA + (unsigned)(layer * LB), xm = xa + (a_rep(xg) ^ cx); (void)xa; (void)xm
        auto gather_img = [&](unsigned o, u32x4 (&a)[5], int sid, unsigned off) {
#pragma unroll 1
            for (unsigned spins = 0; spins < kSpinCap; ++spins) {
                a[0] = ld16_sc1o<-2048>(ws, o); a[1] = ld16_sc1o<-1024>(ws, o); a[2] = ld16_sc1o<0>(ws, o); a[3] = ld16_sc1o<1024>(ws, o); a[4] = ld16_sc1o<2048>(ws, o);
                asm volatile("s_waitcnt vmcnt(0)" : "+v"(a[0]), "+v"(a[1]), "+v"(a[2]), "+v"(a[3]), "+v"(a[4]) :: "memory");
                if (allok(umax(mx4(a[0], mx4(a[1])), mx4(a[2], mx4(a[3], mx4(a[4])))))) break;
                RETRY(sid, spins + 1);
                __builtin_amdgcn_s_sleep(10);
            }
        };
        auto gather_tab = [&](unsigned o, u32x4 (&a)[5], u32x4& r, int sid, unsigned t) {
            wait_until(t);
            a[0] = ld16_sc1o<-2048>(ws, o); a[1] = ld16_sc1o<-1024>(ws, o); a[2] = ld16_sc1o<0>(ws, o); a[3] = ld16_sc1o<1024>(ws, o); a[4] = ld16_sc1o<2048>(ws, o);
            wait_until(t + SCH(S_H4));
            r = ld16_sc1o<3072>(ws, o);
            asm volatile("s_waitcnt vmcnt(1)" : "+v"(a[0]), "+v"(a[1]), "+v"(a[2]), "+v"(a[3]), "+v"(a[4]) :: "memory");
            if (__builtin_expect(!allok(umax(mx4(a[0], mx4(a[1])), mx4(a[2], mx4(a[3], mx4(a[4]))))), 0)) {
                RETRY(sid, 1);
                asm volatile("s_waitcnt vmcnt(0)" : "+v"(r) :: "memory");
                __builtin_amdgcn_s_sleep(4);
                gather_img(o, a, sid, 0);
                r = ld16_sc1o<3072>(ws, o);
            }
        };
        auto late = [&](u32x4 v, unsigned base) {
            int d = -1000;
#pragma unroll
            for (int i = 0; i < 4; ++i) d = imax(d, v[i] == ~0u ? -1000 : (int)(v[i] - base));
            d = wave_imax(d);
            return d < 1000 ? d : 1000;
        };
        auto img_stats = [&](const u32x4 (&a)[5], float& mb, float& rden) {
            float s0 = 0.f, s1 = 0.f, q0 = 0.f, q1 = 0.f;
#pragma unroll
            for (int i = 0; i < 10; ++i) {
                const unsigned u = a[i >> 1][(i & 1) * 2], v = a[i >> 1][(i & 1) * 2 + 1];
                s0 = dot2(u, 0x3f803f80u, s0); q0 = dot2(u, u, q0); s1 = dot2(v, 0x3f803f80u, s1); q1 = dot2(v, v, q1);
            }
            const float mean = sum_x4(s0 + s1) * (1.f / DM);
            ln_consts(mean, sum_x4(q0 + q1) * (1.f / DM) - mean * mean, mb, rden);
        };
        if (!is_mlp) {
            if (wave == 5) {
                PAGE(4);
                LANES(); LAYER();
                const unsigned po = (a_rep((lane >> 2) & 7) ^ cx) + (unsigned)((oi >> 1) * 64 + p4 * 16 + (oi & 1) * 8 + (lane >> 5) * 4);
                if (layer == 0) lds_wait_ge(lb + L_CNT + 40, 1u);
                u32x4 w[5];
#pragma unroll
                for (int j = 0; j < 5; ++j) w[j] = *reinterpret_cast<const u32x4*>(smem + L_WR + j * 256 + (lane >> 2) * 16);
                float xr = layer ? *reinterpret_cast<const float*>(smem + L_XR + lane * 4) : __uint_as_float((unsigned)*reinterpret_cast<const u16*>(smem + L_WR + 1280 + p4 * 8 + kq * 2) << 16);
                float cw = 0.f;
#pragma unroll
                for (int j = 0; j < 20; ++j) cw = dot2(w[j >> 2][j & 3], 0x3f803f80u, cw);
                cw = sum_x4(cw);
                const unsigned tw = (a_rep(lane & 7) ^ cx) + (unsigned)(5120 + xg * 128 + ((h & 3) * 5 + sub) * 4);
                {
                    unsigned o = (a_own(oi) ^ cx) + (unsigned)(layer * LB + 6144 + lane * 16), so = xa + po;
                    unsigned t8 = tag & 0xffu;
                    asm volatile("" : "+v"(xr), "+v"(o), "+v"(so), "+v"(cw), "+s"(t8));
                    if (layer == 0) {
                        if (!(SCH(S_FL) & 224)) lds_wait_ge(lb + L_CNT + 44, 1u);
#pragma unroll 1
                        for (unsigned spins = 0; spins < (kSpinCap << 6) && ref == 0u; ++spins) ref = lds_ld(lb + L_CNT + 8);
                    }
                    if (PRI & 1) __builtin_amdgcn_s_setprio(2);
                    if (!layer) DMARK(35);
                    if (layer == 0 && (SCH(S_FL) & 96)) lds_wait_ge(lb + L_CNT + 44, 1u);
                    wait_until(ref + (layer ? SCH(S_H3) : SCH(S_H30)));
                    if (!layer) DMARK(34);
                    CY(c_a);
                    u32x4 a0, a1;
                    GATHER(8 + 32 * layer, { a0 = ld16_sc1o<0>(ws, o); a1 = ld16_sc1o<1024>(ws, o);
                                 asm volatile("s_waitcnt vmcnt(0)" : "+v"(a0), "+v"(a1) :: "memory"); a0 ^= t8; a1 ^= t8; }, tagok(or4(a0) | or4(a1)));
                    CY(c_b);
                    xr += bfr(red4(asf(a0) + asf(a1)));
                    auto sw = __builtin_amdgcn_permlane16_swap(__float_as_uint(xr), __float_as_uint(xr), false, false);
                    st4_sc1(ws, so, enc(pk2(__uint_as_float(sw[0]), __uint_as_float(sw[1]))));
                    xr = bfr(xr);
                    const unsigned t = now32() + SCH(S_GAP);
                    if ((int)(t - ref - (layer ? SCH(S_XA1) : SCH(S_XA0))) > 0) st4_m<0xffu, 0u>(ws, xa + tw, enc(t + SCH(S_EX)));
                    CY(c_c); CYW(55, c_a, c_b); CYW(56, c_b, c_c);
                    STAMP(8 + 32 * layer); if (layer) DMARK(25); else DMARK(31);
                }
                float rs, xn;
                {
                    unsigned o = xm + (unsigned)(2048 + lane * 16);
                    asm volatile("" : "+v"(o), "+v"(cw));
                    u32x4 a[5], lt; gather_tab(o, a, lt, 9 + 32 * layer, ref + (layer ? SCH(S_XA1) : SCH(S_XA0)) + 8u);
                    float mb, rden; img_stats(a, mb, rden);
                    float r0 = 0.f, r1 = 0.f;
#pragma unroll
                    for (int j = 0; j < 10; ++j) { r0 = dot2(a[j >> 1][(j & 1) * 2], w[j >> 1][(j & 1) * 2], r0); r1 = dot2(a[j >> 1][(j & 1) * 2 + 1], w[j >> 1][(j & 1) * 2 + 1], r1); }
                    const float r = bfr((sum_x4(r0 + r1) - mb * cw) * rden);
                    rs = bfr(frcp(1.f + fexp(-r)));
                    xn = bfr(bfr(xr - mb) * rden);
                    asm volatile("s_waitcnt vmcnt(0)" : "+v"(lt), "+v"(rs), "+v"(xn) :: "memory");
                    ref += (unsigned)imax(late(lt, ref + (layer ? SCH(S_XA1) : SCH(S_XA0))), 0);
                    if (!layer) DMARK(38);
                    if (layer == 0) {
                        if (g == 0) st4_m<1u, 0u>(ws, WS_EP, tag);
                        lds_wait_ge(lb + L_CNT + 44, 2u);
                    }
                    STAMP(9 + 32 * layer); if (!layer) DMARK(37);
                }
                {
                    unsigned o = (a_own(oi) ^ cx) + (unsigned)(layer * LB + lane * 16 + 2048);
                    unsigned so = layer ? (unsigned)((p4 * DM + oi * 4 + (lane >> 5) * 2) * 2) : WS_XB + po;
                    asm volatile("" : "+v"(rs), "+v"(xn), "+v"(o), "+v"(so));
                    wait_until(ref + (layer ? SCH(S_H51) : SCH(S_H50)));
                    CY(c_a);
                    u32x4 a[6];
                    GATHER(13 + 32 * layer, { a[0] = ld16_sc1o<-2048>(ws, o); a[1] = ld16_sc1o<-1024>(ws, o); a[2] = ld16_sc1o<0>(ws, o);
                                 a[3] = ld16_sc1o<1024>(ws, o); a[4] = ld16_sc1o<2048>(ws, o); a[5] = ld16_sc1o<3072>(ws, o);
                                 asm volatile("s_waitcnt vmcnt(0)" : "+v"(a[0]), "+v"(a[1]), "+v"(a[2]), "+v"(a[3]), "+v"(a[4]), "+v"(a[5]) :: "memory"); },
                           allok(umax(umax(mx4(a[0], mx4(a[1])), mx4(a[2], mx4(a[3]))), mx4(a[4], mx4(a[5])))));
                    CY(c_b);
                    const float y = bfr(red4(((asf(a[0]) + asf(a[1])) + (asf(a[2]) + asf(a[3]))) + (asf(a[4]) + asf(a[5]))));
                    xr += bfr(xn + bfr(y * rs));
                    auto sw = __builtin_amdgcn_permlane16_swap(__float_as_uint(xr), __float_as_uint(xr), false, false);
                    const unsigned w = pk2(__uint_as_float(sw[0]), __uint_as_float(sw[1]));
                    if (layer == 0) {
                        st4_sc1(ws, so, enc(w));
                        const unsigned t = now32() + SCH(S_GAP), t2 = ref + SCH(S_XB), tx = ((int)(t - t2) > 0 ? t + SCH(S_EXB) : t2) | 1u;
                        asm volatile("ds_write_b32 %0, %1" :: "v"(lb + L_CNT + 60), "v"(tx) : "memory");
                        if ((sub == 0 && (h & 3) == 0) || (int)(t - t2) > 0) st4_m<0xffu, 0u>(ws, WS_XB + tw, enc(tx));
                    }
                    else {
                        asm volatile("s_mov_b32 exec_lo, 15\n\ts_mov_b32 exec_hi, 15\n\tglobal_store_dword %0, %1, %2 " OSX "\n\ts_mov_b64 exec, -1" :: "v"(so), "v"(w), "s"(out) : "memory");
                    }
                    *reinterpret_cast<float*>(smem + L_XR + lane * 4) = bfr(xr);
                    CY(c_c); CYW(57, c_a, c_b); CYW(58, c_b, c_c);
                    STAMP(13 + 32 * layer); if (!layer) DMARK(36);
                }
            } else if (wave == 4 && layer_ == 0) {
                LANES();
                const u32x4 ones = splat<~0u>();
                if (SCH(S_FL) & 32) lds_wait_ge(lb + L_CNT + 8, 1u);
                if (SCH(S_FL) & 64) { lds_wait_ge(lb + L_CNT + 48, 1u); wait_until(lds_ld(lb + L_CNT + 48) + 30u); }
                if (!(SCH(S_FL) & 128)) {
#pragma unroll
                for (int i = 0; i < 6; ++i) st16_l(ws, (a_own(oi) ^ CX(0)) + (unsigned)(i * 1024 + lane * 16), ones);
                if (oi < 96 && lane < 32) st16_l(ws, WS_XB + (unsigned)((oi / 12) * 8192 + (oi % 12) * 512 + lane * 16), ones);
                asm volatile("s_waitcnt vmcnt(0)\n\tds_write_b32 %0, %1" :: "v"(lb + L_CNT + 44), "v"(1u) : "memory");
                }
                lds_wait_ge(lb + L_CNT + 8, 1u); wait_until(lds_ld(lb + L_CNT + 8) + SCH(S_CL) - 100u);
                if (SCH(S_FL) & 2) {
#pragma unroll
                    for (int i = 0; i < 2; ++i) st16_l(ws, a_own(oi) + (unsigned)(LB + 6144 + i * 1024 + lane * 16), (u32x4){tag + 1u, tag + 1u, tag + 1u, tag + 1u});
                }
                if (oi < 128) { st16_l(ws, WS_XA + LB + (unsigned)(oi * 1536 + lane * 16), ones); if (lane < 32) st16_l(ws, WS_XA + LB + (unsigned)(oi * 1536 + 1024 + lane * 16), ones); }
#pragma unroll
                for (int i = 0; i < 3; ++i) st16_l(ws, a_h1(h, lane >> 4) + (unsigned)(((2 * i + ((lane >> 3) & 1)) * 5 + sub) * 128 + (lane & 7) * 16), ones);
                asm volatile("s_waitcnt vmcnt(0)\n\tds_write_b32 %0, %1" :: "v"(lb + L_CNT + 44), "v"(2u) : "memory");
                STAMP(26);
            }
        } else {
            PAGE(3);
            LAYER();
            if (wave < 4) {
                LANES();
                if (layer == 0) { VMW(0); STAMP(27); lds_wait_ge(lb + L_CNT + 8, 1u); ref = lds_ld(lb + L_CNT + 8); }
                const unsigned gs = xm + (unsigned)(wave * 1280);
                int st;
                asm volatile(ASM_GU : [st] "=&s"(st) : [a0] "v"(lb + L_GU + (unsigned)((wave * 160 + 8 * kq + q4 + 4 * (kq & 1)) * 32 + p4 * 8)), [gs] "v"(gs + lane * 16),
                             [g2] "v"(gs + (unsigned)(1024 + kq * 64 + p4 * 16 + q4 * 4)), [pw] "v"(lb + L_OP + (unsigned)(n * 784 + kq * 192 + wave * 8)), [gw] "v"(lb + L_OP + (unsigned)(4096 + n * 272 + kq * 64 + wave * 4)),
                             [ws] "s"(ws), [tg] "s"(ref + (layer ? SCH(S_XA1) : SCH(S_XA0))), [dbg] "s"(lb + L_DBG + wave * 512 + 256 + (10 + 32 * layer) * 4) : "memory", "scc", "vcc", "v6", "v7", CLOB_8_15, CLOB_16_63);
                STAMP(11 + 32 * layer);
                if (wave == 1 && layer == 0) {
                    u32x4 lt = ld16_sc1(ws, xm + (unsigned)(5120 + lane * 16));
                    asm volatile("s_waitcnt vmcnt(0)" : "+v"(lt) :: "memory");
                    asm volatile("ds_write_b32 %0, %1" :: "v"(lb + L_CNT + 60), "v"((ref + SCH(S_XB) + (unsigned)imax(late(lt, ref + SCH(S_XA0)), 0)) | 1u) : "memory");
                }
            } else {
                LANES();
                if (layer == 0) {
                    lds_wait_ge(lb + L_CNT + 48, 1u);
                    if (wave == 7) DMARK(39);
                }
                const unsigned wdo = lb + L_WD + (unsigned)(4 * kq * 1312 + (160 * (wave & 3) + 4 * p4) * 2);
                unsigned w0 = wdo + q4 * 1312, w1 = wdo + ((q4 + 1) & 3) * 1312, w2 = wdo + ((q4 + 2) & 3) * 1312, w3 = wdo + ((q4 + 3) & 3) * 1312;
                asm volatile(ASM_HS : [w0] "+{v8}"(w0), [w1] "+{v9}"(w1), [w2] "+{v10}"(w2), [w3] "+{v11}"(w3) :
                             [rp] "v"(lb + L_OP + (unsigned)(p4 * 784 + (4 * kq + q4) * 48)), [cp] "v"(lb + L_OP + (unsigned)(4 * 784 + (4 * kq + q4) * 48)),
                             [sq] "v"(lb + L_OP + (unsigned)(4096 + p4 * 288)), [sx] "v"(lb + L_OP + (unsigned)(4096 + p4 * 272 + 64)),
                             [so] "v"(((unsigned)(layer * LB + ((10 * (wave & 1) + q4) * 4 + kq) * GP + (wave & 2) * 4096 + mi * 64 + p4 * 16)) ^ ((unsigned)((q4 * 4 + kq) << 7) | cx)), [ws] "s"(ws)
                             : "memory", "v12", "v13", CLOB_16_63);
                STAMP(12 + 32 * layer); if (!layer) DMARK(32);
            }
        }
        if (layer_ != 0) break;
        unsigned tz = 0u;
        if (wave == 7) lds_wait_ge(lb + L_CNT + 8, 1u);
        if (is_mlp || wave == 7) tz = (lds_ld(lb + L_CNT + 8) + SCH(S_H50) + SCH(S_Y2)) | 1u;
        {
            const unsigned xb = WS_XB + (a_rep(xg) ^ CX(0));
#define QPROJ() do { const int tp = wave == 6 ? 5 : wave; int st_, rf_; \
                    const unsigned a0_ = lb + L_WQ + (unsigned)((8 * kq + q4) * 384 + ((2 * tp) ^ ((q4 >> 1) | ((kq & 1) << 1))) * 32 + p4 * 8); \
                    asm volatile(ASM_QP : [st] "=&s"(st_), [rf] "=&s"(rf_) : [a0] "{v60}"(a0_), [a1] "{v61}"(a0_ ^ 32u), [gs] "{v62}"(xb + (unsigned)(sub * 1024 + lane * 16)), \
                                 [so] "{v63}"((a_h1(h, p4) ^ CX(1)) + (unsigned)((tp * 5 + sub) * 128 + kq * 32 + (q4 & 1) * 16)), \
                                 [ws] "s"(ws), [fl] "s"(lb + L_CNT + 60), [tz] "s"(SCH(S_H50) + SCH(S_Y2)), [dbg] "s"(lb + L_DBG + wave * 512 + 256 + 14 * 4) : "memory", "scc", "vcc", CLOB_16_59); } while (0)
#define ATT_IN() LANES(); \
            ATT_ADDR(1); \
            const unsigned go = (wave < 6 ? (a_h1(h, b) ^ CX(1)) + (unsigned)(wave * 640) : (a_own(oi) ^ CX(1)) + (unsigned)(LB + (wave & 1) * 3072)) + (unsigned)(lane * 16); \
            const unsigned qo = lb + L_Q + (unsigned)((wave & 1) * 192 + (wave >> 1) * 64 + (lane & 1) * 32 + (kq >> 1) * 16 + ((lane >> 1) & 3) * 4 + (kq & 1) * 2); \
            const float cs = *reinterpret_cast<const float*>(smem + L_CS + wave * 128 + (lane & 1) * 64 + ((lane >> 1) & 3) * 16 + kq * 4); \
            int st
#define ATT_OPS : [st] "=&s"(st) : [qa] "{v8}"(qa), [pw] "{v9}"(pw), [pr] "{v10}"(pr), [ow] "{v11}"(ow), [mo] "{v12}"(mo), [r0] "{v5}"(r0), [so] "{v6}"(so), [go] "{v13}"(go), [qo] "{v14}"(qo), [cs] "{v15}"(cs), [ix] "{v7}"(ix), \
                [mlw] "s"(lb + L_ML + wave * 4), [ma] "s"(lb + L_ML), [ws] "s"(ws), [wv] "s"(wave), \
                [cl] "s"((wave + 2) & (sub - 5) & 8), [tg] "s"(SCH(S_H1)), [tz] "s"(is_mlp && tz ? tz + SCH(S_Y3) : 0u), [dbg] "s"(lb + L_DBG + wave * 512 + 256 + 35 * 4) : "memory", "scc", "vcc", CLOB_16_63 TRCL
            if (wave >= 4) {
                ATT_IN();
                unsigned go_ = go, qo_ = qo; float cs_ = cs;
                asm volatile("" : "+{v8}"(qa), "+{v9}"(pw), "+{v10}"(pr), "+{v11}"(ow), "+{v12}"(mo), "+{v5}"(r0), "+{v6}"(so), "+{v13}"(go_), "+{v14}"(qo_), "+{v15}"(cs_), "+{v7}"(ix));
                PAGE(5);
                if (!(wave & 1) && !is_mlp) { QPROJ(); DMARK(21); }
                if (wave == 7) {
                unsigned o = xb + (unsigned)(2048 + lane * 16);
                asm volatile("" : "+v"(o));
                unsigned gx = 0u;
                if (PRI & 2) __builtin_amdgcn_s_setprio(3);
                if (tz) while ((int)(now32() - tz) < 0) __builtin_amdgcn_s_sleep(3);
#pragma unroll 1
                for (unsigned spins = 0; spins < (kSpinCap << 6) && gx == 0u; ++spins) gx = lds_ld(lb + L_CNT + 60);
                u32x4 a[5], r; gather_tab(o, a, r, 14, gx + 4u);
                unsigned k0 = lane & 7 ? 1u : 2u;
                asm volatile("s_waitcnt vmcnt(0)" : "+v"(r), "+v"(k0) :: "memory");
                auto dif = [&](unsigned w, unsigned k) { const unsigned x = w ^ gx, y = w + k; return x < y ? x : y; };
                if (__builtin_expect(__builtin_amdgcn_ballot_w64((dif(r[0], k0) | dif(r[1], 1u) | dif(r[2], 1u) | dif(r[3], 1u)) != 0u) != 0ull, 0)) {
#pragma unroll 1
                    for (unsigned spins = 0; spins < kSpinCap && !allok(lane & 7 ? 0u : r[0]); ++spins) {
                        RETRY(15, spins + 1);
                        __builtin_amdgcn_s_sleep(4);
                        r = ld16_sc1o<3072>(ws, o);
                        asm volatile("s_waitcnt vmcnt(0)" : "+v"(r) :: "memory");
                    }
                    gx = (gx + (unsigned)late(r, gx)) | 1u;
                }
                asm volatile("ds_write_b32 %0, %1" :: "v"(lb + L_CNT + 20), "v"(gx) : "memory");
                STAMP(38); DMARK(28);
                float mb, rden; img_stats(a, mb, rden);
                STAMP(14); DMARK(24);
                if (lane == b) *reinterpret_cast<u32x2*>(smem + L_CNT + 32) = (u32x2){__float_as_uint(mb), __float_as_uint(rden)};
                BAR();
                }
                { const unsigned go = go_, qo = qo_; const float cs = cs_; asm volatile(ASM_ATT_B ATT_OPS); }
            }
            else if (is_mlp) { PAGE(6); ATT_IN(); asm volatile(ASM_ATT_A ATT_OPS); }
            else {
                ATT_IN();
                const unsigned a0 = lb + L_WO + (unsigned)((8 * kq + q4) * 256 + ((2 * wave) ^ (q4 | ((kq & 1) << 2))) * 32 + p4 * 8), h2 = (a_h2(h) ^ CX(1)) + LB; const int ot = (8 * sub + 2 * wave + (q4 & 1)) * 4 + kq, rw = (q4 & 1) ? 3 - p4 : p4;
                unsigned i0 = qa, i1 = pw, i2 = pr, i3 = ow, i4 = mo, i5 = go, i6 = qo; float i7 = cs; int rf;
                unsigned j0 = a0, j1 = a0 ^ 32u, j2 = h2 + (unsigned)((lane >> 3) * 128 + rw * 32 + (q4 & 1) * 16), j3 = h2 + (unsigned)(3072 + ((n >> 3) * 4 + rw) * 128);
                unsigned j4 = (a_own(ot) + (unsigned)(LB + 6144 + h * 64 + rw * 16)) ^ ((unsigned)((ot & 15) << 7) | CX(1));
                asm volatile("" : "+{v8}"(i0), "+{v9}"(i1), "+{v10}"(i2), "+{v11}"(i3), "+{v12}"(i4), "+{v13}"(i5), "+{v14}"(i6), "+{v15}"(i7), "+{v0}"(j0), "+{v1}"(j1), "+{v2}"(j2), "+{v3}"(j3), "+{v4}"(j4), "+{v5}"(r0), "+{v6}"(so), "+{v7}"(ix));
                PAGE(7);
                QPROJ();
                asm volatile(ASM_ATT_P : [st] "=&s"(st), [rf] "=&s"(rf), [t0] "+{v7}"(ix), [qa] "+{v8}"(i0), [pw] "+{v9}"(i1), [pr] "+{v10}"(i2), [ow] "+{v11}"(i3), [mo] "+{v12}"(i4), [go] "+{v13}"(i5), [qo] "+{v14}"(i6), [cs] "+{v15}"(i7)
                             : [a0] "{v0}"(j0), [a1] "{v1}"(j1), [gs] "{v2}"(j2), [gm] "{v3}"(j3), [s2] "{v4}"(j4), [r0] "{v5}"(r0), [so] "{v6}"(so),
                               [mlw] "s"(lb + L_ML + wave * 4), [ma] "s"(lb + L_ML), [ws] "s"(ws),
                               [tg] "s"(SCH(S_H1)), [t2] "s"(SCH(S_H2)), [wv] "s"(wave), [tag] "s"(tag), [dbg] "s"(lb + L_DBG + wave * 512 + 256 + 35 * 4) : "memory", "scc", "vcc", CLOB_16_63 TRCL);
                STAMP(39); DMARK(23);
                break;
            }
            if (wave != 7) STAMP(38);
            DMARK(22); if (is_mlp) DMARK(26);
            ref = lds_ld(lb + L_CNT + 20);
        }
    }
    asm volatile("s_endpgm\n\t.fill 128, 4, 0xbf800000\nmega_pf_end:" ::: "memory");
}

extern "C" void kernel_launch(void* const* d_in, const int* in_sizes, int n_in, void* d_out, int out_size, void* d_ws,
                              size_t ws_size, hipStream_t stream) {
    const u16* x0 = (const u16*)d_in[0];
    const u16* kc = (const u16*)d_in[1];
    const u16* vc = (const u16*)d_in[2];
    const u16* wq = (const u16*)d_in[3];
    const u16* wo = (const u16*)d_in[4];
    const u16* wr = (const u16*)d_in[6];
    const u16* wg = (const u16*)d_in[7];
    const u16* wu = (const u16*)d_in[8];
    const u16* wd = (const u16*)d_in[9];
    (void)hipFuncSetAttribute((const void*)mega_kernel, hipFuncAttributeMaxDynamicSharedMemorySize, L_END);
    mega_kernel<<<256, 512, L_END, stream>>>(kc, vc, (unsigned char*)(((size_t)d_ws + LB - 1) & ~(size_t)(LB - 1)), x0, wq, wg, wu, wo, wr, wd, (u16*)d_out
                                             );
}
```

```cpp
#define SCHED {130,232,300,56,205,271,323,422,10,0,490,3,1822,256,14,0,256,16,175,0,74,65,564,60,4,16,0,9,70,40,16,40,0,4,170,230,455,30,70,0,120,25,44,46}
#define WOMODE 0
#define KAL 4096
#define P_LN 7
#define P_QP 9
#define F7 4
#define DHI 4
#define WQP 4
#define EPF 3
#define LDR 38
#define CA 8
#define A7 13
#define Q7 22
#define L7 25
#define QG 35
#define QW 6
#define ECX "sc1"
#define NN7 18
#define WK -4
#define DNW 21
#define NM7 0
#define TSX 1
#define WSP -1
#define WPRE 0
#define MLS 16
#define MLS1 16
#include <hip/hip_runtime.h>
#include <hip/hip_bf16.h>
#include <stdint.h>

typedef unsigned short u16;
typedef unsigned u32x4 __attribute__((ext_vector_type(4)));
typedef unsigned u32x2 __attribute__((ext_vector_type(2)));
typedef unsigned u32x8 __attribute__((ext_vector_type(8)));
typedef float f32x4 __attribute__((ext_vector_type(4)));
typedef short s16x4 __attribute__((ext_vector_type(4)));
typedef __attribute__((ext_vector_type(4))) __bf16 bf16x4;
typedef __attribute__((ext_vector_type(8))) __bf16 bf16x8;
#define LAS __attribute__((address_space(3)))

constexpr int DM = 640, HQ = 32, SK = 4096, DH = 192, WIN = 1024;
constexpr int QD = HQ * DH;
constexpr float SCALE = 0.07216878364870322f;
constexpr float L2E = 1.44269504088896341f;
constexpr unsigned kSpinCap = 1u << 16;
constexpr int NMLP = 96;
#define TDEPTH 4

#define POL 0
#define WPOL " nt"
enum { S_XA0, S_H50, S_XB, S_H1, S_H2, S_H3, S_XA1, S_H51, S_GAP, S_PA, S_PD, S_WT, S_WD, S_CL, S_H4, S_GS, S_TS, S_TW, S_TR, S_C5, S_H30, S_H20, S_PM, S_EX, S_N0, S_N1, S_NT, S_ND, S_NR, S_PT, S_FL, S_BT, S_G1, S_G2, S_G3, S_X3, S_X4, S_X5, S_X6, S_X7, S_Y0, S_Y1, S_Y2, S_Y3, S_N };
#define S_LT S_GS
#define S_EXB S_NR
#define S_PO S_BT
#define S_MO S_PT
#define S_P7 S_X4
#define S_O7 S_X5

constexpr unsigned LB = 2097152, GP = 16384;
constexpr unsigned WS_XA = 80 * GP;
constexpr unsigned WS_H2 = 84 * GP;
constexpr unsigned WS_XB = 92 * GP;
constexpr unsigned WS_TS = 96 * GP;
constexpr unsigned WS_H1B = LB + 92 * GP;
constexpr unsigned WS_PR = 124 * GP;
constexpr unsigned WS_EP = 2 * LB;
constexpr unsigned WS_AR = WS_EP + 1024;
constexpr unsigned WS_GO = WS_EP + 4096;
constexpr unsigned WS_H1A = WS_EP + 8192;
constexpr unsigned WS_CS = WS_H1A + 160 * 1536;
constexpr unsigned WS_SINK = WS_CS + 32 * 3840;
__device__ __forceinline__ unsigned a_rep(int r) { return (unsigned)((r & 3) * GP + (r >> 2) * 8192); }
__device__ __forceinline__ unsigned a_own(int o) { return (unsigned)(o * GP - (o >= 80 ? 80 * GP - 8192 : 0)); }
__device__ __forceinline__ unsigned a_h2(int h) { return WS_H2 + (unsigned)(((h & 15) >> 1) * GP + (h >> 4) * 8192 + (h & 1) * 4096); }
__device__ __forceinline__ unsigned a_hf(int h) { return WS_XA + a_rep(h >> 2) + (unsigned)((h & 2) * 6 * GP + 6144 + (h & 1) * 1024); }
__device__ __forceinline__ unsigned a_ts(int r) { return WS_TS + (unsigned)(((r & 3) >> 1) * GP + (r >> 2) * 8192 + (r & 1) * 4096); }
__device__ __forceinline__ unsigned a_h1(int h, int row) { return WS_H1B + (unsigned)(((h & 15) * 2 + (row >> 1)) * GP + (h >> 4) * 8192 + (row & 1) * 3840); }
#define CX(layer) ((pol << (12 - (layer))) & 8192u)
constexpr int LEADER = LDR;
#define RST 128
constexpr unsigned WS_DBG = 6680576;
static_assert(WS_SINK + 256 * 128 <= WS_DBG, "ws map");

constexpr int L_WQ = 0;
constexpr int L_WO = 49152;
constexpr int L_GU = 0;
constexpr int L_WD = 40960;
constexpr int OPS = 3200;
constexpr int L_OP = 98304;
constexpr int L_P = L_OP + 8 * OPS;
constexpr int L_Q = L_P + 8 * 1088;
constexpr int L_CS = L_Q + 512;
constexpr int L_ML = L_CS + 1536;
constexpr int L_XR = L_ML + 64;
constexpr int L_CNT = L_XR + 256;
constexpr int L_FW = L_CNT + 64;
constexpr int L_PF = L_FW + 64;
constexpr int L_POL = L_PF + 256;
constexpr int L_WR = L_POL + 32;
constexpr int L_DBG = L_WR + 1536;
constexpr int L_END = L_DBG + 4096;
static_assert(L_CNT + 32 - L_ML == 352, "stats offset");
static_assert(L_END <= 163840 && L_END > 81920 && L_Q % 16 == 0, "LDS");

typedef __attribute__((ext_vector_type(2))) __bf16 bf16x2;
typedef float f32x2 __attribute__((ext_vector_type(2)));
__device__ __forceinline__ unsigned pk2(float a, float b) { return __builtin_bit_cast(unsigned, __builtin_convertvector((f32x2){a, b}, bf16x2)); }
__device__ __forceinline__ float lo16(unsigned w) { return __uint_as_float(w << 16); }
__device__ __forceinline__ float hi16(unsigned w) { return __uint_as_float(w & 0xffff0000u); }
__device__ __forceinline__ float bfr(float f) { return lo16(pk2(f, f)); }
template <int CTRL> __device__ __forceinline__ float dpp(float x) {
    return __builtin_bit_cast(float, __builtin_amdgcn_mov_dpp(__builtin_bit_cast(int, x), CTRL, 0xf, 0xf, true));
}
template <int CTRL> __device__ __forceinline__ unsigned dppu(unsigned x) { return (unsigned)__builtin_amdgcn_mov_dpp((int)x, CTRL, 0xf, 0xf, true); }
__device__ __forceinline__ float row16_sum(float v) {
    v += dpp<0xB1>(v); v += dpp<0x4E>(v); v += dpp<0x141>(v); v += dpp<0x140>(v); return v;
}
__device__ __forceinline__ float sw16_add(float a, float b) { auto s = __builtin_amdgcn_permlane16_swap(__float_as_uint(a), __float_as_uint(b), false, false); return __uint_as_float(s[0]) + __uint_as_float(s[1]); }
__device__ __forceinline__ float sw32_add(float a, float b) { auto s = __builtin_amdgcn_permlane32_swap(__float_as_uint(a), __float_as_uint(b), false, false); return __uint_as_float(s[0]) + __uint_as_float(s[1]); }
__device__ __forceinline__ float wave_sum(float v) { v = row16_sum(v); v = sw16_add(v, v); return sw32_add(v, v); }
__device__ __forceinline__ int imax(int a, int b) { return a > b ? a : b; }
__device__ __forceinline__ unsigned umax(unsigned a, unsigned b) { return a > b ? a : b; }
__device__ __forceinline__ int wave_imax(int v) {
    v = imax(v, __builtin_amdgcn_mov_dpp(v, 0xB1, 0xf, 0xf, true)); v = imax(v, __builtin_amdgcn_mov_dpp(v, 0x4E, 0xf, 0xf, true));
    v = imax(v, __builtin_amdgcn_mov_dpp(v, 0x141, 0xf, 0xf, true)); v = imax(v, __builtin_amdgcn_mov_dpp(v, 0x140, 0xf, 0xf, true));
    return imax(imax(__builtin_amdgcn_readlane(v, 0), __builtin_amdgcn_readlane(v, 16)), imax(__builtin_amdgcn_readlane(v, 32), __builtin_amdgcn_readlane(v, 48)));
}
__device__ __forceinline__ int fkey(int bits) { return bits ^ ((bits >> 31) & 0x7fffffff); }

#define BAR() asm volatile("s_waitcnt lgkmcnt(0)\n\ts_barrier" ::: "memory")
#define VMW(n) asm volatile("s_waitcnt vmcnt(%0)" :: "i"(n) : "memory")

__device__ __forceinline__ u32x4 ld16_sc1(const void* sbase, unsigned voff) {
    u32x4 r; asm volatile("global_load_dwordx4 %0, %1, %2 sc1" : "=v"(r) : "v"(voff), "s"(sbase) : "memory"); return r;
}
template <int IMM> __device__ __forceinline__ u32x4 ld16_sc1o(const void* sbase, unsigned voff) {
    u32x4 r; asm volatile("global_load_dwordx4 %0, %1, %2 offset:%3 sc1" : "=v"(r) : "v"(voff), "s"(sbase), "i"(IMM) : "memory"); return r;
}
template <int IMM> __device__ __forceinline__ u32x2 ld8_sc1o(const void* sbase, unsigned voff) {
    u32x2 r; asm volatile("global_load_dwordx2 %0, %1, %2 offset:%3 sc1" : "=v"(r) : "v"(voff), "s"(sbase), "i"(IMM) : "memory"); return r;
}
__device__ __forceinline__ unsigned ld4_sc1(const void* sbase, unsigned voff) {
    unsigned r; asm volatile("global_load_dword %0, %1, %2 sc1" : "=v"(r) : "v"(voff), "s"(sbase) : "memory"); return r;
}
__device__ __forceinline__ void st16_sc1(void* sbase, unsigned voff, u32x4 v) {
    asm volatile("global_store_dwordx4 %0, %1, %2 sc1\n\ts_nop 1" :: "v"(voff), "v"(v), "s"(sbase) : "memory");
}
#define XSX "sc1 nt"
#define XLX "sc1 nt"
#define LCX "sc1"
__device__ __forceinline__ void st16_e(void* sbase, unsigned voff, u32x4 v) {
    asm volatile("global_store_dwordx4 %0, %1, %2 " ECX "\n\ts_nop 1" :: "v"(voff), "v"(v), "s"(sbase) : "memory");
}
__device__ __forceinline__ void st16_l(void* sbase, unsigned voff, u32x4 v) {
    asm volatile("global_store_dwordx4 %0, %1, %2 " LCX "\n\ts_nop 1" :: "v"(voff), "v"(v), "s"(sbase) : "memory");
}
__device__ __forceinline__ void st16_x(void* sbase, unsigned voff, u32x4 v) {
    asm volatile("global_store_dwordx4 %0, %1, %2 " XSX "\n\ts_nop 1" :: "v"(voff), "v"(v), "s"(sbase) : "memory");
}
template <int IMM> __device__ __forceinline__ u32x4 ld16_xo(const void* sbase, unsigned voff) {
    u32x4 r; asm volatile("global_load_dwordx4 %0, %1, %2 offset:%3 " XLX : "=v"(r) : "v"(voff), "s"(sbase), "i"(IMM) : "memory"); return r;
}
__device__ __forceinline__ u32x2 ld8_xo(const void* sbase, unsigned voff) {
    u32x2 r; asm volatile("global_load_dwordx2 %0, %1, %2 " XLX : "=v"(r) : "v"(voff), "s"(sbase) : "memory"); return r;
}
__device__ __forceinline__ void st8_sc1(void* sbase, unsigned voff, u32x2 v) {
    asm volatile("global_store_dwordx2 %0, %1, %2 sc1" :: "v"(voff), "v"(v), "s"(sbase) : "memory");
}
__device__ __forceinline__ void st4_sc1(void* sbase, unsigned voff, unsigned v) {
    asm volatile("global_store_dword %0, %1, %2 sc1" :: "v"(voff), "v"(v), "s"(sbase) : "memory");
}
__device__ __forceinline__ void st8(void* sbase, unsigned voff, u32x2 v) {
    asm volatile("global_store_dwordx2 %0, %1, %2" :: "v"(voff), "v"(v), "s"(sbase) : "memory");
}
template <int IMM> __device__ __forceinline__ u32x4 ld16_nt(const void* sbase, unsigned voff) {
    return __builtin_nontemporal_load(reinterpret_cast<const u32x4*>(reinterpret_cast<const unsigned char*>(sbase) + voff + IMM));
}
__device__ __forceinline__ u32x4 ld16_v(const void* sbase, unsigned voff) {
    return *reinterpret_cast<const u32x4*>(reinterpret_cast<const unsigned char*>(sbase) + voff);
}
__device__ __forceinline__ void dma16(const void* sbase, unsigned voff, unsigned lds_dst) {
    unsigned keep;
    asm volatile("s_mov_b32 %0, m0\n\ts_mov_b32 m0, %3\n\ts_nop 0\n\tglobal_load_lds_dwordx4 %1, %2" WPOL "\n\ts_mov_b32 m0, %0"
                 : "=&s"(keep) : "v"(voff), "s"(sbase), "s"(lds_dst) : "memory");
}
__device__ __forceinline__ void dma16p(const void* sbase, unsigned voff, unsigned lds_dst) {
    unsigned keep;
    asm volatile("s_mov_b32 %0, m0\n\ts_mov_b32 m0, %3\n\ts_nop 0\n\tglobal_load_lds_dwordx4 %1, %2\n\ts_mov_b32 m0, %0"
                 : "=&s"(keep) : "v"(voff), "s"(sbase), "s"(lds_dst) : "memory");
}
__device__ __forceinline__ void dma4(const void* sbase, unsigned voff, unsigned lds_dst) {
    unsigned keep;
    asm volatile("s_mov_b32 %0, m0\n\ts_mov_b32 m0, %3\n\ts_nop 0\n\tglobal_load_lds_dword %1, %2\n\ts_mov_b32 m0, %0"
                 : "=&s"(keep) : "v"(voff), "s"(sbase), "s"(lds_dst) : "memory");
}
__device__ __forceinline__ void dma4n(const void* sbase, unsigned voff, unsigned lds_dst) {
    unsigned keep;
    asm volatile("s_mov_b32 %0, m0\n\ts_mov_b32 m0, %3\n\ts_nop 0\n\tglobal_load_lds_dword %1, %2 nt\n\ts_mov_b32 m0, %0"
                 : "=&s"(keep) : "v"(voff), "s"(sbase), "s"(lds_dst) : "memory");
}
#define KEEPY " sc1"
__device__ __forceinline__ void pull(const void* sbase, unsigned voff, unsigned lds_dst) {
    unsigned keep;
    asm volatile("s_mov_b32 %0, m0\n\ts_mov_b32 m0, %3\n\ts_nop 0\n\tglobal_load_lds_dword %1, %2 nt\n\tglobal_load_lds_dword %1, %2" KEEPY "\n\ts_mov_b32 m0, %0"
                 : "=&s"(keep) : "v"(voff), "s"(sbase), "s"(lds_dst) : "memory");
}
__device__ __forceinline__ unsigned poll4(void* sbase, unsigned voff) {
    unsigned r;
    asm volatile("global_load_dword %0, %1, %2 sc1\n\ts_waitcnt vmcnt(0)" : "=v"(r) : "v"(voff), "s"(sbase) : "memory");
    return r;
}
__device__ __forceinline__ unsigned enc(unsigned w) { return w < 0xfffffffeu ? w : 0xfffffffeu; }
__device__ __forceinline__ u32x4 enc4(f32x4 c) { return (u32x4){enc(__float_as_uint(c[0])), enc(__float_as_uint(c[1])), enc(__float_as_uint(c[2])), enc(__float_as_uint(c[3]))}; }
__device__ __forceinline__ unsigned mx4(u32x4 a) { return umax(umax(umax(a[0], a[1]), a[2]), a[3]); }
__device__ __forceinline__ unsigned mx4(u32x4 a, unsigned m) { return umax(umax(umax(umax(m, a[0]), a[1]), a[2]), a[3]); }
__device__ __forceinline__ bool allok(unsigned m) { return __builtin_amdgcn_ballot_w64(m == 0xffffffffu) == 0ull; }
__device__ __forceinline__ u32x4 tag4(u32x4 a, unsigned t8) { return (a & 0xffffff00u) | t8; }
__device__ __forceinline__ unsigned or4(u32x4 a) { return a[0] | a[1] | a[2] | a[3]; }
__device__ __forceinline__ bool tagok(unsigned m) { return __builtin_amdgcn_ballot_w64((m & 0xffu) != 0u) == 0ull; }
__device__ __forceinline__ f32x4 asf(u32x4 a) { return __builtin_bit_cast(f32x4, a); }

__device__ __forceinline__ bf16x4 tr64(unsigned lds_addr) { return __builtin_amdgcn_ds_read_tr16_b64_v4bf16((LAS bf16x4*)(size_t)lds_addr); }
__device__ __forceinline__ bf16x8 cat8(bf16x4 a, bf16x4 b) { return __builtin_shufflevector(a, b, 0, 1, 2, 3, 4, 5, 6, 7); }
__device__ __forceinline__ f32x4 mfma32(bf16x8 a, bf16x8 b, f32x4 c) { return __builtin_amdgcn_mfma_f32_16x16x32_bf16(a, b, c, 0, 0, 0); }
__device__ __forceinline__ f32x4 mfma32u(u32x4 a, u32x4 b, f32x4 c) { return mfma32(__builtin_bit_cast(bf16x8, a), __builtin_bit_cast(bf16x8, b), c); }
__device__ __forceinline__ f32x4 mfma32w(bf16x8 a, u32x4 b, f32x4 c) { return mfma32(a, __builtin_bit_cast(bf16x8, b), c); }
__device__ __forceinline__ f32x4 mfma16(bf16x4 a, u32x2 b, f32x4 c) {
    return __builtin_amdgcn_mfma_f32_16x16x16bf16_1k(__builtin_bit_cast(s16x4, a), __builtin_bit_cast(s16x4, b), c, 0, 0, 0);
}
__device__ __forceinline__ unsigned now32() { return (unsigned)__builtin_amdgcn_s_memrealtime(); }
#define WSL 1
__device__ __forceinline__ void wait_until(unsigned t) {
    asm volatile("s_branch 2f\n1:\n\ts_sleep 2\n2:\n\ts_memrealtime vcc\n\ts_waitcnt lgkmcnt(0)\n\ts_sub_i32 vcc_lo, vcc_lo, %0\n\ts_cmp_lt_i32 vcc_lo, -14\n\ts_cbranch_scc1 1b\n\ts_cmp_lt_i32 vcc_lo, 0\n\ts_cbranch_scc0 4f\n"
                 "3:\n\ts_memrealtime vcc\n\ts_waitcnt lgkmcnt(0)\n\ts_sub_i32 vcc_lo, vcc_lo, %0\n\ts_cmp_lt_i32 vcc_lo, 0\n\ts_cbranch_scc1 3b\n4:" :: "s"(t) : "memory", "scc", "vcc");
}
__device__ __forceinline__ unsigned lds_ld(unsigned addr) { unsigned t; asm volatile("ds_read_b32 %0, %1\n\ts_waitcnt lgkmcnt(0)\n\ts_nop 0" : "=v"(t) : "v"(addr) : "memory"); return __builtin_amdgcn_readfirstlane(t); }
__device__ __forceinline__ void lds_wait_ge(unsigned addr, unsigned v) {
#pragma unroll 1
    for (unsigned spins = 0; spins < (kSpinCap << 6); ++spins) { if (lds_ld(addr) >= v) break; __builtin_amdgcn_s_sleep(1); }
}

#define STAMPS 0
#define STAMP(i) do {} while (0)
#define STAMPV(i, t) do {} while (0)
#define RETRY(i, n) do {} while (0)
#define RETRY_ASM ""
#define RETRY_ASM3 ""
#define RETRY_ASM2 ""

#define CY(v) do {} while (0)
#define CYW(slot, a, b) do {} while (0)
#define CYS(slot, a, b) do {} while (0)
__device__ __forceinline__ int swz(int r) { return r ^ (((r >> 3) & 1) << 2); }
__device__ __forceinline__ f32x2 unpk(unsigned w) { return (f32x2){lo16(w), hi16(w)}; }
__device__ __forceinline__ unsigned pk2v(f32x2 v) { return __builtin_bit_cast(unsigned, __builtin_convertvector(v, bf16x2)); }
__device__ __forceinline__ float frcp(float x) { return __builtin_amdgcn_rcpf(x); }
__device__ __forceinline__ float fexp(float x) { return __builtin_amdgcn_exp2f(x * L2E); }

__device__ __forceinline__ void ln_consts(float mean, float var, float& mb, float& rden) {
    const unsigned mv = pk2(mean, __builtin_amdgcn_fmed3f(var, 0.f, 3.0e38f));
    mb = lo16(mv);
    rden = frcp(bfr(__builtin_amdgcn_sqrtf(bfr(hi16(mv) + 1e-5f))));
}
__device__ __forceinline__ void row_stats(u32x4 a, u32x4 c, int lane, float& mb, float& rden) {
    const float c0 = __uint_as_float((unsigned)__builtin_amdgcn_readfirstlane((int)a[0]) << 16);
    const f32x2 c2 = {c0, c0};
    f32x2 s2 = {0.f, 0.f}, q2 = s2, t2 = s2, u2 = s2;
#pragma unroll
    for (int i = 0; i < 4; ++i) { const f32x2 d = unpk(a[i]) - c2, e = unpk(c[i]) - c2; s2 += d; q2 += d * d; t2 += e; u2 += e * e; }
    float s = s2[0] + s2[1], q = q2[0] + q2[1];
    if (lane < 16) { s += t2[0] + t2[1]; q += u2[0] + u2[1]; }
    s = wave_sum(s); q = wave_sum(q);
    const float ms = s * (1.f / DM);
    ln_consts(c0 + ms, (q - s * ms) * (1.f / DM), mb, rden);
}
__device__ __forceinline__ float red3(f32x4 v) { float z = sw16_add(sw32_add(v[0], v[2]), sw32_add(v[1], v[3])); return z + dpp<0x128>(z); }
__device__ __forceinline__ float red4(f32x4 v) { float z = red3(v); return z + dpp<0x124>(z); }
__device__ __forceinline__ float sum_x4(float v) { v += dpp<0x124>(v); v += dpp<0x128>(v); v = sw16_add(v, v); return sw32_add(v, v); }
__device__ __forceinline__ float dot2(unsigned a, unsigned b, float c) { return __builtin_amdgcn_fdot2_f32_bf16(__builtin_bit_cast(bf16x2, a), __builtin_bit_cast(bf16x2, b), c, false); }
__device__ __forceinline__ int kpos(int r) { return (r & 7) | (((r >> 5) & 3) << 3) | (((r >> 3) & 3) << 5); }
__device__ __forceinline__ int gpos(int r) { const int qt = r / 160, i = r - 160 * qt; return 160 * qt + (i < 128 ? kpos(i) : i); }
__device__ __forceinline__ float vmax3(float a, float b, float c) { float r; asm("v_max3_f32 %0, %1, %2, %3" : "=v"(r) : "v"(a), "v"(b), "v"(c)); return r; }
template <unsigned LO, unsigned HI> __device__ __forceinline__ void st16_m(void* sbase, unsigned voff, u32x4 v) {
    asm volatile("s_mov_b32 exec_lo, %3\n\ts_mov_b32 exec_hi, %4\n\tglobal_store_dwordx4 %0, %1, %2 sc1\n\ts_mov_b64 exec, -1\n\ts_nop 0" :: "v"(voff), "v"(v), "s"(sbase), "i"(LO), "i"(HI) : "memory");
}
template <unsigned LO, unsigned HI> __device__ __forceinline__ void st4_m(void* sbase, unsigned voff, unsigned v) {
    asm volatile("s_mov_b32 exec_lo, %3\n\ts_mov_b32 exec_hi, %4\n\tglobal_store_dword %0, %1, %2 sc1\n\ts_mov_b64 exec, -1" :: "v"(voff), "v"(v), "s"(sbase), "i"(LO), "i"(HI) : "memory");
}

#define GATHER(sid, LOADS, OK) do { LOADS; if (__builtin_expect(!(OK), 0)) { for (unsigned spins_ = 1; spins_ < kSpinCap; ++spins_) { RETRY(sid, spins_); __builtin_amdgcn_s_sleep(1); LOADS; if (OK) break; } } } while (0)
#define SCH(i) (sched_c[i])
#define PRIO_MODE 0
#define TAILN 8
#define ABL 0
#define DIRECT 0
#define GMODE 0
#define GP_ 0
#define GID blockIdx.x
#define H1ROW 1
#define P_BA 3
#define WO_AT 7

#define ASM_QK \
    "ds_read_b128 v[48:51], %1 offset:0\n\t" \
    "ds_read_b128 v[52:55], %1 offset:128\n\t" \
    "ds_read_b128 v[56:59], %1 offset:256\n\t" \
    "v_mov_b32 v32, 0\n\t" \
    "v_mov_b32 v33, 0\n\t" \
    "v_mov_b32 v36, 0\n\t" \
    "v_mov_b32 v37, 0\n\t" \
    "v_mov_b32 v40, 0\n\t" \
    "v_mov_b32 v41, 0\n\t" \
    "v_mov_b32 v44, 0\n\t" \
    "v_mov_b32 v45, 0\n\t" \
    "s_waitcnt lgkmcnt(2)\n\t" \
    "v_smfmac_f32_16x16x64_bf16 v[32:35], v[48:51], v[64:71], %2\n\t" \
    "v_smfmac_f32_16x16x64_bf16 v[36:39], v[48:51], v[88:95], %2\n\t" \
    "v_smfmac_f32_16x16x64_bf16 v[40:43], v[48:51], v[112:119], %2\n\t" \
    "v_smfmac_f32_16x16x64_bf16 v[44:47], v[48:51], v[136:143], %2\n\t" \
    "s_waitcnt lgkmcnt(1)\n\t" \
    "v_smfmac_f32_16x16x64_bf16 v[32:35], v[52:55], v[72:79], %2\n\t" \
    "v_smfmac_f32_16x16x64_bf16 v[36:39], v[52:55], v[96:103], %2\n\t" \
    "v_smfmac_f32_16x16x64_bf16 v[40:43], v[52:55], v[120:127], %2\n\t" \
    "v_smfmac_f32_16x16x64_bf16 v[44:47], v[52:55], v[144:151], %2\n\t" \
    "s_waitcnt lgkmcnt(0)\n\t" \
    "v_smfmac_f32_16x16x64_bf16 v[32:35], v[56:59], v[80:87], %2\n\t" \
    "v_smfmac_f32_16x16x64_bf16 v[36:39], v[56:59], v[104:111], %2\n\t" \
    "v_smfmac_f32_16x16x64_bf16 v[40:43], v[56:59], v[128:135], %2\n\t" \
    "v_smfmac_f32_16x16x64_bf16 v[44:47], v[56:59], v[152:159], %2\n\t" \
    "s_nop 4\n\t" \
    "v_add_f32_dpp %0, v32, v33 quad_perm:[0,1,2,3] row_mask:0x1 bank_mask:0xf\n\t" \
    "v_add_f32_dpp %0, v36, v37 quad_perm:[0,1,2,3] row_mask:0x2 bank_mask:0xf\n\t" \
    "v_add_f32_dpp %0, v40, v41 quad_perm:[0,1,2,3] row_mask:0x4 bank_mask:0xf\n\t" \
    "v_add_f32_dpp %0, v44, v45 quad_perm:[0,1,2,3] row_mask:0x8 bank_mask:0xf\n\t"
#define ASM_PVB0 \
    "s_nop 1\n\t" \
    "v_smfmac_f32_16x16x64_bf16 %0, %1, v[160:167], %6 abid:1\n\t" \
    "v_smfmac_f32_16x16x64_bf16 %0, %2, v[168:175], %6 abid:1\n\t" \
    "v_smfmac_f32_16x16x64_bf16 %0, %3, v[176:183], %6 abid:1\n\t" \
    "v_smfmac_f32_16x16x64_bf16 %0, %4, v[184:191], %6 abid:1\n\t" \
    "s_nop 7\n\t" \
    "ds_write_b128 %5, %0 offset:0\n\t"
#define ASM_PVB1 \
    "s_nop 1\n\t" \
    "v_smfmac_f32_16x16x64_bf16 %0, %1, v[192:199], %6 abid:1\n\t" \
    "v_smfmac_f32_16x16x64_bf16 %0, %2, v[200:207], %6 abid:1\n\t" \
    "v_smfmac_f32_16x16x64_bf16 %0, %3, v[208:215], %6 abid:1\n\t" \
    "v_smfmac_f32_16x16x64_bf16 %0, %4, v[216:223], %6 abid:1\n\t" \
    "s_nop 7\n\t" \
    "ds_write_b128 %5, %0 offset:1024\n\t"
#define ASM_PVB2 \
    "s_nop 1\n\t" \
    "v_smfmac_f32_16x16x64_bf16 %0, %1, v[224:231], %6 abid:1\n\t" \
    "v_smfmac_f32_16x16x64_bf16 %0, %2, v[232:239], %6 abid:1\n\t" \
    "v_smfmac_f32_16x16x64_bf16 %0, %3, v[240:247], %6 abid:1\n\t" \
    "v_smfmac_f32_16x16x64_bf16 %0, %4, v[248:255], %6 abid:1\n\t" \
    "s_nop 7\n\t" \
    "ds_write_b128 %5, %0 offset:2048\n\t"
#define ASM_PV20 \
    "v_smfmac_f32_16x16x64_bf16 %0, %1, v[224:231], %6 abid:1\n\t"
#define ASM_PV21 \
    "v_smfmac_f32_16x16x64_bf16 %0, %2, v[232:239], %6 abid:1\n\t"
#define ASM_PV22 \
    "v_smfmac_f32_16x16x64_bf16 %0, %3, v[240:247], %6 abid:1\n\t"
#define ASM_PV23 \
    "v_smfmac_f32_16x16x64_bf16 %0, %4, v[248:255], %6 abid:1\n\t"
#define ASM_ATT_A \
    "s_setprio 2\n\t" \
    "v_mov_b32 v28, 0\n\t" \
    "v_mov_b32 v29, 0\n\t" \
    "v_mov_b32 v32, 0\n\t" \
    "v_mov_b32 v33, 0\n\t" \
    "v_mov_b32 v36, 0\n\t" \
    "v_mov_b32 v37, 0\n\t" \
    "v_mov_b32 v40, 0\n\t" \
    "v_mov_b32 v41, 0\n\t" \
    "v_mov_b32 v48, 0\n\t" \
    "v_mov_b32 v49, 0\n\t" \
    "v_mov_b32 v50, 0\n\t" \
    "v_mov_b32 v51, 0\n\t" \
    "v_mov_b32 v52, 0\n\t" \
    "v_mov_b32 v53, 0\n\t" \
    "v_mov_b32 v54, 0\n\t" \
    "v_mov_b32 v55, 0\n\t" \
    "v_mov_b32 v56, 0\n\t" \
    "v_mov_b32 v57, 0\n\t" \
    "v_mov_b32 v58, 0\n\t" \
    "v_mov_b32 v59, 0\n\t" \
    "v_mov_b32 v16, 0\n\t" \
    "v_mov_b32 v17, 0\n\t" \
    "v_mov_b32 v18, 0\n\t" \
    "v_mov_b32 v19, 0\n\t" \
    "v_mov_b32 v20, %[ma]\n\t" \
    "s_cmp_eq_u32 %[tz], 0\n\t" \
    "s_cbranch_scc1 23f\n\t" \
    "s_branch 22f\n\t" \
    "21:\n\t" \
    "s_sleep 3\n\t" \
    "22:\n\t" \
    "s_memrealtime vcc\n\t" \
    "s_waitcnt lgkmcnt(0)\n\t" \
    "s_sub_i32 vcc_lo, vcc_lo, %[tz]\n\t" \
    "s_cmp_lt_i32 vcc_lo, 0\n\t" \
    "s_cbranch_scc1 21b\n\t" \
    "23:\n\t" \
    "s_mov_b32 vcc_hi, 0x400000\n\t" \
    "0:\n\t" \
    "ds_read_b32 v21, v20 offset:340\n\t" \
    "s_sub_u32 vcc_hi, vcc_hi, 1\n\t" \
    "s_cbranch_scc1 4f\n\t" \
    "s_waitcnt lgkmcnt(0)\n\t" \
    "v_readfirstlane_b32 %[st], v21\n\t" \
    "s_cmp_eq_u32 %[st], 0\n\t" \
    "s_cbranch_scc1 0b\n\t" \
    "s_add_u32 %[st], %[st], %[tg]\n\t" \
    "4:\n\t" \
    "s_memrealtime vcc\n\t" \
    "s_waitcnt lgkmcnt(0)\n\t" \
    "s_sub_i32 vcc_lo, vcc_lo, %[st]\n\t" \
    "s_cmp_lt_i32 vcc_lo, -4000\n\t" \
    "s_cbranch_scc1 10f\n\t" \
    "s_cmp_lt_i32 vcc_lo, 0\n\t" \
    "s_cbranch_scc1 4b\n\t" \
    "10:\n\t" \
    "s_mov_b32 exec_hi, 0xff\n\t" \
    "global_load_dwordx4 v[16:19], %[go], %[ws] sc1\n\t" \
    "s_mov_b32 exec_hi, -1\n\t" \
    "s_barrier\n\t" \
    "ds_read_b64 v[22:23], v20 offset:352\n\t" \
    "ds_read_b32 v24, %[cs]\n\t" \
    "s_mov_b32 %[st], 0x10000\n\t" \
    "s_waitcnt lgkmcnt(0)\n\t" \
    "v_mul_f32 v22, v22, v24\n\t" \
    "v_mul_f32_e64 v22, -v22, v23\n\t" \
    "5:\n\t" \
    "s_waitcnt vmcnt(0)\n\t" \
    "v_max3_u32 v24, v16, v17, v18\n\t" \
    "v_max_u32 v24, v24, v19\n\t" \
    "v_cmp_eq_u32 vcc, -1, v24\n\t" \
    "v_permlane32_swap_b32 v16, v18\n\t" \
    "v_permlane32_swap_b32 v17, v19\n\t" \
    "v_pk_add_f32 v[16:17], v[16:17], v[18:19]\n\t" \
    "s_nop 0\n\t" \
    "s_cbranch_vccnz 6f\n\t" \
    "7:\n\t" \
    "v_permlane16_swap_b32 v16, v17\n\t" \
    "v_add_f32 v16, v16, v17\n\t" \
    "s_nop 1\n\t" \
    "v_add_f32_dpp v16, v16, v16 row_ror:8 row_mask:0xf bank_mask:0xf\n\t" \
    "v_fmac_f32 v22, v16, v23\n\t" \
    "v_cvt_pk_bf16_f32 v22, v22, v22\n\t" \
    "ds_write_b16 %[qo], v22\n\t" \
    "s_waitcnt lgkmcnt(0)\n\t" \
    "s_barrier\n\t" \
    "ds_read_b128 v[16:19], %[qa] offset:0\n\t" \
    "ds_read_b128 v[20:23], %[qa] offset:128\n\t" \
    "ds_read_b128 v[24:27], %[qa] offset:256\n\t" \
    "s_waitcnt lgkmcnt(2)\n\t" \
    "v_smfmac_f32_16x16x64_bf16 v[28:31], v[16:19], v[64:71], v7\n\t" \
    "v_smfmac_f32_16x16x64_bf16 v[32:35], v[16:19], v[88:95], v7\n\t" \
    "v_smfmac_f32_16x16x64_bf16 v[36:39], v[16:19], v[112:119], v7\n\t" \
    "v_smfmac_f32_16x16x64_bf16 v[40:43], v[16:19], v[136:143], v7\n\t" \
    "s_waitcnt lgkmcnt(1)\n\t" \
    "v_smfmac_f32_16x16x64_bf16 v[28:31], v[20:23], v[72:79], v7\n\t" \
    "v_smfmac_f32_16x16x64_bf16 v[32:35], v[20:23], v[96:103], v7\n\t" \
    "v_smfmac_f32_16x16x64_bf16 v[36:39], v[20:23], v[120:127], v7\n\t" \
    "v_smfmac_f32_16x16x64_bf16 v[40:43], v[20:23], v[144:151], v7\n\t" \
    "s_waitcnt lgkmcnt(0)\n\t" \
    "v_smfmac_f32_16x16x64_bf16 v[28:31], v[24:27], v[80:87], v7\n\t" \
    "v_smfmac_f32_16x16x64_bf16 v[32:35], v[24:27], v[104:111], v7\n\t" \
    "v_smfmac_f32_16x16x64_bf16 v[36:39], v[24:27], v[128:135], v7\n\t" \
    "v_smfmac_f32_16x16x64_bf16 v[40:43], v[24:27], v[152:159], v7\n\t" \
    "v_mov_b32 v63, %[mlw]\n\t" \
    "s_nop 3\n\t" \
    "v_add_f32_dpp v60, v28, v29 quad_perm:[0,1,2,3] row_mask:0x1 bank_mask:0xf\n\t" \
    "v_add_f32_dpp v60, v32, v33 quad_perm:[0,1,2,3] row_mask:0x2 bank_mask:0xf\n\t" \
    "v_add_f32_dpp v60, v36, v37 quad_perm:[0,1,2,3] row_mask:0x4 bank_mask:0xf\n\t" \
    "v_add_f32_dpp v60, v40, v41 quad_perm:[0,1,2,3] row_mask:0x8 bank_mask:0xf\n\t" \
    "v_mul_f32 v60, 0x3dd53b94, v60\n\t" \
    "s_nop 1\n\t" \
    "v_max_f32_dpp v61, v60, v60 quad_perm:[1,0,3,2] row_mask:0xf bank_mask:0xf\n\t" \
    "s_nop 1\n\t" \
    "v_max_f32_dpp v61, v61, v61 quad_perm:[2,3,0,1] row_mask:0xf bank_mask:0xf\n\t" \
    "s_nop 1\n\t" \
    "v_max_f32_dpp v61, v61, v61 row_half_mirror row_mask:0xf bank_mask:0xf\n\t" \
    "s_nop 1\n\t" \
    "v_max_f32_dpp v61, v61, v61 row_mirror row_mask:0xf bank_mask:0xf\n\t" \
    "s_nop 1\n\t" \
    "v_max_f32_dpp v61, v61, v61 row_bcast:15 row_mask:0xa bank_mask:0xf\n\t" \
    "s_nop 1\n\t" \
    "v_max_f32_dpp v61, v61, v61 row_bcast:31 row_mask:0xc bank_mask:0xf\n\t" \
    "s_nop 0\n\t" \
    "v_readlane_b32 %[st], v61, 63\n\t" \
    "s_mov_b32 exec_lo, 0\n\t" \
    "s_brev_b32 exec_hi, 1\n\t" \
    "ds_write_b32 v63, v61\n\t" \
    "s_mov_b64 exec, -1\n\t" \
    "v_subrev_f32 v62, %[st], v60\n\t" \
    "v_exp_f32 v62, v62\n\t" \
    "s_nop 0\n\t" \
    "v_cvt_pk_bf16_f32 v60, v62, v62\n\t" \
    "ds_write_b16 %[pw], v60\n\t" \
    "ds_write_b16 %[pw], v60 offset:20\n\t" \
    "ds_read_b128 v[16:19], %[pr] offset:0\n\t" \
    "ds_read_b128 v[20:23], %[pr] offset:272\n\t" \
    "ds_read_b128 v[24:27], %[pr] offset:544\n\t" \
    "ds_read_b128 v[28:31], %[pr] offset:816\n\t" \
    "v_mov_b32 v60, %[ma]\n\t" \
    "s_setprio 0\n\t" \
    "v_add_f32_dpp v62, v62, v62 quad_perm:[1,0,3,2] row_mask:0xf bank_mask:0xf\n\t" \
    "s_nop 1\n\t" \
    "v_add_f32_dpp v62, v62, v62 quad_perm:[2,3,0,1] row_mask:0xf bank_mask:0xf\n\t" \
    "s_nop 1\n\t" \
    "v_add_f32_dpp v62, v62, v62 row_half_mirror row_mask:0xf bank_mask:0xf\n\t" \
    "s_nop 1\n\t" \
    "v_add_f32_dpp v62, v62, v62 row_mirror row_mask:0xf bank_mask:0xf\n\t" \
    "s_waitcnt lgkmcnt(3)\n\t" \
    "v_smfmac_f32_16x16x64_bf16 v[48:51], v[16:19], v[160:167], v7 abid:1\n\t" \
    "v_smfmac_f32_16x16x64_bf16 v[52:55], v[16:19], v[192:199], v7 abid:1\n\t" \
    "v_smfmac_f32_16x16x64_bf16 v[56:59], v[16:19], v[224:231], v7 abid:1\n\t" \
    "v_add_f32_dpp v62, v62, v62 row_bcast:15 row_mask:0xa bank_mask:0xf\n\t" \
    "s_waitcnt lgkmcnt(2)\n\t" \
    "v_smfmac_f32_16x16x64_bf16 v[48:51], v[20:23], v[168:175], v7 abid:1\n\t" \
    "v_smfmac_f32_16x16x64_bf16 v[52:55], v[20:23], v[200:207], v7 abid:1\n\t" \
    "v_smfmac_f32_16x16x64_bf16 v[56:59], v[20:23], v[232:239], v7 abid:1\n\t" \
    "v_add_f32_dpp v62, v62, v62 row_bcast:31 row_mask:0xc bank_mask:0xf\n\t" \
    "s_waitcnt lgkmcnt(1)\n\t" \
    "v_smfmac_f32_16x16x64_bf16 v[48:51], v[24:27], v[176:183], v7 abid:1\n\t" \
    "v_smfmac_f32_16x16x64_bf16 v[52:55], v[24:27], v[208:215], v7 abid:1\n\t" \
    "v_smfmac_f32_16x16x64_bf16 v[56:59], v[24:27], v[240:247], v7 abid:1\n\t" \
    "s_waitcnt lgkmcnt(0)\n\t" \
    "v_smfmac_f32_16x16x64_bf16 v[48:51], v[28:31], v[184:191], v7 abid:1\n\t" \
    "v_smfmac_f32_16x16x64_bf16 v[52:55], v[28:31], v[216:223], v7 abid:1\n\t" \
    "v_smfmac_f32_16x16x64_bf16 v[56:59], v[28:31], v[248:255], v7 abid:1\n\t" \
    "s_barrier\n\t" \
    "ds_read_b128 v[16:19], v60\n\t" \
    "ds_read_b128 v[20:23], v60 offset:16\n\t" \
    "ds_read_b32 v24, %[mo]\n\t" \
    "s_mov_b32 exec_lo, 0\n\t" \
    "s_brev_b32 exec_hi, 1\n\t" \
    "ds_write_b32 v63, v62 offset:32\n\t" \
    "s_mov_b64 exec, -1\n\t" \
    "v_mov_b32_dpp v48, v52 quad_perm:[0,1,2,3] row_mask:0xc bank_mask:0xf\n\t" \
    "v_mov_b32_dpp v49, v53 quad_perm:[0,1,2,3] row_mask:0xc bank_mask:0xf\n\t" \
    "v_mov_b32_dpp v50, v54 quad_perm:[0,1,2,3] row_mask:0xc bank_mask:0xf\n\t" \
    "v_mov_b32_dpp v51, v55 quad_perm:[0,1,2,3] row_mask:0xc bank_mask:0xf\n\t" \
    "ds_write_b128 %[ow], v[48:51] offset:0\n\t" \
    "s_mov_b32 exec_lo, 0x00ff00ff\n\t" \
    "s_mov_b32 exec_hi, 0xff00ff00\n\t" \
    "ds_write_b128 %[ow], v[56:59] offset:2048\n\t" \
    "s_mov_b64 exec, -1\n\t" \
    "s_waitcnt lgkmcnt(3)\n\t" \
    "v_max3_f32 v16, v16, v17, v18\n\t" \
    "v_max3_f32 v19, v19, v20, v21\n\t" \
    "v_max3_f32 v16, v16, v22, v23\n\t" \
    "v_max_f32 v26, v16, v19\n\t" \
    "v_sub_f32 v60, v24, v26\n\t" \
    "v_exp_f32 v60, v60\n\t" \
    "s_nop 0\n\t" \
    "ds_write2_b32 %[mo], v60, v26 offset0:96 offset1:104\n\t" \
    "s_waitcnt lgkmcnt(0)\n\t" \
    "s_barrier\n\t" \
    "ds_read_b128 v[16:19], %[r0]\n\t" \
    "ds_read_b128 v[20:23], %[r0] offset:512\n\t" \
    "s_waitcnt lgkmcnt(0)\n\t" \
    "v_pk_add_f32 v[16:17], v[16:17], v[20:21]\n\t" \
    "v_pk_add_f32 v[18:19], v[18:19], v[22:23]\n\t" \
    "v_pk_mul_f32 v[16:17], v[16:17], v[60:61] op_sel_hi:[1,0]\n\t" \
    "v_pk_mul_f32 v[18:19], v[18:19], v[60:61] op_sel_hi:[1,0]\n\t" \
    "s_nop 1\n\t" \
    "v_permlane32_swap_b32 v16, v18\n\t" \
    "v_permlane32_swap_b32 v17, v19\n\t" \
    "v_pk_add_f32 v[16:17], v[16:17], v[18:19]\n\t" \
    "s_nop 1\n\t" \
    "v_permlane16_swap_b32 v16, v17\n\t" \
    "v_add_f32 v16, v16, v17\n\t" \
    "s_nop 1\n\t" \
    "v_add_f32_dpp v17, v16, v16 row_ror:8 row_mask:0xf bank_mask:0xf\n\t" \
    "v_add_f32_dpp v16, v16, v16 row_ror:8 row_mask:0xf bank_mask:0xf\n\t" \
    "s_nop 1\n\t" \
    "v_permlane16_swap_b32 v16, v17\n\t" \
    "s_mov_b32 exec_lo, 0xff\n\t" \
    "v_cvt_pk_bf16_f32 v16, v16, v17\n\t" \
    "s_mov_b32 exec_hi, 0xff\n\t" \
    "v_min_u32 v16, -2, v16\n\t" \
    "global_store_dword %[so], v16, %[ws] sc1\n\t" \
    "s_mov_b64 exec, -1\n\t" \
    "s_branch 2f\n\t" \
    "6:\n\t" \
    "s_sub_u32 %[st], %[st], 1\n\t" \
    "s_cbranch_scc1 7b\n\t" \
    RETRY_ASM \
    "v_mov_b32 v16, 0\n\t" \
    "v_mov_b32 v17, 0\n\t" \
    "v_mov_b32 v18, 0\n\t" \
    "v_mov_b32 v19, 0\n\t" \
    "s_mov_b32 exec_hi, 0xff\n\t" \
    "global_load_dwordx4 v[16:19], %[go], %[ws] sc1\n\t" \
    "s_mov_b32 exec_hi, -1\n\t" \
    "s_branch 5b\n\t" \
    "2:\n\t"
#define ASM_ATT_B \
    "s_setprio 1\n\t" \
    "v_mov_b32 v28, 0\n\t" \
    "v_mov_b32 v29, 0\n\t" \
    "v_mov_b32 v32, 0\n\t" \
    "v_mov_b32 v33, 0\n\t" \
    "v_mov_b32 v36, 0\n\t" \
    "v_mov_b32 v37, 0\n\t" \
    "v_mov_b32 v40, 0\n\t" \
    "v_mov_b32 v41, 0\n\t" \
    "v_mov_b32 v48, 0\n\t" \
    "v_mov_b32 v49, 0\n\t" \
    "v_mov_b32 v50, 0\n\t" \
    "v_mov_b32 v51, 0\n\t" \
    "v_mov_b32 v52, 0\n\t" \
    "v_mov_b32 v53, 0\n\t" \
    "v_mov_b32 v54, 0\n\t" \
    "v_mov_b32 v55, 0\n\t" \
    "v_mov_b32 v56, 0\n\t" \
    "v_mov_b32 v57, 0\n\t" \
    "v_mov_b32 v58, 0\n\t" \
    "v_mov_b32 v59, 0\n\t" \
    "v_mov_b32 v44, -1\n\t" \
    "v_mov_b32 v45, -1\n\t" \
    "v_mov_b32 v46, -1\n\t" \
    "v_mov_b32 v47, -1\n\t" \
    "s_cmp_gt_u32 %[wv], 5\n\t" \
    "s_cbranch_scc0 8f\n\t" \
    "s_cmp_eq_u32 %[wv], 7\n\t" \
    "s_cbranch_scc1 9f\n\t" \
    "s_barrier\n\t" \
    "s_branch 9f\n\t" \
    "8:\n\t" \
    "v_mov_b32 v16, 0\n\t" \
    "v_mov_b32 v17, 0\n\t" \
    "v_mov_b32 v18, 0\n\t" \
    "v_mov_b32 v19, 0\n\t" \
    "v_mov_b32 v20, %[ma]\n\t" \
    "s_cmp_eq_u32 %[tz], 0\n\t" \
    "s_cbranch_scc1 23f\n\t" \
    "s_branch 22f\n\t" \
    "21:\n\t" \
    "s_sleep 3\n\t" \
    "22:\n\t" \
    "s_memrealtime vcc\n\t" \
    "s_waitcnt lgkmcnt(0)\n\t" \
    "s_sub_i32 vcc_lo, vcc_lo, %[tz]\n\t" \
    "s_cmp_lt_i32 vcc_lo, 0\n\t" \
    "s_cbranch_scc1 21b\n\t" \
    "23:\n\t" \
    "s_mov_b32 vcc_hi, 0x400000\n\t" \
    "0:\n\t" \
    "ds_read_b32 v21, v20 offset:340\n\t" \
    "s_sub_u32 vcc_hi, vcc_hi, 1\n\t" \
    "s_cbranch_scc1 4f\n\t" \
    "s_waitcnt lgkmcnt(0)\n\t" \
    "v_readfirstlane_b32 %[st], v21\n\t" \
    "s_cmp_eq_u32 %[st], 0\n\t" \
    "s_cbranch_scc1 0b\n\t" \
    "s_add_u32 %[st], %[st], %[tg]\n\t" \
    "4:\n\t" \
    "s_memrealtime vcc\n\t" \
    "s_waitcnt lgkmcnt(0)\n\t" \
    "s_sub_i32 vcc_lo, vcc_lo, %[st]\n\t" \
    "s_cmp_lt_i32 vcc_lo, -4000\n\t" \
    "s_cbranch_scc1 10f\n\t" \
    "s_cmp_lt_i32 vcc_lo, 0\n\t" \
    "s_cbranch_scc1 4b\n\t" \
    "10:\n\t" \
    "s_mov_b32 exec_hi, 0xff\n\t" \
    "global_load_dwordx4 v[16:19], %[go], %[ws] sc1\n\t" \
    "s_mov_b32 exec_hi, -1\n\t" \
    "s_barrier\n\t" \
    "ds_read_b64 v[22:23], v20 offset:352\n\t" \
    "ds_read_b32 v24, %[cs]\n\t" \
    "s_mov_b32 %[st], 0x10000\n\t" \
    "s_waitcnt lgkmcnt(0)\n\t" \
    "v_mul_f32 v22, v22, v24\n\t" \
    "v_mul_f32_e64 v22, -v22, v23\n\t" \
    "5:\n\t" \
    "s_waitcnt vmcnt(0)\n\t" \
    "v_max3_u32 v24, v16, v17, v18\n\t" \
    "v_max_u32 v24, v24, v19\n\t" \
    "v_cmp_eq_u32 vcc, -1, v24\n\t" \
    "v_permlane32_swap_b32 v16, v18\n\t" \
    "v_permlane32_swap_b32 v17, v19\n\t" \
    "v_pk_add_f32 v[16:17], v[16:17], v[18:19]\n\t" \
    "s_nop 0\n\t" \
    "s_cbranch_vccnz 6f\n\t" \
    "7:\n\t" \
    "v_permlane16_swap_b32 v16, v17\n\t" \
    "v_add_f32 v16, v16, v17\n\t" \
    "s_nop 1\n\t" \
    "v_add_f32_dpp v16, v16, v16 row_ror:8 row_mask:0xf bank_mask:0xf\n\t" \
    "v_fmac_f32 v22, v16, v23\n\t" \
    "v_cvt_pk_bf16_f32 v22, v22, v22\n\t" \
    "ds_write_b16 %[qo], v22\n\t" \
    "9:\n\t" \
    "s_waitcnt lgkmcnt(0)\n\t" \
    "s_barrier\n\t" \
    "ds_read_b128 v[16:19], %[qa] offset:0\n\t" \
    "ds_read_b128 v[20:23], %[qa] offset:128\n\t" \
    "ds_read_b128 v[24:27], %[qa] offset:256\n\t" \
    "s_cmp_eq_u32 %[cl], 0\n\t" \
    "s_cbranch_scc1 3f\n\t" \
    "global_store_dwordx4 %[go], v[44:47], %[ws] offset:0 sc1\n\t" \
    "global_store_dwordx4 %[go], v[44:47], %[ws] offset:1024 sc1\n\t" \
    "global_store_dwordx4 %[go], v[44:47], %[ws] offset:2048 sc1\n\t" \
    "3:\n\t" \
    "s_waitcnt lgkmcnt(2)\n\t" \
    "v_smfmac_f32_16x16x64_bf16 v[28:31], v[16:19], v[64:71], v7\n\t" \
    "v_smfmac_f32_16x16x64_bf16 v[32:35], v[16:19], v[88:95], v7\n\t" \
    "v_smfmac_f32_16x16x64_bf16 v[36:39], v[16:19], v[112:119], v7\n\t" \
    "v_smfmac_f32_16x16x64_bf16 v[40:43], v[16:19], v[136:143], v7\n\t" \
    "s_waitcnt lgkmcnt(1)\n\t" \
    "v_smfmac_f32_16x16x64_bf16 v[28:31], v[20:23], v[72:79], v7\n\t" \
    "v_smfmac_f32_16x16x64_bf16 v[32:35], v[20:23], v[96:103], v7\n\t" \
    "v_smfmac_f32_16x16x64_bf16 v[36:39], v[20:23], v[120:127], v7\n\t" \
    "v_smfmac_f32_16x16x64_bf16 v[40:43], v[20:23], v[144:151], v7\n\t" \
    "s_waitcnt lgkmcnt(0)\n\t" \
    "v_smfmac_f32_16x16x64_bf16 v[28:31], v[24:27], v[80:87], v7\n\t" \
    "v_smfmac_f32_16x16x64_bf16 v[32:35], v[24:27], v[104:111], v7\n\t" \
    "v_smfmac_f32_16x16x64_bf16 v[36:39], v[24:27], v[128:135], v7\n\t" \
    "v_smfmac_f32_16x16x64_bf16 v[40:43], v[24:27], v[152:159], v7\n\t" \
    "v_mov_b32 v63, %[mlw]\n\t" \
    "s_nop 3\n\t" \
    "v_add_f32_dpp v60, v28, v29 quad_perm:[0,1,2,3] row_mask:0x1 bank_mask:0xf\n\t" \
    "v_add_f32_dpp v60, v32, v33 quad_perm:[0,1,2,3] row_mask:0x2 bank_mask:0xf\n\t" \
    "v_add_f32_dpp v60, v36, v37 quad_perm:[0,1,2,3] row_mask:0x4 bank_mask:0xf\n\t" \
    "v_add_f32_dpp v60, v40, v41 quad_perm:[0,1,2,3] row_mask:0x8 bank_mask:0xf\n\t" \
    "v_mul_f32 v60, 0x3dd53b94, v60\n\t" \
    "s_nop 1\n\t" \
    "v_max_f32_dpp v61, v60, v60 quad_perm:[1,0,3,2] row_mask:0xf bank_mask:0xf\n\t" \
    "s_nop 1\n\t" \
    "v_max_f32_dpp v61, v61, v61 quad_perm:[2,3,0,1] row_mask:0xf bank_mask:0xf\n\t" \
    "s_nop 1\n\t" \
    "v_max_f32_dpp v61, v61, v61 row_half_mirror row_mask:0xf bank_mask:0xf\n\t" \
    "s_nop 1\n\t" \
    "v_max_f32_dpp v61, v61, v61 row_mirror row_mask:0xf bank_mask:0xf\n\t" \
    "s_nop 1\n\t" \
    "v_max_f32_dpp v61, v61, v61 row_bcast:15 row_mask:0xa bank_mask:0xf\n\t" \
    "s_nop 1\n\t" \
    "v_max_f32_dpp v61, v61, v61 row_bcast:31 row_mask:0xc bank_mask:0xf\n\t" \
    "s_nop 0\n\t" \
    "v_readlane_b32 %[st], v61, 63\n\t" \
    "s_mov_b32 exec_lo, 0\n\t" \
    "s_brev_b32 exec_hi, 1\n\t" \
    "ds_write_b32 v63, v61\n\t" \
    "s_mov_b64 exec, -1\n\t" \
    "v_subrev_f32 v62, %[st], v60\n\t" \
    "v_exp_f32 v62, v62\n\t" \
    "s_nop 0\n\t" \
    "v_cvt_pk_bf16_f32 v60, v62, v62\n\t" \
    "ds_write_b16 %[pw], v60\n\t" \
    "ds_write_b16 %[pw], v60 offset:20\n\t" \
    "ds_read_b128 v[16:19], %[pr] offset:0\n\t" \
    "ds_read_b128 v[20:23], %[pr] offset:272\n\t" \
    "ds_read_b128 v[24:27], %[pr] offset:544\n\t" \
    "ds_read_b128 v[28:31], %[pr] offset:816\n\t" \
    "v_mov_b32 v60, %[ma]\n\t" \
    "s_setprio 0\n\t" \
    "v_add_f32_dpp v62, v62, v62 quad_perm:[1,0,3,2] row_mask:0xf bank_mask:0xf\n\t" \
    "s_nop 1\n\t" \
    "v_add_f32_dpp v62, v62, v62 quad_perm:[2,3,0,1] row_mask:0xf bank_mask:0xf\n\t" \
    "s_nop 1\n\t" \
    "v_add_f32_dpp v62, v62, v62 row_half_mirror row_mask:0xf bank_mask:0xf\n\t" \
    "s_nop 1\n\t" \
    "v_add_f32_dpp v62, v62, v62 row_mirror row_mask:0xf bank_mask:0xf\n\t" \
    "s_waitcnt lgkmcnt(3)\n\t" \
    "s_barrier\n\t" \
    "v_smfmac_f32_16x16x64_bf16 v[48:51], v[16:19], v[160:167], v7 abid:1\n\t" \
    "v_smfmac_f32_16x16x64_bf16 v[52:55], v[16:19], v[192:199], v7 abid:1\n\t" \
    "v_smfmac_f32_16x16x64_bf16 v[56:59], v[16:19], v[224:231], v7 abid:1\n\t" \
    "v_add_f32_dpp v62, v62, v62 row_bcast:15 row_mask:0xa bank_mask:0xf\n\t" \
    "s_waitcnt lgkmcnt(2)\n\t" \
    "v_smfmac_f32_16x16x64_bf16 v[48:51], v[20:23], v[168:175], v7 abid:1\n\t" \
    "v_smfmac_f32_16x16x64_bf16 v[52:55], v[20:23], v[200:207], v7 abid:1\n\t" \
    "v_smfmac_f32_16x16x64_bf16 v[56:59], v[20:23], v[232:239], v7 abid:1\n\t" \
    "v_add_f32_dpp v62, v62, v62 row_bcast:31 row_mask:0xc bank_mask:0xf\n\t" \
    "s_waitcnt lgkmcnt(1)\n\t" \
    "v_smfmac_f32_16x16x64_bf16 v[48:51], v[24:27], v[176:183], v7 abid:1\n\t" \
    "v_smfmac_f32_16x16x64_bf16 v[52:55], v[24:27], v[208:215], v7 abid:1\n\t" \
    "v_smfmac_f32_16x16x64_bf16 v[56:59], v[24:27], v[240:247], v7 abid:1\n\t" \
    "s_waitcnt lgkmcnt(0)\n\t" \
    "v_smfmac_f32_16x16x64_bf16 v[48:51], v[28:31], v[184:191], v7 abid:1\n\t" \
    "v_smfmac_f32_16x16x64_bf16 v[52:55], v[28:31], v[216:223], v7 abid:1\n\t" \
    "v_smfmac_f32_16x16x64_bf16 v[56:59], v[28:31], v[248:255], v7 abid:1\n\t" \
    "s_mov_b32 exec_lo, 0\n\t" \
    "s_brev_b32 exec_hi, 1\n\t" \
    "ds_write_b32 v63, v62 offset:32\n\t" \
    "s_mov_b64 exec, -1\n\t" \
    "s_cmp_gt_u32 %[wv], 5\n\t" \
    "s_nop 1\n\t" \
    "v_mov_b32_dpp v48, v52 quad_perm:[0,1,2,3] row_mask:0xc bank_mask:0xf\n\t" \
    "v_mov_b32_dpp v49, v53 quad_perm:[0,1,2,3] row_mask:0xc bank_mask:0xf\n\t" \
    "v_mov_b32_dpp v50, v54 quad_perm:[0,1,2,3] row_mask:0xc bank_mask:0xf\n\t" \
    "v_mov_b32_dpp v51, v55 quad_perm:[0,1,2,3] row_mask:0xc bank_mask:0xf\n\t" \
    "ds_write_b128 %[ow], v[48:51] offset:0\n\t" \
    "s_mov_b32 exec_lo, 0x00ff00ff\n\t" \
    "s_mov_b32 exec_hi, 0xff00ff00\n\t" \
    "ds_write_b128 %[ow], v[56:59] offset:2048\n\t" \
    "s_mov_b64 exec, -1\n\t" \
    "s_cbranch_scc1 1f\n\t" \
    "s_waitcnt lgkmcnt(0)\n\t" \
    "s_barrier\n\t" \
    "ds_read_b128 v[16:19], %[r0]\n\t" \
    "ds_read_b128 v[20:23], %[r0] offset:640\n\t" \
    "ds_read_b32 v60, %[mo] offset:384\n\t" \
    "s_waitcnt lgkmcnt(1)\n\t" \
    "v_pk_add_f32 v[16:17], v[16:17], v[20:21]\n\t" \
    "v_pk_add_f32 v[18:19], v[18:19], v[22:23]\n\t" \
    "s_waitcnt lgkmcnt(0)\n\t" \
    "v_pk_mul_f32 v[16:17], v[16:17], v[60:61] op_sel_hi:[1,0]\n\t" \
    "v_pk_mul_f32 v[18:19], v[18:19], v[60:61] op_sel_hi:[1,0]\n\t" \
    "s_nop 1\n\t" \
    "v_permlane32_swap_b32 v16, v18\n\t" \
    "v_permlane32_swap_b32 v17, v19\n\t" \
    "v_pk_add_f32 v[16:17], v[16:17], v[18:19]\n\t" \
    "s_nop 1\n\t" \
    "v_permlane16_swap_b32 v16, v17\n\t" \
    "v_add_f32 v16, v16, v17\n\t" \
    "s_nop 1\n\t" \
    "v_add_f32_dpp v17, v16, v16 row_ror:8 row_mask:0xf bank_mask:0xf\n\t" \
    "v_add_f32_dpp v16, v16, v16 row_ror:8 row_mask:0xf bank_mask:0xf\n\t" \
    "s_nop 1\n\t" \
    "v_permlane16_swap_b32 v16, v17\n\t" \
    "s_mov_b32 exec_lo, 0xff\n\t" \
    "v_cvt_pk_bf16_f32 v16, v16, v17\n\t" \
    "s_mov_b32 exec_hi, 0xff\n\t" \
    "v_min_u32 v16, -2, v16\n\t" \
    "global_store_dword %[so], v16, %[ws] sc1\n\t" \
    "s_mov_b64 exec, -1\n\t" \
    "s_branch 2f\n\t" \
    "6:\n\t" \
    "s_sub_u32 %[st], %[st], 1\n\t" \
    "s_cbranch_scc1 7b\n\t" \
    RETRY_ASM \
    "v_mov_b32 v16, 0\n\t" \
    "v_mov_b32 v17, 0\n\t" \
    "v_mov_b32 v18, 0\n\t" \
    "v_mov_b32 v19, 0\n\t" \
    "s_mov_b32 exec_hi, 0xff\n\t" \
    "global_load_dwordx4 v[16:19], %[go], %[ws] sc1\n\t" \
    "s_mov_b32 exec_hi, -1\n\t" \
    "s_branch 5b\n\t" \
    "1:\n\t" \
    "s_cmp_eq_u32 %[wv], 7\n\t" \
    "s_waitcnt vmcnt(0) lgkmcnt(0)\n\t" \
    "s_barrier\n\t" \
    "s_cbranch_scc1 2f\n\t" \
    "ds_read_b32 v16, %[mo] offset:32\n\t" \
    "ds_read_b32 v60, %[mo] offset:384\n\t" \
    "ds_read_b32 v26, %[mo] offset:416\n\t" \
    "s_waitcnt lgkmcnt(0)\n\t" \
    "v_min_u32 v26, -2, v26\n\t" \
    "v_mul_f32 v16, v16, v60\n\t" \
    "s_nop 1\n\t" \
    "v_add_f32_dpp v17, v16, v16 row_ror:8 row_mask:0xf bank_mask:0xf\n\t" \
    "v_add_f32_dpp v16, v16, v16 row_ror:8 row_mask:0xf bank_mask:0xf\n\t" \
    "s_nop 1\n\t" \
    "v_permlane16_swap_b32 v16, v17\n\t" \
    "v_add_f32 v18, v16, v17\n\t" \
    "v_add_f32 v16, v16, v17\n\t" \
    "s_nop 1\n\t" \
    "v_permlane32_swap_b32 v16, v18\n\t" \
    "s_mov_b64 exec, 3\n\t" \
    "v_add_f32 v27, v16, v18\n\t" \
    "v_min_u32 v27, -2, v27\n\t" \
    "global_store_dwordx2 %[so], v[26:27], %[ws] sc1\n\t" \
    "s_mov_b64 exec, -1\n\t" \
    "2:\n\t"
#define ASM_QP \
    "s_mov_b32 %[st], 0x10000\n\t" \
    "5:\n\t" \
    "ds_read_b64_tr_b16 v[16:17], %[a0] offset:0\n\t" \
    "ds_read_b64_tr_b16 v[18:19], %[a0] offset:1536\n\t" \
    "ds_read_b64_tr_b16 v[20:21], %[a1] offset:0\n\t" \
    "ds_read_b64_tr_b16 v[22:23], %[a1] offset:1536\n\t" \
    "ds_read_b64_tr_b16 v[24:25], %[a0] offset:12288\n\t" \
    "ds_read_b64_tr_b16 v[26:27], %[a0] offset:13824\n\t" \
    "ds_read_b64_tr_b16 v[28:29], %[a1] offset:12288\n\t" \
    "ds_read_b64_tr_b16 v[30:31], %[a1] offset:13824\n\t" \
    "ds_read_b64_tr_b16 v[32:33], %[a0] offset:24576\n\t" \
    "ds_read_b64_tr_b16 v[34:35], %[a0] offset:26112\n\t" \
    "ds_read_b64_tr_b16 v[36:37], %[a1] offset:24576\n\t" \
    "ds_read_b64_tr_b16 v[38:39], %[a1] offset:26112\n\t" \
    "ds_read_b64_tr_b16 v[40:41], %[a0] offset:36864\n\t" \
    "ds_read_b64_tr_b16 v[42:43], %[a0] offset:38400\n\t" \
    "ds_read_b64_tr_b16 v[44:45], %[a1] offset:36864\n\t" \
    "ds_read_b64_tr_b16 v[46:47], %[a1] offset:38400\n\t" \
    "v_mov_b32 v48, %[fl]\n\t" \
    "v_add_u32 v50, -52, v48\n\t" \
    "s_mov_b32 vcc_hi, 0x100000\n\t" \
    "s_branch 25f\n\t" \
    "24:\n\t" \
    "s_sleep 4\n\t" \
    "25:\n\t" \
    "ds_read_b32 v49, v50\n\t" \
    "s_sub_u32 vcc_hi, vcc_hi, 1\n\t" \
    "s_cbranch_scc1 23f\n\t" \
    "s_waitcnt lgkmcnt(0)\n\t" \
    "v_readfirstlane_b32 %[rf], v49\n\t" \
    "s_cmp_eq_u32 %[rf], 0\n\t" \
    "s_cbranch_scc1 24b\n\t" \
    "s_add_u32 %[rf], %[rf], %[tz]\n\t" \
    "s_branch 22f\n\t" \
    "21:\n\t" \
    "s_sleep 3\n\t" \
    "22:\n\t" \
    "s_memrealtime vcc\n\t" \
    "s_waitcnt lgkmcnt(0)\n\t" \
    "s_sub_i32 vcc_lo, vcc_lo, %[rf]\n\t" \
    "s_cmp_lt_i32 vcc_lo, 0\n\t" \
    "s_cbranch_scc1 21b\n\t" \
    "23:\n\t" \
    "s_mov_b32 vcc_hi, 0x400000\n\t" \
    "0:\n\t" \
    "ds_read_b32 v49, v48\n\t" \
    "s_sub_u32 vcc_hi, vcc_hi, 1\n\t" \
    "s_cbranch_scc1 10f\n\t" \
    "s_waitcnt lgkmcnt(0)\n\t" \
    "v_readfirstlane_b32 %[rf], v49\n\t" \
    "s_cmp_eq_u32 %[rf], 0\n\t" \
    "s_cbranch_scc1 0b\n\t" \
    "4:\n\t" \
    "s_memrealtime vcc\n\t" \
    "s_waitcnt lgkmcnt(0)\n\t" \
    "s_sub_i32 vcc_lo, vcc_lo, %[rf]\n\t" \
    "s_cmp_lt_i32 vcc_lo, 0\n\t" \
    "s_cbranch_scc1 4b\n\t" \
    "10:\n\t" \
    "global_load_dwordx4 v[48:51], %[gs], %[ws] sc1\n\t" \
    "s_waitcnt vmcnt(0)\n\t" \
    "v_mfma_f32_16x16x32_bf16 v[16:19], v[16:19], v[48:51], 0\n\t" \
    "v_mfma_f32_16x16x32_bf16 v[20:23], v[20:23], v[48:51], 0\n\t" \
    "v_mov_b32_dpp v52, v48 row_shl:4 row_mask:0xf bank_mask:0xf\n\t" \
    "v_mov_b32_dpp v53, v49 row_shl:4 row_mask:0xf bank_mask:0xf\n\t" \
    "v_mov_b32_dpp v54, v50 row_shl:4 row_mask:0xf bank_mask:0xf\n\t" \
    "v_mov_b32_dpp v55, v51 row_shl:4 row_mask:0xf bank_mask:0xf\n\t" \
    "v_mov_b32_dpp v56, v48 row_shl:8 row_mask:0xf bank_mask:0xf\n\t" \
    "v_mov_b32_dpp v57, v49 row_shl:8 row_mask:0xf bank_mask:0xf\n\t" \
    "v_mfma_f32_16x16x32_bf16 v[16:19], v[24:27], v[52:55], v[16:19]\n\t" \
    "v_mov_b32_dpp v58, v50 row_shl:8 row_mask:0xf bank_mask:0xf\n\t" \
    "v_mov_b32_dpp v59, v51 row_shl:8 row_mask:0xf bank_mask:0xf\n\t" \
    "v_mfma_f32_16x16x32_bf16 v[20:23], v[28:31], v[52:55], v[20:23]\n\t" \
    "v_mov_b32_dpp v52, v48 row_shl:12 row_mask:0xf bank_mask:0xf\n\t" \
    "v_mov_b32_dpp v53, v49 row_shl:12 row_mask:0xf bank_mask:0xf\n\t" \
    "v_mfma_f32_16x16x32_bf16 v[16:19], v[32:35], v[56:59], v[16:19]\n\t" \
    "v_mov_b32_dpp v54, v50 row_shl:12 row_mask:0xf bank_mask:0xf\n\t" \
    "v_mov_b32_dpp v55, v51 row_shl:12 row_mask:0xf bank_mask:0xf\n\t" \
    "v_mfma_f32_16x16x32_bf16 v[20:23], v[36:39], v[56:59], v[20:23]\n\t" \
    "v_max3_u32 v24, v48, v49, v50\n\t" \
    "v_max_u32 v24, v24, v51\n\t" \
    "v_mfma_f32_16x16x32_bf16 v[16:19], v[40:43], v[52:55], v[16:19]\n\t" \
    "v_cmp_eq_u32 vcc, -1, v24\n\t" \
    "v_mfma_f32_16x16x32_bf16 v[20:23], v[44:47], v[52:55], v[20:23]\n\t" \
    "s_mov_b32 exec_lo, 0x00ff00ff\n\t" \
    "s_mov_b32 exec_hi, 0x00ff00ff\n\t" \
    "s_cbranch_vccnz 6f\n\t" \
    "s_nop 4\n\t" \
    "v_mov_b32_dpp v16, v20 row_shr:4 row_mask:0xf bank_mask:0x2\n\t" \
    "v_mov_b32_dpp v17, v21 row_shr:4 row_mask:0xf bank_mask:0x2\n\t" \
    "v_mov_b32_dpp v18, v22 row_shr:4 row_mask:0xf bank_mask:0x2\n\t" \
    "v_mov_b32_dpp v19, v23 row_shr:4 row_mask:0xf bank_mask:0x2\n\t" \
    "v_min_u32 v16, -2, v16\n\t" \
    "v_min_u32 v17, -2, v17\n\t" \
    "v_min_u32 v18, -2, v18\n\t" \
    "v_min_u32 v19, -2, v19\n\t" \
    "global_store_dwordx4 %[so], v[16:19], %[ws] sc1\n\t" \
    "s_mov_b64 exec, -1\n\t" \
    "s_branch 2f\n\t" \
    "6:\n\t" \
    "s_mov_b64 exec, -1\n\t" \
    "s_sub_u32 %[st], %[st], 1\n\t" \
    "s_cbranch_scc1 2f\n\t" \
    RETRY_ASM \
    "s_sleep 6\n\t" \
    "s_branch 5b\n\t" \
    "2:\n\t"
#define ASM_ATT_P \
    "s_setprio 2\n\t" \
    "v_mov_b32 v28, 0\n\t" \
    "v_mov_b32 v29, 0\n\t" \
    "v_mov_b32 v32, 0\n\t" \
    "v_mov_b32 v33, 0\n\t" \
    "v_mov_b32 v36, 0\n\t" \
    "v_mov_b32 v37, 0\n\t" \
    "v_mov_b32 v40, 0\n\t" \
    "v_mov_b32 v41, 0\n\t" \
    "v_mov_b32 v48, 0\n\t" \
    "v_mov_b32 v49, 0\n\t" \
    "v_mov_b32 v50, 0\n\t" \
    "v_mov_b32 v51, 0\n\t" \
    "v_mov_b32 v52, 0\n\t" \
    "v_mov_b32 v53, 0\n\t" \
    "v_mov_b32 v54, 0\n\t" \
    "v_mov_b32 v55, 0\n\t" \
    "v_mov_b32 v56, 0\n\t" \
    "v_mov_b32 v57, 0\n\t" \
    "v_mov_b32 v58, 0\n\t" \
    "v_mov_b32 v59, 0\n\t" \
    "v_mov_b32 v16, 0\n\t" \
    "v_mov_b32 v17, 0\n\t" \
    "v_mov_b32 v18, 0\n\t" \
    "v_mov_b32 v19, 0\n\t" \
    "v_mov_b32 v20, %[ma]\n\t" \
    "s_mov_b32 vcc_hi, 0x400000\n\t" \
    "0:\n\t" \
    "ds_read_b32 v21, v20 offset:340\n\t" \
    "s_sub_u32 vcc_hi, vcc_hi, 1\n\t" \
    "s_cbranch_scc1 4f\n\t" \
    "s_waitcnt lgkmcnt(0)\n\t" \
    "v_readfirstlane_b32 %[rf], v21\n\t" \
    "s_cmp_eq_u32 %[rf], 0\n\t" \
    "s_cbranch_scc1 0b\n\t" \
    "s_add_u32 %[st], %[rf], %[tg]\n\t" \
    "4:\n\t" \
    "s_memrealtime vcc\n\t" \
    "s_waitcnt lgkmcnt(0)\n\t" \
    "s_sub_i32 vcc_lo, vcc_lo, %[st]\n\t" \
    "s_cmp_lt_i32 vcc_lo, -4000\n\t" \
    "s_cbranch_scc1 10f\n\t" \
    "s_cmp_lt_i32 vcc_lo, 0\n\t" \
    "s_cbranch_scc1 4b\n\t" \
    "10:\n\t" \
    "s_mov_b32 exec_hi, 0xff\n\t" \
    "global_load_dwordx4 v[16:19], %[go], %[ws] sc1\n\t" \
    "s_mov_b32 exec_hi, -1\n\t" \
    "s_barrier\n\t" \
    "ds_read_b64 v[22:23], v20 offset:352\n\t" \
    "ds_read_b32 v24, %[cs]\n\t" \
    "s_mov_b32 %[st], 0x10000\n\t" \
    "s_waitcnt lgkmcnt(0)\n\t" \
    "v_mul_f32 v22, v22, v24\n\t" \
    "v_mul_f32_e64 v22, -v22, v23\n\t" \
    "5:\n\t" \
    "s_waitcnt vmcnt(0)\n\t" \
    "v_max3_u32 v24, v16, v17, v18\n\t" \
    "v_max_u32 v24, v24, v19\n\t" \
    "v_cmp_eq_u32 vcc, -1, v24\n\t" \
    "v_permlane32_swap_b32 v16, v18\n\t" \
    "v_permlane32_swap_b32 v17, v19\n\t" \
    "v_pk_add_f32 v[16:17], v[16:17], v[18:19]\n\t" \
    "s_nop 0\n\t" \
    "s_cbranch_vccnz 6f\n\t" \
    "7:\n\t" \
    "v_permlane16_swap_b32 v16, v17\n\t" \
    "v_add_f32 v16, v16, v17\n\t" \
    "s_nop 1\n\t" \
    "v_add_f32_dpp v16, v16, v16 row_ror:8 row_mask:0xf bank_mask:0xf\n\t" \
    "v_fmac_f32 v22, v16, v23\n\t" \
    "v_cvt_pk_bf16_f32 v22, v22, v22\n\t" \
    "ds_write_b16 %[qo], v22\n\t" \
    "s_waitcnt lgkmcnt(0)\n\t" \
    "s_barrier\n\t" \
    "ds_read_b128 v[16:19], %[qa] offset:0\n\t" \
    "ds_read_b128 v[20:23], %[qa] offset:128\n\t" \
    "ds_read_b128 v[24:27], %[qa] offset:256\n\t" \
    "s_waitcnt lgkmcnt(2)\n\t" \
    "v_smfmac_f32_16x16x64_bf16 v[28:31], v[16:19], v[64:71], v7\n\t" \
    "v_smfmac_f32_16x16x64_bf16 v[32:35], v[16:19], v[88:95], v7\n\t" \
    "v_smfmac_f32_16x16x64_bf16 v[36:39], v[16:19], v[112:119], v7\n\t" \
    "v_smfmac_f32_16x16x64_bf16 v[40:43], v[16:19], v[136:143], v7\n\t" \
    "s_waitcnt lgkmcnt(1)\n\t" \
    "v_smfmac_f32_16x16x64_bf16 v[28:31], v[20:23], v[72:79], v7\n\t" \
    "v_smfmac_f32_16x16x64_bf16 v[32:35], v[20:23], v[96:103], v7\n\t" \
    "v_smfmac_f32_16x16x64_bf16 v[36:39], v[20:23], v[120:127], v7\n\t" \
    "v_smfmac_f32_16x16x64_bf16 v[40:43], v[20:23], v[144:151], v7\n\t" \
    "s_waitcnt lgkmcnt(0)\n\t" \
    "v_smfmac_f32_16x16x64_bf16 v[28:31], v[24:27], v[80:87], v7\n\t" \
    "v_smfmac_f32_16x16x64_bf16 v[32:35], v[24:27], v[104:111], v7\n\t" \
    "v_smfmac_f32_16x16x64_bf16 v[36:39], v[24:27], v[128:135], v7\n\t" \
    "v_smfmac_f32_16x16x64_bf16 v[40:43], v[24:27], v[152:159], v7\n\t" \
    "v_mov_b32 v63, %[mlw]\n\t" \
    "s_nop 3\n\t" \
    "v_add_f32_dpp v60, v28, v29 quad_perm:[0,1,2,3] row_mask:0x1 bank_mask:0xf\n\t" \
    "v_add_f32_dpp v60, v32, v33 quad_perm:[0,1,2,3] row_mask:0x2 bank_mask:0xf\n\t" \
    "v_add_f32_dpp v60, v36, v37 quad_perm:[0,1,2,3] row_mask:0x4 bank_mask:0xf\n\t" \
    "v_add_f32_dpp v60, v40, v41 quad_perm:[0,1,2,3] row_mask:0x8 bank_mask:0xf\n\t" \
    "v_mul_f32 v60, 0x3dd53b94, v60\n\t" \
    "s_nop 1\n\t" \
    "v_max_f32_dpp v61, v60, v60 quad_perm:[1,0,3,2] row_mask:0xf bank_mask:0xf\n\t" \
    "s_nop 1\n\t" \
    "v_max_f32_dpp v61, v61, v61 quad_perm:[2,3,0,1] row_mask:0xf bank_mask:0xf\n\t" \
    "s_nop 1\n\t" \
    "v_max_f32_dpp v61, v61, v61 row_half_mirror row_mask:0xf bank_mask:0xf\n\t" \
    "s_nop 1\n\t" \
    "v_max_f32_dpp v61, v61, v61 row_mirror row_mask:0xf bank_mask:0xf\n\t" \
    "s_nop 1\n\t" \
    "v_max_f32_dpp v61, v61, v61 row_bcast:15 row_mask:0xa bank_mask:0xf\n\t" \
    "s_nop 1\n\t" \
    "v_max_f32_dpp v61, v61, v61 row_bcast:31 row_mask:0xc bank_mask:0xf\n\t" \
    "s_nop 0\n\t" \
    "v_readlane_b32 %[st], v61, 63\n\t" \
    "s_mov_b32 exec_lo, 0\n\t" \
    "s_brev_b32 exec_hi, 1\n\t" \
    "ds_write_b32 v63, v61\n\t" \
    "s_mov_b64 exec, -1\n\t" \
    "v_subrev_f32 v62, %[st], v60\n\t" \
    "v_exp_f32 v62, v62\n\t" \
    "s_nop 0\n\t" \
    "v_cvt_pk_bf16_f32 v60, v62, v62\n\t" \
    "ds_write_b16 %[pw], v60\n\t" \
    "ds_write_b16 %[pw], v60 offset:20\n\t" \
    "ds_read_b128 v[16:19], %[pr] offset:0\n\t" \
    "ds_read_b128 v[20:23], %[pr] offset:272\n\t" \
    "ds_read_b128 v[24:27], %[pr] offset:544\n\t" \
    "ds_read_b128 v[28:31], %[pr] offset:816\n\t" \
    "v_mov_b32 v60, %[ma]\n\t" \
    "s_setprio 0\n\t" \
    "v_add_f32_dpp v62, v62, v62 quad_perm:[1,0,3,2] row_mask:0xf bank_mask:0xf\n\t" \
    "s_nop 1\n\t" \
    "v_add_f32_dpp v62, v62, v62 quad_perm:[2,3,0,1] row_mask:0xf bank_mask:0xf\n\t" \
    "s_nop 1\n\t" \
    "v_add_f32_dpp v62, v62, v62 row_half_mirror row_mask:0xf bank_mask:0xf\n\t" \
    "s_nop 1\n\t" \
    "v_add_f32_dpp v62, v62, v62 row_mirror row_mask:0xf bank_mask:0xf\n\t" \
    "s_waitcnt lgkmcnt(3)\n\t" \
    "v_smfmac_f32_16x16x64_bf16 v[48:51], v[16:19], v[160:167], v7 abid:1\n\t" \
    "v_smfmac_f32_16x16x64_bf16 v[52:55], v[16:19], v[192:199], v7 abid:1\n\t" \
    "v_smfmac_f32_16x16x64_bf16 v[56:59], v[16:19], v[224:231], v7 abid:1\n\t" \
    "v_add_f32_dpp v62, v62, v62 row_bcast:15 row_mask:0xa bank_mask:0xf\n\t" \
    "s_waitcnt lgkmcnt(2)\n\t" \
    "v_smfmac_f32_16x16x64_bf16 v[48:51], v[20:23], v[168:175], v7 abid:1\n\t" \
    "v_smfmac_f32_16x16x64_bf16 v[52:55], v[20:23], v[200:207], v7 abid:1\n\t" \
    "v_smfmac_f32_16x16x64_bf16 v[56:59], v[20:23], v[232:239], v7 abid:1\n\t" \
    "v_add_f32_dpp v62, v62, v62 row_bcast:31 row_mask:0xc bank_mask:0xf\n\t" \
    "s_waitcnt lgkmcnt(1)\n\t" \
    "v_smfmac_f32_16x16x64_bf16 v[48:51], v[24:27], v[176:183], v7 abid:1\n\t" \
    "v_smfmac_f32_16x16x64_bf16 v[52:55], v[24:27], v[208:215], v7 abid:1\n\t" \
    "v_smfmac_f32_16x16x64_bf16 v[56:59], v[24:27], v[240:247], v7 abid:1\n\t" \
    "s_waitcnt lgkmcnt(0)\n\t" \
    "v_smfmac_f32_16x16x64_bf16 v[48:51], v[28:31], v[184:191], v7 abid:1\n\t" \
    "v_smfmac_f32_16x16x64_bf16 v[52:55], v[28:31], v[216:223], v7 abid:1\n\t" \
    "v_smfmac_f32_16x16x64_bf16 v[56:59], v[28:31], v[248:255], v7 abid:1\n\t" \
    "s_barrier\n\t" \
    "ds_read_b128 v[16:19], v60\n\t" \
    "ds_read_b128 v[20:23], v60 offset:16\n\t" \
    "ds_read_b32 v24, %[mo]\n\t" \
    "s_mov_b32 exec_lo, 0\n\t" \
    "s_brev_b32 exec_hi, 1\n\t" \
    "ds_write_b32 v63, v62 offset:32\n\t" \
    "s_mov_b64 exec, -1\n\t" \
    "v_mov_b32_dpp v48, v52 quad_perm:[0,1,2,3] row_mask:0xc bank_mask:0xf\n\t" \
    "v_mov_b32_dpp v49, v53 quad_perm:[0,1,2,3] row_mask:0xc bank_mask:0xf\n\t" \
    "v_mov_b32_dpp v50, v54 quad_perm:[0,1,2,3] row_mask:0xc bank_mask:0xf\n\t" \
    "v_mov_b32_dpp v51, v55 quad_perm:[0,1,2,3] row_mask:0xc bank_mask:0xf\n\t" \
    "ds_write_b128 %[ow], v[48:51] offset:0\n\t" \
    "s_mov_b32 exec_lo, 0x00ff00ff\n\t" \
    "s_mov_b32 exec_hi, 0xff00ff00\n\t" \
    "ds_write_b128 %[ow], v[56:59] offset:2048\n\t" \
    "s_mov_b64 exec, -1\n\t" \
    "s_waitcnt lgkmcnt(3)\n\t" \
    "v_max3_f32 v16, v16, v17, v18\n\t" \
    "v_max3_f32 v19, v19, v20, v21\n\t" \
    "v_max3_f32 v16, v16, v22, v23\n\t" \
    "v_max_f32 v26, v16, v19\n\t" \
    "v_sub_f32 v60, v24, v26\n\t" \
    "v_exp_f32 v60, v60\n\t" \
    "s_nop 0\n\t" \
    "ds_write2_b32 %[mo], v60, v26 offset0:96 offset1:104\n\t" \
    "ds_read_b64_tr_b16 v[28:29], %[a0] offset:0\n\t" \
    "ds_read_b64_tr_b16 v[30:31], %[a0] offset:1024\n\t" \
    "ds_read_b64_tr_b16 v[32:33], %[a1] offset:8192\n\t" \
    "ds_read_b64_tr_b16 v[34:35], %[a1] offset:9216\n\t" \
    "ds_read_b64_tr_b16 v[36:37], %[a0] offset:8192\n\t" \
    "ds_read_b64_tr_b16 v[38:39], %[a0] offset:9216\n\t" \
    "ds_read_b64_tr_b16 v[40:41], %[a1] offset:0\n\t" \
    "ds_read_b64_tr_b16 v[42:43], %[a1] offset:1024\n\t" \
    "ds_read_b64_tr_b16 v[44:45], %[a0] offset:16384\n\t" \
    "ds_read_b64_tr_b16 v[46:47], %[a0] offset:17408\n\t" \
    "ds_read_b64_tr_b16 v[48:49], %[a1] offset:24576\n\t" \
    "ds_read_b64_tr_b16 v[50:51], %[a1] offset:25600\n\t" \
    "ds_read_b64_tr_b16 v[52:53], %[a0] offset:24576\n\t" \
    "ds_read_b64_tr_b16 v[54:55], %[a0] offset:25600\n\t" \
    "ds_read_b64_tr_b16 v[56:57], %[a1] offset:16384\n\t" \
    "ds_read_b64_tr_b16 v[58:59], %[a1] offset:17408\n\t" \
    "s_waitcnt lgkmcnt(0)\n\t" \
    "s_barrier\n\t" \
    "ds_read_b128 v[16:19], %[r0]\n\t" \
    "ds_read_b128 v[20:23], %[r0] offset:512\n\t" \
    "s_waitcnt lgkmcnt(0)\n\t" \
    "v_pk_add_f32 v[16:17], v[16:17], v[20:21]\n\t" \
    "v_pk_add_f32 v[18:19], v[18:19], v[22:23]\n\t" \
    "v_pk_mul_f32 v[16:17], v[16:17], v[60:61] op_sel_hi:[1,0]\n\t" \
    "v_pk_mul_f32 v[18:19], v[18:19], v[60:61] op_sel_hi:[1,0]\n\t" \
    "s_nop 1\n\t" \
    "v_permlane32_swap_b32 v16, v18\n\t" \
    "v_permlane32_swap_b32 v17, v19\n\t" \
    "v_pk_add_f32 v[16:17], v[16:17], v[18:19]\n\t" \
    "s_nop 1\n\t" \
    "v_permlane16_swap_b32 v16, v17\n\t" \
    "v_add_f32 v16, v16, v17\n\t" \
    "s_nop 1\n\t" \
    "v_add_f32_dpp v17, v16, v16 row_ror:8 row_mask:0xf bank_mask:0xf\n\t" \
    "v_add_f32_dpp v16, v16, v16 row_ror:8 row_mask:0xf bank_mask:0xf\n\t" \
    "s_nop 1\n\t" \
    "v_permlane16_swap_b32 v16, v17\n\t" \
    "s_mov_b32 exec_lo, 0xff\n\t" \
    "v_cvt_pk_bf16_f32 v16, v16, v17\n\t" \
    "s_mov_b32 exec_hi, 0xff\n\t" \
    "v_min_u32 v16, -2, v16\n\t" \
    "global_store_dword %[so], v16, %[ws] sc1\n\t" \
    "s_mov_b64 exec, -1\n\t" \
    "ds_read_b64_tr_b16 v[60:61], %[a0] offset:32768\n\t" \
    "ds_read_b64_tr_b16 v[62:63], %[a0] offset:33792\n\t" \
    "s_add_u32 %[rf], %[rf], %[t2]\n\t" \
    "s_mov_b32 %[st], 0x10000\n\t" \
    "14:\n\t" \
    "s_memrealtime vcc\n\t" \
    "s_waitcnt lgkmcnt(0)\n\t" \
    "s_sub_i32 vcc_lo, vcc_lo, %[rf]\n\t" \
    "s_cmp_lt_i32 vcc_lo, -4000\n\t" \
    "s_cbranch_scc1 15f\n\t" \
    "s_cmp_lt_i32 vcc_lo, 0\n\t" \
    "s_cbranch_scc1 14b\n\t" \
    "15:\n\t" \
    "global_load_dwordx4 v[8:11], %[gs], %[ws] offset:0 sc1\n\t" \
    "global_load_dwordx4 v[12:15], %[gs], %[ws] offset:1024 sc1\n\t" \
    "global_load_dwordx4 v[16:19], %[gs], %[ws] offset:2048 sc1\n\t" \
    "global_load_dwordx4 v[24:27], %[gm], %[ws] sc1\n\t" \
    "s_waitcnt vmcnt(3)\n\t" \
    "v_mfma_f32_16x16x32_bf16 v[28:31], v[28:31], v[8:11], 0\n\t" \
    "v_mov_b32_dpp v20, v8 row_half_mirror row_mask:0xf bank_mask:0xf\n\t" \
    "v_mov_b32_dpp v21, v9 row_half_mirror row_mask:0xf bank_mask:0xf\n\t" \
    "v_mfma_f32_16x16x32_bf16 v[32:35], v[32:35], v[8:11], 0\n\t" \
    "v_mov_b32_dpp v22, v10 row_half_mirror row_mask:0xf bank_mask:0xf\n\t" \
    "v_mov_b32_dpp v23, v11 row_half_mirror row_mask:0xf bank_mask:0xf\n\t" \
    "v_max3_u32 %[t0], v8, v9, v10\n\t" \
    "s_waitcnt vmcnt(2)\n\t" \
    "v_mfma_f32_16x16x32_bf16 v[28:31], v[36:39], v[20:23], v[28:31]\n\t" \
    "v_max3_u32 %[t0], %[t0], v11, v12\n\t" \
    "ds_read_b64_tr_b16 v[36:37], %[a1] offset:40960\n\t" \
    "v_mfma_f32_16x16x32_bf16 v[32:35], v[40:43], v[20:23], v[32:35]\n\t" \
    "ds_read_b64_tr_b16 v[38:39], %[a1] offset:41984\n\t" \
    "ds_read_b64_tr_b16 v[40:41], %[a0] offset:40960\n\t" \
    "v_mfma_f32_16x16x32_bf16 v[28:31], v[44:47], v[12:15], v[28:31]\n\t" \
    "v_mov_b32_dpp v20, v12 row_half_mirror row_mask:0xf bank_mask:0xf\n\t" \
    "v_mov_b32_dpp v21, v13 row_half_mirror row_mask:0xf bank_mask:0xf\n\t" \
    "v_mfma_f32_16x16x32_bf16 v[32:35], v[48:51], v[12:15], v[32:35]\n\t" \
    "v_mov_b32_dpp v22, v14 row_half_mirror row_mask:0xf bank_mask:0xf\n\t" \
    "v_mov_b32_dpp v23, v15 row_half_mirror row_mask:0xf bank_mask:0xf\n\t" \
    "ds_read_b64_tr_b16 v[42:43], %[a0] offset:41984\n\t" \
    "ds_read_b64_tr_b16 v[44:45], %[a1] offset:32768\n\t" \
    "v_mfma_f32_16x16x32_bf16 v[28:31], v[52:55], v[20:23], v[28:31]\n\t" \
    "ds_read_b64_tr_b16 v[46:47], %[a1] offset:33792\n\t" \
    "s_waitcnt vmcnt(0)\n\t" \
    "v_max3_u32 %[t0], %[t0], v13, v14\n\t" \
    "v_mfma_f32_16x16x32_bf16 v[32:35], v[56:59], v[20:23], v[32:35]\n\t" \
    "v_max3_u32 v8, v24, v25, v26\n\t" \
    "v_sub_f32 v24, v26, v24\n\t" \
    "v_mfma_f32_16x16x32_bf16 v[28:31], v[60:63], v[16:19], v[28:31]\n\t" \
    "v_mov_b32_dpp v20, v16 row_half_mirror row_mask:0xf bank_mask:0xf\n\t" \
    "v_mov_b32_dpp v21, v17 row_half_mirror row_mask:0xf bank_mask:0xf\n\t" \
    "s_waitcnt lgkmcnt(4)\n\t" \
    "v_mfma_f32_16x16x32_bf16 v[32:35], v[36:39], v[16:19], v[32:35]\n\t" \
    "v_mov_b32_dpp v22, v18 row_half_mirror row_mask:0xf bank_mask:0xf\n\t" \
    "v_mov_b32_dpp v23, v19 row_half_mirror row_mask:0xf bank_mask:0xf\n\t" \
    "v_exp_f32 v24, v24\n\t" \
    "v_max3_u32 %[t0], %[t0], v15, v16\n\t" \
    "s_waitcnt lgkmcnt(0)\n\t" \
    "v_mfma_f32_16x16x32_bf16 v[28:31], v[40:43], v[20:23], v[28:31]\n\t" \
    "v_fmac_f32 v25, v24, v27\n\t" \
    "v_max3_u32 v8, v8, v27, v17\n\t" \
    "v_mfma_f32_16x16x32_bf16 v[32:35], v[44:47], v[20:23], v[32:35]\n\t" \
    "v_rcp_f32 v24, v25\n\t" \
    "v_max3_u32 %[t0], %[t0], v18, v19\n\t" \
    "v_max_u32 v8, v8, %[t0]\n\t" \
    "v_cmp_eq_u32 vcc, -1, v8\n\t" \
    "v_mov_b32 v9, 0xff\n\t" \
    "s_nop 1\n\t" \
    "s_cbranch_vccnz 16f\n\t" \
    "v_mov_b32_dpp v28, v32 quad_perm:[0,1,2,3] row_mask:0xf bank_mask:0xa\n\t" \
    "v_mov_b32_dpp v29, v33 quad_perm:[0,1,2,3] row_mask:0xf bank_mask:0xa\n\t" \
    "v_mov_b32_dpp v30, v34 quad_perm:[0,1,2,3] row_mask:0xf bank_mask:0xa\n\t" \
    "v_mov_b32_dpp v31, v35 quad_perm:[0,1,2,3] row_mask:0xf bank_mask:0xa\n\t" \
    "v_pk_mul_f32 v[28:29], v[28:29], v[24:25] op_sel_hi:[1,0]\n\t" \
    "v_pk_mul_f32 v[30:31], v[30:31], v[24:25] op_sel_hi:[1,0]\n\t" \
    "s_nop 0\n\t" \
    "v_add_f32_dpp v28, v28, v28 row_shl:8 row_mask:0xf bank_mask:0xf\n\t" \
    "v_add_f32_dpp v29, v29, v29 row_shl:8 row_mask:0xf bank_mask:0xf\n\t" \
    "v_add_f32_dpp v30, v30, v30 row_shl:8 row_mask:0xf bank_mask:0xf\n\t" \
    "v_add_f32_dpp v31, v31, v31 row_shl:8 row_mask:0xf bank_mask:0xf\n\t" \
    "s_mov_b32 exec_lo, 0x00ff00ff\n\t" \
    "s_mov_b32 exec_hi, 0x00ff00ff\n\t" \
    "v_bfi_b32 v28, v9, %[tag], v28\n\t" \
    "v_bfi_b32 v29, v9, %[tag], v29\n\t" \
    "v_bfi_b32 v30, v9, %[tag], v30\n\t" \
    "v_bfi_b32 v31, v9, %[tag], v31\n\t" \
    "global_store_dwordx4 %[s2], v[28:31], %[ws] sc1\n\t" \
    "s_mov_b64 exec, -1\n\t" \
    "s_branch 2f\n\t" \
    "6:\n\t" \
    "s_sub_u32 %[st], %[st], 1\n\t" \
    "s_cbranch_scc1 7b\n\t" \
    RETRY_ASM \
    "v_mov_b32 v16, 0\n\t" \
    "v_mov_b32 v17, 0\n\t" \
    "v_mov_b32 v18, 0\n\t" \
    "v_mov_b32 v19, 0\n\t" \
    "s_mov_b32 exec_hi, 0xff\n\t" \
    "global_load_dwordx4 v[16:19], %[go], %[ws] sc1\n\t" \
    "s_mov_b32 exec_hi, -1\n\t" \
    "s_branch 5b\n\t" \
    "16:\n\t" \
    "s_mov_b64 exec, -1\n\t" \
    "s_sub_u32 %[st], %[st], 1\n\t" \
    "s_cbranch_scc1 2f\n\t" \
    RETRY_ASM3 \
    "s_sleep 3\n\t" \
    "ds_read_b64_tr_b16 v[28:29], %[a0] offset:0\n\t" \
    "ds_read_b64_tr_b16 v[30:31], %[a0] offset:1024\n\t" \
    "ds_read_b64_tr_b16 v[32:33], %[a1] offset:8192\n\t" \
    "ds_read_b64_tr_b16 v[34:35], %[a1] offset:9216\n\t" \
    "ds_read_b64_tr_b16 v[36:37], %[a0] offset:8192\n\t" \
    "ds_read_b64_tr_b16 v[38:39], %[a0] offset:9216\n\t" \
    "ds_read_b64_tr_b16 v[40:41], %[a1] offset:0\n\t" \
    "ds_read_b64_tr_b16 v[42:43], %[a1] offset:1024\n\t" \
    "ds_read_b64_tr_b16 v[44:45], %[a0] offset:16384\n\t" \
    "ds_read_b64_tr_b16 v[46:47], %[a0] offset:17408\n\t" \
    "ds_read_b64_tr_b16 v[48:49], %[a1] offset:24576\n\t" \
    "ds_read_b64_tr_b16 v[50:51], %[a1] offset:25600\n\t" \
    "ds_read_b64_tr_b16 v[52:53], %[a0] offset:24576\n\t" \
    "ds_read_b64_tr_b16 v[54:55], %[a0] offset:25600\n\t" \
    "ds_read_b64_tr_b16 v[56:57], %[a1] offset:16384\n\t" \
    "ds_read_b64_tr_b16 v[58:59], %[a1] offset:17408\n\t" \
    "ds_read_b64_tr_b16 v[60:61], %[a0] offset:32768\n\t" \
    "ds_read_b64_tr_b16 v[62:63], %[a0] offset:33792\n\t" \
    "s_waitcnt lgkmcnt(0)\n\t" \
    "s_branch 15b\n\t" \
    "2:\n\t"
#define ASM_OP \
    "s_mov_b32 %[st], 0x10000\n\t" \
    "5:\n\t" \
    "v_mov_b32 v8, %[fl]\n\t" \
    "s_mov_b32 %[ct], 0x400000\n\t" \
    "0:\n\t" \
    "ds_read_b32 v9, v8\n\t" \
    "s_memrealtime vcc\n\t" \
    "s_sub_u32 %[ct], %[ct], 1\n\t" \
    "s_waitcnt lgkmcnt(0)\n\t" \
    "s_cbranch_scc1 10f\n\t" \
    "v_readfirstlane_b32 %[rf], v9\n\t" \
    "s_cmp_eq_u32 %[rf], 0\n\t" \
    "s_cbranch_scc1 0b\n\t" \
    "s_sub_i32 vcc_lo, vcc_lo, %[rf]\n\t" \
    "s_cmp_ge_i32 vcc_lo, -4\n\t" \
    "s_cbranch_scc1 10f\n\t" \
    "4:\n\t" \
    "s_memrealtime vcc\n\t" \
    "s_waitcnt lgkmcnt(0)\n\t" \
    "s_sub_i32 vcc_lo, vcc_lo, %[rf]\n\t" \
    "s_cmp_lt_i32 vcc_lo, 0\n\t" \
    "s_cbranch_scc1 4b\n\t" \
    "10:\n\t" \
    "global_load_dwordx4 v[8:11], %[gs], %[ws] offset:0 sc1\n\t" \
    "global_load_dwordx4 v[12:15], %[gs], %[ws] offset:1024 sc1\n\t" \
    "global_load_dwordx4 v[16:19], %[gs], %[ws] offset:2048 sc1\n\t" \
    "global_load_dwordx4 v[24:27], %[gm], %[ws] sc1\n\t" \
    "v_mov_b32 v28, %[fl]\n\t" \
    "v_add_u32 v28, -8, v28\n\t" \
    "s_mov_b32 %[ct], 0x400000\n\t" \
    "11:\n\t" \
    "ds_read_b32 v29, v28\n\t" \
    "s_sub_u32 %[ct], %[ct], 1\n\t" \
    "s_waitcnt lgkmcnt(0)\n\t" \
    "s_cbranch_scc1 12f\n\t" \
    "v_cmp_eq_u32 vcc, 0, v29\n\t" \
    "s_cbranch_vccnz 11b\n\t" \
    "12:\n\t" \
    "ds_read_b64_tr_b16 v[28:29], %[a0] offset:0\n\t" \
    "ds_read_b64_tr_b16 v[30:31], %[a0] offset:1024\n\t" \
    "ds_read_b64_tr_b16 v[32:33], %[a1] offset:8192\n\t" \
    "ds_read_b64_tr_b16 v[34:35], %[a1] offset:9216\n\t" \
    "ds_read_b64_tr_b16 v[36:37], %[a0] offset:8192\n\t" \
    "ds_read_b64_tr_b16 v[38:39], %[a0] offset:9216\n\t" \
    "ds_read_b64_tr_b16 v[40:41], %[a1] offset:0\n\t" \
    "ds_read_b64_tr_b16 v[42:43], %[a1] offset:1024\n\t" \
    "ds_read_b64_tr_b16 v[44:45], %[a0] offset:16384\n\t" \
    "ds_read_b64_tr_b16 v[46:47], %[a0] offset:17408\n\t" \
    "ds_read_b64_tr_b16 v[48:49], %[a1] offset:24576\n\t" \
    "ds_read_b64_tr_b16 v[50:51], %[a1] offset:25600\n\t" \
    "ds_read_b64_tr_b16 v[52:53], %[a0] offset:24576\n\t" \
    "ds_read_b64_tr_b16 v[54:55], %[a0] offset:25600\n\t" \
    "ds_read_b64_tr_b16 v[56:57], %[a1] offset:16384\n\t" \
    "ds_read_b64_tr_b16 v[58:59], %[a1] offset:17408\n\t" \
    "ds_read_b64_tr_b16 v[60:61], %[a0] offset:32768\n\t" \
    "ds_read_b64_tr_b16 v[62:63], %[a0] offset:33792\n\t" \
    "s_waitcnt vmcnt(3) lgkmcnt(0)\n\t" \
    "v_mfma_f32_16x16x32_bf16 v[28:31], v[28:31], v[8:11], 0\n\t" \
    "v_mov_b32_dpp v20, v8 row_half_mirror row_mask:0xf bank_mask:0xf\n\t" \
    "v_mov_b32_dpp v21, v9 row_half_mirror row_mask:0xf bank_mask:0xf\n\t" \
    "v_mfma_f32_16x16x32_bf16 v[32:35], v[32:35], v[8:11], 0\n\t" \
    "v_mov_b32_dpp v22, v10 row_half_mirror row_mask:0xf bank_mask:0xf\n\t" \
    "v_mov_b32_dpp v23, v11 row_half_mirror row_mask:0xf bank_mask:0xf\n\t" \
    "v_max3_u32 %[t0], v8, v9, v10\n\t" \
    "s_waitcnt vmcnt(2)\n\t" \
    "v_mfma_f32_16x16x32_bf16 v[28:31], v[36:39], v[20:23], v[28:31]\n\t" \
    "v_max3_u32 %[t0], %[t0], v11, v12\n\t" \
    "ds_read_b64_tr_b16 v[36:37], %[a1] offset:40960\n\t" \
    "v_mfma_f32_16x16x32_bf16 v[32:35], v[40:43], v[20:23], v[32:35]\n\t" \
    "ds_read_b64_tr_b16 v[38:39], %[a1] offset:41984\n\t" \
    "ds_read_b64_tr_b16 v[40:41], %[a0] offset:40960\n\t" \
    "v_mfma_f32_16x16x32_bf16 v[28:31], v[44:47], v[12:15], v[28:31]\n\t" \
    "v_mov_b32_dpp v20, v12 row_half_mirror row_mask:0xf bank_mask:0xf\n\t" \
    "v_mov_b32_dpp v21, v13 row_half_mirror row_mask:0xf bank_mask:0xf\n\t" \
    "v_mfma_f32_16x16x32_bf16 v[32:35], v[48:51], v[12:15], v[32:35]\n\t" \
    "v_mov_b32_dpp v22, v14 row_half_mirror row_mask:0xf bank_mask:0xf\n\t" \
    "v_mov_b32_dpp v23, v15 row_half_mirror row_mask:0xf bank_mask:0xf\n\t" \
    "ds_read_b64_tr_b16 v[42:43], %[a0] offset:41984\n\t" \
    "ds_read_b64_tr_b16 v[44:45], %[a1] offset:32768\n\t" \
    "v_mfma_f32_16x16x32_bf16 v[28:31], v[52:55], v[20:23], v[28:31]\n\t" \
    "ds_read_b64_tr_b16 v[46:47], %[a1] offset:33792\n\t" \
    "s_waitcnt vmcnt(0)\n\t" \
    "v_max3_u32 %[t0], %[t0], v13, v14\n\t" \
    "v_mfma_f32_16x16x32_bf16 v[32:35], v[56:59], v[20:23], v[32:35]\n\t" \
    "v_max3_u32 v8, v24, v25, v26\n\t" \
    "v_sub_f32 v24, v26, v24\n\t" \
    "v_mfma_f32_16x16x32_bf16 v[28:31], v[60:63], v[16:19], v[28:31]\n\t" \
    "v_mov_b32_dpp v20, v16 row_half_mirror row_mask:0xf bank_mask:0xf\n\t" \
    "v_mov_b32_dpp v21, v17 row_half_mirror row_mask:0xf bank_mask:0xf\n\t" \
    "s_waitcnt lgkmcnt(4)\n\t" \
    "v_mfma_f32_16x16x32_bf16 v[32:35], v[36:39], v[16:19], v[32:35]\n\t" \
    "v_mov_b32_dpp v22, v18 row_half_mirror row_mask:0xf bank_mask:0xf\n\t" \
    "v_mov_b32_dpp v23, v19 row_half_mirror row_mask:0xf bank_mask:0xf\n\t" \
    "v_exp_f32 v24, v24\n\t" \
    "v_max3_u32 %[t0], %[t0], v15, v16\n\t" \
    "s_waitcnt lgkmcnt(0)\n\t" \
    "v_mfma_f32_16x16x32_bf16 v[28:31], v[40:43], v[20:23], v[28:31]\n\t" \
    "v_fmac_f32 v25, v24, v27\n\t" \
    "v_max3_u32 v8, v8, v27, v17\n\t" \
    "v_mfma_f32_16x16x32_bf16 v[32:35], v[44:47], v[20:23], v[32:35]\n\t" \
    "v_rcp_f32 v24, v25\n\t" \
    "v_max3_u32 %[t0], %[t0], v18, v19\n\t" \
    "v_max_u32 v8, v8, %[t0]\n\t" \
    "v_cmp_eq_u32 vcc, -1, v8\n\t" \
    "v_mov_b32 v9, 0xff\n\t" \
    "s_nop 1\n\t" \
    "s_cbranch_vccnz 6f\n\t" \
    "v_mov_b32_dpp v28, v32 quad_perm:[0,1,2,3] row_mask:0xf bank_mask:0xa\n\t" \
    "v_mov_b32_dpp v29, v33 quad_perm:[0,1,2,3] row_mask:0xf bank_mask:0xa\n\t" \
    "v_mov_b32_dpp v30, v34 quad_perm:[0,1,2,3] row_mask:0xf bank_mask:0xa\n\t" \
    "v_mov_b32_dpp v31, v35 quad_perm:[0,1,2,3] row_mask:0xf bank_mask:0xa\n\t" \
    "v_pk_mul_f32 v[28:29], v[28:29], v[24:25] op_sel_hi:[1,0]\n\t" \
    "v_pk_mul_f32 v[30:31], v[30:31], v[24:25] op_sel_hi:[1,0]\n\t" \
    "s_nop 0\n\t" \
    "v_add_f32_dpp v28, v28, v28 row_shl:8 row_mask:0xf bank_mask:0xf\n\t" \
    "v_add_f32_dpp v29, v29, v29 row_shl:8 row_mask:0xf bank_mask:0xf\n\t" \
    "v_add_f32_dpp v30, v30, v30 row_shl:8 row_mask:0xf bank_mask:0xf\n\t" \
    "v_add_f32_dpp v31, v31, v31 row_shl:8 row_mask:0xf bank_mask:0xf\n\t" \
    "s_mov_b32 exec_lo, 0x00ff00ff\n\t" \
    "s_mov_b32 exec_hi, 0x00ff00ff\n\t" \
    "v_bfi_b32 v28, v9, %[tag], v28\n\t" \
    "v_bfi_b32 v29, v9, %[tag], v29\n\t" \
    "v_bfi_b32 v30, v9, %[tag], v30\n\t" \
    "v_bfi_b32 v31, v9, %[tag], v31\n\t" \
    "global_store_dwordx4 %[so], v[28:31], %[ws] sc1\n\t" \
    "s_mov_b64 exec, -1\n\t" \
    "s_branch 2f\n\t" \
    "6:\n\t" \
    "s_mov_b64 exec, -1\n\t" \
    "s_sub_u32 %[st], %[st], 1\n\t" \
    "s_cbranch_scc1 2f\n\t" \
    RETRY_ASM \
    "s_sleep 6\n\t" \
    "s_branch 5b\n\t" \
    "2:\n\t"
#define ASM_GU \
    "s_mov_b32 %[st], 0x10000\n\t" \
    "5:\n\t" \
    "v_xor_b32 v11, 0x80, %[a0]\n\t" \
    "ds_read_b64_tr_b16 v[16:17], %[a0] offset:0\n\t" \
    "ds_read_b64_tr_b16 v[18:19], v11 offset:0\n\t" \
    "ds_read_b64_tr_b16 v[36:37], %[a0] offset:20480\n\t" \
    "ds_read_b64_tr_b16 v[38:39], v11 offset:20480\n\t" \
    "ds_read_b64_tr_b16 v[20:21], %[a0] offset:1024\n\t" \
    "ds_read_b64_tr_b16 v[22:23], v11 offset:1024\n\t" \
    "ds_read_b64_tr_b16 v[40:41], %[a0] offset:21504\n\t" \
    "ds_read_b64_tr_b16 v[42:43], v11 offset:21504\n\t" \
    "ds_read_b64_tr_b16 v[24:25], %[a0] offset:2048\n\t" \
    "ds_read_b64_tr_b16 v[26:27], v11 offset:2048\n\t" \
    "ds_read_b64_tr_b16 v[44:45], %[a0] offset:22528\n\t" \
    "ds_read_b64_tr_b16 v[46:47], v11 offset:22528\n\t" \
    "ds_read_b64_tr_b16 v[28:29], %[a0] offset:3072\n\t" \
    "ds_read_b64_tr_b16 v[30:31], v11 offset:3072\n\t" \
    "ds_read_b64_tr_b16 v[48:49], %[a0] offset:23552\n\t" \
    "ds_read_b64_tr_b16 v[50:51], v11 offset:23552\n\t" \
    "ds_read_b64_tr_b16 v[32:33], %[a0] offset:4096\n\t" \
    "ds_read_b64_tr_b16 v[34:35], v11 offset:4096\n\t" \
    "ds_read_b64_tr_b16 v[52:53], %[a0] offset:24576\n\t" \
    "ds_read_b64_tr_b16 v[54:55], v11 offset:24576\n\t" \
    "v_mov_b32 v12, 0x3f803f80\n\t" \
    "v_mov_b32 v13, 0x3f803f80\n\t" \
    "v_mov_b32 v14, 0x3f803f80\n\t" \
    "v_mov_b32 v15, 0x3f803f80\n\t" \
    "v_mov_b32 v56, 0x3f803f80\n\t" \
    "v_mov_b32 v57, 0x3f803f80\n\t" \
    "v_mov_b32 v58, 0x3f803f80\n\t" \
    "v_mov_b32 v59, 0x3f803f80\n\t" \
    "4:\n\t" \
    "s_memrealtime vcc\n\t" \
    "s_waitcnt lgkmcnt(0)\n\t" \
    "s_sub_i32 vcc_lo, vcc_lo, %[tg]\n\t" \
    "s_cmp_lt_i32 vcc_lo, 0\n\t" \
    "s_cbranch_scc1 4b\n\t" \
    "global_load_dwordx4 v[6:9], %[gs], %[ws] sc1\n\t" \
    "global_load_dword v10, %[g2], %[ws] sc1\n\t" \
    "s_waitcnt vmcnt(1)\n\t" \
    "v_mov_b32_dpp v12, v6 quad_perm:[0,1,2,3] row_mask:0xf bank_mask:0x1\n\t" \
    "v_mov_b32_dpp v13, v7 quad_perm:[0,1,2,3] row_mask:0xf bank_mask:0x1\n\t" \
    "v_mov_b32_dpp v14, v8 quad_perm:[0,1,2,3] row_mask:0xf bank_mask:0x1\n\t" \
    "v_mov_b32_dpp v15, v9 quad_perm:[0,1,2,3] row_mask:0xf bank_mask:0x1\n\t" \
    "v_mov_b32_dpp v56, v6 row_shl:4 row_mask:0xf bank_mask:0x1\n\t" \
    "v_mov_b32_dpp v57, v7 row_shl:4 row_mask:0xf bank_mask:0x1\n\t" \
    "v_mfma_f32_16x16x32_bf16 v[16:19], v[16:19], v[12:15], 0\n\t" \
    "v_mov_b32_dpp v58, v8 row_shl:4 row_mask:0xf bank_mask:0x1\n\t" \
    "v_mov_b32_dpp v59, v9 row_shl:4 row_mask:0xf bank_mask:0x1\n\t" \
    "v_mfma_f32_16x16x32_bf16 v[36:39], v[36:39], v[12:15], 0\n\t" \
    "v_max3_u32 v11, v6, v7, v8\n\t" \
    "v_mfma_f32_16x16x32_bf16 v[60:63], v[12:15], v[12:15], 0\n\t" \
    "v_mfma_f32_16x16x32_bf16 v[16:19], v[20:23], v[56:59], v[16:19]\n\t" \
    "v_mov_b32_dpp v12, v6 row_shl:8 row_mask:0xf bank_mask:0x1\n\t" \
    "v_mov_b32_dpp v13, v7 row_shl:8 row_mask:0xf bank_mask:0x1\n\t" \
    "v_mfma_f32_16x16x32_bf16 v[36:39], v[40:43], v[56:59], v[36:39]\n\t" \
    "v_mov_b32_dpp v14, v8 row_shl:8 row_mask:0xf bank_mask:0x1\n\t" \
    "v_mov_b32_dpp v15, v9 row_shl:8 row_mask:0xf bank_mask:0x1\n\t" \
    "v_mfma_f32_16x16x32_bf16 v[60:63], v[56:59], v[56:59], v[60:63]\n\t" \
    "s_nop 0\n\t" \
    "v_mfma_f32_16x16x32_bf16 v[16:19], v[24:27], v[12:15], v[16:19]\n\t" \
    "v_mov_b32_dpp v56, v6 row_shl:12 row_mask:0xf bank_mask:0x1\n\t" \
    "v_mov_b32_dpp v57, v7 row_shl:12 row_mask:0xf bank_mask:0x1\n\t" \
    "v_mfma_f32_16x16x32_bf16 v[36:39], v[44:47], v[12:15], v[36:39]\n\t" \
    "v_mov_b32_dpp v58, v8 row_shl:12 row_mask:0xf bank_mask:0x1\n\t" \
    "v_mov_b32_dpp v59, v9 row_shl:12 row_mask:0xf bank_mask:0x1\n\t" \
    "v_mfma_f32_16x16x32_bf16 v[60:63], v[12:15], v[12:15], v[60:63]\n\t" \
    "s_nop 0\n\t" \
    "v_mfma_f32_16x16x32_bf16 v[16:19], v[28:31], v[56:59], v[16:19]\n\t" \
    "s_waitcnt vmcnt(0)\n\t" \
    "v_mov_b32_dpp v12, v10 quad_perm:[0,1,2,3] row_mask:0xf bank_mask:0x1\n\t" \
    "v_mov_b32_dpp v13, v10 row_shl:4 row_mask:0xf bank_mask:0x1\n\t" \
    "v_mfma_f32_16x16x32_bf16 v[36:39], v[48:51], v[56:59], v[36:39]\n\t" \
    "v_mov_b32_dpp v14, v10 row_shl:8 row_mask:0xf bank_mask:0x1\n\t" \
    "v_mov_b32_dpp v15, v10 row_shl:12 row_mask:0xf bank_mask:0x1\n\t" \
    "v_mfma_f32_16x16x32_bf16 v[60:63], v[56:59], v[56:59], v[60:63]\n\t" \
    "v_max3_u32 v11, v11, v9, v10\n\t" \
    "v_mfma_f32_16x16x32_bf16 v[16:19], v[32:35], v[12:15], v[16:19]\n\t" \
    "v_cmp_eq_u32 vcc, -1, v11\n\t" \
    "v_mfma_f32_16x16x32_bf16 v[36:39], v[52:55], v[12:15], v[36:39]\n\t" \
    "v_mfma_f32_16x16x32_bf16 v[60:63], v[12:15], v[12:15], v[60:63]\n\t" \
    "s_mov_b32 exec_lo, 0x001f001f\n\t" \
    "s_mov_b32 exec_hi, 0x001f001f\n\t" \
    "s_cbranch_vccnz 6f\n\t" \
    "7:\n\t" \
    "s_nop 3\n\t" \
    "ds_write2_b32 %[pw], v16, v36 offset0:0 offset1:1\n\t" \
    "ds_write2_b32 %[pw], v17, v37 offset0:12 offset1:13\n\t" \
    "ds_write2_b32 %[pw], v18, v38 offset0:24 offset1:25\n\t" \
    "ds_write2_b32 %[pw], v19, v39 offset0:36 offset1:37\n\t" \
    "ds_write2_b32 %[gw], v60, v61 offset1:4\n\t" \
    "ds_write2_b32 %[gw], v62, v63 offset0:8 offset1:12\n\t" \
    "s_mov_b64 exec, -1\n\t" \
    "s_waitcnt lgkmcnt(0)\n\t" \
    "s_barrier\n\t" \
    "s_branch 2f\n\t" \
    "6:\n\t" \
    "s_sub_u32 %[st], %[st], 1\n\t" \
    "s_cbranch_scc1 7b\n\t" \
    "s_mov_b64 exec, -1\n\t" \
    RETRY_ASM \
    "s_sleep 1\n\t" \
    "s_branch 5b\n\t" \
    "2:\n\t"
#define ASM_HS \
    "ds_read_b64_tr_b16 v[16:17], %[w0] offset:0\n\t" \
    "ds_read_b64_tr_b16 v[18:19], %[w1] offset:32\n\t" \
    "ds_read_b64_tr_b16 v[20:21], %[w2] offset:64\n\t" \
    "ds_read_b64_tr_b16 v[22:23], %[w3] offset:96\n\t" \
    "ds_read_b64_tr_b16 v[24:25], %[w0] offset:128\n\t" \
    "ds_read_b64_tr_b16 v[26:27], %[w1] offset:160\n\t" \
    "ds_read_b64_tr_b16 v[28:29], %[w2] offset:192\n\t" \
    "ds_read_b64_tr_b16 v[30:31], %[w3] offset:224\n\t" \
    "ds_read_b64_tr_b16 v[32:33], %[w0] offset:256\n\t" \
    "ds_read_b64_tr_b16 v[34:35], %[w1] offset:288\n\t" \
    "v_mov_b32 v36, 0\n\t" \
    "v_mov_b32 v37, 0\n\t" \
    "v_mov_b32 v38, 0\n\t" \
    "v_mov_b32 v39, 0\n\t" \
    "v_mov_b32 v40, 0\n\t" \
    "v_mov_b32 v41, 0\n\t" \
    "v_mov_b32 v42, 0\n\t" \
    "v_mov_b32 v43, 0\n\t" \
    "v_add_u32 v12, 0x40000, %[so]\n\t" \
    "v_add_u32 v13, 0x80000, %[so]\n\t" \
    "s_waitcnt lgkmcnt(0)\n\t" \
    "s_barrier\n\t" \
    "ds_read_b128 v[44:47], %[sx]\n\t" \
    "ds_read_b128 v[48:51], %[sq]\n\t" \
    "ds_read_b128 v[52:55], %[cp]\n\t" \
    "ds_read_b128 v[56:59], %[cp] offset:16\n\t" \
    "ds_read_b128 v[60:63], %[rp]\n\t" \
    "ds_read_b128 v[8:11], %[rp] offset:16\n\t" \
    "s_waitcnt lgkmcnt(5)\n\t" \
    "v_pk_add_f32 v[44:45], v[44:45], v[46:47]\n\t" \
    "s_waitcnt lgkmcnt(4)\n\t" \
    "v_pk_add_f32 v[48:49], v[48:49], v[50:51]\n\t" \
    "v_add_f32 v44, v44, v45\n\t" \
    "v_add_f32 v48, v48, v49\n\t" \
    "v_mul_f32 v44, 0x3acccccd, v44\n\t" \
    "v_mul_f32 v48, 0x3acccccd, v48\n\t" \
    "v_fma_f32 v48, -v44, v44, v48\n\t" \
    "v_max_f32 v48, 0, v48\n\t" \
    "v_cvt_pk_bf16_f32 v44, v44, v48\n\t" \
    "v_and_b32 v48, 0xffff0000, v44\n\t" \
    "v_add_f32 v48, 0x3727c5ac, v48\n\t" \
    "v_cvt_pk_bf16_f32 v48, v48, v48\n\t" \
    "v_lshlrev_b32 v48, 16, v48\n\t" \
    "v_sqrt_f32 v48, v48\n\t" \
    "s_waitcnt lgkmcnt(2)\n\t" \
    "v_pk_add_f32 v[52:53], v[52:53], v[54:55]\n\t" \
    "v_cvt_pk_bf16_f32 v48, v48, v48\n\t" \
    "v_lshlrev_b32 v48, 16, v48\n\t" \
    "v_rcp_f32 v48, v48\n\t" \
    "v_pk_add_f32 v[56:57], v[56:57], v[58:59]\n\t" \
    "v_lshlrev_b32 v44, 16, v44\n\t" \
    "s_waitcnt lgkmcnt(0)\n\t" \
    "v_pk_add_f32 v[60:61], v[60:61], v[62:63]\n\t" \
    "v_pk_add_f32 v[8:9], v[8:9], v[10:11]\n\t" \
    "v_pk_add_f32 v[52:53], v[52:53], v[56:57]\n\t" \
    "v_pk_add_f32 v[60:61], v[60:61], v[8:9]\n\t" \
    "v_pk_fma_f32 v[60:61], v[52:53], v[44:45], v[60:61] op_sel_hi:[1,0,1] neg_lo:[1,0,0] neg_hi:[1,0,0]\n\t" \
    "v_pk_mul_f32 v[60:61], v[60:61], v[48:49] op_sel_hi:[1,0]\n\t" \
    "v_cvt_pk_bf16_f32 v60, v60, v61\n\t" \
    "v_lshlrev_b32 v61, 16, v60\n\t" \
    "v_mul_f32 v62, 0xbfb8aa3b, v61\n\t" \
    "v_exp_f32 v62, v62\n\t" \
    "v_and_b32 v60, 0xffff0000, v60\n\t" \
    "v_add_f32 v62, 1.0, v62\n\t" \
    "v_rcp_f32 v62, v62\n\t" \
    "s_nop 0\n\t" \
    "v_mul_f32 v61, v62, v61\n\t" \
    "v_cvt_pk_bf16_f32 v61, v61, v61\n\t" \
    "v_lshlrev_b32 v61, 16, v61\n\t" \
    "v_mul_f32 v60, v60, v61\n\t" \
    "s_nop 1\n\t" \
    "v_mov_b32_dpp v61, v60 row_ror:12 row_mask:0xf bank_mask:0xf\n\t" \
    "v_cvt_pk_bf16_f32 v60, v60, v61\n\t" \
    "s_nop 1\n\t" \
    "v_mov_b32_dpp v36, v60 quad_perm:[0,1,2,3] row_mask:0xf bank_mask:0x1\n\t" \
    "v_mov_b32_dpp v37, v60 row_ror:8 row_mask:0xf bank_mask:0x1\n\t" \
    "v_mov_b32_dpp v38, v60 quad_perm:[0,1,2,3] row_mask:0xf bank_mask:0x2\n\t" \
    "v_mov_b32_dpp v39, v60 row_ror:8 row_mask:0xf bank_mask:0x2\n\t" \
    "v_mov_b32_dpp v40, v60 quad_perm:[0,1,2,3] row_mask:0xf bank_mask:0x4\n\t" \
    "v_mov_b32_dpp v41, v60 row_ror:8 row_mask:0xf bank_mask:0x4\n\t" \
    "v_mfma_f32_16x16x32_bf16 v[44:47], v[16:19], v[36:39], 0\n\t" \
    "v_mov_b32_dpp v42, v60 quad_perm:[0,1,2,3] row_mask:0xf bank_mask:0x8\n\t" \
    "v_mov_b32_dpp v43, v60 row_ror:8 row_mask:0xf bank_mask:0x8\n\t" \
    "v_mfma_f32_16x16x32_bf16 v[48:51], v[24:27], v[36:39], 0\n\t" \
    "v_mfma_f32_16x16x32_bf16 v[52:55], v[32:35], v[36:39], 0\n\t" \
    "v_mfma_f32_16x16x32_bf16 v[44:47], v[20:23], v[40:43], v[44:47]\n\t" \
    "v_mfma_f32_16x16x32_bf16 v[48:51], v[28:31], v[40:43], v[48:51]\n\t" \
    "s_nop 5\n\t" \
    "v_min_u32 v52, -2, v52\n\t" \
    "v_min_u32 v53, -2, v53\n\t" \
    "v_min_u32 v54, -2, v54\n\t" \
    "v_min_u32 v55, -2, v55\n\t" \
    "v_min_u32 v44, -2, v44\n\t" \
    "v_min_u32 v45, -2, v45\n\t" \
    "v_min_u32 v46, -2, v46\n\t" \
    "v_min_u32 v47, -2, v47\n\t" \
    "global_store_dwordx4 %[so], v[44:47], %[ws] sc1\n\t" \
    "v_min_u32 v48, -2, v48\n\t" \
    "v_min_u32 v49, -2, v49\n\t" \
    "v_min_u32 v50, -2, v50\n\t" \
    "v_min_u32 v51, -2, v51\n\t" \
    "global_store_dwordx4 v12, v[48:51], %[ws] sc1\n\t" \
    "s_mov_b32 exec_lo, 0x00ff00ff\n\t" \
    "s_mov_b32 exec_hi, 0x00ff00ff\n\t" \
    "global_store_dwordx4 v13, v[52:55], %[ws] sc1\n\t" \
    "s_mov_b64 exec, -1\n\t"
#define CLOB_32_59 "v32", "v33", "v34", "v35", "v36", "v37", "v38", "v39", "v40", "v41", "v42", "v43", "v44", "v45", "v46", "v47", "v48", "v49", "v50", "v51", "v52", "v53", "v54", "v55", "v56", "v57", "v58", "v59"
#define CLOB_8_15 "v8", "v9", "v10", "v11", "v12", "v13", "v14", "v15"
#define CLOB_16_59 "v16", "v17", "v18", "v19", "v20", "v21", "v22", "v23", "v24", "v25", "v26", "v27", "v28", "v29", "v30", "v31", "v32", "v33", "v34", "v35", "v36", "v37", "v38", "v39", "v40", "v41", "v42", "v43", "v44", "v45", "v46", "v47", "v48", "v49", "v50", "v51", "v52", "v53", "v54", "v55", "v56", "v57", "v58", "v59"
#define CLOB_16_63 "v16", "v17", "v18", "v19", "v20", "v21", "v22", "v23", "v24", "v25", "v26", "v27", "v28", "v29", "v30", "v31", "v32", "v33", "v34", "v35", "v36", "v37", "v38", "v39", "v40", "v41", "v42", "v43", "v44", "v45", "v46", "v47", "v48", "v49", "v50", "v51", "v52", "v53", "v54", "v55", "v56", "v57", "v58", "v59", "v60", "v61", "v62", "v63"
template <int R, int IMM> __device__ __forceinline__ void ldkv(const void* sbase, unsigned voff) {
    if constexpr ((R < 160 && (POL & 1)) || (R >= 160 && (POL & 2)))
        asm volatile("global_load_dwordx4 v[%c2:%c3], %0, %1 offset:%c4" :: "v"(voff), "s"(sbase), "i"(R), "i"(R + 3), "i"(IMM) : "memory");
    else
        asm volatile("global_load_dwordx4 v[%c2:%c3], %0, %1 offset:%c4 nt" :: "v"(voff), "s"(sbase), "i"(R), "i"(R + 3), "i"(IMM) : "memory");
}
template <int A> __device__ __forceinline__ void fix_k() {
    unsigned t0, t1, t2, t3;
    asm volatile("v_mov_b32 %0, v%c4\n\tv_mov_b32 %1, v%c5\n\tv_mov_b32 %2, v%c6\n\tv_mov_b32 %3, v%c7\n\t"
                 "v_mov_b32_dpp v%c4, v%c8 row_ror:8 row_mask:0xf bank_mask:0xc\n\tv_mov_b32_dpp v%c5, v%c9 row_ror:8 row_mask:0xf bank_mask:0xc\n\t"
                 "v_mov_b32_dpp v%c6, v%c10 row_ror:8 row_mask:0xf bank_mask:0xc\n\tv_mov_b32_dpp v%c7, v%c11 row_ror:8 row_mask:0xf bank_mask:0xc\n\t"
                 "v_mov_b32_dpp v%c8, %0 row_ror:8 row_mask:0xf bank_mask:0x3\n\tv_mov_b32_dpp v%c9, %1 row_ror:8 row_mask:0xf bank_mask:0x3\n\t"
                 "v_mov_b32_dpp v%c10, %2 row_ror:8 row_mask:0xf bank_mask:0x3\n\tv_mov_b32_dpp v%c11, %3 row_ror:8 row_mask:0xf bank_mask:0x3"
                 : "=&v"(t0), "=&v"(t1), "=&v"(t2), "=&v"(t3) : "i"(A), "i"(A + 1), "i"(A + 2), "i"(A + 3), "i"(A + 12), "i"(A + 13), "i"(A + 14), "i"(A + 15));
}
#define PACE 1
#define D7 16
template <int D = DHI> __device__ __forceinline__ void pace(unsigned& due, unsigned step) {
    asm volatile("s_waitcnt vmcnt(%2)\n\ts_add_u32 %0, %0, %1\n1:\n\ts_memrealtime vcc\n\ts_waitcnt lgkmcnt(0)\n\ts_lshl_b32 vcc_lo, vcc_lo, 4\n\ts_sub_i32 vcc_lo, vcc_lo, %0\n\ts_cmp_ge_i32 vcc_lo, 0\n\ts_cbranch_scc0 1b"
                 : "+s"(due) : "s"(step), "i"(D) : "memory", "scc", "vcc");
}
__device__ __forceinline__ unsigned pace_t(unsigned& due, unsigned step) {
    unsigned t;
    asm volatile("s_waitcnt vmcnt(%3)\n\ts_add_u32 %0, %0, %2\n1:\n\ts_memrealtime vcc\n\ts_waitcnt lgkmcnt(0)\n\ts_lshl_b32 %1, vcc_lo, 4\n\ts_sub_i32 vcc_lo, %1, %0\n\ts_cmp_ge_i32 vcc_lo, 0\n\ts_cbranch_scc0 1b"
                 : "+s"(due), "=&s"(t) : "s"(step), "i"(DHI) : "memory", "scc", "vcc");
    return t;
}
#define MID 0
#define MIDN 7
#define JE 0
#define PW 0
#define CPF 0
#define PADN 0
#define KF 0
#define W7F 0
#define EPF_PULL 0
#define FIRSTD 0
#define EPM 248
#define EPN 16
#define PULLM 0
#define KEEPX " sc1"
#define LKEEP 0
#define LPN 4
#define TPN 0
#define OPCL
#define STG 2
#define AB(x) 0
#define OWN 0
#define OWNER(h, sub) ((OWN & 1) ? ((h) >> 4) * 80 + ((0x03142 >> (4 * (sub))) & 7) * 16 + ((h) & 15) : (h) * 5 + (sub))
#define PGM 255
#define PAGE(k) do { if (PGM & (1 << (k))) asm volatile("s_branch 1f\n\t.p2align 12\n1:" ::: "memory"); } while (0)
#define TRCL
#define QSP 0
#define XI 14
#define OPA 1
#define EHW 2
#define GPOL 1
#define QZ 0
#define PRI 3
#define P1ONLY 0
#undef NET
#define NET 0
#define NPS 0
#define NP0 0
#define MP0 24
#define MPS 3
static_assert(!WOMODE || (NET <= 3 && MP0 >= 11 && MP0 + (6 - NET) * MPS < 36), "extras");
template <int I, int HI, bool THR, class F> __device__ __forceinline__ void kv_stream_x(const void* kbase, const void* vbase, unsigned ko, unsigned v0, const unsigned (&to)[4], unsigned& due, unsigned& step, F&& extra) {
    if constexpr (I < HI) {
        if constexpr (NPS > 0 && I >= NP0 && (I - NP0) % (NPS > 0 ? NPS : 1) == 0 && (I - NP0) / (NPS > 0 ? NPS : 1) < 6) extra(100 + (I - NP0) / NPS);
        if constexpr (WOMODE && I >= MP0 && (I - MP0) % MPS == 0 && (I - MP0) / MPS < 7 - NET) { extra((I - MP0) / MPS); pace(due, step); }
        if constexpr (I < 24) ldkv<64 + 4 * I, (I % 3) * 128 + ((I / 3) & 1) * 3072>(kbase, ko + to[I / 6]);
        else ldkv<160 + 4 * (I - 24), ((I - 24) & 3) * 384>(vbase, v0 + to[(I - 24) >> 2]);
        if constexpr (I >= 14 && I < 38 && (I - 14) % 6 < 3) fix_k<64 + 4 * (I - 14)>();
        pace(due, step);
        if constexpr (I == XI || I == XI + 5) extra(300 + I - XI);
        if constexpr (I == P_QP || I == 14 || I == 18 || I == 26 || I == 34) extra(I == P_QP ? 216 : I == 14 ? 220 : I == 18 ? 224 : I == 26 ? 228 : 232);
        kv_stream_x<I + 1, HI, THR>(kbase, vbase, ko, v0, to, due, step, extra);
    }
}
template <int I, int HI, int THR> __device__ __forceinline__ void kv_stream(const void* kbase, const void* vbase, unsigned ko, unsigned v0, const unsigned (&to)[4], unsigned& due, unsigned step) {
    if constexpr (I < HI) {
        if constexpr (I < 24) ldkv<64 + 4 * I, (I % 3) * 128 + ((I / 3) & 1) * 3072>(kbase, ko + to[I / 6]);
        else ldkv<160 + 4 * (I - 24), ((I - 24) & 3) * 384>(vbase, v0 + to[(I - 24) >> 2]);
        if constexpr (THR == 1 && I >= P_BA) pace(due, step);
        if constexpr (THR == 2) pace<D7>(due, step);
        if constexpr (THR == 3) pace(due, step);
        kv_stream<I + 1, HI, THR>(kbase, vbase, ko, v0, to, due, step);
    }
}
#define NMS 22
#define NNE 48
#define DSL 0
#define NME 46
static_assert((NM7 == 0 || NN7 % 6 == 0) && NM7 <= NME - NMS && NN7 <= NNE - F7 && NN7 <= 48 - F7, "slots");
template <int I> struct IC { static constexpr int v = I; };
template <int I, int HI, class F> __device__ __forceinline__ void kv_stream7(const void* kbase, const void* vbase, unsigned ko, unsigned v0, unsigned v1, unsigned v2, const unsigned (&to)[4], unsigned& due, unsigned step, F&& nar) {
    if constexpr (I < HI) {
        if constexpr (I < 24) ldkv<64 + 4 * I, (I % 3) * 128 + ((I / 3) & 1) * 3072>(kbase, ko + to[I / 6]);
        else if constexpr (I < 40) ldkv<160 + 4 * (I - 24), ((I - 24) & 3) * 384>(vbase, v0 + to[(I - 24) >> 2]);
        else ldkv<160 + 4 * (I - 24), (I & 3) * 384>(vbase, I < 44 ? v1 : v2);
        if constexpr (((I - F7) * NN7) % (48 - F7) < NN7) nar(IC<((I - F7) * NN7) / (48 - F7)>{});
        if constexpr (NNE < 48 && I < NNE && ((I - F7) * NN7) % (NNE - F7) < NN7) nar(IC<200 + ((I - F7) * NN7) / (NNE - F7)>{});
        if constexpr (I >= NMS && I < NME && ((I - NMS) * NM7) % (NME - NMS) < NM7) nar(IC<100 + ((I - NMS) * NM7) / (NME - NMS)>{});
        if constexpr (I < 47) pace<D7>(due, step);
        kv_stream7<I + 1, HI>(kbase, vbase, ko, v0, v1, v2, to, due, step, nar);
    }
}
template <int I> __device__ __forceinline__ void fix_all() { if constexpr (I < 12) { fix_k<64 + 4 * ((I / 3) * 6 + I % 3)>(); fix_all<I + 1>(); } }
#define LANES() int lane; asm volatile("v_mbcnt_lo_u32_b32 %0, -1, 0\n\tv_mbcnt_hi_u32_b32 %0, -1, %0" : "=v"(lane)); __builtin_assume(lane >= 0 && lane < 64); \
    const int n = lane & 15, kq = lane >> 4, q4 = n >> 2, p4 = lane & 3; (void)n; (void)kq; (void)q4; (void)p4
template <unsigned C> __device__ __forceinline__ u32x4 splat() { unsigned a, b, c, d; asm volatile("v_mov_b32 %0, %4\n\tv_mov_b32 %1, %4\n\tv_mov_b32 %2, %4\n\tv_mov_b32 %3, %4" : "=v"(a), "=v"(b), "=v"(c), "=v"(d) : "i"(C)); return (u32x4){a, b, c, d}; }
static_assert(TDEPTH <= 8 && DHI <= 8, "fix_k schedule");
__device__ __forceinline__ u32x4 ld16_a(const void* sbase, unsigned voff) {
    u32x4 r; asm volatile("global_load_dwordx4 %0, %1, %2" : "=v"(r) : "v"(voff), "s"(sbase) : "memory"); return r;
}

template <bool KEEP = false> __device__ __forceinline__ void narrow(const u16* wg, const u16* wu, unsigned va, unsigned vb, unsigned lb, int s, int rot) {
    const int s10 = (s * 26) >> 8, r = s - 10 * s10, m = r >= 5, c = r - 5 * m, qt = (s10 + rot) & 3;
    if (KEEP) dma16p((m ? wu : wg) + (qt * 160 + (c & 3) * 8) * 1536, c == 4 ? vb : va, lb + L_GU + (unsigned)(m * 20480 + (qt * 5 + c) * 1024)); else
    dma16((m ? wu : wg) + (qt * 160 + (c & 3) * 8) * 1536, c == 4 ? vb : va, lb + L_GU + (unsigned)(m * 20480 + (qt * 5 + c) * 1024));
}
#define NROT 0
#define ROTS(s) (NROT ? ((s) + 7 * (mi & 3)) % (int)SCH(S_N1) : (s))
#define OSX ""
#define LATED 0
#define LDSL 2
template <int P> __device__ __forceinline__ void wo_piece(const u16* wob, unsigned oa, unsigned lb) {
    constexpr int j = P / 6, i = P % 6;
    dma16(wob + (i * 32 + (j & 1) * 16 + ((j >> 1) & 1) * 4 + (j >> 2) * 2) * DM, (j & 2) ? oa ^ 128u : oa, lb + L_WO + (j + 8 * i) * 1024);
}
template <int P, int HI, int SP = WSP> __device__ __forceinline__ void wo_rest(const u16* wob, unsigned oa, unsigned ob, unsigned lb) {
    if constexpr (P < HI) {
        constexpr int j = P / 6, i = P % 6; unsigned t;
        asm volatile("s_add_u32 m0, %3, %4\n\tv_add_u32 %0, %5, %1\n\tglobal_load_lds_dwordx4 %0, %2" WPOL
                     : "=&v"(t) : "v"((j & 2) ? ob : oa), "s"(wob), "s"(lb), "i"(L_WO + (j + 8 * i) * 1024), "i"((i * 32 + (j & 1) * 16 + ((j >> 1) & 1) * 4 + (j >> 2) * 2) * DM * 2) : "memory", "scc");
        if (SP > 0 && P < 47) __builtin_amdgcn_s_sleep(SP);
        if (SP < 0 && (P & 1) && P < 47) __builtin_amdgcn_s_sleep(-SP);
        wo_rest<P + 1, HI, SP>(wob, oa, ob, lb);
    }
}
#define WDP 8
template <int P, int HI> __device__ __forceinline__ void wo_paced(const u16* wob, unsigned oa, unsigned ob, unsigned lb, unsigned& due, unsigned step) {
    if constexpr (P < HI) { pace<WDP>(due, step); wo_rest<P, P + 1, 0>(wob, oa, ob, lb); wo_paced<P + 1, HI>(wob, oa, ob, lb, due, step); }
}
#define M0_KEEP() unsigned m0_; asm volatile("s_mov_b32 %0, m0" : "=s"(m0_) :: "memory")
#define M0_BACK() asm volatile("s_mov_b32 m0, %0" :: "s"(m0_) : "memory")
#define WO_ADDR() const u16* wob = wo + h * DH * DM + sub * 128; const unsigned woa = (unsigned)(((kq >> 1) * 8 + (kq & 1)) * DM * 2 + ((n >> 1) ^ kq) * 32 + (n & 1) * 16)
#define NARROW_ADDR() const int r5_ = swz(lane >> 1); const unsigned nva = (unsigned)(((r5_ & 7) + ((r5_ >> 3) << 5)) * 3072 + mi * 32 + (lane & 1) * 16), \
        nvb = (unsigned)((128 + r5_) * 3072 + mi * 32 + (lane & 1) * 16); const int nrot = 0

#define SLOWL(j) (((((unsigned)(size_t)wg >> 7) + (unsigned)(j)) & 7u) == 3u)
#define N1W ((int)(SLOWL(mi >> 2) ? SCH(S_N0) : SCH(S_N1)))
#define GUPF(rot) do { const int nd = (int)SCH(S_ND); int sh = (rr >> 3) * 5 + sub + (int)(rot); if (sh >= 20) sh -= 20; \
        _Pragma("unroll 1") for (int k_ = 0; k_ < nd; k_ += 21) { const int rw = k_ + lane / 3; \
        const unsigned o = (unsigned)((640 - 20 * nd + sh * nd + (rw < nd ? rw : nd - 1)) * 3072 + xg * 384 + (lane % 3) * 128); \
        if (GPOL) { pull(wg, o, lb + L_PF); pull(wu, o, lb + L_PF); } else { dma4(wg, o, lb + L_PF); dma4(wu, o, lb + L_PF); } } } while (0)
#define EXTRA(k) do { LANES(); \
        if (!is_mlp) { \
            const int j = (k) < 6 ? wave : 7, i = (k) < 6 ? (k) : wave < 5 ? wave : 5, pp = j * 4 + kq, f = (pp & 3) | (((pp >> 3) & 1) << 2); \
            const int dim = ((pp >> 4) & 1) * 16 + (pp & 1) * 8 + ((pp >> 1) & 7); \
            dma16(wo + (h * DH + i * 32) * DM + sub * 128, (unsigned)(dim * DM * 2 + ((n >> 1) ^ f) * 32 + (n & 1) * 16), lb + L_WO + (j + 8 * i) * 1024); \
        } else if ((k) < 4) { \
            const int s = (int)SCH(S_N0) + wave + 7 * (k); \
            if (s < (int)SCH(S_N1)) { NARROW_ADDR(); narrow(wg, wu, nva, nvb, lb, s, nrot); } \
        } else { \
            const int ii = wave + 7 * ((k) - 4), c = ii * 64 + lane; int row = c / 82, cc = c - 82 * row; if (cc > 79) cc = 79; if (row > 15) row = 15; \
            dma16(wd + mi * 16 * DM, (unsigned)((row * DM + cc * 8) * 2), lb + L_WD + ii * 1024); \
        } } while (0)

__global__ void __launch_bounds__(512, 2) __attribute__((amdgpu_num_vgpr(32), aligned(KAL)))
mega_kernel(const u16* __restrict__ kc, const u16* __restrict__ vc, unsigned char* __restrict__ ws, const u16* __restrict__ x0, const u16* __restrict__ wq,
            const u16* __restrict__ wg, const u16* __restrict__ wu, const u16* __restrict__ wo, const u16* __restrict__ wr,
            const u16* __restrict__ wd, u16* __restrict__ out
            ) {
    extern __shared__ __attribute__((aligned(16))) unsigned char smem[];
    constexpr unsigned sched_c[S_N] = SCHED; (void)sched_c;
    const unsigned lb = (unsigned)(size_t)smem;
#define STAMP0() do {} while (0)
    unsigned long long st64 = 0; (void)st64;
    const int wave0 = __builtin_amdgcn_readfirstlane(threadIdx.x >> 6);
    {
        unsigned long long kp, pc; unsigned s0, s1, s2, a0, a1;
        asm volatile("s_load_dwordx2 %0, %7, 0x0\n\ts_getpc_b64 %1\nmega_pf_here:\n\ts_lshr_b32 %2, %8, 3\n\tv_lshl_add_u32 %5, %9, 5, %2\n\tv_lshlrev_b32 %5, 7, %5\n\t"
                     "v_min_u32 %5, mega_pf_end - mega_pf_here - 4, %5\n\tglobal_load_dword v255, %5, %1\n\t"
                     "s_and_b32 %3, %8, 48\n\ts_and_b32 %4, %8, 7\n\ts_lshl1_add_u32 %3, %4, %3\n\ts_and_b32 %4, %2, 25\n\ts_lshl4_add_u32 %3, %3, %4\n\ts_add_u32 %3, %3, 6\n\ts_lshl_b32 %3, %3, 5\n\t"
                     "v_lshrrev_b32 %5, 6, %9\n\tv_add_u32 %5, %2, %5\n\tv_and_or_b32 %5, %5, 31, %3\n\tv_and_b32 %6, 7, %9\n\tv_lshl_add_u32 %5, %5, 4, %6\n\tv_mul_u32_u24 %5, 0x180, %5\n\t"
                     "v_and_or_b32 %5, %9, 48, %5\n\tv_lshlrev_b32 %6, 3, %9\n\tv_and_or_b32 %5, %6, 64, %5\n\ts_waitcnt lgkmcnt(0)\n\t"
                     "global_load_dwordx4 v[64:67], %5, %0 nt\n\tglobal_load_dwordx4 v[68:71], %5, %0 offset:128 nt\n\tglobal_load_dwordx4 v[72:75], %5, %0 offset:256 nt\n\tglobal_load_dwordx4 v[76:79], %5, %0 offset:3072 nt\n\t"
                     "s_memrealtime %1\n\ts_waitcnt lgkmcnt(0)"
                     : "=&s"(kp), "=&s"(pc), "=&s"(s0), "=&s"(s1), "=&s"(s2), "=&v"(a0), "=&v"(a1) : "s"(__builtin_amdgcn_kernarg_segment_ptr()), "s"(blockIdx.x), "v"(threadIdx.x) : "memory", "scc");
        st64 = pc;
    }
    const u32x4 ones = {~0u, ~0u, ~0u, ~0u};
    const f32x4 z4 = {0.f, 0.f, 0.f, 0.f};
    asm volatile("" ::: "v255");
#define WSOFF 0

#define DMARK(k) do {} while (0)
    unsigned to[4];
#pragma unroll
    for (int t = 0; t < 4; ++t) to[t] = ((t * 8 + wave0 + (GID >> 3)) & 31u) * 6144u;
    unsigned step = wave0 == 7 ? SCH(S_P7) : ((GID >> 3) & 7) >= 5 ? SCH(S_PM) : SCH(S_PD);
    unsigned t_start, due, t0, pol = 0u, tag = 0u;
#define START(off) do { t0 = t_start = (unsigned)st64; due = ((t_start + (off)) << 4) + ((((wave0 + STG) & 7) * step) >> 3); STAMPV(10, t_start); STAMP0(); } while (0)
    {
        const int wave = wave0, lane = threadIdx.x & 63;
        const int g = GID, xg = g & 7, rr = g >> 3;
        const int h = xg * 4 + (rr >> 3), sub = rr & 7, b = sub >> 1, half = sub & 1;
        const bool is_mlp = sub >= 5; const int mi = h * 3 + sub - 5, oi = OWNER(h, sub);
        const int n = lane & 15, kq = lane >> 4, q4 = n >> 2, p4 = lane & 3;
        const u16* kbase = kc + ((size_t)(b * HQ + h) * SK + (SK - WIN) + half * 512) * DH;
        const u16* vbase = vc + ((size_t)(b * HQ + h) * SK + (SK - WIN) + half * 512) * DH;
        const unsigned ko = (unsigned)((lane & 7) * 384 + (((lane >> 3) & 1) * 4 + (lane >> 4)) * 16);
        const unsigned v0 = (unsigned)(4 * kq * 384 + n * 16);
        const unsigned v1 = (unsigned)(4 * kq * 384 + 256 + (n & 7) * 16) + (n >= 8 ? to[2] : to[0]), v2 = (unsigned)(4 * kq * 384 + 256 + (n & 7) * 16) + (n >= 8 ? to[3] : to[1]);
        auto lds_zero_nb = [&]() {
            if (threadIdx.x < 16) *reinterpret_cast<unsigned*>(smem + L_CNT + threadIdx.x * 4) = 0u;
            *reinterpret_cast<f32x4*>(smem + L_P + threadIdx.x * 16) = z4;
            if (threadIdx.x < 32) *reinterpret_cast<f32x4*>(smem + L_P + 8192 + threadIdx.x * 16) = z4;
        };
        auto lds_zero = [&]() { lds_zero_nb(); BAR(); };
        auto lds_inc = [&](int idx) {
            asm volatile("s_waitcnt lgkmcnt(0)" ::: "memory");
            if (lane == 0) __hip_atomic_fetch_add(reinterpret_cast<unsigned*>(smem + L_CNT) + idx, 1u, __ATOMIC_RELAXED, __HIP_MEMORY_SCOPE_WORKGROUP);
        };
        const unsigned po = WS_PR + (unsigned)((mi & 1) * LB + ((mi >> 1) & 1) * 8192 + (mi >> 1) * 128);
        auto clean = [&]() {
            if (is_mlp) {
#pragma unroll
                for (int i = 0; i < 3; ++i) {
                    const int c = mi * 3 + i;
                    const int lim = c >= 256 ? (AB(64) ? 0 : 64) : c >= 64 && c < 192 ? ((c & 3) == 3 ? MLS / 2 : AB(4) ? 0 : 64) : (c & 7) >= 6 ? 40 : c < 64 || AB(32) ? (AB(2) ? 0 : 64) : 0;
                    if (lane < lim) st16_e(ws, WS_XA + (unsigned)(c * 1024 + lane * 16), ones);
                }
                if (lane < 16 && !AB(8)) st16_sc1(ws, (po ^ (unsigned)((lane & 8) << 10)) + (unsigned)((lane & 7) * 16), ones);
            }
        };
        auto first7 = [&]() {
            if (CA == 0) clean();
            if (EPF != 3) kv_stream<0, F7, 0>(kbase, vbase, ko, v0, to, due, step);
            START(SCH(S_O7));
            lds_zero_nb();
            __builtin_amdgcn_s_setprio(3);
            {
                unsigned d;
                asm volatile("s_waitcnt lgkmcnt(0)\n\ts_load_dword %0, %2, %3\n\ts_load_dword %1, %2, %4\n\ts_barrier\n\ts_waitcnt lgkmcnt(0)"
                             : "=&s"(tag), "=&s"(d) : "s"(ws), "s"(WS_EP), "s"(is_mlp ? (po | (2 * GP)) & ~16383u : WS_EP) : "memory");
                tag += 0x01010101u;
            }
            STAMP(24);
        };
        int w7 = wave;
        { if (wave == 7) first7(); asm volatile("" : "+s"(w7)); }
        if (w7 != 7) {
            const unsigned o2 = (unsigned)((wave & 3) * 1280 + lane * 16);
            u32x4 xa_ = ones, xc_ = ones;
            START(is_mlp ? SCH(S_MO) : SCH(S_PO));
            if (FIRSTD) { VMW(3); STAMP(36); VMW(0); STAMP(37); }
            lds_zero();
            if (!is_mlp) {
                xa_ = ld16_a(x0, o2); xc_ = ld16_a(x0, lane < 16 ? o2 + 1024 : o2);
#pragma unroll
                for (int i = 0; i < 7; ++i) {
                    int ii = wave + 7 * i; if (ii > 47) ii = 47;
                    const int c = ii * 64 + lane, row = c / 24, u = c - 24 * row;
                    const int f = ((row >> 1) & 1) | (((row >> 3) & 1) << 1);
                    unsigned o = (unsigned)(((sub * 128 + kpos(row)) * QD + h * DH) * 2 + ((u >> 1) ^ f) * 32 + (u & 1) * 16);
                    asm volatile("" : "+v"(o));
                    if (i < 2) pace<DHI + 2>(due, step); else pace(due, step);
                    dma16(wq, o, lb + L_WQ + ii * 1024);
                }
            }
            pace(due, step);
            kv_stream<4, P_LN, 3>(kbase, vbase, ko, v0, to, due, step);
            if (!is_mlp) {
                asm volatile("s_waitcnt vmcnt(%2)" : "+v"(xa_), "+v"(xc_) : "i"(P_LN - (EPF == 3 ? 4 : 0)) : "memory");
                if (wave >= 3) {
                    float mb, rden; row_stats(xa_, xc_, lane, mb, rden);
                    const f32x2 mb2 = {mb, mb}, rd2 = {rden, rden};
#pragma unroll
                    for (int j = 0; j < 4; ++j) { xa_[j] = pk2v(unpk(pk2v(unpk(xa_[j]) - mb2)) * rd2); xc_[j] = pk2v(unpk(pk2v(unpk(xc_[j]) - mb2)) * rd2); }
                    *reinterpret_cast<u32x4*>(smem + L_OP + (wave & 3) * 1344 + lane * 16) = xa_;
                    if (lane < 16) *reinterpret_cast<u32x4*>(smem + L_OP + (wave & 3) * 1344 + 1024 + lane * 16) = xc_;
                }
            }
            lds_inc(1);
            kv_stream<P_LN, P_QP, 1>(kbase, vbase, ko, v0, to, due, step);
            if (!is_mlp && wave < 6) {
                lds_wait_ge(lb + L_CNT + 4, 7u);
                STAMP(1);
                const unsigned a0 = lb + L_WQ + (unsigned)((8 * kq + q4) * 384 + ((2 * wave) ^ ((q4 >> 1) | ((kq & 1) << 1))) * 32 + p4 * 8), a1 = a0 ^ 32u;
                const unsigned char* xp = smem + L_OP + ((n >> 1) & 3) * 1344 + sub * 256 + kq * 64;
                f32x4 c0 = z4, c1 = z4;
#pragma unroll
                for (int s = 0; s < 4; ++s) {
                    const u32x4 bf = n >= 8 ? (u32x4){0x3f803f80u, 0x3f803f80u, 0x3f803f80u, 0x3f803f80u} : *reinterpret_cast<const u32x4*>(xp + s * 16);
                    c0 = mfma32w(cat8(tr64(a0 + s * 12288), tr64(a0 + s * 12288 + 1536)), bf, c0);
                    c1 = mfma32w(cat8(tr64(a1 + s * 12288), tr64(a1 + s * 12288 + 1536)), bf, c1);
                }
                if (n & 1) c0 = c1;
                if (n < 10) *reinterpret_cast<f32x4*>(smem + (n < 8 ? L_OP + 6144 + (n >> 1) * 768 : L_CS + 768) + wave * 128 + kq * 32 + (n & 1) * 16) = c0;
                lds_inc(3);
            }
            kv_stream_x<P_QP, 36, true>(kbase, vbase, ko, v0, to, due, step, [&](int k) {
                if (k >= 300) { if ((k == 305) == is_mlp) { } } else
                if (k >= 200) STAMPV(56 + (k - 216) / 4, now32() - (due >> 4) + t_start + 100u - ((now32() - (due >> 4) + 0x8000000u) & 0xf0000000u)); });
        } else {
            const unsigned t8 = tag & 0xffu;
            WO_ADDR();
            NARROW_ADDR(); (void)nva; (void)nvb; (void)nrot;
            const int ml_ = mi - 12 * xg, l3_ = lane / 3, ne_ = (int)SCH(S_N0), r0e_ = 640 - 6 * LPN * (int)SCH(S_ND);
            const unsigned pe_ = (unsigned)((l3_ < ne_ ? l3_ : 0) * 3072 + xg * 384 + (lane - 3 * l3_) * 128);
            auto nar = [&](auto ic) {
                constexpr int k = decltype(ic)::v;
                auto rows = [&](int j) { const int row = (j * 6 + (ml_ >> 1)) * ne_; if (ne_ && row < r0e_) pull(((ml_ & 1) ? wu : wg) + (row < 640 - ne_ ? row : 640 - ne_) * 1536, pe_, lb + L_PF); };
                if constexpr (k >= 200) { if (is_mlp) rows(k - 200); }
                else if constexpr (k >= 100) { if (is_mlp) narrow<true>(wg, wu, nva, nvb, lb, k - 100, 0); }
                else if (is_mlp) { if (NNE == 48) rows(k); }
                else wo_piece<k>(wob, woa, lb); };
            auto qstore = [&]() {
                if (!is_mlp) {
                    lds_wait_ge(lb + L_CNT + 12, 6u);
                    f32x4 v[3];
#pragma unroll
                    for (int t = 0; t < 3; ++t) v[t] = *reinterpret_cast<const f32x4*>(smem + L_OP + 6144 + lane * 48 + t * 16);
                    unsigned am = 12u << 23;
#pragma unroll
                    for (int i = 0; i < 12; ++i) am = umax(am, __float_as_uint(v[i >> 2][i & 3]) & 0x7fffffffu);
                    am >>= 23;
                    const float sc = __uint_as_float((266u - am) << 23);
                    unsigned u[12], w[6];
#pragma unroll
                    for (int i = 0; i < 12; ++i) u[i] = __float_as_uint(__builtin_amdgcn_fmed3f(__builtin_fmaf(v[i >> 2][i & 3], sc, 12591104.f), 12582912.f, 12599295.f));
#pragma unroll
                    for (int i = 0; i < 6; ++i) w[i] = (u[2 * i] << 18) | ((u[2 * i + 1] & 0x3fffu) << 4) | (i < 4 ? (((am >> (2 * i)) & 3u) << 2) | (t8 & 3u) : t8 & 15u);
                    const u32x4 d = {w[0], w[1], w[2], w[3]}; const u32x2 e = {w[4], w[5]};
                    const unsigned o = WS_H1A + (unsigned)((h * 5 + sub) * 1536);
                    if (!AB(16)) {
                        st16_x(ws, o + (unsigned)(lane * 16), d);
                        asm volatile("global_store_dwordx2 %0, %1, %2 offset:1024 " XSX :: "v"(o + (unsigned)(lane * 8)), "v"(e), "s"(ws) : "memory");
                    }
                }
                STAMP(17);
            };
            if (EPF == 3) pace<D7>(due, step);
            kv_stream7<F7, (CA > F7 ? CA : F7)>(kbase, vbase, ko, v0, v1, v2, to, due, step, nar);
            if (CA) clean();
            kv_stream7<(CA > F7 ? CA : F7), A7>(kbase, vbase, ko, v0, v1, v2, to, due, step, nar);
            if (A7 >= Q7) qstore();
            if (is_mlp) due -= 4 * step;
            kv_stream7<A7, A7 + 4>(kbase, vbase, ko, v0, v1, v2, to, due, step, nar);
            if (is_mlp) {
                unsigned long long c0, c1, c2; unsigned d;
                due += 4 * step;
                VMW(A7 + 4 - CA);
                asm volatile(
                             "s_memtime %0\n\ts_load_dword %3, %4, %5\n\ts_waitcnt lgkmcnt(0)\n\t"
                             "s_memtime %1\n\ts_load_dword %3, %4, %6\n\ts_waitcnt lgkmcnt(0)\n\t"
                             "s_memtime %2\n\ts_waitcnt lgkmcnt(0)"
                             : "=&s"(c0), "=&s"(c1), "=&s"(c2), "=&s"(d) : "s"(ws), "s"(AB(8) ? WS_EP : po), "s"(AB(8) ? WS_EP : po ^ 8192u) : "memory");
                RETRY(48, (unsigned)c1 - (unsigned)c0); RETRY(49, (unsigned)c2 - (unsigned)c1);
                const unsigned arv = (t_start & ~1u) | ((((int)((unsigned)c2 + (unsigned)c0 - 2u * (unsigned)c1) > 0 ? 0u : 1u) ^ (unsigned)((mi >> 1) ^ (xg >> 2))) & 1u);
                if (lane == 0) st8_sc1(ws, WS_AR + (unsigned)((mi >> 2) * 128 + (mi & 3) * 8), (u32x2){tag, arv});
            }
            STAMP(16);
            if (A7 < Q7) { kv_stream7<A7 + 4, Q7>(kbase, vbase, ko, v0, v1, v2, to, due, step, nar); qstore(); }
            kv_stream7<(A7 < Q7 ? Q7 : A7 + 4), L7>(kbase, vbase, ko, v0, v1, v2, to, due, step, nar);
            u32x4 t = ones;
            const unsigned oa = WS_AR + (unsigned)((lane < 48 ? lane >> 1 : 0) * 128 + (lane & 1) * 16);
            if (g == LEADER) t = ld16_sc1(ws, oa);
            kv_stream7<L7, L7 + 4>(kbase, vbase, ko, v0, v1, v2, to, due, step, nar);
            if (g == LEADER) {
                asm volatile("s_waitcnt vmcnt(4)" : "+v"(t) :: "memory");
#pragma unroll 1
                for (unsigned spins = 0; spins < (kSpinCap << 4); ++spins) {
                    if (__builtin_amdgcn_ballot_w64(t[0] != tag || t[2] != tag) == 0ull) break;
                    RETRY(3, spins + 1);
                    __builtin_amdgcn_s_sleep(8);
                    t = ld16_sc1(ws, oa);
                    asm volatile("s_waitcnt vmcnt(0)" : "+v"(t) :: "memory");
                }
                t0 = t_start + (unsigned)wave_imax(imax((int)(t[1] - t_start), (int)(t[3] - t_start)));
                const int v0_ = __builtin_popcountll(__builtin_amdgcn_ballot_w64((t[1] & 1u) != 0u) & 0xffffffffffffull), v1_ = __builtin_popcountll(__builtin_amdgcn_ballot_w64((t[3] & 1u) != 0u) & 0xffffffffffffull);
                RETRY(50, v0_ + 100); RETRY(51, v1_ + 100);
                t0 = (t0 & ~7u) | (v0_ > 24 ? 2u : 0u) | (v1_ > 24 ? 4u : 0u);
                if (SCH(S_FL) & 256) { RETRY(5, (t0 ^ (SCH(S_FL) >> 8)) & 2u); RETRY(6, (t0 ^ (SCH(S_FL) >> 8)) & 4u); t0 = (t0 & ~7u) | ((SCH(S_FL) >> 8) & 6u); }
                if (lane < 8) st8_sc1(ws, WS_GO + (unsigned)(lane * 128), (u32x2){tag, t0});
                STAMP(29);
            }
#define LDQ() do { a4 = ld16_xo<0>(ws, o4); a1 = ld8_xo(ws, o1); b4 = ld16_xo<0>(ws, p4_); b1 = ld8_xo(ws, p1); } while (0)
#define WTQ(n) asm volatile("s_waitcnt vmcnt(%4)" : "+v"(a4), "+v"(a1), "+v"(b4), "+v"(b1) : "i"(n) : "memory")
            kv_stream7<L7 + 4, QG>(kbase, vbase, ko, v0, v1, v2, to, due, step, nar);
            {
                u32x2 r = ld8_sc1o<0>(ws, WS_GO + (unsigned)(xg * 128));
                const unsigned ob = WS_H1A + (unsigned)(h * 7680), o4 = ob + (unsigned)(kq * 1536 + (b * 16 + n) * 16), o1 = ob + (unsigned)(kq * 1536 + 1024 + (b * 16 + n) * 8);
                const unsigned p4_ = ob + (unsigned)(6144 + (b * 16 + n) * 16), p1 = ob + (unsigned)(7168 + (b * 16 + n) * 8);
                u32x4 a4, b4; u32x2 a1, b1;
                LDQ();
                kv_stream7<QG, QG + QW>(kbase, vbase, ko, v0, v1, v2, to, due, step, nar);
                WTQ(QW);
#pragma unroll 1
                for (unsigned spins = 0; spins < (kSpinCap << 4); ++spins) {
                    if (tagok((or4((a4 ^ t8) | (b4 ^ t8)) & 3u) | (((a1[0] ^ t8) | (a1[1] ^ t8) | (b1[0] ^ t8) | (b1[1] ^ t8)) & 15u)) || AB(16)) break;
                    RETRY(1, spins + 1);
                    __builtin_amdgcn_s_sleep(8);
                    LDQ();
                    WTQ(0);
                }
                float q[12];
                auto dec = [&](u32x4 d, u32x2 e, bool first) {
                    const unsigned x = ((d[0] >> 2) & 3u) | (d[1] & 12u) | ((d[2] & 12u) << 2) | ((d[3] & 12u) << 4);
                    float sc = __uint_as_float((x - 12u) << 23);
                    if (!first && kq) sc = 0.f;
                    const float of = -8192.f * sc;
#pragma unroll
                    for (int i = 0; i < 12; ++i) {
                        const unsigned w = i < 8 ? d[i >> 1] : e[(i >> 1) - 4];
                        const float y = __builtin_fmaf((float)((i & 1) ? (w >> 4) & 0x3fffu : w >> 18), sc, of);
                        q[i] = first ? y : q[i] + y;
                    }
                };
                dec(a4, a1, true); dec(b4, b1, false);
#pragma unroll
                for (int t = 0; t < 3; ++t) {
                    const float z = sw16_add(sw32_add(q[4 * t], q[4 * t + 2]), sw32_add(q[4 * t + 1], q[4 * t + 3]));
                    const int c = 3 * n + t;
                    *reinterpret_cast<u16*>(smem + L_Q + ((c >> 3) & 1) * 192 + (c >> 4) * 64 + (c & 1) * 32 + ((c >> 1) & 3) * 4 + (kq >> 1) * 16 + (kq & 1) * 2) = (u16)pk2(z, z);
                }
                STAMP(30);
                asm volatile("" : "+v"(r));
#pragma unroll 1
                for (unsigned spins = 0; spins < (kSpinCap << 4); ++spins) {
                    t0 = __builtin_amdgcn_readfirstlane(r[1]);
                    if (__builtin_amdgcn_readfirstlane(r[0]) == tag) break;
                    RETRY(2, spins + 1);
                    __builtin_amdgcn_s_sleep(8);
                    r = ld8_sc1o<0>(ws, WS_GO + (unsigned)(xg * 128));
                    asm volatile("s_waitcnt vmcnt(0)" : "+v"(r) :: "memory");
                }
            }
            pol = t0;
            asm volatile("ds_write2_b32 %0, %1, %2 offset1:3\n\tds_write2_b32 %0, %3, %2 offset0:1 offset1:5" :: "v"(lb + L_POL), "v"(pol), "v"(0u), "v"(tag) : "memory");
            if (t0 - t_start + 8u > 500u || (SCH(S_FL) & 4096)) t0 = t_start;
            asm volatile("ds_write_b32 %0, %1" :: "v"(lb + L_CNT + 36), "v"(t0 | 1u) : "memory");
            lds_inc(0);
            STAMP(2);
            kv_stream7<QG + QW, 48>(kbase, vbase, ko, v0, v1, v2, to, due, step, nar);
            if (AB(1024)) {
                unsigned long long pc; unsigned len;
                asm volatile("s_getpc_b64 %0\nmega_pf_two:\n\ts_mov_b32 %1, mega_pf_end - mega_pf_two - 4" : "=s"(pc), "=s"(len));
                const unsigned o = (unsigned)((lane * 16 + (rr & 15)) * 128);
                dma4((const void*)pc, o < len ? o : len, lb + L_PF);
            }
            if (!is_mlp) {
                dma16p(wr, (unsigned)(lane * 16), lb + L_WR); dma4(wr, (unsigned)(1024 + lane * 4), lb + L_WR + 1024);
                dma4(x0, (unsigned)((((lane >> 1) & 3) * DM + oi * 4) * 2 + (lane & 1) * 4), lb + L_WR + 1280);
                dma4n(out, (unsigned)(xg * 128 + (lane & 1) * 4096), lb + L_PF);
            } else if (!AB(8192)) {
                dma4n(wd + mi * 16 * DM, (unsigned)((lane & 1) * 20476), lb + L_PF);
                if (!AB(16384)) { const unsigned o = (unsigned)(((lane & 7) * 91 + 2) * 3072 + mi * 32); dma4n(wg, o, lb + L_PF); dma4n(wu, o, lb + L_PF); }
            }
            VMW(16);
            fix_all<0>();
            VMW(0);
            STAMP(19);
            __builtin_amdgcn_s_setprio(0);
        }
    }

#define IDS() int g = GID; asm volatile("" : "+s"(g)); int wave = wave0; asm volatile("" : "+s"(wave)); \
        const int xg = g & 7, rr = g >> 3, h = xg * 4 + (rr >> 3), sub = rr & 7, b = sub >> 1; \
        const bool is_mlp = sub >= 5; const int mi = h * 3 + sub - 5, oi = OWNER(h, sub); (void)mi; (void)oi; (void)b
#define ATT_ADDR(layer) \
        unsigned qa = lb + (unsigned)(L_Q + kq * 32 + (lane & 1) * 16), ix = (unsigned)((p4 & 1) * 0xaaaa + p4 * 0x55550000) ^ 0x44444444u; \
        unsigned pw = lb + L_P + (unsigned)(wave * 1088 + ((kq & 1) * 2 + (p4 >> 1)) * 272 + (p4 & 1) * 128 + (q4 >> 1) * 64 + (kq >> 1) * 32 + (q4 & 1) * 8); \
        unsigned pr = lb + L_P + (unsigned)(wave * 1088 + (lane >> 2) * 16); \
        unsigned ow = lb + L_OP + (unsigned)(wave * OPS + lane * 16); \
        unsigned mlw = lb + L_ML + (unsigned)(wave * 4), mo = lb + L_ML + (unsigned)((lane >> 3) * 4), ma = lb + L_ML; \
        unsigned r0 = lb + L_OP + (unsigned)((lane >> 3) * OPS + (wave < 4 ? (wave >> 1) * 128 : 2048) + (wave & 1) * 256 + (lane & 7) * 16), r1 = r0 + (wave < 4 ? 1536u : 640u); \
        unsigned so = (a_h2(h) ^ (layer ? CX(1) : 0u)) + (unsigned)(layer * LB + (wave < 6 ? ((wave >> 1) * 8 + (lane >> 5) * 4 + (wave & 1) * 2 + (sub & 1)) * 128 + b * 32 + (lane & 7) * 4 \
                                                                                  : 3072 + (((sub & 1) ^ (lane & 1)) * 4 + b) * (layer ? MLS1 : MLS) + (lane & 1) * 8)); \
        (void)mlw; (void)ma; (void)r1

    if (PADN) asm volatile("s_branch 1f\n\t.fill %0, 4, 0xbf800000\n1:" :: "i"(PADN) : "memory");
    {
        IDS(); LANES();
        ATT_ADDR(0);
        asm volatile("" : "+v"(qa), "+v"(pw), "+v"(pr), "+v"(ow), "+v"(mlw), "+v"(mo), "+v"(ma), "+v"(r0), "+v"(r1), "+v"(so), "+v"(ix));
        const u16* vbase = vc + ((size_t)(b * HQ + h) * SK + (SK - WIN) + (sub & 1) * 512) * DH;
        unsigned v1 = (unsigned)(4 * kq * 384 + 256 + (n & 7) * 16), v2 = v1 + (n >= 8 ? to[3] : to[1]);
        v1 += n >= 8 ? to[2] : to[0];
        auto tail1 = [&]() { ldkv<224, 0>(vbase, v1); pace(due, step); ldkv<228, 384>(vbase, v1); pace(due, step); ldkv<232, 768>(vbase, v1); pace(due, step); ldkv<236, 1152>(vbase, v1); };
        unsigned dt = 0u;
        auto tail2 = [&]() { if (EHW == 3) dt = pace_t(due, step); else pace(due, step); ldkv<240, 0>(vbase, v2); };
        auto tail3 = [&]() {
            if (EHW == 2) dt = pace_t(due, step); else pace(due, step);
            ldkv<244, 384>(vbase, v2);
            if (EHW == 1) dt = pace_t(due, step); else pace(due, step);
            ldkv<248, 768>(vbase, v2); dt = pace_t(due, step) - dt; ldkv<252, 1152>(vbase, v2);
            asm volatile("s_mov_b64 exec, 1\n\tds_add_u32 %0, %1 offset:336\n\ts_mov_b64 exec, -1" :: "v"(ma), "v"(1u) : "memory");
            STAMP(3);
        };
        if (wave != 7) {
            const unsigned t = lds_ld(lb + L_CNT + 36);
            if (t) due += (t - t_start) << 4;
            step = (step * SCH(S_X3)) >> 8;
            if (wave == 6 && !is_mlp) step -= SCH(S_X6);
        }
        const bool early = wave == 7 || lds_ld(lb + L_CNT) != 0u;
        if (!early) {
            const unsigned v0 = (unsigned)(4 * kq * 384 + n * 16) + to[3];
            ldkv<208, 0>(vbase, v0); pace(due, step); ldkv<212, 384>(vbase, v0); pace(due, step); ldkv<216, 768>(vbase, v0); pace(due, step); ldkv<220, 1152>(vbase, v0); pace(due, step);
            tail1(); tail2(); tail3();
            lds_wait_ge(lb + L_CNT, 1u);
        }
        pol = lds_ld(lb + L_POL); tag = lds_ld(lb + L_POL + 4);
        so ^= CX(0);
        STAMP(4);
        PAGE(0);
        float sa;
        asm volatile(ASM_QK : "=&v"(sa) : "v"(qa), "v"(ix) : "memory", CLOB_32_59);
        const float s = bfr(bfr(sa) * SCALE) * L2E;
        float mt, p; int ms;
        asm volatile("s_nop 1\n\tv_max_f32_dpp %0, %3, %3 quad_perm:[1,0,3,2] row_mask:0xf bank_mask:0xf\n\ts_nop 1\n\t"
                     "v_max_f32_dpp %0, %0, %0 quad_perm:[2,3,0,1] row_mask:0xf bank_mask:0xf\n\ts_nop 1\n\t"
                     "v_max_f32_dpp %0, %0, %0 row_half_mirror row_mask:0xf bank_mask:0xf\n\ts_nop 1\n\t"
                     "v_max_f32_dpp %0, %0, %0 row_mirror row_mask:0xf bank_mask:0xf\n\ts_nop 1\n\t"
                     "v_max_f32_dpp %0, %0, %0 row_bcast:15 row_mask:0xa bank_mask:0xf\n\ts_nop 1\n\t"
                     "v_max_f32_dpp %0, %0, %0 row_bcast:31 row_mask:0xc bank_mask:0xf\n\ts_nop 0\n\t"
                     "v_readlane_b32 %2, %0, 63\n\ts_nop 1\n\t"
                     "v_subrev_f32 %1, %2, %3\n\tv_exp_f32 %1, %1\n\ts_nop 0"
                     : "=&v"(mt), "=&v"(p), "=&s"(ms) : "v"(s));
        u32x4 bm[4];
        {
            unsigned pb;
            asm volatile("v_cvt_pk_bf16_f32 %5, %4, %4\n\tds_write_b16 %6, %5\n\tds_write_b16 %6, %5 offset:20\n\t"
                     "ds_read_b128 %0, %7\n\tds_read_b128 %1, %7 offset:272\n\tds_read_b128 %2, %7 offset:544\n\tds_read_b128 %3, %7 offset:816\n\t"
                     "v_add_f32_dpp %4, %4, %4 quad_perm:[1,0,3,2] row_mask:0xf bank_mask:0xf\n\ts_nop 1\n\t"
                     "v_add_f32_dpp %4, %4, %4 quad_perm:[2,3,0,1] row_mask:0xf bank_mask:0xf\n\ts_nop 1\n\t"
                     "v_add_f32_dpp %4, %4, %4 row_half_mirror row_mask:0xf bank_mask:0xf\n\ts_nop 1\n\t"
                     "v_add_f32_dpp %4, %4, %4 row_mirror row_mask:0xf bank_mask:0xf\n\ts_nop 1\n\t"
                     "v_add_f32_dpp %4, %4, %4 row_bcast:15 row_mask:0xa bank_mask:0xf\n\ts_nop 1\n\t"
                     "v_add_f32_dpp %4, %4, %4 row_bcast:31 row_mask:0xc bank_mask:0xf\n\t"
                     "s_mov_b32 exec_lo, 0\n\ts_brev_b32 exec_hi, 1\n\tds_write2_b32 %9, %8, %4 offset1:8\n\ts_mov_b64 exec, -1\n\ts_waitcnt lgkmcnt(0)"
                     : "=&v"(bm[0]), "=&v"(bm[1]), "=&v"(bm[2]), "=&v"(bm[3]), "+v"(p), "=&v"(pb)
                     : "v"(pw), "v"(pr), "v"(mt), "v"(mlw) : "memory");
        }
#define PVP(name) asm volatile(name : "+v"(da) : "v"(bm[0]), "v"(bm[1]), "v"(bm[2]), "v"(bm[3]), "v"(ow), "v"(ix) : "memory")
#define PVB(name) do { f32x4 da = z4; PVP(name); } while (0)
        int w7 = wave;
        if (wave == 7) {
            PVB(ASM_PVB0); PVB(ASM_PVB1); PVB(ASM_PVB2);
            asm volatile("s_waitcnt lgkmcnt(0)\n\ts_mov_b64 exec, 1\n\tds_add_u32 %0, %1 offset:372\n\tds_add_u32 %0, %1 offset:348\n\ts_mov_b64 exec, -1" :: "v"(ma), "v"(1u) : "memory");
        }
        asm volatile("" : "+s"(w7));
        if (w7 != 7) {
            LANES();
            {
                const unsigned v0 = (unsigned)(4 * kq * 384 + n * 16) + to[3];
                if (early) { ldkv<208, 0>(vbase, v0); pace(due, step); ldkv<212, 384>(vbase, v0); VMW(6); }
                if (early) { pace(due, step); ldkv<216, 768>(vbase, v0); }
                PVB(ASM_PVB0);
                if (early) { pace(due, step); ldkv<220, 1152>(vbase, v0); pace(due, step); tail1(); }
            }
            VMW(4);
            PVB(ASM_PVB1);
#define MEET(off) do { unsigned t_; int c_; \
            asm volatile("s_waitcnt lgkmcnt(0)\n\ts_mov_b64 exec, 1\n\tds_add_u32 %2, %3 offset:" #off "\n\ts_mov_b64 exec, -1\n\ts_mov_b32 %1, 0x400000\n1:\n\tds_read_b32 %0, %2 offset:" #off "\n\ts_sub_u32 %1, %1, 1\n\ts_cbranch_scc1 2f\n\t" \
                         "s_waitcnt lgkmcnt(0)\n\tv_cmp_gt_u32 vcc, 8, %0\n\ts_cbranch_vccnz 1b\n2:" : "=&v"(t_), "=&s"(c_) : "v"(ma), "v"(1u) : "memory", "scc", "vcc"); } while (0)
            asm volatile("s_waitcnt lgkmcnt(0)\n\ts_mov_b64 exec, 1\n\tds_add_u32 %0, %1 offset:372\n\ts_mov_b64 exec, -1" :: "v"(ma), "v"(1u) : "memory");
            f32x4 da = z4; int la;
            {
                unsigned hfa = (a_hf(h) ^ CX(0)) + (unsigned)(lane * 128 + ((sub - lane) & 7) * 4), hb = lds_ld(lb + L_CNT + 36);
                const unsigned tw_ = SCH(S_TW);
                asm volatile("" : "+v"(da), "+v"(hfa), "+v"(hb));
                if (early) { tail2(); tail3(); } else dt = 0u;
                unsigned ar = 1u, mx, dv = ((dt * tw_) >> 8) + (is_mlp ? SCH(S_G1) : 0u) - hb; int th;
                asm volatile("s_waitcnt vmcnt(%4)\n\ts_memrealtime vcc\n\ts_mov_b64 exec, 1\n\ts_waitcnt lgkmcnt(0)\n\tv_add_u32 %1, vcc_lo, %3\n\t"
                             "ds_max_u32 %2, %1 offset:%5\n\tds_add_rtn_u32 %0, %2, %0 offset:376\n\tds_read_b32 %1, %2 offset:%5\n\ts_mov_b64 exec, -1"
                             : "+v"(ar), "=&v"(mx) : "v"(ma), "v"(dv), "i"(EHW), "i"(L_POL + 20 - L_ML) : "memory", "scc", "vcc");
                PVP(ASM_PV20); PVP(ASM_PV21); if (EHW <= 2) PVP(ASM_PV22);
                asm volatile("s_waitcnt lgkmcnt(0)\n\tv_add_u32 %3, %3, %4\n\tv_readfirstlane_b32 %0, %2\n\tv_readfirstlane_b32 %1, %3\n\ts_cmp_lg_u32 %0, 7\n\ts_cbranch_scc1 2f\n\t"
                             "s_mov_b64 exec, 31\n\tv_and_b32 %3, -2, %1\n\tglobal_store_dword %5, %3, %7 sc1\n\tv_or_b32 %2, 1, %1\n\tds_write_b32 %6, %2 offset:%8\n\ts_mov_b64 exec, -1\n2:"
                             : "=&s"(la), "=&s"(th), "+v"(ar), "+v"(mx) : "v"(hb), "v"(hfa), "v"(ma), "s"(ws), "i"(L_POL + 12 - L_ML) : "memory", "scc");
            }
            lds_wait_ge(lb + L_CNT + 52, 8u);
            const f32x4 m0 = *(const LAS f32x4*)(size_t)ma, m1 = *(const LAS f32x4*)(size_t)(ma + 16);
            const float M = vmax3(vmax3(m0[0], m0[1], m0[2]), vmax3(m0[3], m1[0], m1[1]), vmax3(m1[2], m1[3], m1[3]));
            float fj = __builtin_amdgcn_exp2f(*(const LAS float*)(size_t)mo - M);
            auto combine = [&]() {
                const f32x4 a = *(const LAS f32x4*)(size_t)r0, c = *(const LAS f32x4*)(size_t)r1;
                const float z = red3((a + c) * fj);
                auto sw = __builtin_amdgcn_permlane16_swap(__float_as_uint(z), __float_as_uint(z), false, false);
                return enc(pk2(__uint_as_float(sw[0]), __uint_as_float(sw[1])));
            };
            if (wave < 4) st4_m<0x000000ffu, 0x000000ffu>(ws, so, combine());
            else if (wave == 6) {
                float L = *(const LAS float*)(size_t)(mo + 32) * fj;
                L += dpp<0x128>(L); L = sw16_add(L, L); L = sw32_add(L, L);
                asm volatile("s_mov_b64 exec, 3\n\tglobal_store_dwordx2 %0, %1, %2 sc1\n\ts_mov_b64 exec, -1" :: "v"(so), "v"((u32x2){enc(__float_as_uint(M)), enc(__float_as_uint(L))}), "s"(ws) : "memory");
            } else la += 8;
            const unsigned h2 = a_h2(h) ^ CX(0); const int ot = (8 * sub + 2 * wave + (q4 & 1)) * 4 + kq, rw = (q4 & 1) ? 3 - p4 : p4;
            unsigned j0 = lb + L_WO + (unsigned)((8 * kq + q4) * 256 + ((2 * wave) ^ (q4 | ((kq & 1) << 2))) * 32 + p4 * 8), j1 = j0 ^ 32u;
            unsigned j2 = h2 + (unsigned)((lane >> 3) * 128 + rw * 32 + (q4 & 1) * 16), j3 = h2 + (unsigned)(3072 + ((n >> 3) * 4 + rw) * MLS);
            unsigned j4 = (a_own(ot) + (unsigned)(6144 + h * 64 + rw * 16)) ^ ((unsigned)((ot & 15) << 7) | CX(0));
            if (OPA) asm volatile("" : "+v"(fj), "+{v0}"(j0), "+{v1}"(j1), "+{v2}"(j2), "+{v3}"(j3), "+{v4}"(j4)); else asm volatile("" : "+v"(fj));
            STAMP(6);
            asm volatile("s_waitcnt vmcnt(2)\n\ts_cmp_eq_u32 %0, 7\n\ts_cbranch_scc1 1f\n\ts_waitcnt vmcnt(1)\n\ts_sub_u32 %0, %0, 8\n\ts_cmp_gt_u32 %0, 6\n\ts_cbranch_scc1 1f\n\ts_waitcnt vmcnt(0)\n1:" : "+s"(la) :: "memory", "scc");
            if (EHW > 2) PVP(ASM_PV22);
            STAMP(20); DMARK(46);
            asm volatile(ASM_PV23 "s_nop 7\n\tds_write_b128 %5, %0 offset:2048" : "+v"(da) : "v"(bm[0]), "v"(bm[1]), "v"(bm[2]), "v"(bm[3]), "v"(ow), "v"(ix) : "memory");
            if ((wave & 6) == 4) { MEET(348); st4_m<0x000000ffu, 0x000000ffu>(ws, so, combine()); STAMP(5); asm volatile("s_mov_b64 exec, 1\n\tds_add_u32 %0, %1 offset:344\n\ts_mov_b64 exec, -1" :: "v"(ma), "v"(1u) : "memory"); }
            else asm volatile("s_mov_b64 exec, 1\n\tds_add_u32 %0, %1 offset:348\n\ts_mov_b64 exec, -1" :: "v"(ma), "v"(1u) : "memory");
            if (is_mlp && wave < 4) {
                LANES();
                NARROW_ADDR();
                unsigned va = nva + (unsigned)(wave * 160 * 3072), vb = nvb + (unsigned)(wave * 160 * 3072), lw = lb + (unsigned)(L_GU + wave * 5120);
                asm volatile("" : "+v"(va), "+v"(vb), "+s"(lw));
                M0_KEEP();
                lds_wait_ge(lb + L_CNT + 24, 2u);
                lds_wait_ge(lb + L_CNT + 40, 1u);
                if (SCH(S_LT)) wait_until(now32() + SCH(S_LT));
                STAMP(18);
                if (wave >= (int)(SCH(S_X7) >> 3))
#pragma unroll
                for (int c = 0; c < 5; ++c) {
                    unsigned t;
                    asm volatile("s_add_u32 m0, %4, %5\n\tv_add_u32 %0, %6, %1\n\tglobal_load_lds_dwordx4 %0, %2" WPOL "\n\ts_add_u32 m0, %4, %7\n\ts_nop 0\n\tglobal_load_lds_dwordx4 %0, %3" WPOL
                                 : "=&v"(t) : "v"(c == 4 ? vb : va), "s"(wg), "s"(wu), "s"(lw), "i"(c * 1024), "i"((c & 3) * 8 * 3072), "i"(c * 1024 + 20480) : "memory", "scc");
                }
                M0_BACK();
                asm volatile("s_mov_b64 exec, 1\n\tds_add_u32 %0, %1 offset:364\n\ts_mov_b64 exec, -1" :: "v"(ma), "v"(1u) : "memory");
                STAMP(37);
                if (P1ONLY) { VMW(0); STAMP(27); }
            }
            if (!P1ONLY && !is_mlp && wave < 4) {
                int st, rf, ct; unsigned t0;
                asm volatile(ASM_OP : [st] "=&s"(st), [rf] "=&s"(rf), [ct] "=&s"(ct), [t0] "=&{v7}"(t0) : [a0] "{v0}"(j0), [a1] "{v1}"(j1), [gs] "{v2}"(j2), [gm] "{v3}"(j3), [so] "{v4}"(j4),
                             [ws] "s"(ws), [fl] "s"(lb + L_CNT + 48), [tag] "s"(tag), [dbg] "s"(lb + L_DBG + wave * 512 + 256 + 7 * 4) : "memory", "scc", "vcc", CLOB_8_15, CLOB_16_63 OPCL);
                STAMP(7); DMARK(30);
            }
        }
    }
    {
        IDS();
        if (wave == 7) {
            PAGE(1);
            STAMP(15);
            LANES();
            {
                __builtin_amdgcn_s_setprio(3);
                unsigned th = 0u;
                auto hint = [&]() { lds_wait_ge(lb + L_POL + 12, 1u); th = lds_ld(lb + L_POL + 12); };
                unsigned hfa = (a_hf(h) ^ CX(0)) + (unsigned)(lane * 128 + ((sub - lane) & 7) * 4), hb = lds_ld(lb + L_CNT + 36), ma = lb + L_ML;
                asm volatile("" : "+v"(hfa), "+v"(hb), "+v"(ma));
#define ARRIVE(off, W) do { \
                    unsigned ar = 1u, mx, dv = (off) - hb; int la, th_; \
                    asm volatile("s_memrealtime vcc\n\ts_mov_b64 exec, 1\n\ts_waitcnt lgkmcnt(0)\n\tv_add_u32 %1, vcc_lo, %3\n\t" \
                                 "ds_max_u32 %2, %1 offset:%4\n\tds_add_rtn_u32 %0, %2, %0 offset:376\n\tds_read_b32 %1, %2 offset:%4\n\ts_mov_b64 exec, -1" \
                                 : "+v"(ar), "=&v"(mx) : "v"(ma), "v"(dv), "i"(L_POL + 20 - L_ML) : "memory", "scc", "vcc"); \
                    asm volatile("s_waitcnt lgkmcnt(0)\n\tv_add_u32 %3, %3, %4\n\tv_readfirstlane_b32 %0, %2\n\tv_readfirstlane_b32 %1, %3\n\ts_cmp_eq_u32 %0, 7\n\ts_cbranch_scc1 1f\n\ts_waitcnt vmcnt(%9)\n\ts_branch 2f\n" \
                                 "1:\n\ts_mov_b64 exec, 31\n\tv_and_b32 %3, -2, %1\n\tglobal_store_dword %5, %3, %7 sc1\n\tv_or_b32 %2, 1, %1\n\tds_write_b32 %6, %2 offset:%8\n\ts_mov_b64 exec, -1\n\ts_waitcnt vmcnt(%10)\n2:" \
                                 : "=&s"(la), "=&s"(th_), "+v"(ar), "+v"(mx) : "v"(hb), "v"(hfa), "v"(ma), "s"(ws), "i"(L_POL + 12 - L_ML), "i"(W), "i"((W) | 1) : "memory", "scc"); \
                    if (STAMPS && __builtin_amdgcn_readfirstlane(la) == 7) { STAMPV(21, th_); RETRY(20, 1); } \
                } while (0)
                if (!is_mlp) {
                    WO_ADDR();
                    unsigned oa = woa, ob = woa ^ 128u;
                    asm volatile("" : "+v"(oa), "+v"(ob), "+s"(wob));
                    if (SCH(S_FL) & 128) {
                        const u32x4 ones = splat<~0u>();
#pragma unroll
                        for (int i = 0; i < 6; ++i) st16_l(ws, (a_own(oi) ^ CX(0)) + (unsigned)(i * 1024 + lane * 16), ones);
                        if (oi < 96 && lane < 32) st16_l(ws, WS_XB + (unsigned)((oi / 12) * 8192 + (oi % 12) * 512 + lane * 16), ones);
                    }
                    M0_KEEP();
                    if (WPRE) {
                        unsigned dw = (t0 + SCH(S_WD)) << 4;
                        wo_paced<NN7, NN7 + WPRE>(wob, oa, ob, lb, dw, SCH(S_TR));
                    }
                    if (NN7 + WPRE < 48) { if (SCH(S_WT) > 7u) lds_wait_ge(lb + L_CNT + 56, SCH(S_WT) - 7u); else lds_wait_ge(lb + L_CNT + 16, SCH(S_WT)); }
                    STAMP(35);
                    if (WK < 0) { wo_rest<NN7 + WPRE, NN7 + WPRE - WK>(wob, oa, ob, lb); ARRIVE(SCH(S_G3) - 100u, 63); }
                    wo_rest<NN7 + WPRE - (WK < 0 ? WK : 0), 48>(wob, oa, ob, lb);
                    M0_BACK();
                    STAMP(36);
                    if (WK < 0) VMW(0); else { VMW(WK < 0 ? 0 : WK); ARRIVE(SCH(S_G3) - 100u, 0); }
                    STAMP(33); DMARK(44);
                    asm volatile("ds_write_b32 %0, %1" :: "v"(lb + L_CNT + 40), "v"(1u) : "memory");
                    if (lane < 48) st16_sc1(ws, WS_CS + (unsigned)(((h * 6 + (lane >> 3)) * 5 + sub) * 128 + (lane & 7) * 16), tag4(*reinterpret_cast<const u32x4*>(smem + L_CS + 768 + lane * 16), tag & 0xffu));
                } else {
                    ARRIVE(0u, 63);
                    {
                        NARROW_ADDR();
                        unsigned t = now32(); const unsigned ts = SCH(S_TS); const int se = (int)(SCH(S_X7) >> 3) * 10;
#pragma unroll 1
                        for (int s = NM7; s < se; ++s) { narrow<true>(wg, wu, nva, nvb, lb, s, nrot); t += ts; wait_until(t); }
                        STAMP(47);
                    }
                    const int ml = mi - 12 * xg, nl = (int)SCH(S_ND), l3 = lane / 3;
                    unsigned pa = (unsigned)((l3 < nl ? l3 : 0) * 3072 + xg * 384 + (lane - 3 * l3) * 128);
                    const u16* pb = ((ml & 1) ? wu : wg) + (640 - (6 - (ml >> 1)) * LPN * nl) * 1536;
                    auto late = [&]() {
                        unsigned t = now32();
                        if (nl) {
#pragma unroll
                            for (int k = 0; k < LPN; ++k) { pull(pb + k * nl * 1536, pa, lb + L_PF); if (SCH(S_PA) && k < LPN - 1) { t += SCH(S_PA); wait_until(t); } }
                        }
                    };
                    if (SCH(S_PA)) late();
                    const u16* wdb = wd + mi * 16 * DM;
                    auto down = [&]() {
                        unsigned c16 = (unsigned)lane * 16u, t, keep; int k;
                        asm volatile("s_mov_b32 %2, m0\n\ts_mov_b32 m0, %5\n\ts_mov_b32 %3, 20\n"
                                     "1:\n\tv_mul_u32_u24 %1, 0xc7d, %0\n\tv_lshrrev_b32 %1, 22, %1\n\tv_lshlrev_b32 %1, 5, %1\n\tv_sub_u32 %1, %0, %1\n\tv_min_u32 %1, 0x4ff0, %1\n\t"
                                     "global_load_lds_dwordx4 %1, %4" WPOL "\n\ts_add_u32 m0, m0, 0x400\n\tv_add_u32 %0, 0x400, %0\n\ts_sleep %6\n\ts_sub_u32 %3, %3, 1\n\ts_cbranch_scc0 1b\n\ts_mov_b32 m0, %2"
                                     : "+v"(c16), "=&v"(t), "=&s"(keep), "=&s"(k) : "s"(wdb), "s"(lb + L_WD), "i"(DSL) : "memory", "scc");
                    };
                    asm volatile("" : "+v"(pa), "+s"(wdb));
                    if (DSL) { down(); STAMP(46); }
                    if (SCH(S_N1) > 15u) { lds_wait_ge(lb + L_CNT + 24, 2u); if (SCH(S_NT)) wait_until(now32() + SCH(S_NT)); }
                    else if (SCH(S_N1) > 14u) { hint(); wait_until(th + SCH(S_NT)); }
                    else if (SCH(S_N1) > 7u) lds_wait_ge(lb + L_CNT + 56, SCH(S_N1) - 6u); else lds_wait_ge(lb + L_CNT + 16, SCH(S_N1));
                    if (!SCH(S_PA)) late();
                    {
                        if (!DSL) down();
                        VMW(DSL ? 0 : DNW);
                        asm volatile("ds_write_b32 %0, %1" :: "v"(lb + L_CNT + 40), "v"(1u) : "memory");
                        VMW(0);
                    }
                    STAMP(34);
                    asm volatile("ds_write_b32 %0, %1" :: "v"(lb + L_CNT + 48), "v"(1u) : "memory");
                }
                __builtin_amdgcn_s_setprio(0);
            }
        }
        if (wave == 6) {
            PAGE(2);
            LANES();
            if (P1ONLY) {} else {
            if (sub < 5) {
                int d = 0; int left;
                const unsigned o = (a_hf(h) ^ CX(0)) + (unsigned)(sub * 128);
                const int tk = sub * 4 + ((lane >> 3) & 3);
                unsigned ta = (a_ts(lane & 7) ^ CX(0)) + (unsigned)((lane < 32 ? tk + (tk / 5) * 3 : (5 + tk + (tk / 3) * 5) & 31) * 128 + h * 4), fa = lb + L_CNT + 48, t;
                asm volatile("" : "+v"(ta), "+v"(fa));
                {
                    u32x8 e; unsigned x;
                    asm volatile("s_movk_i32 %1, 4000\n1:\n\ts_load_dwordx8 s[4:11], %4, %5 glc\n\tds_read_b32 %3, %6 offset:%7\n\ts_memrealtime vcc\n\ts_sub_u32 %1, %1, 1\n\ts_waitcnt lgkmcnt(0)\n\ts_cbranch_scc1 2f\n\t"
                                 "v_readfirstlane_b32 s4, %3\n\ts_or_b32 vcc_hi, s5, s6\n\ts_or_b32 %2, s7, s8\n\ts_or_b32 vcc_hi, vcc_hi, s9\n\ts_or_b32 %2, %2, s10\n\ts_or_b32 vcc_hi, vcc_hi, s11\n\ts_orn2_b32 %2, %2, s4\n\t"
                                 "s_or_b32 vcc_hi, vcc_hi, %2\n\ts_bitcmp1_b32 vcc_hi, 0\n\ts_cbranch_scc1 1b\n2:\n\ts_mov_b32 %2, vcc_lo"
                                 : "=&{s[4:11]}"(e), "=&s"(left), "=&s"(t), "=&v"(x) : "s"(ws), "s"(o), "v"(fa), "i"(L_POL + 12 - L_CNT - 48) : "memory", "scc", "vcc");
                    asm volatile("s_sub_i32 s4, s4, %2\n\ts_sub_i32 s5, s5, %2\n\ts_sub_i32 s6, s6, %2\n\ts_sub_i32 s7, s7, %2\n\ts_max_i32 s4, s4, s5\n\ts_sub_i32 s8, s8, %2\n\ts_max_i32 s6, s6, s7\n\ts_sub_i32 s9, s9, %2\n\t"
                                 "s_max_i32 s4, s4, s6\n\ts_sub_i32 s10, s10, %2\n\ts_max_i32 s8, s8, s9\n\ts_sub_i32 s11, s11, %2\n\ts_max_i32 s4, s4, s8\n\ts_max_i32 s10, s10, s11\n\ts_max_i32 %0, s4, s10"
                                 : "=s"(d), "+{s[4:11]}"(e) : "s"(t) : "scc");
                }
                if (__builtin_expect(left < 0, 0)) {
#pragma unroll 1
                    for (unsigned spins = 0; spins < kSpinCap; ++spins) {
                        if (allok(poll4(ws, o + (unsigned)((lane & 7) * 4)))) break;
                    }
                    t = now32(); d = 0;
                    RETRY(21, 1);
                }
                d += (int)SCH(S_H20);
                if (TSX && d < (int)SCH(S_G2) - TSX) d = (int)SCH(S_G2) - TSX;
                DMARK(45);
                const unsigned tg = (t + (unsigned)(d > (int)SCH(S_G2) && d < 1000 ? d : (int)SCH(S_G2))) | 1u;
                asm volatile("ds_write_b32 %0, %1" :: "v"(fa), "v"(tg) : "memory");
                STAMP(25);
                st4_m<~0u, 0u>(ws, ta, enc(t + (unsigned)d));
                if (sub < 3) { wait_until(tg + 4u); st4_m<0u, ~0u>(ws, ta, enc(t + (unsigned)d)); }
                STAMPV(28, tg);
            } else lds_wait_ge(lb + L_CNT + 28, 8u);
            unsigned t; int d;
            const unsigned fl0 = lds_ld(lb + L_CNT + 36) + SCH(S_WD);
            {
                const unsigned o = (a_ts(xg) ^ CX(0)) + (unsigned)(rr * 128);
                unsigned r, lo; int left;
                asm volatile("s_movk_i32 %2, 4000\n1:\n\ts_load_dwordx16 s[68:83], %4, %5 glc\n\ts_load_dwordx16 s[84:99], %4, %6 glc\n\ts_memrealtime vcc\n\ts_sub_u32 %2, %2, 1\n\ts_waitcnt lgkmcnt(0)\n\ts_cbranch_scc1 2f\n\t"
                             "s_max_u32 s68, s68, s84\n\ts_max_u32 s69, s69, s85\n\ts_max_u32 s70, s70, s86\n\ts_max_u32 s71, s71, s87\n\ts_max_u32 s72, s72, s88\n\ts_max_u32 s73, s73, s89\n\ts_max_u32 s74, s74, s90\n\ts_max_u32 s75, s75, s91\n\ts_max_u32 s76, s76, s92\n\ts_max_u32 s77, s77, s93\n\ts_max_u32 s78, s78, s94\n\ts_max_u32 s79, s79, s95\n\ts_max_u32 s80, s80, s96\n\ts_max_u32 s81, s81, s97\n\ts_max_u32 s82, s82, s98\n\ts_max_u32 s83, s83, s99\n\ts_max_u32 s68, s68, s76\n\ts_max_u32 s69, s69, s77\n\ts_max_u32 s70, s70, s78\n\ts_max_u32 s71, s71, s79\n\ts_max_u32 s72, s72, s80\n\ts_max_u32 s73, s73, s81\n\ts_max_u32 s74, s74, s82\n\ts_max_u32 s75, s75, s83\n\ts_max_u32 s84, s68, s72\n\ts_max_u32 s85, s69, s73\n\ts_max_u32 s86, s70, s74\n\ts_max_u32 s87, s71, s75\n\ts_max_u32 s84, s84, s86\n\ts_max_u32 s85, s85, s87\n\ts_max_u32 %0, s84, s85\n\t"
                             "s_cmp_eq_u32 %0, -1\n\ts_cbranch_scc1 1b\n2:\n\ts_mov_b32 %1, vcc_lo\n\t"
                             "s_min_u32 s68, s68, s69\n\ts_min_u32 s70, s70, s71\n\ts_min_u32 s72, s72, s73\n\ts_min_u32 s74, s74, s75\n\ts_min_u32 s68, s68, s70\n\ts_min_u32 s72, s72, s74\n\ts_min_u32 %[lo], s68, s72"
                             : "=&s"(r), "=&s"(t), "=&s"(left), [lo] "=&s"(lo) : "s"(ws), "s"(o), "s"(o + 64u) : "memory", "scc", "vcc", "s68", "s69", "s70", "s71", "s72", "s73", "s74", "s75", "s76", "s77", "s78", "s79", "s80", "s81", "s82", "s83", "s84", "s85", "s86", "s87", "s88", "s89", "s90", "s91", "s92", "s93", "s94", "s95", "s96", "s97", "s98", "s99");
                if (__builtin_expect(left < 0, 0)) {
#pragma unroll 1
                    for (unsigned spins = 0; spins < kSpinCap; ++spins) {
                        if (allok(poll4(ws, o + (unsigned)((lane & 31) * 4)))) break;
                    }
                    lo = r = t = now32();
                    RETRY(22, 1);
                }
                const unsigned sp = r - lo, ex = sp < SCH(S_Y0) ? ((SCH(S_Y0) - sp) * 5u) >> 3 : 0u;
                d = (int)(r + (ex < SCH(S_Y1) ? ex : SCH(S_Y1)) - t);
                if (SCH(S_WD)) d = imax(d, (int)(fl0 - t));
            }
            asm volatile("ds_write_b32 %0, %1" :: "v"(lb + L_CNT + 8), "v"((t + (unsigned)(d < 150 ? d : 0)) | 1u) : "memory");
            STAMP(23); if (!is_mlp) DMARK(33);
            STAMPV(31, t + (unsigned)(d < 150 ? d : 0));
            }
        }
    }

    unsigned ref = 0;
#pragma unroll 1
    for (int layer_ = 0; layer_ < (P1ONLY ? 0 : 2); ++layer_) {
        IDS();
#define LAYER() int layer = layer_; asm volatile("" : "+s"(layer)); const unsigned cx = CX(layer), xa = WS_XA + (unsigned)(layer * LB), xm = xa + (a_rep(xg) ^ cx); (void)xa; (void)xm
        auto gather_img = [&](unsigned o, u32x4 (&a)[5], int sid, unsigned off) {
#pragma unroll 1
            for (unsigned spins = 0; spins < kSpinCap; ++spins) {
                a[0] = ld16_sc1o<-2048>(ws, o); a[1] = ld16_sc1o<-1024>(ws, o); a[2] = ld16_sc1o<0>(ws, o); a[3] = ld16_sc1o<1024>(ws, o); a[4] = ld16_sc1o<2048>(ws, o);
                asm volatile("s_waitcnt vmcnt(0)" : "+v"(a[0]), "+v"(a[1]), "+v"(a[2]), "+v"(a[3]), "+v"(a[4]) :: "memory");
                if (allok(umax(mx4(a[0], mx4(a[1])), mx4(a[2], mx4(a[3], mx4(a[4])))))) break;
                RETRY(sid, spins + 1);
                __builtin_amdgcn_s_sleep(10);
            }
        };
        auto gather_tab = [&](unsigned o, u32x4 (&a)[5], u32x4& r, int sid, unsigned t) {
            wait_until(t);
            a[0] = ld16_sc1o<-2048>(ws, o); a[1] = ld16_sc1o<-1024>(ws, o); a[2] = ld16_sc1o<0>(ws, o); a[3] = ld16_sc1o<1024>(ws, o); a[4] = ld16_sc1o<2048>(ws, o);
            wait_until(t + SCH(S_H4));
            r = ld16_sc1o<3072>(ws, o);
            asm volatile("s_waitcnt vmcnt(1)" : "+v"(a[0]), "+v"(a[1]), "+v"(a[2]), "+v"(a[3]), "+v"(a[4]) :: "memory");
            if (__builtin_expect(!allok(umax(mx4(a[0], mx4(a[1])), mx4(a[2], mx4(a[3], mx4(a[4]))))), 0)) {
                RETRY(sid, 1);
                asm volatile("s_waitcnt vmcnt(0)" : "+v"(r) :: "memory");
                __builtin_amdgcn_s_sleep(4);
                gather_img(o, a, sid, 0);
                r = ld16_sc1o<3072>(ws, o);
            }
        };
        auto late = [&](u32x4 v, unsigned base) {
            int d = -1000;
#pragma unroll
            for (int i = 0; i < 4; ++i) d = imax(d, v[i] == ~0u ? -1000 : (int)(v[i] - base));
            d = wave_imax(d);
            return d < 1000 ? d : 1000;
        };
        auto img_stats = [&](const u32x4 (&a)[5], float& mb, float& rden) {
            float s0 = 0.f, s1 = 0.f, q0 = 0.f, q1 = 0.f;
#pragma unroll
            for (int i = 0; i < 10; ++i) {
                const unsigned u = a[i >> 1][(i & 1) * 2], v = a[i >> 1][(i & 1) * 2 + 1];
                s0 = dot2(u, 0x3f803f80u, s0); q0 = dot2(u, u, q0); s1 = dot2(v, 0x3f803f80u, s1); q1 = dot2(v, v, q1);
            }
            const float mean = sum_x4(s0 + s1) * (1.f / DM);
            ln_consts(mean, sum_x4(q0 + q1) * (1.f / DM) - mean * mean, mb, rden);
        };
        if (!is_mlp) {
            if (wave == 5) {
                PAGE(4);
                LANES(); LAYER();
                const unsigned po = (a_rep((lane >> 2) & 7) ^ cx) + (unsigned)((oi >> 1) * 64 + p4 * 16 + (oi & 1) * 8 + (lane >> 5) * 4);
                if (layer == 0) lds_wait_ge(lb + L_CNT + 40, 1u);
                u32x4 w[5];
#pragma unroll
                for (int j = 0; j < 5; ++j) w[j] = *reinterpret_cast<const u32x4*>(smem + L_WR + j * 256 + (lane >> 2) * 16);
                float xr = layer ? *reinterpret_cast<const float*>(smem + L_XR + lane * 4) : __uint_as_float((unsigned)*reinterpret_cast<const u16*>(smem + L_WR + 1280 + p4 * 8 + kq * 2) << 16);
                float cw = 0.f;
#pragma unroll
                for (int j = 0; j < 20; ++j) cw = dot2(w[j >> 2][j & 3], 0x3f803f80u, cw);
                cw = sum_x4(cw);
                const unsigned tw = (a_rep(lane & 7) ^ cx) + (unsigned)(5120 + xg * 128 + ((h & 3) * 5 + sub) * 4);
                {
                    unsigned o = (a_own(oi) ^ cx) + (unsigned)(layer * LB + 6144 + lane * 16), so = xa + po;
                    unsigned t8 = tag & 0xffu;
                    asm volatile("" : "+v"(xr), "+v"(o), "+v"(so), "+v"(cw), "+s"(t8));
                    if (layer == 0) {
                        if (!(SCH(S_FL) & 224)) lds_wait_ge(lb + L_CNT + 44, 1u);
#pragma unroll 1
                        for (unsigned spins = 0; spins < (kSpinCap << 6) && ref == 0u; ++spins) ref = lds_ld(lb + L_CNT + 8);
                    }
                    if (PRI & 1) __builtin_amdgcn_s_setprio(2);
                    if (!layer) DMARK(35);
                    if (layer == 0 && (SCH(S_FL) & 96)) lds_wait_ge(lb + L_CNT + 44, 1u);
                    wait_until(ref + (layer ? SCH(S_H3) : SCH(S_H30)));
                    if (!layer) DMARK(34);
                    CY(c_a);
                    u32x4 a0, a1;
                    GATHER(8 + 32 * layer, { a0 = ld16_sc1o<0>(ws, o); a1 = ld16_sc1o<1024>(ws, o);
                                 asm volatile("s_waitcnt vmcnt(0)" : "+v"(a0), "+v"(a1) :: "memory"); a0 ^= t8; a1 ^= t8; }, tagok(or4(a0) | or4(a1)));
                    CY(c_b);
                    xr += bfr(red4(asf(a0) + asf(a1)));
                    auto sw = __builtin_amdgcn_permlane16_swap(__float_as_uint(xr), __float_as_uint(xr), false, false);
                    st4_sc1(ws, so, enc(pk2(__uint_as_float(sw[0]), __uint_as_float(sw[1]))));
                    xr = bfr(xr);
                    const unsigned t = now32() + SCH(S_GAP);
                    if ((int)(t - ref - (layer ? SCH(S_XA1) : SCH(S_XA0))) > 0) st4_m<0xffu, 0u>(ws, xa + tw, enc(t + SCH(S_EX)));
                    CY(c_c); CYW(55, c_a, c_b); CYW(56, c_b, c_c);
                    STAMP(8 + 32 * layer); if (layer) DMARK(25); else DMARK(31);
                }
                float rs, xn;
                {
                    unsigned o = xm + (unsigned)(2048 + lane * 16);
                    asm volatile("" : "+v"(o), "+v"(cw));
                    u32x4 a[5], lt; gather_tab(o, a, lt, 9 + 32 * layer, ref + (layer ? SCH(S_XA1) : SCH(S_XA0)) + 8u);
                    float mb, rden; img_stats(a, mb, rden);
                    float r0 = 0.f, r1 = 0.f;
#pragma unroll
                    for (int j = 0; j < 10; ++j) { r0 = dot2(a[j >> 1][(j & 1) * 2], w[j >> 1][(j & 1) * 2], r0); r1 = dot2(a[j >> 1][(j & 1) * 2 + 1], w[j >> 1][(j & 1) * 2 + 1], r1); }
                    const float r = bfr((sum_x4(r0 + r1) - mb * cw) * rden);
                    rs = bfr(frcp(1.f + fexp(-r)));
                    xn = bfr(bfr(xr - mb) * rden);
                    asm volatile("s_waitcnt vmcnt(0)" : "+v"(lt), "+v"(rs), "+v"(xn) :: "memory");
                    ref += (unsigned)imax(late(lt, ref + (layer ? SCH(S_XA1) : SCH(S_XA0))), 0);
                    if (!layer) DMARK(38);
                    if (layer == 0) {
                        if (g == 0) st4_m<1u, 0u>(ws, WS_EP, tag);
                        lds_wait_ge(lb + L_CNT + 44, 2u);
                    }
                    STAMP(9 + 32 * layer); if (!layer) DMARK(37);
                }
                {
                    unsigned o = (a_own(oi) ^ cx) + (unsigned)(layer * LB + lane * 16 + 2048);
                    unsigned so = layer ? (unsigned)((p4 * DM + oi * 4 + (lane >> 5) * 2) * 2) : WS_XB + po;
                    asm volatile("" : "+v"(rs), "+v"(xn), "+v"(o), "+v"(so));
                    wait_until(ref + (layer ? SCH(S_H51) : SCH(S_H50)));
                    CY(c_a);
                    u32x4 a[6];
                    GATHER(13 + 32 * layer, { a[0] = ld16_sc1o<-2048>(ws, o); a[1] = ld16_sc1o<-1024>(ws, o); a[2] = ld16_sc1o<0>(ws, o);
                                 a[3] = ld16_sc1o<1024>(ws, o); a[4] = ld16_sc1o<2048>(ws, o); a[5] = ld16_sc1o<3072>(ws, o);
                                 asm volatile("s_waitcnt vmcnt(0)" : "+v"(a[0]), "+v"(a[1]), "+v"(a[2]), "+v"(a[3]), "+v"(a[4]), "+v"(a[5]) :: "memory"); },
                           allok(umax(umax(mx4(a[0], mx4(a[1])), mx4(a[2], mx4(a[3]))), mx4(a[4], mx4(a[5])))));
                    CY(c_b);
                    const float y = bfr(red4(((asf(a[0]) + asf(a[1])) + (asf(a[2]) + asf(a[3]))) + (asf(a[4]) + asf(a[5]))));
                    xr += bfr(xn + bfr(y * rs));
                    auto sw = __builtin_amdgcn_permlane16_swap(__float_as_uint(xr), __float_as_uint(xr), false, false);
                    const unsigned w = pk2(__uint_as_float(sw[0]), __uint_as_float(sw[1]));
                    if (layer == 0) {
                        st4_sc1(ws, so, enc(w));
                        const unsigned t = now32() + SCH(S_GAP), t2 = ref + SCH(S_XB), tx = ((int)(t - t2) > 0 ? t + SCH(S_EXB) : t2) | 1u;
                        asm volatile("ds_write_b32 %0, %1" :: "v"(lb + L_CNT + 60), "v"(tx) : "memory");
                        if ((sub == 0 && (h & 3) == 0) || (int)(t - t2) > 0) st4_m<0xffu, 0u>(ws, WS_XB + tw, enc(tx));
                    }
                    else {
                        asm volatile("s_mov_b32 exec_lo, 15\n\ts_mov_b32 exec_hi, 15\n\tglobal_store_dword %0, %1, %2 " OSX "\n\ts_mov_b64 exec, -1" :: "v"(so), "v"(w), "s"(out) : "memory");
                    }
                    *reinterpret_cast<float*>(smem + L_XR + lane * 4) = bfr(xr);
                    CY(c_c); CYW(57, c_a, c_b); CYW(58, c_b, c_c);
                    STAMP(13 + 32 * layer); if (!layer) DMARK(36);
                }
            } else if (wave == 4 && layer_ == 0) {
                LANES();
                const u32x4 ones = splat<~0u>();
                if (SCH(S_FL) & 32) lds_wait_ge(lb + L_CNT + 8, 1u);
                if (SCH(S_FL) & 64) { lds_wait_ge(lb + L_CNT + 48, 1u); wait_until(lds_ld(lb + L_CNT + 48) + 30u); }
                if (!(SCH(S_FL) & 128)) {
#pragma unroll
                for (int i = 0; i < 6; ++i) st16_l(ws, (a_own(oi) ^ CX(0)) + (unsigned)(i * 1024 + lane * 16), ones);
                if (oi < 96 && lane < 32) st16_l(ws, WS_XB + (unsigned)((oi / 12) * 8192 + (oi % 12) * 512 + lane * 16), ones);
                asm volatile("s_waitcnt vmcnt(0)\n\tds_write_b32 %0, %1" :: "v"(lb + L_CNT + 44), "v"(1u) : "memory");
                }
                lds_wait_ge(lb + L_CNT + 8, 1u); wait_until(lds_ld(lb + L_CNT + 8) + SCH(S_CL) - 100u);
                if (SCH(S_FL) & 2) {
#pragma unroll
                    for (int i = 0; i < 2; ++i) st16_l(ws, a_own(oi) + (unsigned)(LB + 6144 + i * 1024 + lane * 16), (u32x4){tag + 1u, tag + 1u, tag + 1u, tag + 1u});
                }
                if (oi < 128) { st16_l(ws, WS_XA + LB + (unsigned)(oi * 1536 + lane * 16), ones); if (lane < 32) st16_l(ws, WS_XA + LB + (unsigned)(oi * 1536 + 1024 + lane * 16), ones); }
#pragma unroll
                for (int i = 0; i < 3; ++i) st16_l(ws, a_h1(h, lane >> 4) + (unsigned)(((2 * i + ((lane >> 3) & 1)) * 5 + sub) * 128 + (lane & 7) * 16), ones);
                asm volatile("s_waitcnt vmcnt(0)\n\tds_write_b32 %0, %1" :: "v"(lb + L_CNT + 44), "v"(2u) : "memory");
                STAMP(26);
            }
        } else {
            PAGE(3);
            LAYER();
            if (wave < 4) {
                LANES();
                if (layer == 0) { VMW(0); STAMP(27); lds_wait_ge(lb + L_CNT + 8, 1u); ref = lds_ld(lb + L_CNT + 8); }
                const unsigned gs = xm + (unsigned)(wave * 1280);
                int st;
                asm volatile(ASM_GU : [st] "=&s"(st) : [a0] "v"(lb + L_GU + (unsigned)((wave * 160 + 8 * kq + q4 + 4 * (kq & 1)) * 32 + p4 * 8)), [gs] "v"(gs + lane * 16),
                             [g2] "v"(gs + (unsigned)(1024 + kq * 64 + p4 * 16 + q4 * 4)), [pw] "v"(lb + L_OP + (unsigned)(n * 784 + kq * 192 + wave * 8)), [gw] "v"(lb + L_OP + (unsigned)(4096 + n * 272 + kq * 64 + wave * 4)),
                             [ws] "s"(ws), [tg] "s"(ref + (layer ? SCH(S_XA1) : SCH(S_XA0))), [dbg] "s"(lb + L_DBG + wave * 512 + 256 + (10 + 32 * layer) * 4) : "memory", "scc", "vcc", "v6", "v7", CLOB_8_15, CLOB_16_63);
                STAMP(11 + 32 * layer);
                if (wave == 1 && layer == 0) {
                    u32x4 lt = ld16_sc1(ws, xm + (unsigned)(5120 + lane * 16));
                    asm volatile("s_waitcnt vmcnt(0)" : "+v"(lt) :: "memory");
                    asm volatile("ds_write_b32 %0, %1" :: "v"(lb + L_CNT + 60), "v"((ref + SCH(S_XB) + (unsigned)imax(late(lt, ref + SCH(S_XA0)), 0)) | 1u) : "memory");
                }
            } else {
                LANES();
                if (layer == 0) {
                    lds_wait_ge(lb + L_CNT + 48, 1u);
                    if (wave == 7) DMARK(39);
                }
                const unsigned wdo = lb + L_WD + (unsigned)(4 * kq * 1312 + (160 * (wave & 3) + 4 * p4) * 2);
                unsigned w0 = wdo + q4 * 1312, w1 = wdo + ((q4 + 1) & 3) * 1312, w2 = wdo + ((q4 + 2) & 3) * 1312, w3 = wdo + ((q4 + 3) & 3) * 1312;
                asm volatile(ASM_HS : [w0] "+{v8}"(w0), [w1] "+{v9}"(w1), [w2] "+{v10}"(w2), [w3] "+{v11}"(w3) :
                             [rp] "v"(lb + L_OP + (unsigned)(p4 * 784 + (4 * kq + q4) * 48)), [cp] "v"(lb + L_OP + (unsigned)(4 * 784 + (4 * kq + q4) * 48)),
                             [sq] "v"(lb + L_OP + (unsigned)(4096 + p4 * 288)), [sx] "v"(lb + L_OP + (unsigned)(4096 + p4 * 272 + 64)),
                             [so] "v"(((unsigned)(layer * LB + ((10 * (wave & 1) + q4) * 4 + kq) * GP + (wave & 2) * 4096 + mi * 64 + p4 * 16)) ^ ((unsigned)((q4 * 4 + kq) << 7) | cx)), [ws] "s"(ws)
                             : "memory", "v12", "v13", CLOB_16_63);
                STAMP(12 + 32 * layer); if (!layer) DMARK(32);
            }
        }
        if (layer_ != 0) break;
        unsigned tz = 0u;
        if (wave == 7) lds_wait_ge(lb + L_CNT + 8, 1u);
        if (is_mlp || wave == 7) tz = (lds_ld(lb + L_CNT + 8) + SCH(S_H50) + SCH(S_Y2)) | 1u;
        if (wave == (is_mlp ? 2 : 7)) {
            LANES();
            unsigned o = WS_CS + (unsigned)((h * 6 + (lane < 48 ? lane >> 3 : 0)) * 640 + (lane & 7) * 16); const unsigned t8 = tag & 0xffu;
            asm volatile("" : "+v"(o));
            { const unsigned tc = tz - SCH(S_H50) - SCH(S_Y2) + SCH(S_TR); while ((int)(now32() - tc) < 0) __builtin_amdgcn_s_sleep(3); }
            u32x4 a[5];
#pragma unroll 1
            for (unsigned spins = 0; spins < kSpinCap; ++spins) {
                a[0] = ld16_sc1o<0>(ws, o); a[1] = ld16_sc1o<128>(ws, o); a[2] = ld16_sc1o<256>(ws, o); a[3] = ld16_sc1o<384>(ws, o); a[4] = ld16_sc1o<512>(ws, o);
                asm volatile("s_waitcnt vmcnt(0)" : "+v"(a[0]), "+v"(a[1]), "+v"(a[2]), "+v"(a[3]), "+v"(a[4]) :: "memory");
#pragma unroll
                for (int i = 0; i < 5; ++i) a[i] ^= t8;
                if (tagok(or4(a[0] | a[1] | a[2] | a[3] | a[4]))) break;
                RETRY(4, spins + 1);
                __builtin_amdgcn_s_sleep(4);
            }
            if (lane < 48) *reinterpret_cast<f32x4*>(smem + L_CS + (lane >> 3) * 128 + (lane & 1) * 64 + ((lane >> 1) & 3) * 16) = ((asf(a[0]) + asf(a[1])) + (asf(a[2]) + asf(a[3]))) + asf(a[4]);
            STAMP(32);
        }
        {
            const unsigned xb = WS_XB + (a_rep(xg) ^ CX(0));
#define QPROJ() do { const int tp = wave == 6 ? 5 : wave; int st_, rf_; \
                    const unsigned a0_ = lb + L_WQ + (unsigned)((8 * kq + q4) * 384 + ((2 * tp) ^ ((q4 >> 1) | ((kq & 1) << 1))) * 32 + p4 * 8); \
                    asm volatile(ASM_QP : [st] "=&s"(st_), [rf] "=&s"(rf_) : [a0] "{v60}"(a0_), [a1] "{v61}"(a0_ ^ 32u), [gs] "{v62}"(xb + (unsigned)(sub * 1024 + lane * 16)), \
                                 [so] "{v63}"((a_h1(h, p4) ^ CX(1)) + (unsigned)((tp * 5 + sub) * 128 + kq * 32 + (q4 & 1) * 16)), \
                                 [ws] "s"(ws), [fl] "s"(lb + L_CNT + 60), [tz] "s"(SCH(S_H50) + SCH(S_Y2)), [dbg] "s"(lb + L_DBG + wave * 512 + 256 + 14 * 4) : "memory", "scc", "vcc", CLOB_16_59); } while (0)
#define ATT_IN() LANES(); \
            ATT_ADDR(1); \
            const unsigned go = (wave < 6 ? (a_h1(h, b) ^ CX(1)) + (unsigned)(wave * 640) : (a_own(oi) ^ CX(1)) + (unsigned)(LB + (wave & 1) * 3072)) + (unsigned)(lane * 16); \
            const unsigned qo = lb + L_Q + (unsigned)((wave & 1) * 192 + (wave >> 1) * 64 + (lane & 1) * 32 + (kq >> 1) * 16 + ((lane >> 1) & 3) * 4 + (kq & 1) * 2); \
            const unsigned cs = lb + L_CS + (unsigned)(wave * 128 + (lane & 1) * 64 + ((lane >> 1) & 3) * 16 + kq * 4); \
            int st
#define ATT_OPS : [st] "=&s"(st) : [qa] "{v8}"(qa), [pw] "{v9}"(pw), [pr] "{v10}"(pr), [ow] "{v11}"(ow), [mo] "{v12}"(mo), [r0] "{v5}"(r0), [so] "{v6}"(so), [go] "{v13}"(go), [qo] "{v14}"(qo), [cs] "{v15}"(cs), [ix] "{v7}"(ix), \
                [mlw] "s"(lb + L_ML + wave * 4), [ma] "s"(lb + L_ML), [ws] "s"(ws), [wv] "s"(wave), \
                [cl] "s"((wave + 2) & (sub - 5) & 8), [tg] "s"(SCH(S_H1)), [tz] "s"(is_mlp && tz ? tz + SCH(S_Y3) : 0u), [dbg] "s"(lb + L_DBG + wave * 512 + 256 + 35 * 4) : "memory", "scc", "vcc", CLOB_16_63 TRCL
            if (wave >= 4) {
                ATT_IN();
                unsigned go_ = go, qo_ = qo, cs_ = cs;
                asm volatile("" : "+{v8}"(qa), "+{v9}"(pw), "+{v10}"(pr), "+{v11}"(ow), "+{v12}"(mo), "+{v5}"(r0), "+{v6}"(so), "+{v13}"(go_), "+{v14}"(qo_), "+{v15}"(cs_), "+{v7}"(ix));
                PAGE(5);
                if (!(wave & 1) && !is_mlp) { QPROJ(); DMARK(21); }
                if (wave == 7) {
                unsigned o = xb + (unsigned)(2048 + lane * 16);
                asm volatile("" : "+v"(o));
                unsigned gx = 0u;
                if (PRI & 2) __builtin_amdgcn_s_setprio(3);
                if (tz) while ((int)(now32() - tz) < 0) __builtin_amdgcn_s_sleep(3);
#pragma unroll 1
                for (unsigned spins = 0; spins < (kSpinCap << 6) && gx == 0u; ++spins) gx = lds_ld(lb + L_CNT + 60);
                u32x4 a[5], r; gather_tab(o, a, r, 14, gx + 4u);
                unsigned k0 = lane & 7 ? 1u : 2u;
                asm volatile("s_waitcnt vmcnt(0)" : "+v"(r), "+v"(k0) :: "memory");
                auto dif = [&](unsigned w, unsigned k) { const unsigned x = w ^ gx, y = w + k; return x < y ? x : y; };
                if (__builtin_expect(__builtin_amdgcn_ballot_w64((dif(r[0], k0) | dif(r[1], 1u) | dif(r[2], 1u) | dif(r[3], 1u)) != 0u) != 0ull, 0)) {
#pragma unroll 1
                    for (unsigned spins = 0; spins < kSpinCap && !allok(lane & 7 ? 0u : r[0]); ++spins) {
                        RETRY(15, spins + 1);
                        __builtin_amdgcn_s_sleep(4);
                        r = ld16_sc1o<3072>(ws, o);
                        asm volatile("s_waitcnt vmcnt(0)" : "+v"(r) :: "memory");
                    }
                    gx = (gx + (unsigned)late(r, gx)) | 1u;
                }
                asm volatile("ds_write_b32 %0, %1" :: "v"(lb + L_CNT + 20), "v"(gx) : "memory");
                STAMP(38); DMARK(28);
                float mb, rden; img_stats(a, mb, rden);
                STAMP(14); DMARK(24);
                if (lane == b) *reinterpret_cast<u32x2*>(smem + L_CNT + 32) = (u32x2){__float_as_uint(mb), __float_as_uint(rden)};
                BAR();
                }
                { const unsigned go = go_, qo = qo_, cs = cs_; asm volatile(ASM_ATT_B ATT_OPS); }
            }
            else if (is_mlp) { PAGE(6); ATT_IN(); asm volatile(ASM_ATT_A ATT_OPS); }
            else {
                ATT_IN();
                const unsigned a0 = lb + L_WO + (unsigned)((8 * kq + q4) * 256 + ((2 * wave) ^ (q4 | ((kq & 1) << 2))) * 32 + p4 * 8), h2 = (a_h2(h) ^ CX(1)) + LB; const int ot = (8 * sub + 2 * wave + (q4 & 1)) * 4 + kq, rw = (q4 & 1) ? 3 - p4 : p4;
                unsigned i0 = qa, i1 = pw, i2 = pr, i3 = ow, i4 = mo, i5 = go, i6 = qo, i7 = cs; int rf;
                unsigned j0 = a0, j1 = a0 ^ 32u, j2 = h2 + (unsigned)((lane >> 3) * 128 + rw * 32 + (q4 & 1) * 16), j3 = h2 + (unsigned)(3072 + ((n >> 3) * 4 + rw) * MLS1);
                unsigned j4 = (a_own(ot) + (unsigned)(LB + 6144 + h * 64 + rw * 16)) ^ ((unsigned)((ot & 15) << 7) | CX(1));
                asm volatile("" : "+{v8}"(i0), "+{v9}"(i1), "+{v10}"(i2), "+{v11}"(i3), "+{v12}"(i4), "+{v13}"(i5), "+{v14}"(i6), "+{v15}"(i7), "+{v0}"(j0), "+{v1}"(j1), "+{v2}"(j2), "+{v3}"(j3), "+{v4}"(j4), "+{v5}"(r0), "+{v6}"(so), "+{v7}"(ix));
                PAGE(7);
                QPROJ();
                asm volatile(ASM_ATT_P : [st] "=&s"(st), [rf] "=&s"(rf), [t0] "+{v7}"(ix), [qa] "+{v8}"(i0), [pw] "+{v9}"(i1), [pr] "+{v10}"(i2), [ow] "+{v11}"(i3), [mo] "+{v12}"(i4), [go] "+{v13}"(i5), [qo] "+{v14}"(i6), [cs] "+{v15}"(i7)
                             : [a0] "{v0}"(j0), [a1] "{v1}"(j1), [gs] "{v2}"(j2), [gm] "{v3}"(j3), [s2] "{v4}"(j4), [r0] "{v5}"(r0), [so] "{v6}"(so),
                               [mlw] "s"(lb + L_ML + wave * 4), [ma] "s"(lb + L_ML), [ws] "s"(ws),
                               [tg] "s"(SCH(S_H1)), [t2] "s"(SCH(S_H2)), [wv] "s"(wave), [tag] "s"(tag), [dbg] "s"(lb + L_DBG + wave * 512 + 256 + 35 * 4) : "memory", "scc", "vcc", CLOB_16_63 TRCL);
                STAMP(39); DMARK(23);
                break;
            }
            if (wave != 7) STAMP(38);
            DMARK(22); if (is_mlp) DMARK(26);
            ref = lds_ld(lb + L_CNT + 20);
        }
    }
    asm volatile("s_endpgm\n\t.fill 128, 4, 0xbf800000\nmega_pf_end:" ::: "memory");
}

extern "C" void kernel_launch(void* const* d_in, const int* in_sizes, int n_in, void* d_out, int out_size, void* d_ws,
                              size_t ws_size, hipStream_t stream) {
    const u16* x0 = (const u16*)d_in[0];
    const u16* kc = (const u16*)d_in[1];
    const u16* vc = (const u16*)d_in[2];
    const u16* wq = (const u16*)d_in[3];
    const u16* wo = (const u16*)d_in[4];
    const u16* wr = (const u16*)d_in[6];
    const u16* wg = (const u16*)d_in[7];
    const u16* wu = (const u16*)d_in[8];
    const u16* wd = (const u16*)d_in[9];
    (void)hipFuncSetAttribute((const void*)mega_kernel, hipFuncAttributeMaxDynamicSharedMemorySize, L_END);
    mega_kernel<<<256, 512, L_END, stream>>>(kc, vc, (unsigned char*)(((size_t)d_ws + LB - 1) & ~(size_t)(LB - 1)), x0, wq, wg, wu, wo, wr, wd, (u16*)d_out
                                             );
}
```

```cpp
#define SCHED {131,233,301,56,205,272,324,424,10,0,490,3,1822,266,14,0,256,16,150,0,75,67,564,60,4,16,0,9,70,40,16,40,0,4,170,230,455,30,70,0,120,30,44,46}
#define WOMODE 0
#define KAL 4096
#define P_LN 7
#define P_QP 9
#define F7 4
#define DHI 4
#define WQP 4
#define EPF 3
#define LDR 38
#define CA 10
#define A7 13
#define Q7 28
#define L7 28
#define QG 37
#define QW 5
#define ECX "sc1"
#define NN7 18
#define WK -4
#define DNW 21
#define NM7 0
#define TSX 1
#define WSP -1
#define WPRE 0
#define MLS 16
#define MLS1 16
#include <hip/hip_runtime.h>
#include <hip/hip_bf16.h>
#include <stdint.h>

typedef unsigned short u16;
typedef unsigned u32x4 __attribute__((ext_vector_type(4)));
typedef unsigned u32x2 __attribute__((ext_vector_type(2)));
typedef unsigned u32x8 __attribute__((ext_vector_type(8)));
typedef float f32x4 __attribute__((ext_vector_type(4)));
typedef short s16x4 __attribute__((ext_vector_type(4)));
typedef __attribute__((ext_vector_type(4))) __bf16 bf16x4;
typedef __attribute__((ext_vector_type(8))) __bf16 bf16x8;
#define LAS __attribute__((address_space(3)))

constexpr int DM = 640, HQ = 32, SK = 4096, DH = 192, WIN = 1024;
constexpr int QD = HQ * DH;
constexpr float SCALE = 0.07216878364870322f;
constexpr float L2E = 1.44269504088896341f;
constexpr unsigned kSpinCap = 1u << 16;
constexpr int NMLP = 96;
#define TDEPTH 4

#define POL 0
#define WPOL " nt"
enum { S_XA0, S_H50, S_XB, S_H1, S_H2, S_H3, S_XA1, S_H51, S_GAP, S_PA, S_PD, S_WT, S_WD, S_CL, S_H4, S_GS, S_TS, S_TW, S_TR, S_C5, S_H30, S_H20, S_PM, S_EX, S_N0, S_N1, S_NT, S_ND, S_NR, S_PT, S_FL, S_BT, S_G1, S_G2, S_G3, S_X3, S_X4, S_X5, S_X6, S_X7, S_Y0, S_Y1, S_Y2, S_Y3, S_N };
#define S_LT S_GS
#define S_EXB S_NR
#define S_PO S_BT
#define S_MO S_PT
#define S_P7 S_X4
#define S_O7 S_X5

constexpr unsigned LB = 2097152, GP = 16384;
constexpr unsigned WS_XA = 80 * GP;
constexpr unsigned WS_H2 = 84 * GP;
constexpr unsigned WS_XB = 92 * GP;
constexpr unsigned WS_TS = 96 * GP;
constexpr unsigned WS_H1B = LB + 92 * GP;
constexpr unsigned WS_PR = 124 * GP;
constexpr unsigned WS_EP = 2 * LB;
constexpr unsigned WS_AR = WS_EP + 1024;
constexpr unsigned WS_GO = WS_EP + 4096;
constexpr unsigned WS_H1A = WS_EP + 8192;
constexpr unsigned WS_CS = WS_H1A + 160 * 1792;
constexpr unsigned WS_SINK = WS_CS + 32 * 3840;
__device__ __forceinline__ unsigned a_rep(int r) { return (unsigned)((r & 3) * GP + (r >> 2) * 8192); }
__device__ __forceinline__ unsigned a_own(int o) { return (unsigned)(o * GP - (o >= 80 ? 80 * GP - 8192 : 0)); }
__device__ __forceinline__ unsigned a_h2(int h) { return WS_H2 + (unsigned)(((h & 15) >> 1) * GP + (h >> 4) * 8192 + (h & 1) * 4096); }
__device__ __forceinline__ unsigned a_hf(int h) { return WS_XA + a_rep(h >> 2) + (unsigned)((h & 2) * 6 * GP + 6144 + (h & 1) * 1024); }
__device__ __forceinline__ unsigned a_ts(int r) { return WS_TS + (unsigned)(((r & 3) >> 1) * GP + (r >> 2) * 8192 + (r & 1) * 4096); }
__device__ __forceinline__ unsigned a_h1(int h, int row) { return WS_H1B + (unsigned)(((h & 15) * 2 + (row >> 1)) * GP + (h >> 4) * 8192 + (row & 1) * 3840); }
#define CX(layer) ((pol << (12 - (layer))) & 8192u)
constexpr int LEADER = LDR;
#define RST 128
constexpr unsigned WS_DBG = 6680576;
static_assert(WS_SINK + 256 * 128 <= WS_DBG, "ws map");

constexpr int L_WQ = 0;
constexpr int L_WO = 49152;
constexpr int L_GU = 0;
constexpr int L_WD = 40960;
constexpr int OPS = 3200;
constexpr int L_OP = 98304;
constexpr int L_P = L_OP + 8 * OPS;
constexpr int L_Q = L_P + 8 * 1088;
constexpr int L_CS = L_Q + 512;
constexpr int L_ML = L_CS + 1536;
constexpr int L_XR = L_ML + 64;
constexpr int L_CNT = L_XR + 256;
constexpr int L_FW = L_CNT + 64;
constexpr int L_PF = L_FW + 64;
constexpr int L_POL = L_PF + 256;
constexpr int L_WR = L_POL + 32;
constexpr int L_DBG = L_WR + 1536;
constexpr int L_END = L_DBG + 4096;
static_assert(L_CNT + 32 - L_ML == 352, "stats offset");
static_assert(L_END <= 163840 && L_END > 81920 && L_Q % 16 == 0, "LDS");

typedef __attribute__((ext_vector_type(2))) __bf16 bf16x2;
typedef float f32x2 __attribute__((ext_vector_type(2)));
__device__ __forceinline__ unsigned pk2(float a, float b) { return __builtin_bit_cast(unsigned, __builtin_convertvector((f32x2){a, b}, bf16x2)); }
__device__ __forceinline__ float lo16(unsigned w) { return __uint_as_float(w << 16); }
__device__ __forceinline__ float hi16(unsigned w) { return __uint_as_float(w & 0xffff0000u); }
__device__ __forceinline__ float bfr(float f) { return lo16(pk2(f, f)); }
template <int CTRL> __device__ __forceinline__ float dpp(float x) {
    return __builtin_bit_cast(float, __builtin_amdgcn_mov_dpp(__builtin_bit_cast(int, x), CTRL, 0xf, 0xf, true));
}
template <int CTRL> __device__ __forceinline__ unsigned dppu(unsigned x) { return (unsigned)__builtin_amdgcn_mov_dpp((int)x, CTRL, 0xf, 0xf, true); }
__device__ __forceinline__ float row16_sum(float v) {
    v += dpp<0xB1>(v); v += dpp<0x4E>(v); v += dpp<0x141>(v); v += dpp<0x140>(v); return v;
}
__device__ __forceinline__ float sw16_add(float a, float b) { auto s = __builtin_amdgcn_permlane16_swap(__float_as_uint(a), __float_as_uint(b), false, false); return __uint_as_float(s[0]) + __uint_as_float(s[1]); }
__device__ __forceinline__ float sw32_add(float a, float b) { auto s = __builtin_amdgcn_permlane32_swap(__float_as_uint(a), __float_as_uint(b), false, false); return __uint_as_float(s[0]) + __uint_as_float(s[1]); }
__device__ __forceinline__ float wave_sum(float v) { v = row16_sum(v); v = sw16_add(v, v); return sw32_add(v, v); }
__device__ __forceinline__ int imax(int a, int b) { return a > b ? a : b; }
__device__ __forceinline__ unsigned umax(unsigned a, unsigned b) { return a > b ? a : b; }
__device__ __forceinline__ int wave_imax(int v) {
    v = imax(v, __builtin_amdgcn_mov_dpp(v, 0xB1, 0xf, 0xf, true)); v = imax(v, __builtin_amdgcn_mov_dpp(v, 0x4E, 0xf, 0xf, true));
    v = imax(v, __builtin_amdgcn_mov_dpp(v, 0x141, 0xf, 0xf, true)); v = imax(v, __builtin_amdgcn_mov_dpp(v, 0x140, 0xf, 0xf, true));
    return imax(imax(__builtin_amdgcn_readlane(v, 0), __builtin_amdgcn_readlane(v, 16)), imax(__builtin_amdgcn_readlane(v, 32), __builtin_amdgcn_readlane(v, 48)));
}
__device__ __forceinline__ int fkey(int bits) { return bits ^ ((bits >> 31) & 0x7fffffff); }

#define BAR() asm volatile("s_waitcnt lgkmcnt(0)\n\ts_barrier" ::: "memory")
#define VMW(n) asm volatile("s_waitcnt vmcnt(%0)" :: "i"(n) : "memory")

__device__ __forceinline__ u32x4 ld16_sc1(const void* sbase, unsigned voff) {
    u32x4 r; asm volatile("global_load_dwordx4 %0, %1, %2 sc1" : "=v"(r) : "v"(voff), "s"(sbase) : "memory"); return r;
}
template <int IMM> __device__ __forceinline__ u32x4 ld16_sc1o(const void* sbase, unsigned voff) {
    u32x4 r; asm volatile("global_load_dwordx4 %0, %1, %2 offset:%3 sc1" : "=v"(r) : "v"(voff), "s"(sbase), "i"(IMM) : "memory"); return r;
}
template <int IMM> __device__ __forceinline__ u32x2 ld8_sc1o(const void* sbase, unsigned voff) {
    u32x2 r; asm volatile("global_load_dwordx2 %0, %1, %2 offset:%3 sc1" : "=v"(r) : "v"(voff), "s"(sbase), "i"(IMM) : "memory"); return r;
}
__device__ __forceinline__ unsigned ld4_sc1(const void* sbase, unsigned voff) {
    unsigned r; asm volatile("global_load_dword %0, %1, %2 sc1" : "=v"(r) : "v"(voff), "s"(sbase) : "memory"); return r;
}
__device__ __forceinline__ void st16_sc1(void* sbase, unsigned voff, u32x4 v) {
    asm volatile("global_store_dwordx4 %0, %1, %2 sc1\n\ts_nop 1" :: "v"(voff), "v"(v), "s"(sbase) : "memory");
}
#define XSX "sc1 nt"
#define XLX "sc1 nt"
#define LCX "sc1"
__device__ __forceinline__ void st16_e(void* sbase, unsigned voff, u32x4 v) {
    asm volatile("global_store_dwordx4 %0, %1, %2 " ECX "\n\ts_nop 1" :: "v"(voff), "v"(v), "s"(sbase) : "memory");
}
__device__ __forceinline__ void st16_l(void* sbase, unsigned voff, u32x4 v) {
    asm volatile("global_store_dwordx4 %0, %1, %2 " LCX "\n\ts_nop 1" :: "v"(voff), "v"(v), "s"(sbase) : "memory");
}
__device__ __forceinline__ void st16_x(void* sbase, unsigned voff, u32x4 v) {
    asm volatile("global_store_dwordx4 %0, %1, %2 " XSX "\n\ts_nop 1" :: "v"(voff), "v"(v), "s"(sbase) : "memory");
}
template <int IMM> __device__ __forceinline__ u32x4 ld16_xo(const void* sbase, unsigned voff) {
    u32x4 r; asm volatile("global_load_dwordx4 %0, %1, %2 offset:%3 " XLX : "=v"(r) : "v"(voff), "s"(sbase), "i"(IMM) : "memory"); return r;
}
typedef unsigned u32x3 __attribute__((ext_vector_type(3)));
__device__ __forceinline__ u32x3 ld12_xo(const void* sbase, unsigned voff) {
    u32x3 r; asm volatile("global_load_dwordx3 %0, %1, %2 " XLX : "=v"(r) : "v"(voff), "s"(sbase) : "memory"); return r;
}
__device__ __forceinline__ void st8_sc1(void* sbase, unsigned voff, u32x2 v) {
    asm volatile("global_store_dwordx2 %0, %1, %2 sc1" :: "v"(voff), "v"(v), "s"(sbase) : "memory");
}
__device__ __forceinline__ void st4_sc1(void* sbase, unsigned voff, unsigned v) {
    asm volatile("global_store_dword %0, %1, %2 sc1" :: "v"(voff), "v"(v), "s"(sbase) : "memory");
}
__device__ __forceinline__ void st8(void* sbase, unsigned voff, u32x2 v) {
    asm volatile("global_store_dwordx2 %0, %1, %2" :: "v"(voff), "v"(v), "s"(sbase) : "memory");
}
template <int IMM> __device__ __forceinline__ u32x4 ld16_nt(const void* sbase, unsigned voff) {
    return __builtin_nontemporal_load(reinterpret_cast<const u32x4*>(reinterpret_cast<const unsigned char*>(sbase) + voff + IMM));
}
__device__ __forceinline__ u32x4 ld16_v(const void* sbase, unsigned voff) {
    return *reinterpret_cast<const u32x4*>(reinterpret_cast<const unsigned char*>(sbase) + voff);
}
__device__ __forceinline__ void dma16(const void* sbase, unsigned voff, unsigned lds_dst) {
    unsigned keep;
    asm volatile("s_mov_b32 %0, m0\n\ts_mov_b32 m0, %3\n\ts_nop 0\n\tglobal_load_lds_dwordx4 %1, %2" WPOL "\n\ts_mov_b32 m0, %0"
                 : "=&s"(keep) : "v"(voff), "s"(sbase), "s"(lds_dst) : "memory");
}
__device__ __forceinline__ void dma16p(const void* sbase, unsigned voff, unsigned lds_dst) {
    unsigned keep;
    asm volatile("s_mov_b32 %0, m0\n\ts_mov_b32 m0, %3\n\ts_nop 0\n\tglobal_load_lds_dwordx4 %1, %2\n\ts_mov_b32 m0, %0"
                 : "=&s"(keep) : "v"(voff), "s"(sbase), "s"(lds_dst) : "memory");
}
__device__ __forceinline__ void dma4(const void* sbase, unsigned voff, unsigned lds_dst) {
    unsigned keep;
    asm volatile("s_mov_b32 %0, m0\n\ts_mov_b32 m0, %3\n\ts_nop 0\n\tglobal_load_lds_dword %1, %2\n\ts_mov_b32 m0, %0"
                 : "=&s"(keep) : "v"(voff), "s"(sbase), "s"(lds_dst) : "memory");
}
__device__ __forceinline__ void dma4n(const void* sbase, unsigned voff, unsigned lds_dst) {
    unsigned keep;
    asm volatile("s_mov_b32 %0, m0\n\ts_mov_b32 m0, %3\n\ts_nop 0\n\tglobal_load_lds_dword %1, %2 nt\n\ts_mov_b32 m0, %0"
                 : "=&s"(keep) : "v"(voff), "s"(sbase), "s"(lds_dst) : "memory");
}
#define KEEPY " sc1"
__device__ __forceinline__ void pull(const void* sbase, unsigned voff, unsigned lds_dst) {
    unsigned keep;
    asm volatile("s_mov_b32 %0, m0\n\ts_mov_b32 m0, %3\n\ts_nop 0\n\tglobal_load_lds_dword %1, %2 nt\n\tglobal_load_lds_dword %1, %2" KEEPY "\n\ts_mov_b32 m0, %0"
                 : "=&s"(keep) : "v"(voff), "s"(sbase), "s"(lds_dst) : "memory");
}
__device__ __forceinline__ unsigned poll4(void* sbase, unsigned voff) {
    unsigned r;
    asm volatile("global_load_dword %0, %1, %2 sc1\n\ts_waitcnt vmcnt(0)" : "=v"(r) : "v"(voff), "s"(sbase) : "memory");
    return r;
}
__device__ __forceinline__ unsigned enc(unsigned w) { return w < 0xfffffffeu ? w : 0xfffffffeu; }
__device__ __forceinline__ u32x4 enc4(f32x4 c) { return (u32x4){enc(__float_as_uint(c[0])), enc(__float_as_uint(c[1])), enc(__float_as_uint(c[2])), enc(__float_as_uint(c[3]))}; }
__device__ __forceinline__ unsigned mx4(u32x4 a) { return umax(umax(umax(a[0], a[1]), a[2]), a[3]); }
__device__ __forceinline__ unsigned mx4(u32x4 a, unsigned m) { return umax(umax(umax(umax(m, a[0]), a[1]), a[2]), a[3]); }
__device__ __forceinline__ bool allok(unsigned m) { return __builtin_amdgcn_ballot_w64(m == 0xffffffffu) == 0ull; }
__device__ __forceinline__ u32x4 tag4(u32x4 a, unsigned t8) { return (a & 0xffffff00u) | t8; }
__device__ __forceinline__ unsigned or4(u32x4 a) { return a[0] | a[1] | a[2] | a[3]; }
__device__ __forceinline__ bool tagok(unsigned m) { return __builtin_amdgcn_ballot_w64((m & 0xffu) != 0u) == 0ull; }
__device__ __forceinline__ f32x4 asf(u32x4 a) { return __builtin_bit_cast(f32x4, a); }

__device__ __forceinline__ bf16x4 tr64(unsigned lds_addr) { return __builtin_amdgcn_ds_read_tr16_b64_v4bf16((LAS bf16x4*)(size_t)lds_addr); }
__device__ __forceinline__ bf16x8 cat8(bf16x4 a, bf16x4 b) { return __builtin_shufflevector(a, b, 0, 1, 2, 3, 4, 5, 6, 7); }
__device__ __forceinline__ f32x4 mfma32(bf16x8 a, bf16x8 b, f32x4 c) { return __builtin_amdgcn_mfma_f32_16x16x32_bf16(a, b, c, 0, 0, 0); }
__device__ __forceinline__ f32x4 mfma32u(u32x4 a, u32x4 b, f32x4 c) { return mfma32(__builtin_bit_cast(bf16x8, a), __builtin_bit_cast(bf16x8, b), c); }
__device__ __forceinline__ f32x4 mfma32w(bf16x8 a, u32x4 b, f32x4 c) { return mfma32(a, __builtin_bit_cast(bf16x8, b), c); }
__device__ __forceinline__ f32x4 mfma16(bf16x4 a, u32x2 b, f32x4 c) {
    return __builtin_amdgcn_mfma_f32_16x16x16bf16_1k(__builtin_bit_cast(s16x4, a), __builtin_bit_cast(s16x4, b), c, 0, 0, 0);
}
__device__ __forceinline__ unsigned now32() { return (unsigned)__builtin_amdgcn_s_memrealtime(); }
#define WSL 1
__device__ __forceinline__ void wait_until(unsigned t) {
    asm volatile("s_branch 2f\n1:\n\ts_sleep 2\n2:\n\ts_memrealtime vcc\n\ts_waitcnt lgkmcnt(0)\n\ts_sub_i32 vcc_lo, vcc_lo, %0\n\ts_cmp_lt_i32 vcc_lo, -14\n\ts_cbranch_scc1 1b\n\ts_cmp_lt_i32 vcc_lo, 0\n\ts_cbranch_scc0 4f\n"
                 "3:\n\ts_memrealtime vcc\n\ts_waitcnt lgkmcnt(0)\n\ts_sub_i32 vcc_lo, vcc_lo, %0\n\ts_cmp_lt_i32 vcc_lo, 0\n\ts_cbranch_scc1 3b\n4:" :: "s"(t) : "memory", "scc", "vcc");
}
__device__ __forceinline__ unsigned lds_ld(unsigned addr) { unsigned t; asm volatile("ds_read_b32 %0, %1\n\ts_waitcnt lgkmcnt(0)\n\ts_nop 0" : "=v"(t) : "v"(addr) : "memory"); return __builtin_amdgcn_readfirstlane(t); }
__device__ __forceinline__ void lds_wait_ge(unsigned addr, unsigned v) {
#pragma unroll 1
    for (unsigned spins = 0; spins < (kSpinCap << 6); ++spins) { if (lds_ld(addr) >= v) break; __builtin_amdgcn_s_sleep(1); }
}

#define STAMPS 0
#define STAMP(i) do {} while (0)
#define STAMPV(i, t) do {} while (0)
#define RETRY(i, n) do {} while (0)
#define RETRY_ASM ""
#define RETRY_ASM3 ""
#define RETRY_ASM2 ""

#define CY(v) do {} while (0)
#define CYW(slot, a, b) do {} while (0)
#define CYS(slot, a, b) do {} while (0)
__device__ __forceinline__ int swz(int r) { return r ^ (((r >> 3) & 1) << 2); }
__device__ __forceinline__ f32x2 unpk(unsigned w) { return (f32x2){lo16(w), hi16(w)}; }
__device__ __forceinline__ unsigned pk2v(f32x2 v) { return __builtin_bit_cast(unsigned, __builtin_convertvector(v, bf16x2)); }
__device__ __forceinline__ float frcp(float x) { return __builtin_amdgcn_rcpf(x); }
__device__ __forceinline__ float fexp(float x) { return __builtin_amdgcn_exp2f(x * L2E); }

__device__ __forceinline__ void ln_consts(float mean, float var, float& mb, float& rden) {
    const unsigned mv = pk2(mean, __builtin_amdgcn_fmed3f(var, 0.f, 3.0e38f));
    mb = lo16(mv);
    rden = frcp(bfr(__builtin_amdgcn_sqrtf(bfr(hi16(mv) + 1e-5f))));
}
__device__ __forceinline__ void row_stats(u32x4 a, u32x4 c, int lane, float& mb, float& rden) {
    const float c0 = __uint_as_float((unsigned)__builtin_amdgcn_readfirstlane((int)a[0]) << 16);
    const f32x2 c2 = {c0, c0};
    f32x2 s2 = {0.f, 0.f}, q2 = s2, t2 = s2, u2 = s2;
#pragma unroll
    for (int i = 0; i < 4; ++i) { const f32x2 d = unpk(a[i]) - c2, e = unpk(c[i]) - c2; s2 += d; q2 += d * d; t2 += e; u2 += e * e; }
    float s = s2[0] + s2[1], q = q2[0] + q2[1];
    if (lane < 16) { s += t2[0] + t2[1]; q += u2[0] + u2[1]; }
    s = wave_sum(s); q = wave_sum(q);
    const float ms = s * (1.f / DM);
    ln_consts(c0 + ms, (q - s * ms) * (1.f / DM), mb, rden);
}
__device__ __forceinline__ float red3(f32x4 v) { float z = sw16_add(sw32_add(v[0], v[2]), sw32_add(v[1], v[3])); return z + dpp<0x128>(z); }
__device__ __forceinline__ float red4(f32x4 v) { float z = red3(v); return z + dpp<0x124>(z); }
__device__ __forceinline__ float sum_x4(float v) { v += dpp<0x124>(v); v += dpp<0x128>(v); v = sw16_add(v, v); return sw32_add(v, v); }
__device__ __forceinline__ float dot2(unsigned a, unsigned b, float c) { return __builtin_amdgcn_fdot2_f32_bf16(__builtin_bit_cast(bf16x2, a), __builtin_bit_cast(bf16x2, b), c, false); }
__device__ __forceinline__ int kpos(int r) { return (r & 7) | (((r >> 5) & 3) << 3) | (((r >> 3) & 3) << 5); }
__device__ __forceinline__ int gpos(int r) { const int qt = r / 160, i = r - 160 * qt; return 160 * qt + (i < 128 ? kpos(i) : i); }
__device__ __forceinline__ float vmax3(float a, float b, float c) { float r; asm("v_max3_f32 %0, %1, %2, %3" : "=v"(r) : "v"(a), "v"(b), "v"(c)); return r; }
template <unsigned LO, unsigned HI> __device__ __forceinline__ void st16_m(void* sbase, unsigned voff, u32x4 v) {
    asm volatile("s_mov_b32 exec_lo, %3\n\ts_mov_b32 exec_hi, %4\n\tglobal_store_dwordx4 %0, %1, %2 sc1\n\ts_mov_b64 exec, -1\n\ts_nop 0" :: "v"(voff), "v"(v), "s"(sbase), "i"(LO), "i"(HI) : "memory");
}
template <unsigned LO, unsigned HI> __device__ __forceinline__ void st4_m(void* sbase, unsigned voff, unsigned v) {
    asm volatile("s_mov_b32 exec_lo, %3\n\ts_mov_b32 exec_hi, %4\n\tglobal_store_dword %0, %1, %2 sc1\n\ts_mov_b64 exec, -1" :: "v"(voff), "v"(v), "s"(sbase), "i"(LO), "i"(HI) : "memory");
}

#define GATHER(sid, LOADS, OK) do { LOADS; if (__builtin_expect(!(OK), 0)) { for (unsigned spins_ = 1; spins_ < kSpinCap; ++spins_) { RETRY(sid, spins_); __builtin_amdgcn_s_sleep(1); LOADS; if (OK) break; } } } while (0)
#define SCH(i) (sched_c[i])
#define PRIO_MODE 0
#define TAILN 8
#define ABL 0
#define DIRECT 0
#define GMODE 0
#define GP_ 0
#define GID blockIdx.x
#define H1ROW 1
#define P_BA 3
#define WO_AT 7

#define ASM_QK \
    "ds_read_b128 v[48:51], %1 offset:0\n\t" \
    "ds_read_b128 v[52:55], %1 offset:128\n\t" \
    "ds_read_b128 v[56:59], %1 offset:256\n\t" \
    "v_mov_b32 v32, 0\n\t" \
    "v_mov_b32 v33, 0\n\t" \
    "v_mov_b32 v36, 0\n\t" \
    "v_mov_b32 v37, 0\n\t" \
    "v_mov_b32 v40, 0\n\t" \
    "v_mov_b32 v41, 0\n\t" \
    "v_mov_b32 v44, 0\n\t" \
    "v_mov_b32 v45, 0\n\t" \
    "s_waitcnt lgkmcnt(2)\n\t" \
    "v_smfmac_f32_16x16x64_bf16 v[32:35], v[48:51], v[64:71], %2\n\t" \
    "v_smfmac_f32_16x16x64_bf16 v[36:39], v[48:51], v[88:95], %2\n\t" \
    "v_smfmac_f32_16x16x64_bf16 v[40:43], v[48:51], v[112:119], %2\n\t" \
    "v_smfmac_f32_16x16x64_bf16 v[44:47], v[48:51], v[136:143], %2\n\t" \
    "s_waitcnt lgkmcnt(1)\n\t" \
    "v_smfmac_f32_16x16x64_bf16 v[32:35], v[52:55], v[72:79], %2\n\t" \
    "v_smfmac_f32_16x16x64_bf16 v[36:39], v[52:55], v[96:103], %2\n\t" \
    "v_smfmac_f32_16x16x64_bf16 v[40:43], v[52:55], v[120:127], %2\n\t" \
    "v_smfmac_f32_16x16x64_bf16 v[44:47], v[52:55], v[144:151], %2\n\t" \
    "s_waitcnt lgkmcnt(0)\n\t" \
    "v_smfmac_f32_16x16x64_bf16 v[32:35], v[56:59], v[80:87], %2\n\t" \
    "v_smfmac_f32_16x16x64_bf16 v[36:39], v[56:59], v[104:111], %2\n\t" \
    "v_smfmac_f32_16x16x64_bf16 v[40:43], v[56:59], v[128:135], %2\n\t" \
    "v_smfmac_f32_16x16x64_bf16 v[44:47], v[56:59], v[152:159], %2\n\t" \
    "s_nop 4\n\t" \
    "v_add_f32_dpp %0, v32, v33 quad_perm:[0,1,2,3] row_mask:0x1 bank_mask:0xf\n\t" \
    "v_add_f32_dpp %0, v36, v37 quad_perm:[0,1,2,3] row_mask:0x2 bank_mask:0xf\n\t" \
    "v_add_f32_dpp %0, v40, v41 quad_perm:[0,1,2,3] row_mask:0x4 bank_mask:0xf\n\t" \
    "v_add_f32_dpp %0, v44, v45 quad_perm:[0,1,2,3] row_mask:0x8 bank_mask:0xf\n\t"
#define ASM_PVB0 \
    "s_nop 1\n\t" \
    "v_smfmac_f32_16x16x64_bf16 %0, %1, v[160:167], %6 abid:1\n\t" \
    "v_smfmac_f32_16x16x64_bf16 %0, %2, v[168:175], %6 abid:1\n\t" \
    "v_smfmac_f32_16x16x64_bf16 %0, %3, v[176:183], %6 abid:1\n\t" \
    "v_smfmac_f32_16x16x64_bf16 %0, %4, v[184:191], %6 abid:1\n\t" \
    "s_nop 7\n\t" \
    "ds_write_b128 %5, %0 offset:0\n\t"
#define ASM_PVB1 \
    "s_nop 1\n\t" \
    "v_smfmac_f32_16x16x64_bf16 %0, %1, v[192:199], %6 abid:1\n\t" \
    "v_smfmac_f32_16x16x64_bf16 %0, %2, v[200:207], %6 abid:1\n\t" \
    "v_smfmac_f32_16x16x64_bf16 %0, %3, v[208:215], %6 abid:1\n\t" \
    "v_smfmac_f32_16x16x64_bf16 %0, %4, v[216:223], %6 abid:1\n\t" \
    "s_nop 7\n\t" \
    "ds_write_b128 %5, %0 offset:1024\n\t"
#define ASM_PVB2 \
    "s_nop 1\n\t" \
    "v_smfmac_f32_16x16x64_bf16 %0, %1, v[224:231], %6 abid:1\n\t" \
    "v_smfmac_f32_16x16x64_bf16 %0, %2, v[232:239], %6 abid:1\n\t" \
    "v_smfmac_f32_16x16x64_bf16 %0, %3, v[240:247], %6 abid:1\n\t" \
    "v_smfmac_f32_16x16x64_bf16 %0, %4, v[248:255], %6 abid:1\n\t" \
    "s_nop 7\n\t" \
    "ds_write_b128 %5, %0 offset:2048\n\t"
#define ASM_PV20 \
    "v_smfmac_f32_16x16x64_bf16 %0, %1, v[224:231], %6 abid:1\n\t"
#define ASM_PV21 \
    "v_smfmac_f32_16x16x64_bf16 %0, %2, v[232:239], %6 abid:1\n\t"
#define ASM_PV22 \
    "v_smfmac_f32_16x16x64_bf16 %0, %3, v[240:247], %6 abid:1\n\t"
#define ASM_PV23 \
    "v_smfmac_f32_16x16x64_bf16 %0, %4, v[248:255], %6 abid:1\n\t"
#define ASM_ATT_A \
    "s_setprio 2\n\t" \
    "v_mov_b32 v28, 0\n\t" \
    "v_mov_b32 v29, 0\n\t" \
    "v_mov_b32 v32, 0\n\t" \
    "v_mov_b32 v33, 0\n\t" \
    "v_mov_b32 v36, 0\n\t" \
    "v_mov_b32 v37, 0\n\t" \
    "v_mov_b32 v40, 0\n\t" \
    "v_mov_b32 v41, 0\n\t" \
    "v_mov_b32 v48, 0\n\t" \
    "v_mov_b32 v49, 0\n\t" \
    "v_mov_b32 v50, 0\n\t" \
    "v_mov_b32 v51, 0\n\t" \
    "v_mov_b32 v52, 0\n\t" \
    "v_mov_b32 v53, 0\n\t" \
    "v_mov_b32 v54, 0\n\t" \
    "v_mov_b32 v55, 0\n\t" \
    "v_mov_b32 v56, 0\n\t" \
    "v_mov_b32 v57, 0\n\t" \
    "v_mov_b32 v58, 0\n\t" \
    "v_mov_b32 v59, 0\n\t" \
    "v_mov_b32 v16, 0\n\t" \
    "v_mov_b32 v17, 0\n\t" \
    "v_mov_b32 v18, 0\n\t" \
    "v_mov_b32 v19, 0\n\t" \
    "v_mov_b32 v20, %[ma]\n\t" \
    "s_cmp_eq_u32 %[tz], 0\n\t" \
    "s_cbranch_scc1 23f\n\t" \
    "s_branch 22f\n\t" \
    "21:\n\t" \
    "s_sleep 3\n\t" \
    "22:\n\t" \
    "s_memrealtime vcc\n\t" \
    "s_waitcnt lgkmcnt(0)\n\t" \
    "s_sub_i32 vcc_lo, vcc_lo, %[tz]\n\t" \
    "s_cmp_lt_i32 vcc_lo, 0\n\t" \
    "s_cbranch_scc1 21b\n\t" \
    "23:\n\t" \
    "s_mov_b32 vcc_hi, 0x400000\n\t" \
    "0:\n\t" \
    "ds_read_b32 v21, v20 offset:340\n\t" \
    "s_sub_u32 vcc_hi, vcc_hi, 1\n\t" \
    "s_cbranch_scc1 4f\n\t" \
    "s_waitcnt lgkmcnt(0)\n\t" \
    "v_readfirstlane_b32 %[st], v21\n\t" \
    "s_cmp_eq_u32 %[st], 0\n\t" \
    "s_cbranch_scc1 0b\n\t" \
    "s_add_u32 %[st], %[st], %[tg]\n\t" \
    "4:\n\t" \
    "s_memrealtime vcc\n\t" \
    "s_waitcnt lgkmcnt(0)\n\t" \
    "s_sub_i32 vcc_lo, vcc_lo, %[st]\n\t" \
    "s_cmp_lt_i32 vcc_lo, -4000\n\t" \
    "s_cbranch_scc1 10f\n\t" \
    "s_cmp_lt_i32 vcc_lo, 0\n\t" \
    "s_cbranch_scc1 4b\n\t" \
    "10:\n\t" \
    "s_mov_b32 exec_hi, 0xff\n\t" \
    "global_load_dwordx4 v[16:19], %[go], %[ws] sc1\n\t" \
    "s_mov_b32 exec_hi, -1\n\t" \
    "s_barrier\n\t" \
    "ds_read_b64 v[22:23], v20 offset:352\n\t" \
    "ds_read_b32 v24, %[cs]\n\t" \
    "s_mov_b32 %[st], 0x10000\n\t" \
    "s_waitcnt lgkmcnt(0)\n\t" \
    "v_mul_f32 v22, v22, v24\n\t" \
    "v_mul_f32_e64 v22, -v22, v23\n\t" \
    "5:\n\t" \
    "s_waitcnt vmcnt(0)\n\t" \
    "v_max3_u32 v24, v16, v17, v18\n\t" \
    "v_max_u32 v24, v24, v19\n\t" \
    "v_cmp_eq_u32 vcc, -1, v24\n\t" \
    "v_permlane32_swap_b32 v16, v18\n\t" \
    "v_permlane32_swap_b32 v17, v19\n\t" \
    "v_pk_add_f32 v[16:17], v[16:17], v[18:19]\n\t" \
    "s_nop 0\n\t" \
    "s_cbranch_vccnz 6f\n\t" \
    "7:\n\t" \
    "v_permlane16_swap_b32 v16, v17\n\t" \
    "v_add_f32 v16, v16, v17\n\t" \
    "s_nop 1\n\t" \
    "v_add_f32_dpp v16, v16, v16 row_ror:8 row_mask:0xf bank_mask:0xf\n\t" \
    "v_fmac_f32 v22, v16, v23\n\t" \
    "v_cvt_pk_bf16_f32 v22, v22, v22\n\t" \
    "ds_write_b16 %[qo], v22\n\t" \
    "s_waitcnt lgkmcnt(0)\n\t" \
    "s_barrier\n\t" \
    "ds_read_b128 v[16:19], %[qa] offset:0\n\t" \
    "ds_read_b128 v[20:23], %[qa] offset:128\n\t" \
    "ds_read_b128 v[24:27], %[qa] offset:256\n\t" \
    "s_waitcnt lgkmcnt(2)\n\t" \
    "v_smfmac_f32_16x16x64_bf16 v[28:31], v[16:19], v[64:71], v7\n\t" \
    "v_smfmac_f32_16x16x64_bf16 v[32:35], v[16:19], v[88:95], v7\n\t" \
    "v_smfmac_f32_16x16x64_bf16 v[36:39], v[16:19], v[112:119], v7\n\t" \
    "v_smfmac_f32_16x16x64_bf16 v[40:43], v[16:19], v[136:143], v7\n\t" \
    "s_waitcnt lgkmcnt(1)\n\t" \
    "v_smfmac_f32_16x16x64_bf16 v[28:31], v[20:23], v[72:79], v7\n\t" \
    "v_smfmac_f32_16x16x64_bf16 v[32:35], v[20:23], v[96:103], v7\n\t" \
    "v_smfmac_f32_16x16x64_bf16 v[36:39], v[20:23], v[120:127], v7\n\t" \
    "v_smfmac_f32_16x16x64_bf16 v[40:43], v[20:23], v[144:151], v7\n\t" \
    "s_waitcnt lgkmcnt(0)\n\t" \
    "v_smfmac_f32_16x16x64_bf16 v[28:31], v[24:27], v[80:87], v7\n\t" \
    "v_smfmac_f32_16x16x64_bf16 v[32:35], v[24:27], v[104:111], v7\n\t" \
    "v_smfmac_f32_16x16x64_bf16 v[36:39], v[24:27], v[128:135], v7\n\t" \
    "v_smfmac_f32_16x16x64_bf16 v[40:43], v[24:27], v[152:159], v7\n\t" \
    "v_mov_b32 v63, %[mlw]\n\t" \
    "s_nop 3\n\t" \
    "v_add_f32_dpp v60, v28, v29 quad_perm:[0,1,2,3] row_mask:0x1 bank_mask:0xf\n\t" \
    "v_add_f32_dpp v60, v32, v33 quad_perm:[0,1,2,3] row_mask:0x2 bank_mask:0xf\n\t" \
    "v_add_f32_dpp v60, v36, v37 quad_perm:[0,1,2,3] row_mask:0x4 bank_mask:0xf\n\t" \
    "v_add_f32_dpp v60, v40, v41 quad_perm:[0,1,2,3] row_mask:0x8 bank_mask:0xf\n\t" \
    "v_mul_f32 v60, 0x3dd53b94, v60\n\t" \
    "s_nop 1\n\t" \
    "v_max_f32_dpp v61, v60, v60 quad_perm:[1,0,3,2] row_mask:0xf bank_mask:0xf\n\t" \
    "s_nop 1\n\t" \
    "v_max_f32_dpp v61, v61, v61 quad_perm:[2,3,0,1] row_mask:0xf bank_mask:0xf\n\t" \
    "s_nop 1\n\t" \
    "v_max_f32_dpp v61, v61, v61 row_half_mirror row_mask:0xf bank_mask:0xf\n\t" \
    "s_nop 1\n\t" \
    "v_max_f32_dpp v61, v61, v61 row_mirror row_mask:0xf bank_mask:0xf\n\t" \
    "s_nop 1\n\t" \
    "v_max_f32_dpp v61, v61, v61 row_bcast:15 row_mask:0xa bank_mask:0xf\n\t" \
    "s_nop 1\n\t" \
    "v_max_f32_dpp v61, v61, v61 row_bcast:31 row_mask:0xc bank_mask:0xf\n\t" \
    "s_nop 0\n\t" \
    "v_readlane_b32 %[st], v61, 63\n\t" \
    "s_mov_b32 exec_lo, 0\n\t" \
    "s_brev_b32 exec_hi, 1\n\t" \
    "ds_write_b32 v63, v61\n\t" \
    "s_mov_b64 exec, -1\n\t" \
    "v_subrev_f32 v62, %[st], v60\n\t" \
    "v_exp_f32 v62, v62\n\t" \
    "s_nop 0\n\t" \
    "v_cvt_pk_bf16_f32 v60, v62, v62\n\t" \
    "ds_write_b16 %[pw], v60\n\t" \
    "ds_write_b16 %[pw], v60 offset:20\n\t" \
    "ds_read_b128 v[16:19], %[pr] offset:0\n\t" \
    "ds_read_b128 v[20:23], %[pr] offset:272\n\t" \
    "ds_read_b128 v[24:27], %[pr] offset:544\n\t" \
    "ds_read_b128 v[28:31], %[pr] offset:816\n\t" \
    "v_mov_b32 v60, %[ma]\n\t" \
    "s_setprio 0\n\t" \
    "v_add_f32_dpp v62, v62, v62 quad_perm:[1,0,3,2] row_mask:0xf bank_mask:0xf\n\t" \
    "s_nop 1\n\t" \
    "v_add_f32_dpp v62, v62, v62 quad_perm:[2,3,0,1] row_mask:0xf bank_mask:0xf\n\t" \
    "s_nop 1\n\t" \
    "v_add_f32_dpp v62, v62, v62 row_half_mirror row_mask:0xf bank_mask:0xf\n\t" \
    "s_nop 1\n\t" \
    "v_add_f32_dpp v62, v62, v62 row_mirror row_mask:0xf bank_mask:0xf\n\t" \
    "s_waitcnt lgkmcnt(3)\n\t" \
    "v_smfmac_f32_16x16x64_bf16 v[48:51], v[16:19], v[160:167], v7 abid:1\n\t" \
    "v_smfmac_f32_16x16x64_bf16 v[52:55], v[16:19], v[192:199], v7 abid:1\n\t" \
    "v_smfmac_f32_16x16x64_bf16 v[56:59], v[16:19], v[224:231], v7 abid:1\n\t" \
    "v_add_f32_dpp v62, v62, v62 row_bcast:15 row_mask:0xa bank_mask:0xf\n\t" \
    "s_waitcnt lgkmcnt(2)\n\t" \
    "v_smfmac_f32_16x16x64_bf16 v[48:51], v[20:23], v[168:175], v7 abid:1\n\t" \
    "v_smfmac_f32_16x16x64_bf16 v[52:55], v[20:23], v[200:207], v7 abid:1\n\t" \
    "v_smfmac_f32_16x16x64_bf16 v[56:59], v[20:23], v[232:239], v7 abid:1\n\t" \
    "v_add_f32_dpp v62, v62, v62 row_bcast:31 row_mask:0xc bank_mask:0xf\n\t" \
    "s_waitcnt lgkmcnt(1)\n\t" \
    "v_smfmac_f32_16x16x64_bf16 v[48:51], v[24:27], v[176:183], v7 abid:1\n\t" \
    "v_smfmac_f32_16x16x64_bf16 v[52:55], v[24:27], v[208:215], v7 abid:1\n\t" \
    "v_smfmac_f32_16x16x64_bf16 v[56:59], v[24:27], v[240:247], v7 abid:1\n\t" \
    "s_waitcnt lgkmcnt(0)\n\t" \
    "v_smfmac_f32_16x16x64_bf16 v[48:51], v[28:31], v[184:191], v7 abid:1\n\t" \
    "v_smfmac_f32_16x16x64_bf16 v[52:55], v[28:31], v[216:223], v7 abid:1\n\t" \
    "v_smfmac_f32_16x16x64_bf16 v[56:59], v[28:31], v[248:255], v7 abid:1\n\t" \
    "s_barrier\n\t" \
    "ds_read_b128 v[16:19], v60\n\t" \
    "ds_read_b128 v[20:23], v60 offset:16\n\t" \
    "ds_read_b32 v24, %[mo]\n\t" \
    "s_mov_b32 exec_lo, 0\n\t" \
    "s_brev_b32 exec_hi, 1\n\t" \
    "ds_write_b32 v63, v62 offset:32\n\t" \
    "s_mov_b64 exec, -1\n\t" \
    "v_mov_b32_dpp v48, v52 quad_perm:[0,1,2,3] row_mask:0xc bank_mask:0xf\n\t" \
    "v_mov_b32_dpp v49, v53 quad_perm:[0,1,2,3] row_mask:0xc bank_mask:0xf\n\t" \
    "v_mov_b32_dpp v50, v54 quad_perm:[0,1,2,3] row_mask:0xc bank_mask:0xf\n\t" \
    "v_mov_b32_dpp v51, v55 quad_perm:[0,1,2,3] row_mask:0xc bank_mask:0xf\n\t" \
    "ds_write_b128 %[ow], v[48:51] offset:0\n\t" \
    "s_mov_b32 exec_lo, 0x00ff00ff\n\t" \
    "s_mov_b32 exec_hi, 0xff00ff00\n\t" \
    "ds_write_b128 %[ow], v[56:59] offset:2048\n\t" \
    "s_mov_b64 exec, -1\n\t" \
    "s_waitcnt lgkmcnt(3)\n\t" \
    "v_max3_f32 v16, v16, v17, v18\n\t" \
    "v_max3_f32 v19, v19, v20, v21\n\t" \
    "v_max3_f32 v16, v16, v22, v23\n\t" \
    "v_max_f32 v26, v16, v19\n\t" \
    "v_sub_f32 v60, v24, v26\n\t" \
    "v_exp_f32 v60, v60\n\t" \
    "s_nop 0\n\t" \
    "ds_write2_b32 %[mo], v60, v26 offset0:96 offset1:104\n\t" \
    "s_waitcnt lgkmcnt(0)\n\t" \
    "s_barrier\n\t" \
    "ds_read_b128 v[16:19], %[r0]\n\t" \
    "ds_read_b128 v[20:23], %[r0] offset:512\n\t" \
    "s_waitcnt lgkmcnt(0)\n\t" \
    "v_pk_add_f32 v[16:17], v[16:17], v[20:21]\n\t" \
    "v_pk_add_f32 v[18:19], v[18:19], v[22:23]\n\t" \
    "v_pk_mul_f32 v[16:17], v[16:17], v[60:61] op_sel_hi:[1,0]\n\t" \
    "v_pk_mul_f32 v[18:19], v[18:19], v[60:61] op_sel_hi:[1,0]\n\t" \
    "s_nop 1\n\t" \
    "v_permlane32_swap_b32 v16, v18\n\t" \
    "v_permlane32_swap_b32 v17, v19\n\t" \
    "v_pk_add_f32 v[16:17], v[16:17], v[18:19]\n\t" \
    "s_nop 1\n\t" \
    "v_permlane16_swap_b32 v16, v17\n\t" \
    "v_add_f32 v16, v16, v17\n\t" \
    "s_nop 1\n\t" \
    "v_add_f32_dpp v17, v16, v16 row_ror:8 row_mask:0xf bank_mask:0xf\n\t" \
    "v_add_f32_dpp v16, v16, v16 row_ror:8 row_mask:0xf bank_mask:0xf\n\t" \
    "s_nop 1\n\t" \
    "v_permlane16_swap_b32 v16, v17\n\t" \
    "s_mov_b32 exec_lo, 0xff\n\t" \
    "v_cvt_pk_bf16_f32 v16, v16, v17\n\t" \
    "s_mov_b32 exec_hi, 0xff\n\t" \
    "v_min_u32 v16, -2, v16\n\t" \
    "global_store_dword %[so], v16, %[ws] sc1\n\t" \
    "s_mov_b64 exec, -1\n\t" \
    "s_branch 2f\n\t" \
    "6:\n\t" \
    "s_sub_u32 %[st], %[st], 1\n\t" \
    "s_cbranch_scc1 7b\n\t" \
    RETRY_ASM \
    "v_mov_b32 v16, 0\n\t" \
    "v_mov_b32 v17, 0\n\t" \
    "v_mov_b32 v18, 0\n\t" \
    "v_mov_b32 v19, 0\n\t" \
    "s_mov_b32 exec_hi, 0xff\n\t" \
    "global_load_dwordx4 v[16:19], %[go], %[ws] sc1\n\t" \
    "s_mov_b32 exec_hi, -1\n\t" \
    "s_branch 5b\n\t" \
    "2:\n\t"
#define ASM_ATT_B \
    "s_setprio 1\n\t" \
    "v_mov_b32 v28, 0\n\t" \
    "v_mov_b32 v29, 0\n\t" \
    "v_mov_b32 v32, 0\n\t" \
    "v_mov_b32 v33, 0\n\t" \
    "v_mov_b32 v36, 0\n\t" \
    "v_mov_b32 v37, 0\n\t" \
    "v_mov_b32 v40, 0\n\t" \
    "v_mov_b32 v41, 0\n\t" \
    "v_mov_b32 v48, 0\n\t" \
    "v_mov_b32 v49, 0\n\t" \
    "v_mov_b32 v50, 0\n\t" \
    "v_mov_b32 v51, 0\n\t" \
    "v_mov_b32 v52, 0\n\t" \
    "v_mov_b32 v53, 0\n\t" \
    "v_mov_b32 v54, 0\n\t" \
    "v_mov_b32 v55, 0\n\t" \
    "v_mov_b32 v56, 0\n\t" \
    "v_mov_b32 v57, 0\n\t" \
    "v_mov_b32 v58, 0\n\t" \
    "v_mov_b32 v59, 0\n\t" \
    "v_mov_b32 v44, -1\n\t" \
    "v_mov_b32 v45, -1\n\t" \
    "v_mov_b32 v46, -1\n\t" \
    "v_mov_b32 v47, -1\n\t" \
    "s_cmp_gt_u32 %[wv], 5\n\t" \
    "s_cbranch_scc0 8f\n\t" \
    "s_cmp_eq_u32 %[wv], 7\n\t" \
    "s_cbranch_scc1 9f\n\t" \
    "s_barrier\n\t" \
    "s_branch 9f\n\t" \
    "8:\n\t" \
    "v_mov_b32 v16, 0\n\t" \
    "v_mov_b32 v17, 0\n\t" \
    "v_mov_b32 v18, 0\n\t" \
    "v_mov_b32 v19, 0\n\t" \
    "v_mov_b32 v20, %[ma]\n\t" \
    "s_cmp_eq_u32 %[tz], 0\n\t" \
    "s_cbranch_scc1 23f\n\t" \
    "s_branch 22f\n\t" \
    "21:\n\t" \
    "s_sleep 3\n\t" \
    "22:\n\t" \
    "s_memrealtime vcc\n\t" \
    "s_waitcnt lgkmcnt(0)\n\t" \
    "s_sub_i32 vcc_lo, vcc_lo, %[tz]\n\t" \
    "s_cmp_lt_i32 vcc_lo, 0\n\t" \
    "s_cbranch_scc1 21b\n\t" \
    "23:\n\t" \
    "s_mov_b32 vcc_hi, 0x400000\n\t" \
    "0:\n\t" \
    "ds_read_b32 v21, v20 offset:340\n\t" \
    "s_sub_u32 vcc_hi, vcc_hi, 1\n\t" \
    "s_cbranch_scc1 4f\n\t" \
    "s_waitcnt lgkmcnt(0)\n\t" \
    "v_readfirstlane_b32 %[st], v21\n\t" \
    "s_cmp_eq_u32 %[st], 0\n\t" \
    "s_cbranch_scc1 0b\n\t" \
    "s_add_u32 %[st], %[st], %[tg]\n\t" \
    "4:\n\t" \
    "s_memrealtime vcc\n\t" \
    "s_waitcnt lgkmcnt(0)\n\t" \
    "s_sub_i32 vcc_lo, vcc_lo, %[st]\n\t" \
    "s_cmp_lt_i32 vcc_lo, -4000\n\t" \
    "s_cbranch_scc1 10f\n\t" \
    "s_cmp_lt_i32 vcc_lo, 0\n\t" \
    "s_cbranch_scc1 4b\n\t" \
    "10:\n\t" \
    "s_mov_b32 exec_hi, 0xff\n\t" \
    "global_load_dwordx4 v[16:19], %[go], %[ws] sc1\n\t" \
    "s_mov_b32 exec_hi, -1\n\t" \
    "s_barrier\n\t" \
    "ds_read_b64 v[22:23], v20 offset:352\n\t" \
    "ds_read_b32 v24, %[cs]\n\t" \
    "s_mov_b32 %[st], 0x10000\n\t" \
    "s_waitcnt lgkmcnt(0)\n\t" \
    "v_mul_f32 v22, v22, v24\n\t" \
    "v_mul_f32_e64 v22, -v22, v23\n\t" \
    "5:\n\t" \
    "s_waitcnt vmcnt(0)\n\t" \
    "v_max3_u32 v24, v16, v17, v18\n\t" \
    "v_max_u32 v24, v24, v19\n\t" \
    "v_cmp_eq_u32 vcc, -1, v24\n\t" \
    "v_permlane32_swap_b32 v16, v18\n\t" \
    "v_permlane32_swap_b32 v17, v19\n\t" \
    "v_pk_add_f32 v[16:17], v[16:17], v[18:19]\n\t" \
    "s_nop 0\n\t" \
    "s_cbranch_vccnz 6f\n\t" \
    "7:\n\t" \
    "v_permlane16_swap_b32 v16, v17\n\t" \
    "v_add_f32 v16, v16, v17\n\t" \
    "s_nop 1\n\t" \
    "v_add_f32_dpp v16, v16, v16 row_ror:8 row_mask:0xf bank_mask:0xf\n\t" \
    "v_fmac_f32 v22, v16, v23\n\t" \
    "v_cvt_pk_bf16_f32 v22, v22, v22\n\t" \
    "ds_write_b16 %[qo], v22\n\t" \
    "9:\n\t" \
    "s_waitcnt lgkmcnt(0)\n\t" \
    "s_barrier\n\t" \
    "ds_read_b128 v[16:19], %[qa] offset:0\n\t" \
    "ds_read_b128 v[20:23], %[qa] offset:128\n\t" \
    "ds_read_b128 v[24:27], %[qa] offset:256\n\t" \
    "s_cmp_eq_u32 %[cl], 0\n\t" \
    "s_cbranch_scc1 3f\n\t" \
    "global_store_dwordx4 %[go], v[44:47], %[ws] offset:0 sc1\n\t" \
    "global_store_dwordx4 %[go], v[44:47], %[ws] offset:1024 sc1\n\t" \
    "global_store_dwordx4 %[go], v[44:47], %[ws] offset:2048 sc1\n\t" \
    "3:\n\t" \
    "s_waitcnt lgkmcnt(2)\n\t" \
    "v_smfmac_f32_16x16x64_bf16 v[28:31], v[16:19], v[64:71], v7\n\t" \
    "v_smfmac_f32_16x16x64_bf16 v[32:35], v[16:19], v[88:95], v7\n\t" \
    "v_smfmac_f32_16x16x64_bf16 v[36:39], v[16:19], v[112:119], v7\n\t" \
    "v_smfmac_f32_16x16x64_bf16 v[40:43], v[16:19], v[136:143], v7\n\t" \
    "s_waitcnt lgkmcnt(1)\n\t" \
    "v_smfmac_f32_16x16x64_bf16 v[28:31], v[20:23], v[72:79], v7\n\t" \
    "v_smfmac_f32_16x16x64_bf16 v[32:35], v[20:23], v[96:103], v7\n\t" \
    "v_smfmac_f32_16x16x64_bf16 v[36:39], v[20:23], v[120:127], v7\n\t" \
    "v_smfmac_f32_16x16x64_bf16 v[40:43], v[20:23], v[144:151], v7\n\t" \
    "s_waitcnt lgkmcnt(0)\n\t" \
    "v_smfmac_f32_16x16x64_bf16 v[28:31], v[24:27], v[80:87], v7\n\t" \
    "v_smfmac_f32_16x16x64_bf16 v[32:35], v[24:27], v[104:111], v7\n\t" \
    "v_smfmac_f32_16x16x64_bf16 v[36:39], v[24:27], v[128:135], v7\n\t" \
    "v_smfmac_f32_16x16x64_bf16 v[40:43], v[24:27], v[152:159], v7\n\t" \
    "v_mov_b32 v63, %[mlw]\n\t" \
    "s_nop 3\n\t" \
    "v_add_f32_dpp v60, v28, v29 quad_perm:[0,1,2,3] row_mask:0x1 bank_mask:0xf\n\t" \
    "v_add_f32_dpp v60, v32, v33 quad_perm:[0,1,2,3] row_mask:0x2 bank_mask:0xf\n\t" \
    "v_add_f32_dpp v60, v36, v37 quad_perm:[0,1,2,3] row_mask:0x4 bank_mask:0xf\n\t" \
    "v_add_f32_dpp v60, v40, v41 quad_perm:[0,1,2,3] row_mask:0x8 bank_mask:0xf\n\t" \
    "v_mul_f32 v60, 0x3dd53b94, v60\n\t" \
    "s_nop 1\n\t" \
    "v_max_f32_dpp v61, v60, v60 quad_perm:[1,0,3,2] row_mask:0xf bank_mask:0xf\n\t" \
    "s_nop 1\n\t" \
    "v_max_f32_dpp v61, v61, v61 quad_perm:[2,3,0,1] row_mask:0xf bank_mask:0xf\n\t" \
    "s_nop 1\n\t" \
    "v_max_f32_dpp v61, v61, v61 row_half_mirror row_mask:0xf bank_mask:0xf\n\t" \
    "s_nop 1\n\t" \
    "v_max_f32_dpp v61, v61, v61 row_mirror row_mask:0xf bank_mask:0xf\n\t" \
    "s_nop 1\n\t" \
    "v_max_f32_dpp v61, v61, v61 row_bcast:15 row_mask:0xa bank_mask:0xf\n\t" \
    "s_nop 1\n\t" \
    "v_max_f32_dpp v61, v61, v61 row_bcast:31 row_mask:0xc bank_mask:0xf\n\t" \
    "s_nop 0\n\t" \
    "v_readlane_b32 %[st], v61, 63\n\t" \
    "s_mov_b32 exec_lo, 0\n\t" \
    "s_brev_b32 exec_hi, 1\n\t" \
    "ds_write_b32 v63, v61\n\t" \
    "s_mov_b64 exec, -1\n\t" \
    "v_subrev_f32 v62, %[st], v60\n\t" \
    "v_exp_f32 v62, v62\n\t" \
    "s_nop 0\n\t" \
    "v_cvt_pk_bf16_f32 v60, v62, v62\n\t" \
    "ds_write_b16 %[pw], v60\n\t" \
    "ds_write_b16 %[pw], v60 offset:20\n\t" \
    "ds_read_b128 v[16:19], %[pr] offset:0\n\t" \
    "ds_read_b128 v[20:23], %[pr] offset:272\n\t" \
    "ds_read_b128 v[24:27], %[pr] offset:544\n\t" \
    "ds_read_b128 v[28:31], %[pr] offset:816\n\t" \
    "v_mov_b32 v60, %[ma]\n\t" \
    "s_setprio 0\n\t" \
    "v_add_f32_dpp v62, v62, v62 quad_perm:[1,0,3,2] row_mask:0xf bank_mask:0xf\n\t" \
    "s_nop 1\n\t" \
    "v_add_f32_dpp v62, v62, v62 quad_perm:[2,3,0,1] row_mask:0xf bank_mask:0xf\n\t" \
    "s_nop 1\n\t" \
    "v_add_f32_dpp v62, v62, v62 row_half_mirror row_mask:0xf bank_mask:0xf\n\t" \
    "s_nop 1\n\t" \
    "v_add_f32_dpp v62, v62, v62 row_mirror row_mask:0xf bank_mask:0xf\n\t" \
    "s_waitcnt lgkmcnt(3)\n\t" \
    "s_barrier\n\t" \
    "v_smfmac_f32_16x16x64_bf16 v[48:51], v[16:19], v[160:167], v7 abid:1\n\t" \
    "v_smfmac_f32_16x16x64_bf16 v[52:55], v[16:19], v[192:199], v7 abid:1\n\t" \
    "v_smfmac_f32_16x16x64_bf16 v[56:59], v[16:19], v[224:231], v7 abid:1\n\t" \
    "v_add_f32_dpp v62, v62, v62 row_bcast:15 row_mask:0xa bank_mask:0xf\n\t" \
    "s_waitcnt lgkmcnt(2)\n\t" \
    "v_smfmac_f32_16x16x64_bf16 v[48:51], v[20:23], v[168:175], v7 abid:1\n\t" \
    "v_smfmac_f32_16x16x64_bf16 v[52:55], v[20:23], v[200:207], v7 abid:1\n\t" \
    "v_smfmac_f32_16x16x64_bf16 v[56:59], v[20:23], v[232:239], v7 abid:1\n\t" \
    "v_add_f32_dpp v62, v62, v62 row_bcast:31 row_mask:0xc bank_mask:0xf\n\t" \
    "s_waitcnt lgkmcnt(1)\n\t" \
    "v_smfmac_f32_16x16x64_bf16 v[48:51], v[24:27], v[176:183], v7 abid:1\n\t" \
    "v_smfmac_f32_16x16x64_bf16 v[52:55], v[24:27], v[208:215], v7 abid:1\n\t" \
    "v_smfmac_f32_16x16x64_bf16 v[56:59], v[24:27], v[240:247], v7 abid:1\n\t" \
    "s_waitcnt lgkmcnt(0)\n\t" \
    "v_smfmac_f32_16x16x64_bf16 v[48:51], v[28:31], v[184:191], v7 abid:1\n\t" \
    "v_smfmac_f32_16x16x64_bf16 v[52:55], v[28:31], v[216:223], v7 abid:1\n\t" \
    "v_smfmac_f32_16x16x64_bf16 v[56:59], v[28:31], v[248:255], v7 abid:1\n\t" \
    "s_mov_b32 exec_lo, 0\n\t" \
    "s_brev_b32 exec_hi, 1\n\t" \
    "ds_write_b32 v63, v62 offset:32\n\t" \
    "s_mov_b64 exec, -1\n\t" \
    "s_cmp_gt_u32 %[wv], 5\n\t" \
    "s_nop 1\n\t" \
    "v_mov_b32_dpp v48, v52 quad_perm:[0,1,2,3] row_mask:0xc bank_mask:0xf\n\t" \
    "v_mov_b32_dpp v49, v53 quad_perm:[0,1,2,3] row_mask:0xc bank_mask:0xf\n\t" \
    "v_mov_b32_dpp v50, v54 quad_perm:[0,1,2,3] row_mask:0xc bank_mask:0xf\n\t" \
    "v_mov_b32_dpp v51, v55 quad_perm:[0,1,2,3] row_mask:0xc bank_mask:0xf\n\t" \
    "ds_write_b128 %[ow], v[48:51] offset:0\n\t" \
    "s_mov_b32 exec_lo, 0x00ff00ff\n\t" \
    "s_mov_b32 exec_hi, 0xff00ff00\n\t" \
    "ds_write_b128 %[ow], v[56:59] offset:2048\n\t" \
    "s_mov_b64 exec, -1\n\t" \
    "s_cbranch_scc1 1f\n\t" \
    "s_waitcnt lgkmcnt(0)\n\t" \
    "s_barrier\n\t" \
    "ds_read_b128 v[16:19], %[r0]\n\t" \
    "ds_read_b128 v[20:23], %[r0] offset:640\n\t" \
    "ds_read_b32 v60, %[mo] offset:384\n\t" \
    "s_waitcnt lgkmcnt(1)\n\t" \
    "v_pk_add_f32 v[16:17], v[16:17], v[20:21]\n\t" \
    "v_pk_add_f32 v[18:19], v[18:19], v[22:23]\n\t" \
    "s_waitcnt lgkmcnt(0)\n\t" \
    "v_pk_mul_f32 v[16:17], v[16:17], v[60:61] op_sel_hi:[1,0]\n\t" \
    "v_pk_mul_f32 v[18:19], v[18:19], v[60:61] op_sel_hi:[1,0]\n\t" \
    "s_nop 1\n\t" \
    "v_permlane32_swap_b32 v16, v18\n\t" \
    "v_permlane32_swap_b32 v17, v19\n\t" \
    "v_pk_add_f32 v[16:17], v[16:17], v[18:19]\n\t" \
    "s_nop 1\n\t" \
    "v_permlane16_swap_b32 v16, v17\n\t" \
    "v_add_f32 v16, v16, v17\n\t" \
    "s_nop 1\n\t" \
    "v_add_f32_dpp v17, v16, v16 row_ror:8 row_mask:0xf bank_mask:0xf\n\t" \
    "v_add_f32_dpp v16, v16, v16 row_ror:8 row_mask:0xf bank_mask:0xf\n\t" \
    "s_nop 1\n\t" \
    "v_permlane16_swap_b32 v16, v17\n\t" \
    "s_mov_b32 exec_lo, 0xff\n\t" \
    "v_cvt_pk_bf16_f32 v16, v16, v17\n\t" \
    "s_mov_b32 exec_hi, 0xff\n\t" \
    "v_min_u32 v16, -2, v16\n\t" \
    "global_store_dword %[so], v16, %[ws] sc1\n\t" \
    "s_mov_b64 exec, -1\n\t" \
    "s_branch 2f\n\t" \
    "6:\n\t" \
    "s_sub_u32 %[st], %[st], 1\n\t" \
    "s_cbranch_scc1 7b\n\t" \
    RETRY_ASM \
    "v_mov_b32 v16, 0\n\t" \
    "v_mov_b32 v17, 0\n\t" \
    "v_mov_b32 v18, 0\n\t" \
    "v_mov_b32 v19, 0\n\t" \
    "s_mov_b32 exec_hi, 0xff\n\t" \
    "global_load_dwordx4 v[16:19], %[go], %[ws] sc1\n\t" \
    "s_mov_b32 exec_hi, -1\n\t" \
    "s_branch 5b\n\t" \
    "1:\n\t" \
    "s_cmp_eq_u32 %[wv], 7\n\t" \
    "s_waitcnt vmcnt(0) lgkmcnt(0)\n\t" \
    "s_barrier\n\t" \
    "s_cbranch_scc1 2f\n\t" \
    "ds_read_b32 v16, %[mo] offset:32\n\t" \
    "ds_read_b32 v60, %[mo] offset:384\n\t" \
    "ds_read_b32 v26, %[mo] offset:416\n\t" \
    "s_waitcnt lgkmcnt(0)\n\t" \
    "v_min_u32 v26, -2, v26\n\t" \
    "v_mul_f32 v16, v16, v60\n\t" \
    "s_nop 1\n\t" \
    "v_add_f32_dpp v17, v16, v16 row_ror:8 row_mask:0xf bank_mask:0xf\n\t" \
    "v_add_f32_dpp v16, v16, v16 row_ror:8 row_mask:0xf bank_mask:0xf\n\t" \
    "s_nop 1\n\t" \
    "v_permlane16_swap_b32 v16, v17\n\t" \
    "v_add_f32 v18, v16, v17\n\t" \
    "v_add_f32 v16, v16, v17\n\t" \
    "s_nop 1\n\t" \
    "v_permlane32_swap_b32 v16, v18\n\t" \
    "s_mov_b64 exec, 3\n\t" \
    "v_add_f32 v27, v16, v18\n\t" \
    "v_min_u32 v27, -2, v27\n\t" \
    "global_store_dwordx2 %[so], v[26:27], %[ws] sc1\n\t" \
    "s_mov_b64 exec, -1\n\t" \
    "2:\n\t"
#define ASM_QP \
    "s_mov_b32 %[st], 0x10000\n\t" \
    "5:\n\t" \
    "ds_read_b64_tr_b16 v[16:17], %[a0] offset:0\n\t" \
    "ds_read_b64_tr_b16 v[18:19], %[a0] offset:1536\n\t" \
    "ds_read_b64_tr_b16 v[20:21], %[a1] offset:0\n\t" \
    "ds_read_b64_tr_b16 v[22:23], %[a1] offset:1536\n\t" \
    "ds_read_b64_tr_b16 v[24:25], %[a0] offset:12288\n\t" \
    "ds_read_b64_tr_b16 v[26:27], %[a0] offset:13824\n\t" \
    "ds_read_b64_tr_b16 v[28:29], %[a1] offset:12288\n\t" \
    "ds_read_b64_tr_b16 v[30:31], %[a1] offset:13824\n\t" \
    "ds_read_b64_tr_b16 v[32:33], %[a0] offset:24576\n\t" \
    "ds_read_b64_tr_b16 v[34:35], %[a0] offset:26112\n\t" \
    "ds_read_b64_tr_b16 v[36:37], %[a1] offset:24576\n\t" \
    "ds_read_b64_tr_b16 v[38:39], %[a1] offset:26112\n\t" \
    "ds_read_b64_tr_b16 v[40:41], %[a0] offset:36864\n\t" \
    "ds_read_b64_tr_b16 v[42:43], %[a0] offset:38400\n\t" \
    "ds_read_b64_tr_b16 v[44:45], %[a1] offset:36864\n\t" \
    "ds_read_b64_tr_b16 v[46:47], %[a1] offset:38400\n\t" \
    "v_mov_b32 v48, %[fl]\n\t" \
    "v_add_u32 v50, -52, v48\n\t" \
    "s_mov_b32 vcc_hi, 0x100000\n\t" \
    "s_branch 25f\n\t" \
    "24:\n\t" \
    "s_sleep 4\n\t" \
    "25:\n\t" \
    "ds_read_b32 v49, v50\n\t" \
    "s_sub_u32 vcc_hi, vcc_hi, 1\n\t" \
    "s_cbranch_scc1 23f\n\t" \
    "s_waitcnt lgkmcnt(0)\n\t" \
    "v_readfirstlane_b32 %[rf], v49\n\t" \
    "s_cmp_eq_u32 %[rf], 0\n\t" \
    "s_cbranch_scc1 24b\n\t" \
    "s_add_u32 %[rf], %[rf], %[tz]\n\t" \
    "s_branch 22f\n\t" \
    "21:\n\t" \
    "s_sleep 3\n\t" \
    "22:\n\t" \
    "s_memrealtime vcc\n\t" \
    "s_waitcnt lgkmcnt(0)\n\t" \
    "s_sub_i32 vcc_lo, vcc_lo, %[rf]\n\t" \
    "s_cmp_lt_i32 vcc_lo, 0\n\t" \
    "s_cbranch_scc1 21b\n\t" \
    "23:\n\t" \
    "s_mov_b32 vcc_hi, 0x400000\n\t" \
    "0:\n\t" \
    "ds_read_b32 v49, v48\n\t" \
    "s_sub_u32 vcc_hi, vcc_hi, 1\n\t" \
    "s_cbranch_scc1 10f\n\t" \
    "s_waitcnt lgkmcnt(0)\n\t" \
    "v_readfirstlane_b32 %[rf], v49\n\t" \
    "s_cmp_eq_u32 %[rf], 0\n\t" \
    "s_cbranch_scc1 0b\n\t" \
    "4:\n\t" \
    "s_memrealtime vcc\n\t" \
    "s_waitcnt lgkmcnt(0)\n\t" \
    "s_sub_i32 vcc_lo, vcc_lo, %[rf]\n\t" \
    "s_cmp_lt_i32 vcc_lo, 0\n\t" \
    "s_cbranch_scc1 4b\n\t" \
    "10:\n\t" \
    "global_load_dwordx4 v[48:51], %[gs], %[ws] sc1\n\t" \
    "s_waitcnt vmcnt(0)\n\t" \
    "v_mfma_f32_16x16x32_bf16 v[16:19], v[16:19], v[48:51], 0\n\t" \
    "v_mfma_f32_16x16x32_bf16 v[20:23], v[20:23], v[48:51], 0\n\t" \
    "v_mov_b32_dpp v52, v48 row_shl:4 row_mask:0xf bank_mask:0xf\n\t" \
    "v_mov_b32_dpp v53, v49 row_shl:4 row_mask:0xf bank_mask:0xf\n\t" \
    "v_mov_b32_dpp v54, v50 row_shl:4 row_mask:0xf bank_mask:0xf\n\t" \
    "v_mov_b32_dpp v55, v51 row_shl:4 row_mask:0xf bank_mask:0xf\n\t" \
    "v_mov_b32_dpp v56, v48 row_shl:8 row_mask:0xf bank_mask:0xf\n\t" \
    "v_mov_b32_dpp v57, v49 row_shl:8 row_mask:0xf bank_mask:0xf\n\t" \
    "v_mfma_f32_16x16x32_bf16 v[16:19], v[24:27], v[52:55], v[16:19]\n\t" \
    "v_mov_b32_dpp v58, v50 row_shl:8 row_mask:0xf bank_mask:0xf\n\t" \
    "v_mov_b32_dpp v59, v51 row_shl:8 row_mask:0xf bank_mask:0xf\n\t" \
    "v_mfma_f32_16x16x32_bf16 v[20:23], v[28:31], v[52:55], v[20:23]\n\t" \
    "v_mov_b32_dpp v52, v48 row_shl:12 row_mask:0xf bank_mask:0xf\n\t" \
    "v_mov_b32_dpp v53, v49 row_shl:12 row_mask:0xf bank_mask:0xf\n\t" \
    "v_mfma_f32_16x16x32_bf16 v[16:19], v[32:35], v[56:59], v[16:19]\n\t" \
    "v_mov_b32_dpp v54, v50 row_shl:12 row_mask:0xf bank_mask:0xf\n\t" \
    "v_mov_b32_dpp v55, v51 row_shl:12 row_mask:0xf bank_mask:0xf\n\t" \
    "v_mfma_f32_16x16x32_bf16 v[20:23], v[36:39], v[56:59], v[20:23]\n\t" \
    "v_max3_u32 v24, v48, v49, v50\n\t" \
    "v_max_u32 v24, v24, v51\n\t" \
    "v_mfma_f32_16x16x32_bf16 v[16:19], v[40:43], v[52:55], v[16:19]\n\t" \
    "v_cmp_eq_u32 vcc, -1, v24\n\t" \
    "v_mfma_f32_16x16x32_bf16 v[20:23], v[44:47], v[52:55], v[20:23]\n\t" \
    "s_mov_b32 exec_lo, 0x00ff00ff\n\t" \
    "s_mov_b32 exec_hi, 0x00ff00ff\n\t" \
    "s_cbranch_vccnz 6f\n\t" \
    "s_nop 4\n\t" \
    "v_mov_b32_dpp v16, v20 row_shr:4 row_mask:0xf bank_mask:0x2\n\t" \
    "v_mov_b32_dpp v17, v21 row_shr:4 row_mask:0xf bank_mask:0x2\n\t" \
    "v_mov_b32_dpp v18, v22 row_shr:4 row_mask:0xf bank_mask:0x2\n\t" \
    "v_mov_b32_dpp v19, v23 row_shr:4 row_mask:0xf bank_mask:0x2\n\t" \
    "v_min_u32 v16, -2, v16\n\t" \
    "v_min_u32 v17, -2, v17\n\t" \
    "v_min_u32 v18, -2, v18\n\t" \
    "v_min_u32 v19, -2, v19\n\t" \
    "global_store_dwordx4 %[so], v[16:19], %[ws] sc1\n\t" \
    "s_mov_b64 exec, -1\n\t" \
    "s_branch 2f\n\t" \
    "6:\n\t" \
    "s_mov_b64 exec, -1\n\t" \
    "s_sub_u32 %[st], %[st], 1\n\t" \
    "s_cbranch_scc1 2f\n\t" \
    RETRY_ASM \
    "s_sleep 6\n\t" \
    "s_branch 5b\n\t" \
    "2:\n\t"
#define ASM_ATT_P \
    "s_setprio 2\n\t" \
    "v_mov_b32 v28, 0\n\t" \
    "v_mov_b32 v29, 0\n\t" \
    "v_mov_b32 v32, 0\n\t" \
    "v_mov_b32 v33, 0\n\t" \
    "v_mov_b32 v36, 0\n\t" \
    "v_mov_b32 v37, 0\n\t" \
    "v_mov_b32 v40, 0\n\t" \
    "v_mov_b32 v41, 0\n\t" \
    "v_mov_b32 v48, 0\n\t" \
    "v_mov_b32 v49, 0\n\t" \
    "v_mov_b32 v50, 0\n\t" \
    "v_mov_b32 v51, 0\n\t" \
    "v_mov_b32 v52, 0\n\t" \
    "v_mov_b32 v53, 0\n\t" \
    "v_mov_b32 v54, 0\n\t" \
    "v_mov_b32 v55, 0\n\t" \
    "v_mov_b32 v56, 0\n\t" \
    "v_mov_b32 v57, 0\n\t" \
    "v_mov_b32 v58, 0\n\t" \
    "v_mov_b32 v59, 0\n\t" \
    "v_mov_b32 v16, 0\n\t" \
    "v_mov_b32 v17, 0\n\t" \
    "v_mov_b32 v18, 0\n\t" \
    "v_mov_b32 v19, 0\n\t" \
    "v_mov_b32 v20, %[ma]\n\t" \
    "s_mov_b32 vcc_hi, 0x400000\n\t" \
    "0:\n\t" \
    "ds_read_b32 v21, v20 offset:340\n\t" \
    "s_sub_u32 vcc_hi, vcc_hi, 1\n\t" \
    "s_cbranch_scc1 4f\n\t" \
    "s_waitcnt lgkmcnt(0)\n\t" \
    "v_readfirstlane_b32 %[rf], v21\n\t" \
    "s_cmp_eq_u32 %[rf], 0\n\t" \
    "s_cbranch_scc1 0b\n\t" \
    "s_add_u32 %[st], %[rf], %[tg]\n\t" \
    "4:\n\t" \
    "s_memrealtime vcc\n\t" \
    "s_waitcnt lgkmcnt(0)\n\t" \
    "s_sub_i32 vcc_lo, vcc_lo, %[st]\n\t" \
    "s_cmp_lt_i32 vcc_lo, -4000\n\t" \
    "s_cbranch_scc1 10f\n\t" \
    "s_cmp_lt_i32 vcc_lo, 0\n\t" \
    "s_cbranch_scc1 4b\n\t" \
    "10:\n\t" \
    "s_mov_b32 exec_hi, 0xff\n\t" \
    "global_load_dwordx4 v[16:19], %[go], %[ws] sc1\n\t" \
    "s_mov_b32 exec_hi, -1\n\t" \
    "s_barrier\n\t" \
    "ds_read_b64 v[22:23], v20 offset:352\n\t" \
    "ds_read_b32 v24, %[cs]\n\t" \
    "s_mov_b32 %[st], 0x10000\n\t" \
    "s_waitcnt lgkmcnt(0)\n\t" \
    "v_mul_f32 v22, v22, v24\n\t" \
    "v_mul_f32_e64 v22, -v22, v23\n\t" \
    "5:\n\t" \
    "s_waitcnt vmcnt(0)\n\t" \
    "v_max3_u32 v24, v16, v17, v18\n\t" \
    "v_max_u32 v24, v24, v19\n\t" \
    "v_cmp_eq_u32 vcc, -1, v24\n\t" \
    "v_permlane32_swap_b32 v16, v18\n\t" \
    "v_permlane32_swap_b32 v17, v19\n\t" \
    "v_pk_add_f32 v[16:17], v[16:17], v[18:19]\n\t" \
    "s_nop 0\n\t" \
    "s_cbranch_vccnz 6f\n\t" \
    "7:\n\t" \
    "v_permlane16_swap_b32 v16, v17\n\t" \
    "v_add_f32 v16, v16, v17\n\t" \
    "s_nop 1\n\t" \
    "v_add_f32_dpp v16, v16, v16 row_ror:8 row_mask:0xf bank_mask:0xf\n\t" \
    "v_fmac_f32 v22, v16, v23\n\t" \
    "v_cvt_pk_bf16_f32 v22, v22, v22\n\t" \
    "ds_write_b16 %[qo], v22\n\t" \
    "s_waitcnt lgkmcnt(0)\n\t" \
    "s_barrier\n\t" \
    "ds_read_b128 v[16:19], %[qa] offset:0\n\t" \
    "ds_read_b128 v[20:23], %[qa] offset:128\n\t" \
    "ds_read_b128 v[24:27], %[qa] offset:256\n\t" \
    "s_waitcnt lgkmcnt(2)\n\t" \
    "v_smfmac_f32_16x16x64_bf16 v[28:31], v[16:19], v[64:71], v7\n\t" \
    "v_smfmac_f32_16x16x64_bf16 v[32:35], v[16:19], v[88:95], v7\n\t" \
    "v_smfmac_f32_16x16x64_bf16 v[36:39], v[16:19], v[112:119], v7\n\t" \
    "v_smfmac_f32_16x16x64_bf16 v[40:43], v[16:19], v[136:143], v7\n\t" \
    "s_waitcnt lgkmcnt(1)\n\t" \
    "v_smfmac_f32_16x16x64_bf16 v[28:31], v[20:23], v[72:79], v7\n\t" \
    "v_smfmac_f32_16x16x64_bf16 v[32:35], v[20:23], v[96:103], v7\n\t" \
    "v_smfmac_f32_16x16x64_bf16 v[36:39], v[20:23], v[120:127], v7\n\t" \
    "v_smfmac_f32_16x16x64_bf16 v[40:43], v[20:23], v[144:151], v7\n\t" \
    "s_waitcnt lgkmcnt(0)\n\t" \
    "v_smfmac_f32_16x16x64_bf16 v[28:31], v[24:27], v[80:87], v7\n\t" \
    "v_smfmac_f32_16x16x64_bf16 v[32:35], v[24:27], v[104:111], v7\n\t" \
    "v_smfmac_f32_16x16x64_bf16 v[36:39], v[24:27], v[128:135], v7\n\t" \
    "v_smfmac_f32_16x16x64_bf16 v[40:43], v[24:27], v[152:159], v7\n\t" \
    "v_mov_b32 v63, %[mlw]\n\t" \
    "s_nop 3\n\t" \
    "v_add_f32_dpp v60, v28, v29 quad_perm:[0,1,2,3] row_mask:0x1 bank_mask:0xf\n\t" \
    "v_add_f32_dpp v60, v32, v33 quad_perm:[0,1,2,3] row_mask:0x2 bank_mask:0xf\n\t" \
    "v_add_f32_dpp v60, v36, v37 quad_perm:[0,1,2,3] row_mask:0x4 bank_mask:0xf\n\t" \
    "v_add_f32_dpp v60, v40, v41 quad_perm:[0,1,2,3] row_mask:0x8 bank_mask:0xf\n\t" \
    "v_mul_f32 v60, 0x3dd53b94, v60\n\t" \
    "s_nop 1\n\t" \
    "v_max_f32_dpp v61, v60, v60 quad_perm:[1,0,3,2] row_mask:0xf bank_mask:0xf\n\t" \
    "s_nop 1\n\t" \
    "v_max_f32_dpp v61, v61, v61 quad_perm:[2,3,0,1] row_mask:0xf bank_mask:0xf\n\t" \
    "s_nop 1\n\t" \
    "v_max_f32_dpp v61, v61, v61 row_half_mirror row_mask:0xf bank_mask:0xf\n\t" \
    "s_nop 1\n\t" \
    "v_max_f32_dpp v61, v61, v61 row_mirror row_mask:0xf bank_mask:0xf\n\t" \
    "s_nop 1\n\t" \
    "v_max_f32_dpp v61, v61, v61 row_bcast:15 row_mask:0xa bank_mask:0xf\n\t" \
    "s_nop 1\n\t" \
    "v_max_f32_dpp v61, v61, v61 row_bcast:31 row_mask:0xc bank_mask:0xf\n\t" \
    "s_nop 0\n\t" \
    "v_readlane_b32 %[st], v61, 63\n\t" \
    "s_mov_b32 exec_lo, 0\n\t" \
    "s_brev_b32 exec_hi, 1\n\t" \
    "ds_write_b32 v63, v61\n\t" \
    "s_mov_b64 exec, -1\n\t" \
    "v_subrev_f32 v62, %[st], v60\n\t" \
    "v_exp_f32 v62, v62\n\t" \
    "s_nop 0\n\t" \
    "v_cvt_pk_bf16_f32 v60, v62, v62\n\t" \
    "ds_write_b16 %[pw], v60\n\t" \
    "ds_write_b16 %[pw], v60 offset:20\n\t" \
    "ds_read_b128 v[16:19], %[pr] offset:0\n\t" \
    "ds_read_b128 v[20:23], %[pr] offset:272\n\t" \
    "ds_read_b128 v[24:27], %[pr] offset:544\n\t" \
    "ds_read_b128 v[28:31], %[pr] offset:816\n\t" \
    "v_mov_b32 v60, %[ma]\n\t" \
    "s_setprio 0\n\t" \
    "v_add_f32_dpp v62, v62, v62 quad_perm:[1,0,3,2] row_mask:0xf bank_mask:0xf\n\t" \
    "s_nop 1\n\t" \
    "v_add_f32_dpp v62, v62, v62 quad_perm:[2,3,0,1] row_mask:0xf bank_mask:0xf\n\t" \
    "s_nop 1\n\t" \
    "v_add_f32_dpp v62, v62, v62 row_half_mirror row_mask:0xf bank_mask:0xf\n\t" \
    "s_nop 1\n\t" \
    "v_add_f32_dpp v62, v62, v62 row_mirror row_mask:0xf bank_mask:0xf\n\t" \
    "s_waitcnt lgkmcnt(3)\n\t" \
    "v_smfmac_f32_16x16x64_bf16 v[48:51], v[16:19], v[160:167], v7 abid:1\n\t" \
    "v_smfmac_f32_16x16x64_bf16 v[52:55], v[16:19], v[192:199], v7 abid:1\n\t" \
    "v_smfmac_f32_16x16x64_bf16 v[56:59], v[16:19], v[224:231], v7 abid:1\n\t" \
    "v_add_f32_dpp v62, v62, v62 row_bcast:15 row_mask:0xa bank_mask:0xf\n\t" \
    "s_waitcnt lgkmcnt(2)\n\t" \
    "v_smfmac_f32_16x16x64_bf16 v[48:51], v[20:23], v[168:175], v7 abid:1\n\t" \
    "v_smfmac_f32_16x16x64_bf16 v[52:55], v[20:23], v[200:207], v7 abid:1\n\t" \
    "v_smfmac_f32_16x16x64_bf16 v[56:59], v[20:23], v[232:239], v7 abid:1\n\t" \
    "v_add_f32_dpp v62, v62, v62 row_bcast:31 row_mask:0xc bank_mask:0xf\n\t" \
    "s_waitcnt lgkmcnt(1)\n\t" \
    "v_smfmac_f32_16x16x64_bf16 v[48:51], v[24:27], v[176:183], v7 abid:1\n\t" \
    "v_smfmac_f32_16x16x64_bf16 v[52:55], v[24:27], v[208:215], v7 abid:1\n\t" \
    "v_smfmac_f32_16x16x64_bf16 v[56:59], v[24:27], v[240:247], v7 abid:1\n\t" \
    "s_waitcnt lgkmcnt(0)\n\t" \
    "v_smfmac_f32_16x16x64_bf16 v[48:51], v[28:31], v[184:191], v7 abid:1\n\t" \
    "v_smfmac_f32_16x16x64_bf16 v[52:55], v[28:31], v[216:223], v7 abid:1\n\t" \
    "v_smfmac_f32_16x16x64_bf16 v[56:59], v[28:31], v[248:255], v7 abid:1\n\t" \
    "s_barrier\n\t" \
    "ds_read_b128 v[16:19], v60\n\t" \
    "ds_read_b128 v[20:23], v60 offset:16\n\t" \
    "ds_read_b32 v24, %[mo]\n\t" \
    "s_mov_b32 exec_lo, 0\n\t" \
    "s_brev_b32 exec_hi, 1\n\t" \
    "ds_write_b32 v63, v62 offset:32\n\t" \
    "s_mov_b64 exec, -1\n\t" \
    "v_mov_b32_dpp v48, v52 quad_perm:[0,1,2,3] row_mask:0xc bank_mask:0xf\n\t" \
    "v_mov_b32_dpp v49, v53 quad_perm:[0,1,2,3] row_mask:0xc bank_mask:0xf\n\t" \
    "v_mov_b32_dpp v50, v54 quad_perm:[0,1,2,3] row_mask:0xc bank_mask:0xf\n\t" \
    "v_mov_b32_dpp v51, v55 quad_perm:[0,1,2,3] row_mask:0xc bank_mask:0xf\n\t" \
    "ds_write_b128 %[ow], v[48:51] offset:0\n\t" \
    "s_mov_b32 exec_lo, 0x00ff00ff\n\t" \
    "s_mov_b32 exec_hi, 0xff00ff00\n\t" \
    "ds_write_b128 %[ow], v[56:59] offset:2048\n\t" \
    "s_mov_b64 exec, -1\n\t" \
    "s_waitcnt lgkmcnt(3)\n\t" \
    "v_max3_f32 v16, v16, v17, v18\n\t" \
    "v_max3_f32 v19, v19, v20, v21\n\t" \
    "v_max3_f32 v16, v16, v22, v23\n\t" \
    "v_max_f32 v26, v16, v19\n\t" \
    "v_sub_f32 v60, v24, v26\n\t" \
    "v_exp_f32 v60, v60\n\t" \
    "s_nop 0\n\t" \
    "ds_write2_b32 %[mo], v60, v26 offset0:96 offset1:104\n\t" \
    "ds_read_b64_tr_b16 v[28:29], %[a0] offset:0\n\t" \
    "ds_read_b64_tr_b16 v[30:31], %[a0] offset:1024\n\t" \
    "ds_read_b64_tr_b16 v[32:33], %[a1] offset:8192\n\t" \
    "ds_read_b64_tr_b16 v[34:35], %[a1] offset:9216\n\t" \
    "ds_read_b64_tr_b16 v[36:37], %[a0] offset:8192\n\t" \
    "ds_read_b64_tr_b16 v[38:39], %[a0] offset:9216\n\t" \
    "ds_read_b64_tr_b16 v[40:41], %[a1] offset:0\n\t" \
    "ds_read_b64_tr_b16 v[42:43], %[a1] offset:1024\n\t" \
    "ds_read_b64_tr_b16 v[44:45], %[a0] offset:16384\n\t" \
    "ds_read_b64_tr_b16 v[46:47], %[a0] offset:17408\n\t" \
    "ds_read_b64_tr_b16 v[48:49], %[a1] offset:24576\n\t" \
    "ds_read_b64_tr_b16 v[50:51], %[a1] offset:25600\n\t" \
    "ds_read_b64_tr_b16 v[52:53], %[a0] offset:24576\n\t" \
    "ds_read_b64_tr_b16 v[54:55], %[a0] offset:25600\n\t" \
    "ds_read_b64_tr_b16 v[56:57], %[a1] offset:16384\n\t" \
    "ds_read_b64_tr_b16 v[58:59], %[a1] offset:17408\n\t" \
    "s_waitcnt lgkmcnt(0)\n\t" \
    "s_barrier\n\t" \
    "ds_read_b128 v[16:19], %[r0]\n\t" \
    "ds_read_b128 v[20:23], %[r0] offset:512\n\t" \
    "s_waitcnt lgkmcnt(0)\n\t" \
    "v_pk_add_f32 v[16:17], v[16:17], v[20:21]\n\t" \
    "v_pk_add_f32 v[18:19], v[18:19], v[22:23]\n\t" \
    "v_pk_mul_f32 v[16:17], v[16:17], v[60:61] op_sel_hi:[1,0]\n\t" \
    "v_pk_mul_f32 v[18:19], v[18:19], v[60:61] op_sel_hi:[1,0]\n\t" \
    "s_nop 1\n\t" \
    "v_permlane32_swap_b32 v16, v18\n\t" \
    "v_permlane32_swap_b32 v17, v19\n\t" \
    "v_pk_add_f32 v[16:17], v[16:17], v[18:19]\n\t" \
    "s_nop 1\n\t" \
    "v_permlane16_swap_b32 v16, v17\n\t" \
    "v_add_f32 v16, v16, v17\n\t" \
    "s_nop 1\n\t" \
    "v_add_f32_dpp v17, v16, v16 row_ror:8 row_mask:0xf bank_mask:0xf\n\t" \
    "v_add_f32_dpp v16, v16, v16 row_ror:8 row_mask:0xf bank_mask:0xf\n\t" \
    "s_nop 1\n\t" \
    "v_permlane16_swap_b32 v16, v17\n\t" \
    "s_mov_b32 exec_lo, 0xff\n\t" \
    "v_cvt_pk_bf16_f32 v16, v16, v17\n\t" \
    "s_mov_b32 exec_hi, 0xff\n\t" \
    "v_min_u32 v16, -2, v16\n\t" \
    "global_store_dword %[so], v16, %[ws] sc1\n\t" \
    "s_mov_b64 exec, -1\n\t" \
    "ds_read_b64_tr_b16 v[60:61], %[a0] offset:32768\n\t" \
    "ds_read_b64_tr_b16 v[62:63], %[a0] offset:33792\n\t" \
    "s_add_u32 %[rf], %[rf], %[t2]\n\t" \
    "s_mov_b32 %[st], 0x10000\n\t" \
    "14:\n\t" \
    "s_memrealtime vcc\n\t" \
    "s_waitcnt lgkmcnt(0)\n\t" \
    "s_sub_i32 vcc_lo, vcc_lo, %[rf]\n\t" \
    "s_cmp_lt_i32 vcc_lo, -4000\n\t" \
    "s_cbranch_scc1 15f\n\t" \
    "s_cmp_lt_i32 vcc_lo, 0\n\t" \
    "s_cbranch_scc1 14b\n\t" \
    "15:\n\t" \
    "global_load_dwordx4 v[8:11], %[gs], %[ws] offset:0 sc1\n\t" \
    "global_load_dwordx4 v[12:15], %[gs], %[ws] offset:1024 sc1\n\t" \
    "global_load_dwordx4 v[16:19], %[gs], %[ws] offset:2048 sc1\n\t" \
    "global_load_dwordx4 v[24:27], %[gm], %[ws] sc1\n\t" \
    "s_waitcnt vmcnt(3)\n\t" \
    "v_mfma_f32_16x16x32_bf16 v[28:31], v[28:31], v[8:11], 0\n\t" \
    "v_mov_b32_dpp v20, v8 row_half_mirror row_mask:0xf bank_mask:0xf\n\t" \
    "v_mov_b32_dpp v21, v9 row_half_mirror row_mask:0xf bank_mask:0xf\n\t" \
    "v_mfma_f32_16x16x32_bf16 v[32:35], v[32:35], v[8:11], 0\n\t" \
    "v_mov_b32_dpp v22, v10 row_half_mirror row_mask:0xf bank_mask:0xf\n\t" \
    "v_mov_b32_dpp v23, v11 row_half_mirror row_mask:0xf bank_mask:0xf\n\t" \
    "v_max3_u32 %[t0], v8, v9, v10\n\t" \
    "s_waitcnt vmcnt(2)\n\t" \
    "v_mfma_f32_16x16x32_bf16 v[28:31], v[36:39], v[20:23], v[28:31]\n\t" \
    "v_max3_u32 %[t0], %[t0], v11, v12\n\t" \
    "ds_read_b64_tr_b16 v[36:37], %[a1] offset:40960\n\t" \
    "v_mfma_f32_16x16x32_bf16 v[32:35], v[40:43], v[20:23], v[32:35]\n\t" \
    "ds_read_b64_tr_b16 v[38:39], %[a1] offset:41984\n\t" \
    "ds_read_b64_tr_b16 v[40:41], %[a0] offset:40960\n\t" \
    "v_mfma_f32_16x16x32_bf16 v[28:31], v[44:47], v[12:15], v[28:31]\n\t" \
    "v_mov_b32_dpp v20, v12 row_half_mirror row_mask:0xf bank_mask:0xf\n\t" \
    "v_mov_b32_dpp v21, v13 row_half_mirror row_mask:0xf bank_mask:0xf\n\t" \
    "v_mfma_f32_16x16x32_bf16 v[32:35], v[48:51], v[12:15], v[32:35]\n\t" \
    "v_mov_b32_dpp v22, v14 row_half_mirror row_mask:0xf bank_mask:0xf\n\t" \
    "v_mov_b32_dpp v23, v15 row_half_mirror row_mask:0xf bank_mask:0xf\n\t" \
    "ds_read_b64_tr_b16 v[42:43], %[a0] offset:41984\n\t" \
    "ds_read_b64_tr_b16 v[44:45], %[a1] offset:32768\n\t" \
    "v_mfma_f32_16x16x32_bf16 v[28:31], v[52:55], v[20:23], v[28:31]\n\t" \
    "ds_read_b64_tr_b16 v[46:47], %[a1] offset:33792\n\t" \
    "s_waitcnt vmcnt(0)\n\t" \
    "v_max3_u32 %[t0], %[t0], v13, v14\n\t" \
    "v_mfma_f32_16x16x32_bf16 v[32:35], v[56:59], v[20:23], v[32:35]\n\t" \
    "v_max3_u32 v8, v24, v25, v26\n\t" \
    "v_sub_f32 v24, v26, v24\n\t" \
    "v_mfma_f32_16x16x32_bf16 v[28:31], v[60:63], v[16:19], v[28:31]\n\t" \
    "v_mov_b32_dpp v20, v16 row_half_mirror row_mask:0xf bank_mask:0xf\n\t" \
    "v_mov_b32_dpp v21, v17 row_half_mirror row_mask:0xf bank_mask:0xf\n\t" \
    "s_waitcnt lgkmcnt(4)\n\t" \
    "v_mfma_f32_16x16x32_bf16 v[32:35], v[36:39], v[16:19], v[32:35]\n\t" \
    "v_mov_b32_dpp v22, v18 row_half_mirror row_mask:0xf bank_mask:0xf\n\t" \
    "v_mov_b32_dpp v23, v19 row_half_mirror row_mask:0xf bank_mask:0xf\n\t" \
    "v_exp_f32 v24, v24\n\t" \
    "v_max3_u32 %[t0], %[t0], v15, v16\n\t" \
    "s_waitcnt lgkmcnt(0)\n\t" \
    "v_mfma_f32_16x16x32_bf16 v[28:31], v[40:43], v[20:23], v[28:31]\n\t" \
    "v_fmac_f32 v25, v24, v27\n\t" \
    "v_max3_u32 v8, v8, v27, v17\n\t" \
    "v_mfma_f32_16x16x32_bf16 v[32:35], v[44:47], v[20:23], v[32:35]\n\t" \
    "v_rcp_f32 v24, v25\n\t" \
    "v_max3_u32 %[t0], %[t0], v18, v19\n\t" \
    "v_max_u32 v8, v8, %[t0]\n\t" \
    "v_cmp_eq_u32 vcc, -1, v8\n\t" \
    "v_mov_b32 v9, 0xff\n\t" \
    "s_nop 1\n\t" \
    "s_cbranch_vccnz 16f\n\t" \
    "v_mov_b32_dpp v28, v32 quad_perm:[0,1,2,3] row_mask:0xf bank_mask:0xa\n\t" \
    "v_mov_b32_dpp v29, v33 quad_perm:[0,1,2,3] row_mask:0xf bank_mask:0xa\n\t" \
    "v_mov_b32_dpp v30, v34 quad_perm:[0,1,2,3] row_mask:0xf bank_mask:0xa\n\t" \
    "v_mov_b32_dpp v31, v35 quad_perm:[0,1,2,3] row_mask:0xf bank_mask:0xa\n\t" \
    "v_pk_mul_f32 v[28:29], v[28:29], v[24:25] op_sel_hi:[1,0]\n\t" \
    "v_pk_mul_f32 v[30:31], v[30:31], v[24:25] op_sel_hi:[1,0]\n\t" \
    "s_nop 0\n\t" \
    "v_add_f32_dpp v28, v28, v28 row_shl:8 row_mask:0xf bank_mask:0xf\n\t" \
    "v_add_f32_dpp v29, v29, v29 row_shl:8 row_mask:0xf bank_mask:0xf\n\t" \
    "v_add_f32_dpp v30, v30, v30 row_shl:8 row_mask:0xf bank_mask:0xf\n\t" \
    "v_add_f32_dpp v31, v31, v31 row_shl:8 row_mask:0xf bank_mask:0xf\n\t" \
    "s_mov_b32 exec_lo, 0x00ff00ff\n\t" \
    "s_mov_b32 exec_hi, 0x00ff00ff\n\t" \
    "v_bfi_b32 v28, v9, %[tag], v28\n\t" \
    "v_bfi_b32 v29, v9, %[tag], v29\n\t" \
    "v_bfi_b32 v30, v9, %[tag], v30\n\t" \
    "v_bfi_b32 v31, v9, %[tag], v31\n\t" \
    "global_store_dwordx4 %[s2], v[28:31], %[ws] sc1\n\t" \
    "s_mov_b64 exec, -1\n\t" \
    "s_branch 2f\n\t" \
    "6:\n\t" \
    "s_sub_u32 %[st], %[st], 1\n\t" \
    "s_cbranch_scc1 7b\n\t" \
    RETRY_ASM \
    "v_mov_b32 v16, 0\n\t" \
    "v_mov_b32 v17, 0\n\t" \
    "v_mov_b32 v18, 0\n\t" \
    "v_mov_b32 v19, 0\n\t" \
    "s_mov_b32 exec_hi, 0xff\n\t" \
    "global_load_dwordx4 v[16:19], %[go], %[ws] sc1\n\t" \
    "s_mov_b32 exec_hi, -1\n\t" \
    "s_branch 5b\n\t" \
    "16:\n\t" \
    "s_mov_b64 exec, -1\n\t" \
    "s_sub_u32 %[st], %[st], 1\n\t" \
    "s_cbranch_scc1 2f\n\t" \
    RETRY_ASM3 \
    "s_sleep 3\n\t" \
    "ds_read_b64_tr_b16 v[28:29], %[a0] offset:0\n\t" \
    "ds_read_b64_tr_b16 v[30:31], %[a0] offset:1024\n\t" \
    "ds_read_b64_tr_b16 v[32:33], %[a1] offset:8192\n\t" \
    "ds_read_b64_tr_b16 v[34:35], %[a1] offset:9216\n\t" \
    "ds_read_b64_tr_b16 v[36:37], %[a0] offset:8192\n\t" \
    "ds_read_b64_tr_b16 v[38:39], %[a0] offset:9216\n\t" \
    "ds_read_b64_tr_b16 v[40:41], %[a1] offset:0\n\t" \
    "ds_read_b64_tr_b16 v[42:43], %[a1] offset:1024\n\t" \
    "ds_read_b64_tr_b16 v[44:45], %[a0] offset:16384\n\t" \
    "ds_read_b64_tr_b16 v[46:47], %[a0] offset:17408\n\t" \
    "ds_read_b64_tr_b16 v[48:49], %[a1] offset:24576\n\t" \
    "ds_read_b64_tr_b16 v[50:51], %[a1] offset:25600\n\t" \
    "ds_read_b64_tr_b16 v[52:53], %[a0] offset:24576\n\t" \
    "ds_read_b64_tr_b16 v[54:55], %[a0] offset:25600\n\t" \
    "ds_read_b64_tr_b16 v[56:57], %[a1] offset:16384\n\t" \
    "ds_read_b64_tr_b16 v[58:59], %[a1] offset:17408\n\t" \
    "ds_read_b64_tr_b16 v[60:61], %[a0] offset:32768\n\t" \
    "ds_read_b64_tr_b16 v[62:63], %[a0] offset:33792\n\t" \
    "s_waitcnt lgkmcnt(0)\n\t" \
    "s_branch 15b\n\t" \
    "2:\n\t"
#define ASM_OP \
    "s_mov_b32 %[st], 0x10000\n\t" \
    "5:\n\t" \
    "v_mov_b32 v8, %[fl]\n\t" \
    "s_mov_b32 %[ct], 0x400000\n\t" \
    "0:\n\t" \
    "ds_read_b32 v9, v8\n\t" \
    "s_memrealtime vcc\n\t" \
    "s_sub_u32 %[ct], %[ct], 1\n\t" \
    "s_waitcnt lgkmcnt(0)\n\t" \
    "s_cbranch_scc1 10f\n\t" \
    "v_readfirstlane_b32 %[rf], v9\n\t" \
    "s_cmp_eq_u32 %[rf], 0\n\t" \
    "s_cbranch_scc1 0b\n\t" \
    "s_sub_i32 vcc_lo, vcc_lo, %[rf]\n\t" \
    "s_cmp_ge_i32 vcc_lo, -4\n\t" \
    "s_cbranch_scc1 10f\n\t" \
    "4:\n\t" \
    "s_memrealtime vcc\n\t" \
    "s_waitcnt lgkmcnt(0)\n\t" \
    "s_sub_i32 vcc_lo, vcc_lo, %[rf]\n\t" \
    "s_cmp_lt_i32 vcc_lo, 0\n\t" \
    "s_cbranch_scc1 4b\n\t" \
    "10:\n\t" \
    "global_load_dwordx4 v[8:11], %[gs], %[ws] offset:0 sc1\n\t" \
    "global_load_dwordx4 v[12:15], %[gs], %[ws] offset:1024 sc1\n\t" \
    "global_load_dwordx4 v[16:19], %[gs], %[ws] offset:2048 sc1\n\t" \
    "global_load_dwordx4 v[24:27], %[gm], %[ws] sc1\n\t" \
    "v_mov_b32 v28, %[fl]\n\t" \
    "v_add_u32 v28, -8, v28\n\t" \
    "s_mov_b32 %[ct], 0x400000\n\t" \
    "11:\n\t" \
    "ds_read_b32 v29, v28\n\t" \
    "s_sub_u32 %[ct], %[ct], 1\n\t" \
    "s_waitcnt lgkmcnt(0)\n\t" \
    "s_cbranch_scc1 12f\n\t" \
    "v_cmp_eq_u32 vcc, 0, v29\n\t" \
    "s_cbranch_vccnz 11b\n\t" \
    "12:\n\t" \
    "ds_read_b64_tr_b16 v[28:29], %[a0] offset:0\n\t" \
    "ds_read_b64_tr_b16 v[30:31], %[a0] offset:1024\n\t" \
    "ds_read_b64_tr_b16 v[32:33], %[a1] offset:8192\n\t" \
    "ds_read_b64_tr_b16 v[34:35], %[a1] offset:9216\n\t" \
    "ds_read_b64_tr_b16 v[36:37], %[a0] offset:8192\n\t" \
    "ds_read_b64_tr_b16 v[38:39], %[a0] offset:9216\n\t" \
    "ds_read_b64_tr_b16 v[40:41], %[a1] offset:0\n\t" \
    "ds_read_b64_tr_b16 v[42:43], %[a1] offset:1024\n\t" \
    "ds_read_b64_tr_b16 v[44:45], %[a0] offset:16384\n\t" \
    "ds_read_b64_tr_b16 v[46:47], %[a0] offset:17408\n\t" \
    "ds_read_b64_tr_b16 v[48:49], %[a1] offset:24576\n\t" \
    "ds_read_b64_tr_b16 v[50:51], %[a1] offset:25600\n\t" \
    "ds_read_b64_tr_b16 v[52:53], %[a0] offset:24576\n\t" \
    "ds_read_b64_tr_b16 v[54:55], %[a0] offset:25600\n\t" \
    "ds_read_b64_tr_b16 v[56:57], %[a1] offset:16384\n\t" \
    "ds_read_b64_tr_b16 v[58:59], %[a1] offset:17408\n\t" \
    "ds_read_b64_tr_b16 v[60:61], %[a0] offset:32768\n\t" \
    "ds_read_b64_tr_b16 v[62:63], %[a0] offset:33792\n\t" \
    "s_waitcnt vmcnt(3) lgkmcnt(0)\n\t" \
    "v_mfma_f32_16x16x32_bf16 v[28:31], v[28:31], v[8:11], 0\n\t" \
    "v_mov_b32_dpp v20, v8 row_half_mirror row_mask:0xf bank_mask:0xf\n\t" \
    "v_mov_b32_dpp v21, v9 row_half_mirror row_mask:0xf bank_mask:0xf\n\t" \
    "v_mfma_f32_16x16x32_bf16 v[32:35], v[32:35], v[8:11], 0\n\t" \
    "v_mov_b32_dpp v22, v10 row_half_mirror row_mask:0xf bank_mask:0xf\n\t" \
    "v_mov_b32_dpp v23, v11 row_half_mirror row_mask:0xf bank_mask:0xf\n\t" \
    "v_max3_u32 %[t0], v8, v9, v10\n\t" \
    "s_waitcnt vmcnt(2)\n\t" \
    "v_mfma_f32_16x16x32_bf16 v[28:31], v[36:39], v[20:23], v[28:31]\n\t" \
    "v_max3_u32 %[t0], %[t0], v11, v12\n\t" \
    "ds_read_b64_tr_b16 v[36:37], %[a1] offset:40960\n\t" \
    "v_mfma_f32_16x16x32_bf16 v[32:35], v[40:43], v[20:23], v[32:35]\n\t" \
    "ds_read_b64_tr_b16 v[38:39], %[a1] offset:41984\n\t" \
    "ds_read_b64_tr_b16 v[40:41], %[a0] offset:40960\n\t" \
    "v_mfma_f32_16x16x32_bf16 v[28:31], v[44:47], v[12:15], v[28:31]\n\t" \
    "v_mov_b32_dpp v20, v12 row_half_mirror row_mask:0xf bank_mask:0xf\n\t" \
    "v_mov_b32_dpp v21, v13 row_half_mirror row_mask:0xf bank_mask:0xf\n\t" \
    "v_mfma_f32_16x16x32_bf16 v[32:35], v[48:51], v[12:15], v[32:35]\n\t" \
    "v_mov_b32_dpp v22, v14 row_half_mirror row_mask:0xf bank_mask:0xf\n\t" \
    "v_mov_b32_dpp v23, v15 row_half_mirror row_mask:0xf bank_mask:0xf\n\t" \
    "ds_read_b64_tr_b16 v[42:43], %[a0] offset:41984\n\t" \
    "ds_read_b64_tr_b16 v[44:45], %[a1] offset:32768\n\t" \
    "v_mfma_f32_16x16x32_bf16 v[28:31], v[52:55], v[20:23], v[28:31]\n\t" \
    "ds_read_b64_tr_b16 v[46:47], %[a1] offset:33792\n\t" \
    "s_waitcnt vmcnt(0)\n\t" \
    "v_max3_u32 %[t0], %[t0], v13, v14\n\t" \
    "v_mfma_f32_16x16x32_bf16 v[32:35], v[56:59], v[20:23], v[32:35]\n\t" \
    "v_max3_u32 v8, v24, v25, v26\n\t" \
    "v_sub_f32 v24, v26, v24\n\t" \
    "v_mfma_f32_16x16x32_bf16 v[28:31], v[60:63], v[16:19], v[28:31]\n\t" \
    "v_mov_b32_dpp v20, v16 row_half_mirror row_mask:0xf bank_mask:0xf\n\t" \
    "v_mov_b32_dpp v21, v17 row_half_mirror row_mask:0xf bank_mask:0xf\n\t" \
    "s_waitcnt lgkmcnt(4)\n\t" \
    "v_mfma_f32_16x16x32_bf16 v[32:35], v[36:39], v[16:19], v[32:35]\n\t" \
    "v_mov_b32_dpp v22, v18 row_half_mirror row_mask:0xf bank_mask:0xf\n\t" \
    "v_mov_b32_dpp v23, v19 row_half_mirror row_mask:0xf bank_mask:0xf\n\t" \
    "v_exp_f32 v24, v24\n\t" \
    "v_max3_u32 %[t0], %[t0], v15, v16\n\t" \
    "s_waitcnt lgkmcnt(0)\n\t" \
    "v_mfma_f32_16x16x32_bf16 v[28:31], v[40:43], v[20:23], v[28:31]\n\t" \
    "v_fmac_f32 v25, v24, v27\n\t" \
    "v_max3_u32 v8, v8, v27, v17\n\t" \
    "v_mfma_f32_16x16x32_bf16 v[32:35], v[44:47], v[20:23], v[32:35]\n\t" \
    "v_rcp_f32 v24, v25\n\t" \
    "v_max3_u32 %[t0], %[t0], v18, v19\n\t" \
    "v_max_u32 v8, v8, %[t0]\n\t" \
    "v_cmp_eq_u32 vcc, -1, v8\n\t" \
    "v_mov_b32 v9, 0xff\n\t" \
    "s_nop 1\n\t" \
    "s_cbranch_vccnz 6f\n\t" \
    "v_mov_b32_dpp v28, v32 quad_perm:[0,1,2,3] row_mask:0xf bank_mask:0xa\n\t" \
    "v_mov_b32_dpp v29, v33 quad_perm:[0,1,2,3] row_mask:0xf bank_mask:0xa\n\t" \
    "v_mov_b32_dpp v30, v34 quad_perm:[0,1,2,3] row_mask:0xf bank_mask:0xa\n\t" \
    "v_mov_b32_dpp v31, v35 quad_perm:[0,1,2,3] row_mask:0xf bank_mask:0xa\n\t" \
    "v_pk_mul_f32 v[28:29], v[28:29], v[24:25] op_sel_hi:[1,0]\n\t" \
    "v_pk_mul_f32 v[30:31], v[30:31], v[24:25] op_sel_hi:[1,0]\n\t" \
    "s_nop 0\n\t" \
    "v_add_f32_dpp v28, v28, v28 row_shl:8 row_mask:0xf bank_mask:0xf\n\t" \
    "v_add_f32_dpp v29, v29, v29 row_shl:8 row_mask:0xf bank_mask:0xf\n\t" \
    "v_add_f32_dpp v30, v30, v30 row_shl:8 row_mask:0xf bank_mask:0xf\n\t" \
    "v_add_f32_dpp v31, v31, v31 row_shl:8 row_mask:0xf bank_mask:0xf\n\t" \
    "s_mov_b32 exec_lo, 0x00ff00ff\n\t" \
    "s_mov_b32 exec_hi, 0x00ff00ff\n\t" \
    "v_bfi_b32 v28, v9, %[tag], v28\n\t" \
    "v_bfi_b32 v29, v9, %[tag], v29\n\t" \
    "v_bfi_b32 v30, v9, %[tag], v30\n\t" \
    "v_bfi_b32 v31, v9, %[tag], v31\n\t" \
    "global_store_dwordx4 %[so], v[28:31], %[ws] sc1\n\t" \
    "s_mov_b64 exec, -1\n\t" \
    "s_branch 2f\n\t" \
    "6:\n\t" \
    "s_mov_b64 exec, -1\n\t" \
    "s_sub_u32 %[st], %[st], 1\n\t" \
    "s_cbranch_scc1 2f\n\t" \
    RETRY_ASM \
    "s_sleep 6\n\t" \
    "s_branch 5b\n\t" \
    "2:\n\t"
#define ASM_GU \
    "s_mov_b32 %[st], 0x10000\n\t" \
    "5:\n\t" \
    "v_xor_b32 v11, 0x80, %[a0]\n\t" \
    "v_mov_b32 v12, 0x3f803f80\n\t" \
    "v_mov_b32 v13, 0x3f803f80\n\t" \
    "v_mov_b32 v14, 0x3f803f80\n\t" \
    "v_mov_b32 v15, 0x3f803f80\n\t" \
    "v_mov_b32 v56, 0x3f803f80\n\t" \
    "v_mov_b32 v57, 0x3f803f80\n\t" \
    "v_mov_b32 v58, 0x3f803f80\n\t" \
    "v_mov_b32 v59, 0x3f803f80\n\t" \
    "s_mov_b32 vcc_hi, 0\n\t" \
    "s_branch 8f\n\t" \
    "9:\n\t" \
    "4:\n\t" \
    "s_memrealtime vcc\n\t" \
    "s_waitcnt lgkmcnt(0)\n\t" \
    "s_sub_i32 vcc_lo, vcc_lo, %[tg]\n\t" \
    "s_cmp_lt_i32 vcc_lo, 0\n\t" \
    "s_cbranch_scc1 4b\n\t" \
    "global_load_dwordx4 v[6:9], %[gs], %[ws] sc1\n\t" \
    "global_load_dword v10, %[g2], %[ws] sc1\n\t" \
    "s_mov_b32 vcc_hi, 1\n\t" \
    "s_waitcnt vmcnt(2)\n\t" \
    "8:\n\t" \
    "ds_read_b64_tr_b16 v[16:17], %[a0] offset:0\n\t" \
    "ds_read_b64_tr_b16 v[18:19], v11 offset:0\n\t" \
    "ds_read_b64_tr_b16 v[36:37], %[a0] offset:20480\n\t" \
    "ds_read_b64_tr_b16 v[38:39], v11 offset:20480\n\t" \
    "ds_read_b64_tr_b16 v[20:21], %[a0] offset:1024\n\t" \
    "ds_read_b64_tr_b16 v[22:23], v11 offset:1024\n\t" \
    "ds_read_b64_tr_b16 v[40:41], %[a0] offset:21504\n\t" \
    "ds_read_b64_tr_b16 v[42:43], v11 offset:21504\n\t" \
    "ds_read_b64_tr_b16 v[24:25], %[a0] offset:2048\n\t" \
    "ds_read_b64_tr_b16 v[26:27], v11 offset:2048\n\t" \
    "ds_read_b64_tr_b16 v[44:45], %[a0] offset:22528\n\t" \
    "ds_read_b64_tr_b16 v[46:47], v11 offset:22528\n\t" \
    "ds_read_b64_tr_b16 v[28:29], %[a0] offset:3072\n\t" \
    "ds_read_b64_tr_b16 v[30:31], v11 offset:3072\n\t" \
    "ds_read_b64_tr_b16 v[48:49], %[a0] offset:23552\n\t" \
    "ds_read_b64_tr_b16 v[50:51], v11 offset:23552\n\t" \
    "ds_read_b64_tr_b16 v[32:33], %[a0] offset:4096\n\t" \
    "ds_read_b64_tr_b16 v[34:35], v11 offset:4096\n\t" \
    "ds_read_b64_tr_b16 v[52:53], %[a0] offset:24576\n\t" \
    "ds_read_b64_tr_b16 v[54:55], v11 offset:24576\n\t" \
    "s_cmp_eq_u32 vcc_hi, 0\n\t" \
    "s_cbranch_scc1 9b\n\t" \
    "s_waitcnt vmcnt(1) lgkmcnt(0)\n\t" \
    "v_mov_b32_dpp v12, v6 quad_perm:[0,1,2,3] row_mask:0xf bank_mask:0x1\n\t" \
    "v_mov_b32_dpp v13, v7 quad_perm:[0,1,2,3] row_mask:0xf bank_mask:0x1\n\t" \
    "v_mov_b32_dpp v14, v8 quad_perm:[0,1,2,3] row_mask:0xf bank_mask:0x1\n\t" \
    "v_mov_b32_dpp v15, v9 quad_perm:[0,1,2,3] row_mask:0xf bank_mask:0x1\n\t" \
    "v_mov_b32_dpp v56, v6 row_shl:4 row_mask:0xf bank_mask:0x1\n\t" \
    "v_mov_b32_dpp v57, v7 row_shl:4 row_mask:0xf bank_mask:0x1\n\t" \
    "v_mfma_f32_16x16x32_bf16 v[16:19], v[16:19], v[12:15], 0\n\t" \
    "v_mov_b32_dpp v58, v8 row_shl:4 row_mask:0xf bank_mask:0x1\n\t" \
    "v_mov_b32_dpp v59, v9 row_shl:4 row_mask:0xf bank_mask:0x1\n\t" \
    "v_mfma_f32_16x16x32_bf16 v[36:39], v[36:39], v[12:15], 0\n\t" \
    "v_max3_u32 v11, v6, v7, v8\n\t" \
    "v_mfma_f32_16x16x32_bf16 v[60:63], v[12:15], v[12:15], 0\n\t" \
    "v_mfma_f32_16x16x32_bf16 v[16:19], v[20:23], v[56:59], v[16:19]\n\t" \
    "v_mov_b32_dpp v12, v6 row_shl:8 row_mask:0xf bank_mask:0x1\n\t" \
    "v_mov_b32_dpp v13, v7 row_shl:8 row_mask:0xf bank_mask:0x1\n\t" \
    "v_mfma_f32_16x16x32_bf16 v[36:39], v[40:43], v[56:59], v[36:39]\n\t" \
    "v_mov_b32_dpp v14, v8 row_shl:8 row_mask:0xf bank_mask:0x1\n\t" \
    "v_mov_b32_dpp v15, v9 row_shl:8 row_mask:0xf bank_mask:0x1\n\t" \
    "v_mfma_f32_16x16x32_bf16 v[60:63], v[56:59], v[56:59], v[60:63]\n\t" \
    "s_nop 0\n\t" \
    "v_mfma_f32_16x16x32_bf16 v[16:19], v[24:27], v[12:15], v[16:19]\n\t" \
    "v_mov_b32_dpp v56, v6 row_shl:12 row_mask:0xf bank_mask:0x1\n\t" \
    "v_mov_b32_dpp v57, v7 row_shl:12 row_mask:0xf bank_mask:0x1\n\t" \
    "v_mfma_f32_16x16x32_bf16 v[36:39], v[44:47], v[12:15], v[36:39]\n\t" \
    "v_mov_b32_dpp v58, v8 row_shl:12 row_mask:0xf bank_mask:0x1\n\t" \
    "v_mov_b32_dpp v59, v9 row_shl:12 row_mask:0xf bank_mask:0x1\n\t" \
    "v_mfma_f32_16x16x32_bf16 v[60:63], v[12:15], v[12:15], v[60:63]\n\t" \
    "s_nop 0\n\t" \
    "v_mfma_f32_16x16x32_bf16 v[16:19], v[28:31], v[56:59], v[16:19]\n\t" \
    "s_waitcnt vmcnt(0)\n\t" \
    "v_mov_b32_dpp v12, v10 quad_perm:[0,1,2,3] row_mask:0xf bank_mask:0x1\n\t" \
    "v_mov_b32_dpp v13, v10 row_shl:4 row_mask:0xf bank_mask:0x1\n\t" \
    "v_mfma_f32_16x16x32_bf16 v[36:39], v[48:51], v[56:59], v[36:39]\n\t" \
    "v_mov_b32_dpp v14, v10 row_shl:8 row_mask:0xf bank_mask:0x1\n\t" \
    "v_mov_b32_dpp v15, v10 row_shl:12 row_mask:0xf bank_mask:0x1\n\t" \
    "v_mfma_f32_16x16x32_bf16 v[60:63], v[56:59], v[56:59], v[60:63]\n\t" \
    "v_max3_u32 v11, v11, v9, v10\n\t" \
    "v_mfma_f32_16x16x32_bf16 v[16:19], v[32:35], v[12:15], v[16:19]\n\t" \
    "v_cmp_eq_u32 vcc, -1, v11\n\t" \
    "v_mfma_f32_16x16x32_bf16 v[36:39], v[52:55], v[12:15], v[36:39]\n\t" \
    "v_mfma_f32_16x16x32_bf16 v[60:63], v[12:15], v[12:15], v[60:63]\n\t" \
    "s_mov_b32 exec_lo, 0x001f001f\n\t" \
    "s_mov_b32 exec_hi, 0x001f001f\n\t" \
    "s_cbranch_vccnz 6f\n\t" \
    "7:\n\t" \
    "s_nop 3\n\t" \
    "ds_write2_b32 %[pw], v16, v36 offset0:0 offset1:1\n\t" \
    "ds_write2_b32 %[pw], v17, v37 offset0:12 offset1:13\n\t" \
    "ds_write2_b32 %[pw], v18, v38 offset0:24 offset1:25\n\t" \
    "ds_write2_b32 %[pw], v19, v39 offset0:36 offset1:37\n\t" \
    "ds_write2_b32 %[gw], v60, v61 offset1:4\n\t" \
    "ds_write2_b32 %[gw], v62, v63 offset0:8 offset1:12\n\t" \
    "s_mov_b64 exec, -1\n\t" \
    "s_waitcnt lgkmcnt(0)\n\t" \
    "s_barrier\n\t" \
    "s_branch 2f\n\t" \
    "6:\n\t" \
    "s_sub_u32 %[st], %[st], 1\n\t" \
    "s_cbranch_scc1 7b\n\t" \
    "s_mov_b64 exec, -1\n\t" \
    RETRY_ASM \
    "s_sleep 1\n\t" \
    "s_branch 5b\n\t" \
    "2:\n\t"
#define ASM_HS \
    "ds_read_b64_tr_b16 v[16:17], %[w0] offset:0\n\t" \
    "ds_read_b64_tr_b16 v[18:19], %[w1] offset:32\n\t" \
    "ds_read_b64_tr_b16 v[20:21], %[w2] offset:64\n\t" \
    "ds_read_b64_tr_b16 v[22:23], %[w3] offset:96\n\t" \
    "ds_read_b64_tr_b16 v[24:25], %[w0] offset:128\n\t" \
    "ds_read_b64_tr_b16 v[26:27], %[w1] offset:160\n\t" \
    "ds_read_b64_tr_b16 v[28:29], %[w2] offset:192\n\t" \
    "ds_read_b64_tr_b16 v[30:31], %[w3] offset:224\n\t" \
    "ds_read_b64_tr_b16 v[32:33], %[w0] offset:256\n\t" \
    "ds_read_b64_tr_b16 v[34:35], %[w1] offset:288\n\t" \
    "v_mov_b32 v36, 0\n\t" \
    "v_mov_b32 v37, 0\n\t" \
    "v_mov_b32 v38, 0\n\t" \
    "v_mov_b32 v39, 0\n\t" \
    "v_mov_b32 v40, 0\n\t" \
    "v_mov_b32 v41, 0\n\t" \
    "v_mov_b32 v42, 0\n\t" \
    "v_mov_b32 v43, 0\n\t" \
    "v_add_u32 v12, 0x40000, %[so]\n\t" \
    "v_add_u32 v13, 0x80000, %[so]\n\t" \
    "s_waitcnt lgkmcnt(0)\n\t" \
    "s_barrier\n\t" \
    "ds_read_b128 v[44:47], %[sx]\n\t" \
    "ds_read_b128 v[48:51], %[sq]\n\t" \
    "ds_read_b128 v[52:55], %[cp]\n\t" \
    "ds_read_b128 v[56:59], %[cp] offset:16\n\t" \
    "ds_read_b128 v[60:63], %[rp]\n\t" \
    "ds_read_b128 v[8:11], %[rp] offset:16\n\t" \
    "s_waitcnt lgkmcnt(5)\n\t" \
    "v_pk_add_f32 v[44:45], v[44:45], v[46:47]\n\t" \
    "s_waitcnt lgkmcnt(4)\n\t" \
    "v_pk_add_f32 v[48:49], v[48:49], v[50:51]\n\t" \
    "v_add_f32 v44, v44, v45\n\t" \
    "v_add_f32 v48, v48, v49\n\t" \
    "v_mul_f32 v44, 0x3acccccd, v44\n\t" \
    "v_mul_f32 v48, 0x3acccccd, v48\n\t" \
    "v_fma_f32 v48, -v44, v44, v48\n\t" \
    "v_max_f32 v48, 0, v48\n\t" \
    "v_cvt_pk_bf16_f32 v44, v44, v48\n\t" \
    "v_and_b32 v48, 0xffff0000, v44\n\t" \
    "v_add_f32 v48, 0x3727c5ac, v48\n\t" \
    "v_cvt_pk_bf16_f32 v48, v48, v48\n\t" \
    "v_lshlrev_b32 v48, 16, v48\n\t" \
    "v_sqrt_f32 v48, v48\n\t" \
    "s_waitcnt lgkmcnt(2)\n\t" \
    "v_pk_add_f32 v[52:53], v[52:53], v[54:55]\n\t" \
    "v_cvt_pk_bf16_f32 v48, v48, v48\n\t" \
    "v_lshlrev_b32 v48, 16, v48\n\t" \
    "v_rcp_f32 v48, v48\n\t" \
    "v_pk_add_f32 v[56:57], v[56:57], v[58:59]\n\t" \
    "v_lshlrev_b32 v44, 16, v44\n\t" \
    "s_waitcnt lgkmcnt(0)\n\t" \
    "v_pk_add_f32 v[60:61], v[60:61], v[62:63]\n\t" \
    "v_pk_add_f32 v[8:9], v[8:9], v[10:11]\n\t" \
    "v_pk_add_f32 v[52:53], v[52:53], v[56:57]\n\t" \
    "v_pk_add_f32 v[60:61], v[60:61], v[8:9]\n\t" \
    "v_pk_fma_f32 v[60:61], v[52:53], v[44:45], v[60:61] op_sel_hi:[1,0,1] neg_lo:[1,0,0] neg_hi:[1,0,0]\n\t" \
    "v_pk_mul_f32 v[60:61], v[60:61], v[48:49] op_sel_hi:[1,0]\n\t" \
    "v_cvt_pk_bf16_f32 v60, v60, v61\n\t" \
    "v_lshlrev_b32 v61, 16, v60\n\t" \
    "v_mul_f32 v62, 0xbfb8aa3b, v61\n\t" \
    "v_exp_f32 v62, v62\n\t" \
    "v_and_b32 v60, 0xffff0000, v60\n\t" \
    "v_add_f32 v62, 1.0, v62\n\t" \
    "v_rcp_f32 v62, v62\n\t" \
    "s_nop 0\n\t" \
    "v_mul_f32 v61, v62, v61\n\t" \
    "v_cvt_pk_bf16_f32 v61, v61, v61\n\t" \
    "v_lshlrev_b32 v61, 16, v61\n\t" \
    "v_mul_f32 v60, v60, v61\n\t" \
    "s_nop 1\n\t" \
    "v_mov_b32_dpp v61, v60 row_ror:12 row_mask:0xf bank_mask:0xf\n\t" \
    "v_cvt_pk_bf16_f32 v60, v60, v61\n\t" \
    "s_nop 1\n\t" \
    "v_mov_b32_dpp v36, v60 quad_perm:[0,1,2,3] row_mask:0xf bank_mask:0x1\n\t" \
    "v_mov_b32_dpp v37, v60 row_ror:8 row_mask:0xf bank_mask:0x1\n\t" \
    "v_mov_b32_dpp v38, v60 quad_perm:[0,1,2,3] row_mask:0xf bank_mask:0x2\n\t" \
    "v_mov_b32_dpp v39, v60 row_ror:8 row_mask:0xf bank_mask:0x2\n\t" \
    "v_mov_b32_dpp v40, v60 quad_perm:[0,1,2,3] row_mask:0xf bank_mask:0x4\n\t" \
    "v_mov_b32_dpp v41, v60 row_ror:8 row_mask:0xf bank_mask:0x4\n\t" \
    "v_mfma_f32_16x16x32_bf16 v[44:47], v[16:19], v[36:39], 0\n\t" \
    "v_mov_b32_dpp v42, v60 quad_perm:[0,1,2,3] row_mask:0xf bank_mask:0x8\n\t" \
    "v_mov_b32_dpp v43, v60 row_ror:8 row_mask:0xf bank_mask:0x8\n\t" \
    "v_mfma_f32_16x16x32_bf16 v[48:51], v[24:27], v[36:39], 0\n\t" \
    "v_mfma_f32_16x16x32_bf16 v[52:55], v[32:35], v[36:39], 0\n\t" \
    "v_mfma_f32_16x16x32_bf16 v[44:47], v[20:23], v[40:43], v[44:47]\n\t" \
    "v_mfma_f32_16x16x32_bf16 v[48:51], v[28:31], v[40:43], v[48:51]\n\t" \
    "s_nop 5\n\t" \
    "v_min_u32 v52, -2, v52\n\t" \
    "v_min_u32 v53, -2, v53\n\t" \
    "v_min_u32 v54, -2, v54\n\t" \
    "v_min_u32 v55, -2, v55\n\t" \
    "v_min_u32 v44, -2, v44\n\t" \
    "v_min_u32 v45, -2, v45\n\t" \
    "v_min_u32 v46, -2, v46\n\t" \
    "v_min_u32 v47, -2, v47\n\t" \
    "global_store_dwordx4 %[so], v[44:47], %[ws] sc1\n\t" \
    "v_min_u32 v48, -2, v48\n\t" \
    "v_min_u32 v49, -2, v49\n\t" \
    "v_min_u32 v50, -2, v50\n\t" \
    "v_min_u32 v51, -2, v51\n\t" \
    "global_store_dwordx4 v12, v[48:51], %[ws] sc1\n\t" \
    "s_mov_b32 exec_lo, 0x00ff00ff\n\t" \
    "s_mov_b32 exec_hi, 0x00ff00ff\n\t" \
    "global_store_dwordx4 v13, v[52:55], %[ws] sc1\n\t" \
    "s_mov_b64 exec, -1\n\t"
#define CLOB_32_59 "v32", "v33", "v34", "v35", "v36", "v37", "v38", "v39", "v40", "v41", "v42", "v43", "v44", "v45", "v46", "v47", "v48", "v49", "v50", "v51", "v52", "v53", "v54", "v55", "v56", "v57", "v58", "v59"
#define CLOB_8_15 "v8", "v9", "v10", "v11", "v12", "v13", "v14", "v15"
#define CLOB_16_59 "v16", "v17", "v18", "v19", "v20", "v21", "v22", "v23", "v24", "v25", "v26", "v27", "v28", "v29", "v30", "v31", "v32", "v33", "v34", "v35", "v36", "v37", "v38", "v39", "v40", "v41", "v42", "v43", "v44", "v45", "v46", "v47", "v48", "v49", "v50", "v51", "v52", "v53", "v54", "v55", "v56", "v57", "v58", "v59"
#define CLOB_16_63 "v16", "v17", "v18", "v19", "v20", "v21", "v22", "v23", "v24", "v25", "v26", "v27", "v28", "v29", "v30", "v31", "v32", "v33", "v34", "v35", "v36", "v37", "v38", "v39", "v40", "v41", "v42", "v43", "v44", "v45", "v46", "v47", "v48", "v49", "v50", "v51", "v52", "v53", "v54", "v55", "v56", "v57", "v58", "v59", "v60", "v61", "v62", "v63"
template <int R, int IMM> __device__ __forceinline__ void ldkv(const void* sbase, unsigned voff) {
    if constexpr ((R < 160 && (POL & 1)) || (R >= 160 && (POL & 2)))
        asm volatile("global_load_dwordx4 v[%c2:%c3], %0, %1 offset:%c4" :: "v"(voff), "s"(sbase), "i"(R), "i"(R + 3), "i"(IMM) : "memory");
    else
        asm volatile("global_load_dwordx4 v[%c2:%c3], %0, %1 offset:%c4 nt" :: "v"(voff), "s"(sbase), "i"(R), "i"(R + 3), "i"(IMM) : "memory");
}
template <int A> __device__ __forceinline__ void fix_k() {
    unsigned t0, t1, t2, t3;
    asm volatile("v_mov_b32 %0, v%c4\n\tv_mov_b32 %1, v%c5\n\tv_mov_b32 %2, v%c6\n\tv_mov_b32 %3, v%c7\n\t"
                 "v_mov_b32_dpp v%c4, v%c8 row_ror:8 row_mask:0xf bank_mask:0xc\n\tv_mov_b32_dpp v%c5, v%c9 row_ror:8 row_mask:0xf bank_mask:0xc\n\t"
                 "v_mov_b32_dpp v%c6, v%c10 row_ror:8 row_mask:0xf bank_mask:0xc\n\tv_mov_b32_dpp v%c7, v%c11 row_ror:8 row_mask:0xf bank_mask:0xc\n\t"
                 "v_mov_b32_dpp v%c8, %0 row_ror:8 row_mask:0xf bank_mask:0x3\n\tv_mov_b32_dpp v%c9, %1 row_ror:8 row_mask:0xf bank_mask:0x3\n\t"
                 "v_mov_b32_dpp v%c10, %2 row_ror:8 row_mask:0xf bank_mask:0x3\n\tv_mov_b32_dpp v%c11, %3 row_ror:8 row_mask:0xf bank_mask:0x3"
                 : "=&v"(t0), "=&v"(t1), "=&v"(t2), "=&v"(t3) : "i"(A), "i"(A + 1), "i"(A + 2), "i"(A + 3), "i"(A + 12), "i"(A + 13), "i"(A + 14), "i"(A + 15));
}
#define PACE 1
#define D7 16
template <int D = DHI> __device__ __forceinline__ void pace(unsigned& due, unsigned step) {
    asm volatile("s_waitcnt vmcnt(%2)\n\ts_add_u32 %0, %0, %1\n1:\n\ts_memrealtime vcc\n\ts_waitcnt lgkmcnt(0)\n\ts_lshl_b32 vcc_lo, vcc_lo, 4\n\ts_sub_i32 vcc_lo, vcc_lo, %0\n\ts_cmp_ge_i32 vcc_lo, 0\n\ts_cbranch_scc0 1b"
                 : "+s"(due) : "s"(step), "i"(D) : "memory", "scc", "vcc");
}
__device__ __forceinline__ unsigned pace_t(unsigned& due, unsigned step) {
    unsigned t;
    asm volatile("s_waitcnt vmcnt(%3)\n\ts_add_u32 %0, %0, %2\n1:\n\ts_memrealtime vcc\n\ts_waitcnt lgkmcnt(0)\n\ts_lshl_b32 %1, vcc_lo, 4\n\ts_sub_i32 vcc_lo, %1, %0\n\ts_cmp_ge_i32 vcc_lo, 0\n\ts_cbranch_scc0 1b"
                 : "+s"(due), "=&s"(t) : "s"(step), "i"(DHI) : "memory", "scc", "vcc");
    return t;
}
#define MID 0
#define MIDN 7
#define JE 0
#define PW 0
#define CPF 0
#define PADN 0
#define KF 0
#define W7F 0
#define EPF_PULL 0
#define FIRSTD 0
#define EPM 248
#define EPN 16
#define PULLM 0
#define KEEPX " sc1"
#define LKEEP 0
#define LPN 4
#define TPN 0
#define OPCL
#define STG 2
#define AB(x) 0
#define OWN 0
#define OWNER(h, sub) ((OWN & 1) ? ((h) >> 4) * 80 + ((0x03142 >> (4 * (sub))) & 7) * 16 + ((h) & 15) : (h) * 5 + (sub))
#define PGM 255
#define PAGE(k) do { if (PGM & (1 << (k))) asm volatile("s_branch 1f\n\t.p2align 12\n1:" ::: "memory"); } while (0)
#define TRCL
#define QSP 0
#define XI 14
#define OPA 1
#define EHW 2
#define GPOL 1
#define QZ 0
#define PRI 3
#define P1ONLY 0
#undef NET
#define NET 0
#define NPS 0
#define NP0 0
#define MP0 24
#define MPS 3
static_assert(!WOMODE || (NET <= 3 && MP0 >= 11 && MP0 + (6 - NET) * MPS < 36), "extras");
template <int I, int HI, bool THR, class F> __device__ __forceinline__ void kv_stream_x(const void* kbase, const void* vbase, unsigned ko, unsigned v0, const unsigned (&to)[4], unsigned& due, unsigned& step, F&& extra) {
    if constexpr (I < HI) {
        if constexpr (NPS > 0 && I >= NP0 && (I - NP0) % (NPS > 0 ? NPS : 1) == 0 && (I - NP0) / (NPS > 0 ? NPS : 1) < 6) extra(100 + (I - NP0) / NPS);
        if constexpr (WOMODE && I >= MP0 && (I - MP0) % MPS == 0 && (I - MP0) / MPS < 7 - NET) { extra((I - MP0) / MPS); pace(due, step); }
        if constexpr (I < 24) ldkv<64 + 4 * I, (I % 3) * 128 + ((I / 3) & 1) * 3072>(kbase, ko + to[I / 6]);
        else ldkv<160 + 4 * (I - 24), ((I - 24) & 3) * 384>(vbase, v0 + to[(I - 24) >> 2]);
        if constexpr (I >= 14 && I < 38 && (I - 14) % 6 < 3) fix_k<64 + 4 * (I - 14)>();
        pace(due, step);
        if constexpr (I == XI || I == XI + 5) extra(300 + I - XI);
        if constexpr (I == P_QP || I == 14 || I == 18 || I == 26 || I == 34) extra(I == P_QP ? 216 : I == 14 ? 220 : I == 18 ? 224 : I == 26 ? 228 : 232);
        kv_stream_x<I + 1, HI, THR>(kbase, vbase, ko, v0, to, due, step, extra);
    }
}
template <int I, int HI, int THR> __device__ __forceinline__ void kv_stream(const void* kbase, const void* vbase, unsigned ko, unsigned v0, const unsigned (&to)[4], unsigned& due, unsigned step) {
    if constexpr (I < HI) {
        if constexpr (I < 24) ldkv<64 + 4 * I, (I % 3) * 128 + ((I / 3) & 1) * 3072>(kbase, ko + to[I / 6]);
        else ldkv<160 + 4 * (I - 24), ((I - 24) & 3) * 384>(vbase, v0 + to[(I - 24) >> 2]);
        if constexpr (THR == 1 && I >= P_BA) pace(due, step);
        if constexpr (THR == 2) pace<D7>(due, step);
        if constexpr (THR == 3) pace(due, step);
        kv_stream<I + 1, HI, THR>(kbase, vbase, ko, v0, to, due, step);
    }
}
#define NMS 22
#define NNE 48
#define DSL 0
#define NME 46
static_assert((NM7 == 0 || NN7 % 6 == 0) && NM7 <= NME - NMS && NN7 <= NNE - F7 && NN7 <= 48 - F7, "slots");
template <int I> struct IC { static constexpr int v = I; };
template <int I, int HI, class F> __device__ __forceinline__ void kv_stream7(const void* kbase, const void* vbase, unsigned ko, unsigned v0, unsigned v1, unsigned v2, const unsigned (&to)[4], unsigned& due, unsigned step, F&& nar) {
    if constexpr (I < HI) {
        if constexpr (I < 24) ldkv<64 + 4 * I, (I % 3) * 128 + ((I / 3) & 1) * 3072>(kbase, ko + to[I / 6]);
        else if constexpr (I < 40) ldkv<160 + 4 * (I - 24), ((I - 24) & 3) * 384>(vbase, v0 + to[(I - 24) >> 2]);
        else ldkv<160 + 4 * (I - 24), (I & 3) * 384>(vbase, I < 44 ? v1 : v2);
        if constexpr (((I - F7) * NN7) % (48 - F7) < NN7) nar(IC<((I - F7) * NN7) / (48 - F7)>{});
        if constexpr (NNE < 48 && I < NNE && ((I - F7) * NN7) % (NNE - F7) < NN7) nar(IC<200 + ((I - F7) * NN7) / (NNE - F7)>{});
        if constexpr (I >= NMS && I < NME && ((I - NMS) * NM7) % (NME - NMS) < NM7) nar(IC<100 + ((I - NMS) * NM7) / (NME - NMS)>{});
        if constexpr (I < 47) pace<D7>(due, step);
        kv_stream7<I + 1, HI>(kbase, vbase, ko, v0, v1, v2, to, due, step, nar);
    }
}
template <int I> __device__ __forceinline__ void fix_all() { if constexpr (I < 12) { fix_k<64 + 4 * ((I / 3) * 6 + I % 3)>(); fix_all<I + 1>(); } }
#define LANES() int lane; asm volatile("v_mbcnt_lo_u32_b32 %0, -1, 0\n\tv_mbcnt_hi_u32_b32 %0, -1, %0" : "=v"(lane)); __builtin_assume(lane >= 0 && lane < 64); \
    const int n = lane & 15, kq = lane >> 4, q4 = n >> 2, p4 = lane & 3; (void)n; (void)kq; (void)q4; (void)p4
template <unsigned C> __device__ __forceinline__ u32x4 splat() { unsigned a, b, c, d; asm volatile("v_mov_b32 %0, %4\n\tv_mov_b32 %1, %4\n\tv_mov_b32 %2, %4\n\tv_mov_b32 %3, %4" : "=v"(a), "=v"(b), "=v"(c), "=v"(d) : "i"(C)); return (u32x4){a, b, c, d}; }
static_assert(TDEPTH <= 8 && DHI <= 8, "fix_k schedule");
__device__ __forceinline__ u32x4 ld16_a(const void* sbase, unsigned voff) {
    u32x4 r; asm volatile("global_load_dwordx4 %0, %1, %2" : "=v"(r) : "v"(voff), "s"(sbase) : "memory"); return r;
}

template <bool KEEP = false> __device__ __forceinline__ void narrow(const u16* wg, const u16* wu, unsigned va, unsigned vb, unsigned lb, int s, int rot) {
    const int s10 = (s * 26) >> 8, r = s - 10 * s10, m = r >= 5, c = r - 5 * m, qt = (s10 + rot) & 3;
    if (KEEP) dma16p((m ? wu : wg) + (qt * 160 + (c & 3) * 8) * 1536, c == 4 ? vb : va, lb + L_GU + (unsigned)(m * 20480 + (qt * 5 + c) * 1024)); else
    dma16((m ? wu : wg) + (qt * 160 + (c & 3) * 8) * 1536, c == 4 ? vb : va, lb + L_GU + (unsigned)(m * 20480 + (qt * 5 + c) * 1024));
}
#define NROT 0
#define ROTS(s) (NROT ? ((s) + 7 * (mi & 3)) % (int)SCH(S_N1) : (s))
#define OSX ""
#define LATED 0
#define LDSL 2
template <int P> __device__ __forceinline__ void wo_piece(const u16* wob, unsigned oa, unsigned lb) {
    constexpr int j = P / 6, i = P % 6;
    dma16(wob + (i * 32 + (j & 1) * 16 + ((j >> 1) & 1) * 4 + (j >> 2) * 2) * DM, (j & 2) ? oa ^ 128u : oa, lb + L_WO + (j + 8 * i) * 1024);
}
template <int P, int HI, int SP = WSP> __device__ __forceinline__ void wo_rest(const u16* wob, unsigned oa, unsigned ob, unsigned lb) {
    if constexpr (P < HI) {
        constexpr int j = P / 6, i = P % 6; unsigned t;
        asm volatile("s_add_u32 m0, %3, %4\n\tv_add_u32 %0, %5, %1\n\tglobal_load_lds_dwordx4 %0, %2" WPOL
                     : "=&v"(t) : "v"((j & 2) ? ob : oa), "s"(wob), "s"(lb), "i"(L_WO + (j + 8 * i) * 1024), "i"((i * 32 + (j & 1) * 16 + ((j >> 1) & 1) * 4 + (j >> 2) * 2) * DM * 2) : "memory", "scc");
        if (SP > 0 && P < 47) __builtin_amdgcn_s_sleep(SP);
        if (SP < 0 && (P & 1) && P < 47) __builtin_amdgcn_s_sleep(-SP);
        wo_rest<P + 1, HI, SP>(wob, oa, ob, lb);
    }
}
#define WDP 8
template <int P, int HI> __device__ __forceinline__ void wo_paced(const u16* wob, unsigned oa, unsigned ob, unsigned lb, unsigned& due, unsigned step) {
    if constexpr (P < HI) { pace<WDP>(due, step); wo_rest<P, P + 1, 0>(wob, oa, ob, lb); wo_paced<P + 1, HI>(wob, oa, ob, lb, due, step); }
}
#define M0_KEEP() unsigned m0_; asm volatile("s_mov_b32 %0, m0" : "=s"(m0_) :: "memory")
#define M0_BACK() asm volatile("s_mov_b32 m0, %0" :: "s"(m0_) : "memory")
#define WO_ADDR() const u16* wob = wo + h * DH * DM + sub * 128; const unsigned woa = (unsigned)(((kq >> 1) * 8 + (kq & 1)) * DM * 2 + ((n >> 1) ^ kq) * 32 + (n & 1) * 16)
#define NARROW_ADDR() const int r5_ = swz(lane >> 1); const unsigned nva = (unsigned)(((r5_ & 7) + ((r5_ >> 3) << 5)) * 3072 + mi * 32 + (lane & 1) * 16), \
        nvb = (unsigned)((128 + r5_) * 3072 + mi * 32 + (lane & 1) * 16); const int nrot = 0

#define SLOWL(j) (((((unsigned)(size_t)wg >> 7) + (unsigned)(j)) & 7u) == 3u)
#define N1W ((int)(SLOWL(mi >> 2) ? SCH(S_N0) : SCH(S_N1)))
#define GUPF(rot) do { const int nd = (int)SCH(S_ND); int sh = (rr >> 3) * 5 + sub + (int)(rot); if (sh >= 20) sh -= 20; \
        _Pragma("unroll 1") for (int k_ = 0; k_ < nd; k_ += 21) { const int rw = k_ + lane / 3; \
        const unsigned o = (unsigned)((640 - 20 * nd + sh * nd + (rw < nd ? rw : nd - 1)) * 3072 + xg * 384 + (lane % 3) * 128); \
        if (GPOL) { pull(wg, o, lb + L_PF); pull(wu, o, lb + L_PF); } else { dma4(wg, o, lb + L_PF); dma4(wu, o, lb + L_PF); } } } while (0)
#define EXTRA(k) do { LANES(); \
        if (!is_mlp) { \
            const int j = (k) < 6 ? wave : 7, i = (k) < 6 ? (k) : wave < 5 ? wave : 5, pp = j * 4 + kq, f = (pp & 3) | (((pp >> 3) & 1) << 2); \
            const int dim = ((pp >> 4) & 1) * 16 + (pp & 1) * 8 + ((pp >> 1) & 7); \
            dma16(wo + (h * DH + i * 32) * DM + sub * 128, (unsigned)(dim * DM * 2 + ((n >> 1) ^ f) * 32 + (n & 1) * 16), lb + L_WO + (j + 8 * i) * 1024); \
        } else if ((k) < 4) { \
            const int s = (int)SCH(S_N0) + wave + 7 * (k); \
            if (s < (int)SCH(S_N1)) { NARROW_ADDR(); narrow(wg, wu, nva, nvb, lb, s, nrot); } \
        } else { \
            const int ii = wave + 7 * ((k) - 4), c = ii * 64 + lane; int row = c / 82, cc = c - 82 * row; if (cc > 79) cc = 79; if (row > 15) row = 15; \
            dma16(wd + mi * 16 * DM, (unsigned)((row * DM + cc * 8) * 2), lb + L_WD + ii * 1024); \
        } } while (0)

__global__ void __launch_bounds__(512, 2) __attribute__((amdgpu_num_vgpr(32), aligned(KAL)))
mega_kernel(const u16* __restrict__ kc, const u16* __restrict__ vc, unsigned char* __restrict__ ws, const u16* __restrict__ x0, const u16* __restrict__ wq,
            const u16* __restrict__ wg, const u16* __restrict__ wu, const u16* __restrict__ wo, const u16* __restrict__ wr,
            const u16* __restrict__ wd, u16* __restrict__ out
            ) {
    extern __shared__ __attribute__((aligned(16))) unsigned char smem[];
    constexpr unsigned sched_c[S_N] = SCHED; (void)sched_c;
    const unsigned lb = (unsigned)(size_t)smem;
#define STAMP0() do {} while (0)
    unsigned long long st64 = 0; (void)st64;
    const int wave0 = __builtin_amdgcn_readfirstlane(threadIdx.x >> 6);
    {
        unsigned long long kp, pc; unsigned s0, s1, s2, a0, a1;
        asm volatile("s_load_dwordx2 %0, %7, 0x0\n\ts_getpc_b64 %1\nmega_pf_here:\n\ts_lshr_b32 %2, %8, 3\n\tv_lshl_add_u32 %5, %9, 5, %2\n\tv_lshlrev_b32 %5, 7, %5\n\t"
                     "v_min_u32 %5, mega_pf_end - mega_pf_here - 4, %5\n\tglobal_load_dword v255, %5, %1\n\t"
                     "s_and_b32 %3, %8, 48\n\ts_and_b32 %4, %8, 7\n\ts_lshl1_add_u32 %3, %4, %3\n\ts_and_b32 %4, %2, 25\n\ts_lshl4_add_u32 %3, %3, %4\n\ts_add_u32 %3, %3, 6\n\ts_lshl_b32 %3, %3, 5\n\t"
                     "v_lshrrev_b32 %5, 6, %9\n\tv_add_u32 %5, %2, %5\n\tv_and_or_b32 %5, %5, 31, %3\n\tv_and_b32 %6, 7, %9\n\tv_lshl_add_u32 %5, %5, 4, %6\n\tv_mul_u32_u24 %5, 0x180, %5\n\t"
                     "v_and_or_b32 %5, %9, 48, %5\n\tv_lshlrev_b32 %6, 3, %9\n\tv_and_or_b32 %5, %6, 64, %5\n\ts_waitcnt lgkmcnt(0)\n\t"
                     "global_load_dwordx4 v[64:67], %5, %0 nt\n\tglobal_load_dwordx4 v[68:71], %5, %0 offset:128 nt\n\tglobal_load_dwordx4 v[72:75], %5, %0 offset:256 nt\n\tglobal_load_dwordx4 v[76:79], %5, %0 offset:3072 nt\n\t"
                     "s_memrealtime %1\n\ts_waitcnt lgkmcnt(0)"
                     : "=&s"(kp), "=&s"(pc), "=&s"(s0), "=&s"(s1), "=&s"(s2), "=&v"(a0), "=&v"(a1) : "s"(__builtin_amdgcn_kernarg_segment_ptr()), "s"(blockIdx.x), "v"(threadIdx.x) : "memory", "scc");
        st64 = pc;
    }
    const u32x4 ones = {~0u, ~0u, ~0u, ~0u};
    const f32x4 z4 = {0.f, 0.f, 0.f, 0.f};
    asm volatile("" ::: "v255");
#define WSOFF 0

#define DMARK(k) do {} while (0)
    unsigned to[4];
#pragma unroll
    for (int t = 0; t < 4; ++t) to[t] = ((t * 8 + wave0 + (GID >> 3)) & 31u) * 6144u;
    unsigned step = wave0 == 7 ? SCH(S_P7) : ((GID >> 3) & 7) >= 5 ? SCH(S_PM) : SCH(S_PD);
    unsigned t_start, due, t0, pol = 0u, tag = 0u;
#define START(off) do { t0 = t_start = (unsigned)st64; due = ((t_start + (off)) << 4) + ((((wave0 + STG) & 7) * step) >> 3); STAMPV(10, t_start); STAMP0(); } while (0)
    {
        const int wave = wave0, lane = threadIdx.x & 63;
        const int g = GID, xg = g & 7, rr = g >> 3;
        const int h = xg * 4 + (rr >> 3), sub = rr & 7, b = sub >> 1, half = sub & 1;
        const bool is_mlp = sub >= 5; const int mi = h * 3 + sub - 5, oi = OWNER(h, sub);
        const int n = lane & 15, kq = lane >> 4, q4 = n >> 2, p4 = lane & 3;
        const u16* kbase = kc + ((size_t)(b * HQ + h) * SK + (SK - WIN) + half * 512) * DH;
        const u16* vbase = vc + ((size_t)(b * HQ + h) * SK + (SK - WIN) + half * 512) * DH;
        const unsigned ko = (unsigned)((lane & 7) * 384 + (((lane >> 3) & 1) * 4 + (lane >> 4)) * 16);
        const unsigned v0 = (unsigned)(4 * kq * 384 + n * 16);
        const unsigned v1 = (unsigned)(4 * kq * 384 + 256 + (n & 7) * 16) + (n >= 8 ? to[2] : to[0]), v2 = (unsigned)(4 * kq * 384 + 256 + (n & 7) * 16) + (n >= 8 ? to[3] : to[1]);
        auto lds_zero_nb = [&]() {
            if (threadIdx.x < 16) *reinterpret_cast<unsigned*>(smem + L_CNT + threadIdx.x * 4) = 0u;
            *reinterpret_cast<f32x4*>(smem + L_P + threadIdx.x * 16) = z4;
            if (threadIdx.x < 32) *reinterpret_cast<f32x4*>(smem + L_P + 8192 + threadIdx.x * 16) = z4;
        };
        auto lds_zero = [&]() { lds_zero_nb(); BAR(); };
        auto lds_inc = [&](int idx) {
            asm volatile("s_waitcnt lgkmcnt(0)" ::: "memory");
            if (lane == 0) __hip_atomic_fetch_add(reinterpret_cast<unsigned*>(smem + L_CNT) + idx, 1u, __ATOMIC_RELAXED, __HIP_MEMORY_SCOPE_WORKGROUP);
        };
        const unsigned po = WS_PR + (unsigned)((mi & 1) * LB + ((mi >> 1) & 1) * 8192 + (mi >> 1) * 128);
        auto clean = [&]() {
            if (is_mlp) {
#pragma unroll
                for (int i = 0; i < 3; ++i) {
                    const int c = mi * 3 + i;
                    const int lim = c >= 256 ? (AB(64) ? 0 : 64) : c >= 64 && c < 192 ? ((c & 3) == 3 ? MLS / 2 : AB(4) ? 0 : 64) : (c & 7) >= 6 ? 40 : c < 64 || AB(32) ? (AB(2) ? 0 : 64) : 0;
                    if (lane < lim) st16_e(ws, WS_XA + (unsigned)(c * 1024 + lane * 16), ones);
                }
                if (lane < 16 && !AB(8)) st16_sc1(ws, (po ^ (unsigned)((lane & 8) << 10)) + (unsigned)((lane & 7) * 16), ones);
            }
        };
        auto first7 = [&]() {
            if (CA == 0) clean();
            if (EPF != 3) kv_stream<0, F7, 0>(kbase, vbase, ko, v0, to, due, step);
            START(SCH(S_O7));
            lds_zero_nb();
            __builtin_amdgcn_s_setprio(3);
            {
                unsigned d;
                asm volatile("s_waitcnt lgkmcnt(0)\n\ts_load_dword %0, %2, %3\n\ts_load_dword %1, %2, %4\n\ts_barrier\n\ts_waitcnt lgkmcnt(0)"
                             : "=&s"(tag), "=&s"(d) : "s"(ws), "s"(WS_EP), "s"(is_mlp ? (po | (2 * GP)) & ~16383u : WS_EP) : "memory");
                tag += 0x01010101u;
            }
            STAMP(24);
        };
        int w7 = wave;
        { if (wave == 7) first7(); asm volatile("" : "+s"(w7)); }
        if (w7 != 7) {
            const unsigned o2 = (unsigned)((wave & 3) * 1280 + lane * 16);
            u32x4 xa_ = ones, xc_ = ones;
            START(is_mlp ? SCH(S_MO) : SCH(S_PO));
            if (FIRSTD) { VMW(3); STAMP(36); VMW(0); STAMP(37); }
            lds_zero();
            if (!is_mlp) {
                xa_ = ld16_a(x0, o2); xc_ = ld16_a(x0, lane < 16 ? o2 + 1024 : o2);
#pragma unroll
                for (int i = 0; i < 7; ++i) {
                    int ii = wave + 7 * i; if (ii > 47) ii = 47;
                    const int c = ii * 64 + lane, row = c / 24, u = c - 24 * row;
                    const int f = ((row >> 1) & 1) | (((row >> 3) & 1) << 1);
                    unsigned o = (unsigned)(((sub * 128 + kpos(row)) * QD + h * DH) * 2 + ((u >> 1) ^ f) * 32 + (u & 1) * 16);
                    asm volatile("" : "+v"(o));
                    if (i < 2) pace<DHI + 2>(due, step); else pace(due, step);
                    dma16(wq, o, lb + L_WQ + ii * 1024);
                }
            }
            pace(due, step);
            kv_stream<4, P_LN, 3>(kbase, vbase, ko, v0, to, due, step);
            if (!is_mlp) {
                asm volatile("s_waitcnt vmcnt(%2)" : "+v"(xa_), "+v"(xc_) : "i"(P_LN - (EPF == 3 ? 4 : 0)) : "memory");
                if (wave >= 3) {
                    float mb, rden; row_stats(xa_, xc_, lane, mb, rden);
                    const f32x2 mb2 = {mb, mb}, rd2 = {rden, rden};
#pragma unroll
                    for (int j = 0; j < 4; ++j) { xa_[j] = pk2v(unpk(pk2v(unpk(xa_[j]) - mb2)) * rd2); xc_[j] = pk2v(unpk(pk2v(unpk(xc_[j]) - mb2)) * rd2); }
                    *reinterpret_cast<u32x4*>(smem + L_OP + (wave & 3) * 1344 + lane * 16) = xa_;
                    if (lane < 16) *reinterpret_cast<u32x4*>(smem + L_OP + (wave & 3) * 1344 + 1024 + lane * 16) = xc_;
                }
            }
            lds_inc(1);
            kv_stream<P_LN, P_QP, 1>(kbase, vbase, ko, v0, to, due, step);
            if (!is_mlp && wave < 6) {
                lds_wait_ge(lb + L_CNT + 4, 7u);
                STAMP(1);
                const unsigned a0 = lb + L_WQ + (unsigned)((8 * kq + q4) * 384 + ((2 * wave) ^ ((q4 >> 1) | ((kq & 1) << 1))) * 32 + p4 * 8), a1 = a0 ^ 32u;
                const unsigned char* xp = smem + L_OP + ((n >> 1) & 3) * 1344 + sub * 256 + kq * 64;
                f32x4 c0 = z4, c1 = z4;
#pragma unroll
                for (int s = 0; s < 4; ++s) {
                    const u32x4 bf = n >= 8 ? (u32x4){0x3f803f80u, 0x3f803f80u, 0x3f803f80u, 0x3f803f80u} : *reinterpret_cast<const u32x4*>(xp + s * 16);
                    c0 = mfma32w(cat8(tr64(a0 + s * 12288), tr64(a0 + s * 12288 + 1536)), bf, c0);
                    c1 = mfma32w(cat8(tr64(a1 + s * 12288), tr64(a1 + s * 12288 + 1536)), bf, c1);
                }
                if (n & 1) c0 = c1;
                if (n < 10) *reinterpret_cast<f32x4*>(smem + (n < 8 ? L_OP + 6144 + (n >> 1) * 768 : L_CS + 768) + wave * 128 + kq * 32 + (n & 1) * 16) = c0;
                lds_inc(3);
            }
            kv_stream_x<P_QP, 36, true>(kbase, vbase, ko, v0, to, due, step, [&](int k) {
                if (k >= 300) { if ((k == 305) == is_mlp) { } } else
                if (k >= 200) STAMPV(56 + (k - 216) / 4, now32() - (due >> 4) + t_start + 100u - ((now32() - (due >> 4) + 0x8000000u) & 0xf0000000u)); });
        } else {
            const unsigned t8 = tag & 0xffu;
            WO_ADDR();
            NARROW_ADDR(); (void)nva; (void)nvb; (void)nrot;
            const int ml_ = mi - 12 * xg, l3_ = lane / 3, ne_ = (int)SCH(S_N0), r0e_ = 640 - 6 * LPN * (int)SCH(S_ND);
            const unsigned pe_ = (unsigned)((l3_ < ne_ ? l3_ : 0) * 3072 + xg * 384 + (lane - 3 * l3_) * 128);
            auto nar = [&](auto ic) {
                constexpr int k = decltype(ic)::v;
                auto rows = [&](int j) { const int row = (j * 6 + (ml_ >> 1)) * ne_; if (ne_ && row < r0e_) pull(((ml_ & 1) ? wu : wg) + (row < 640 - ne_ ? row : 640 - ne_) * 1536, pe_, lb + L_PF); };
                if constexpr (k >= 200) { if (is_mlp) rows(k - 200); }
                else if constexpr (k >= 100) { if (is_mlp) narrow<true>(wg, wu, nva, nvb, lb, k - 100, 0); }
                else if (is_mlp) { if (NNE == 48) rows(k); }
                else wo_piece<k>(wob, woa, lb); };
            auto qstore = [&]() {
                if (!is_mlp) {
                    lds_wait_ge(lb + L_CNT + 12, 6u);
                    f32x4 v[3];
#pragma unroll
                    for (int t = 0; t < 3; ++t) v[t] = *reinterpret_cast<const f32x4*>(smem + L_OP + 6144 + lane * 48 + t * 16);
                    unsigned am = 14u << 23;
#pragma unroll
                    for (int i = 0; i < 12; ++i) am = umax(am, __float_as_uint(v[i >> 2][i & 3]) & 0x7fffffffu);
                    am >>= 23;
                    const float sc = __uint_as_float((268u - am) << 23);
                    unsigned u[12], w[6];
#pragma unroll
                    for (int i = 0; i < 12; ++i) u[i] = __float_as_uint(__builtin_amdgcn_fmed3f(__builtin_fmaf(v[i >> 2][i & 3], sc, 12615680.f), 12582912.f, 12648447.f));
#pragma unroll
                    for (int i = 0; i < 6; ++i) w[i] = (u[2 * i] << 16) | (u[2 * i + 1] & 0xfff0u) | (i < 4 ? (((am >> (2 * i)) & 3u) << 2) | (t8 & 3u) : t8 & 15u);
                    unsigned lo = t8;
#pragma unroll
                    for (int i = 0; i < 6; ++i) lo |= (u[2 * i + 1] & 15u) << (8 + 4 * i);
                    const u32x4 d = {w[0], w[1], w[2], w[3]}; const u32x3 e = {w[4], w[5], lo};
                    const unsigned o = WS_H1A + (unsigned)((h * 5 + sub) * 1792);
                    if (!AB(16)) {
                        st16_x(ws, o + (unsigned)(lane * 16), d);
                        asm volatile("global_store_dwordx3 %0, %1, %2 offset:1024 " XSX "\n\ts_nop 1" :: "v"(o + (unsigned)(lane * 12)), "v"(e), "s"(ws) : "memory");
                    }
                }
                STAMP(17);
            };
            if (EPF == 3) pace<D7>(due, step);
            kv_stream7<F7, (CA > F7 ? CA : F7)>(kbase, vbase, ko, v0, v1, v2, to, due, step, nar);
            if (CA) clean();
            kv_stream7<(CA > F7 ? CA : F7), A7>(kbase, vbase, ko, v0, v1, v2, to, due, step, nar);
            if (A7 >= Q7) qstore();
            if (is_mlp) due -= 4 * step;
            kv_stream7<A7, A7 + 4>(kbase, vbase, ko, v0, v1, v2, to, due, step, nar);
            if (is_mlp) {
                unsigned long long c0, c1, c2; unsigned d;
                due += 4 * step;
                VMW(A7 + 4 - CA);
                asm volatile(
                             "s_memtime %0\n\ts_load_dword %3, %4, %5\n\ts_waitcnt lgkmcnt(0)\n\t"
                             "s_memtime %1\n\ts_load_dword %3, %4, %6\n\ts_waitcnt lgkmcnt(0)\n\t"
                             "s_memtime %2\n\ts_waitcnt lgkmcnt(0)"
                             : "=&s"(c0), "=&s"(c1), "=&s"(c2), "=&s"(d) : "s"(ws), "s"(AB(8) ? WS_EP : po), "s"(AB(8) ? WS_EP : po ^ 8192u) : "memory");
                RETRY(48, (unsigned)c1 - (unsigned)c0); RETRY(49, (unsigned)c2 - (unsigned)c1);
                const unsigned arv = (t_start & ~1u) | ((((int)((unsigned)c2 + (unsigned)c0 - 2u * (unsigned)c1) > 0 ? 0u : 1u) ^ (unsigned)((mi >> 1) ^ (xg >> 2))) & 1u);
                if (lane == 0) st8_sc1(ws, WS_AR + (unsigned)((mi >> 2) * 128 + (mi & 3) * 8), (u32x2){tag, arv});
            }
            STAMP(16);
            if (A7 < Q7) { kv_stream7<A7 + 4, Q7>(kbase, vbase, ko, v0, v1, v2, to, due, step, nar); qstore(); }
            kv_stream7<(A7 < Q7 ? Q7 : A7 + 4), L7>(kbase, vbase, ko, v0, v1, v2, to, due, step, nar);
            u32x4 t = ones;
            const unsigned oa = WS_AR + (unsigned)((lane < 48 ? lane >> 1 : 0) * 128 + (lane & 1) * 16);
            if (g == LEADER) t = ld16_sc1(ws, oa);
            kv_stream7<L7, L7 + 4>(kbase, vbase, ko, v0, v1, v2, to, due, step, nar);
            if (g == LEADER) {
                asm volatile("s_waitcnt vmcnt(4)" : "+v"(t) :: "memory");
#pragma unroll 1
                for (unsigned spins = 0; spins < (kSpinCap << 4); ++spins) {
                    if (__builtin_amdgcn_ballot_w64(t[0] != tag || t[2] != tag) == 0ull) break;
                    RETRY(3, spins + 1);
                    __builtin_amdgcn_s_sleep(8);
                    t = ld16_sc1(ws, oa);
                    asm volatile("s_waitcnt vmcnt(0)" : "+v"(t) :: "memory");
                }
                t0 = t_start + (unsigned)wave_imax(imax((int)(t[1] - t_start), (int)(t[3] - t_start)));
                const int v0_ = __builtin_popcountll(__builtin_amdgcn_ballot_w64((t[1] & 1u) != 0u) & 0xffffffffffffull), v1_ = __builtin_popcountll(__builtin_amdgcn_ballot_w64((t[3] & 1u) != 0u) & 0xffffffffffffull);
                RETRY(50, v0_ + 100); RETRY(51, v1_ + 100);
                t0 = (t0 & ~7u) | (v0_ > 24 ? 2u : 0u) | (v1_ > 24 ? 4u : 0u);
                if (SCH(S_FL) & 256) { RETRY(5, (t0 ^ (SCH(S_FL) >> 8)) & 2u); RETRY(6, (t0 ^ (SCH(S_FL) >> 8)) & 4u); t0 = (t0 & ~7u) | ((SCH(S_FL) >> 8) & 6u); }
                if (lane < 8) st8_sc1(ws, WS_GO + (unsigned)(lane * 128), (u32x2){tag, t0});
                STAMP(29);
            }
#define LDQ() do { a4 = ld16_xo<0>(ws, o4); a1 = ld12_xo(ws, o1); b4 = ld16_xo<0>(ws, p4_); b1 = ld12_xo(ws, p1); } while (0)
#define WTQ(n) asm volatile("s_waitcnt vmcnt(%4)" : "+v"(a4), "+v"(a1), "+v"(b4), "+v"(b1) : "i"(n) : "memory")
            kv_stream7<L7 + 4, QG>(kbase, vbase, ko, v0, v1, v2, to, due, step, nar);
            {
                u32x2 r = ld8_sc1o<0>(ws, WS_GO + (unsigned)(xg * 128));
                const unsigned ob = WS_H1A + (unsigned)(h * 8960), o4 = ob + (unsigned)(kq * 1792 + (b * 16 + n) * 16), o1 = ob + (unsigned)(kq * 1792 + 1024 + (b * 16 + n) * 12);
                const unsigned p4_ = ob + (unsigned)(7168 + (b * 16 + n) * 16), p1 = ob + (unsigned)(8192 + (b * 16 + n) * 12);
                u32x4 a4, b4; u32x3 a1, b1;
                LDQ();
                kv_stream7<QG, QG + QW>(kbase, vbase, ko, v0, v1, v2, to, due, step, nar);
                WTQ(QW);
#pragma unroll 1
                for (unsigned spins = 0; spins < (kSpinCap << 4); ++spins) {
                    if (tagok((or4((a4 ^ t8) | (b4 ^ t8)) & 3u) | (((a1[0] ^ t8) | (a1[1] ^ t8) | (b1[0] ^ t8) | (b1[1] ^ t8)) & 15u) | (a1[2] ^ t8) | (b1[2] ^ t8)) || AB(16)) break;
                    RETRY(1, spins + 1);
                    __builtin_amdgcn_s_sleep(8);
                    LDQ();
                    WTQ(0);
                }
                float q[12];
                auto dec = [&](u32x4 d, u32x3 e, bool first) {
                    const unsigned x = ((d[0] >> 2) & 3u) | (d[1] & 12u) | ((d[2] & 12u) << 2) | ((d[3] & 12u) << 4);
                    float sc = __uint_as_float((x - 14u) << 23);
                    if (!first && kq) sc = 0.f;
                    const float of = -32768.f * sc;
#pragma unroll
                    for (int i = 0; i < 12; ++i) {
                        const unsigned w = i < 8 ? d[i >> 1] : e[(i >> 1) - 4];
                        const float y = __builtin_fmaf((float)((i & 1) ? (w & 0xfff0u) | ((e[2] >> (8 + 4 * (i >> 1))) & 15u) : w >> 16), sc, of);
                        q[i] = first ? y : q[i] + y;
                    }
                };
                dec(a4, a1, true); dec(b4, b1, false);
#pragma unroll
                for (int t = 0; t < 3; ++t) {
                    const float z = sw16_add(sw32_add(q[4 * t], q[4 * t + 2]), sw32_add(q[4 * t + 1], q[4 * t + 3]));
                    const int c = 3 * n + t;
                    *reinterpret_cast<u16*>(smem + L_Q + ((c >> 3) & 1) * 192 + (c >> 4) * 64 + (c & 1) * 32 + ((c >> 1) & 3) * 4 + (kq >> 1) * 16 + (kq & 1) * 2) = (u16)pk2(z, z);
                }
                STAMP(30);
                asm volatile("" : "+v"(r));
#pragma unroll 1
                for (unsigned spins = 0; spins < (kSpinCap << 4); ++spins) {
                    t0 = __builtin_amdgcn_readfirstlane(r[1]);
                    if (__builtin_amdgcn_readfirstlane(r[0]) == tag) break;
                    RETRY(2, spins + 1);
                    __builtin_amdgcn_s_sleep(8);
                    r = ld8_sc1o<0>(ws, WS_GO + (unsigned)(xg * 128));
                    asm volatile("s_waitcnt vmcnt(0)" : "+v"(r) :: "memory");
                }
            }
            pol = t0;
            asm volatile("ds_write2_b32 %0, %1, %2 offset1:3\n\tds_write2_b32 %0, %3, %2 offset0:1 offset1:5" :: "v"(lb + L_POL), "v"(pol), "v"(0u), "v"(tag) : "memory");
            if (t0 - t_start + 8u > 500u || (SCH(S_FL) & 4096)) t0 = t_start;
            asm volatile("ds_write_b32 %0, %1" :: "v"(lb + L_CNT + 36), "v"(t0 | 1u) : "memory");
            lds_inc(0);
            STAMP(2);
            kv_stream7<QG + QW, 48>(kbase, vbase, ko, v0, v1, v2, to, due, step, nar);
            if (AB(1024)) {
                unsigned long long pc; unsigned len;
                asm volatile("s_getpc_b64 %0\nmega_pf_two:\n\ts_mov_b32 %1, mega_pf_end - mega_pf_two - 4" : "=s"(pc), "=s"(len));
                const unsigned o = (unsigned)((lane * 16 + (rr & 15)) * 128);
                dma4((const void*)pc, o < len ? o : len, lb + L_PF);
            }
            if (!is_mlp) {
                dma16p(wr, (unsigned)(lane * 16), lb + L_WR); dma4(wr, (unsigned)(1024 + lane * 4), lb + L_WR + 1024);
                dma4(x0, (unsigned)((((lane >> 1) & 3) * DM + oi * 4) * 2 + (lane & 1) * 4), lb + L_WR + 1280);
                dma4n(out, (unsigned)(xg * 128 + (lane & 1) * 4096), lb + L_PF);
            } else if (!AB(8192)) {
                dma4n(wd + mi * 16 * DM, (unsigned)((lane & 1) * 20476), lb + L_PF);
                if (!AB(16384)) { const unsigned o = (unsigned)(((lane & 7) * 91 + 2) * 3072 + mi * 32); dma4n(wg, o, lb + L_PF); dma4n(wu, o, lb + L_PF); }
            }
            VMW(16);
            fix_all<0>();
            VMW(0);
            STAMP(19);
            __builtin_amdgcn_s_setprio(0);
        }
    }

#define IDS() int g = GID; asm volatile("" : "+s"(g)); int wave = wave0; asm volatile("" : "+s"(wave)); \
        const int xg = g & 7, rr = g >> 3, h = xg * 4 + (rr >> 3), sub = rr & 7, b = sub >> 1; \
        const bool is_mlp = sub >= 5; const int mi = h * 3 + sub - 5, oi = OWNER(h, sub); (void)mi; (void)oi; (void)b
#define ATT_ADDR(layer) \
        unsigned qa = lb + (unsigned)(L_Q + kq * 32 + (lane & 1) * 16), ix = (unsigned)((p4 & 1) * 0xaaaa + p4 * 0x55550000) ^ 0x44444444u; \
        unsigned pw = lb + L_P + (unsigned)(wave * 1088 + ((kq & 1) * 2 + (p4 >> 1)) * 272 + (p4 & 1) * 128 + (q4 >> 1) * 64 + (kq >> 1) * 32 + (q4 & 1) * 8); \
        unsigned pr = lb + L_P + (unsigned)(wave * 1088 + (lane >> 2) * 16); \
        unsigned ow = lb + L_OP + (unsigned)(wave * OPS + lane * 16); \
        unsigned mlw = lb + L_ML + (unsigned)(wave * 4), mo = lb + L_ML + (unsigned)((lane >> 3) * 4), ma = lb + L_ML; \
        unsigned r0 = lb + L_OP + (unsigned)((lane >> 3) * OPS + (wave < 4 ? (wave >> 1) * 128 : 2048) + (wave & 1) * 256 + (lane & 7) * 16), r1 = r0 + (wave < 4 ? 1536u : 640u); \
        unsigned so = (a_h2(h) ^ (layer ? CX(1) : 0u)) + (unsigned)(layer * LB + (wave < 6 ? ((wave >> 1) * 8 + (lane >> 5) * 4 + (wave & 1) * 2 + (sub & 1)) * 128 + b * 32 + (lane & 7) * 4 \
                                                                                  : 3072 + (((sub & 1) ^ (lane & 1)) * 4 + b) * (layer ? MLS1 : MLS) + (lane & 1) * 8)); \
        (void)mlw; (void)ma; (void)r1

    if (PADN) asm volatile("s_branch 1f\n\t.fill %0, 4, 0xbf800000\n1:" :: "i"(PADN) : "memory");
    {
        IDS(); LANES();
        ATT_ADDR(0);
        asm volatile("" : "+v"(qa), "+v"(pw), "+v"(pr), "+v"(ow), "+v"(mlw), "+v"(mo), "+v"(ma), "+v"(r0), "+v"(r1), "+v"(so), "+v"(ix));
        const u16* vbase = vc + ((size_t)(b * HQ + h) * SK + (SK - WIN) + (sub & 1) * 512) * DH;
        unsigned v1 = (unsigned)(4 * kq * 384 + 256 + (n & 7) * 16), v2 = v1 + (n >= 8 ? to[3] : to[1]);
        v1 += n >= 8 ? to[2] : to[0];
        auto tail1 = [&]() { ldkv<224, 0>(vbase, v1); pace(due, step); ldkv<228, 384>(vbase, v1); pace(due, step); ldkv<232, 768>(vbase, v1); pace(due, step); ldkv<236, 1152>(vbase, v1); };
        unsigned dt = 0u;
        auto tail2 = [&]() { if (EHW == 3) dt = pace_t(due, step); else pace(due, step); ldkv<240, 0>(vbase, v2); };
        auto tail3 = [&]() {
            if (EHW == 2) dt = pace_t(due, step); else pace(due, step);
            ldkv<244, 384>(vbase, v2);
            if (EHW == 1) dt = pace_t(due, step); else pace(due, step);
            ldkv<248, 768>(vbase, v2); dt = pace_t(due, step) - dt; ldkv<252, 1152>(vbase, v2);
            asm volatile("s_mov_b64 exec, 1\n\tds_add_u32 %0, %1 offset:336\n\ts_mov_b64 exec, -1" :: "v"(ma), "v"(1u) : "memory");
            STAMP(3);
        };
        if (wave != 7) {
            const unsigned t = lds_ld(lb + L_CNT + 36);
            if (t) due += (t - t_start) << 4;
            step = (step * SCH(S_X3)) >> 8;
            if (wave == 6 && !is_mlp) step -= SCH(S_X6);
        }
        const bool early = wave == 7 || lds_ld(lb + L_CNT) != 0u;
        if (!early) {
            const unsigned v0 = (unsigned)(4 * kq * 384 + n * 16) + to[3];
            ldkv<208, 0>(vbase, v0); pace(due, step); ldkv<212, 384>(vbase, v0); pace(due, step); ldkv<216, 768>(vbase, v0); pace(due, step); ldkv<220, 1152>(vbase, v0); pace(due, step);
            tail1(); tail2(); tail3();
            lds_wait_ge(lb + L_CNT, 1u);
        }
        pol = lds_ld(lb + L_POL); tag = lds_ld(lb + L_POL + 4);
        so ^= CX(0);
        STAMP(4);
        PAGE(0);
        float sa;
        asm volatile(ASM_QK : "=&v"(sa) : "v"(qa), "v"(ix) : "memory", CLOB_32_59);
        const float s = bfr(bfr(sa) * SCALE) * L2E;
        float mt, p; int ms;
        asm volatile("s_nop 1\n\tv_max_f32_dpp %0, %3, %3 quad_perm:[1,0,3,2] row_mask:0xf bank_mask:0xf\n\ts_nop 1\n\t"
                     "v_max_f32_dpp %0, %0, %0 quad_perm:[2,3,0,1] row_mask:0xf bank_mask:0xf\n\ts_nop 1\n\t"
                     "v_max_f32_dpp %0, %0, %0 row_half_mirror row_mask:0xf bank_mask:0xf\n\ts_nop 1\n\t"
                     "v_max_f32_dpp %0, %0, %0 row_mirror row_mask:0xf bank_mask:0xf\n\ts_nop 1\n\t"
                     "v_max_f32_dpp %0, %0, %0 row_bcast:15 row_mask:0xa bank_mask:0xf\n\ts_nop 1\n\t"
                     "v_max_f32_dpp %0, %0, %0 row_bcast:31 row_mask:0xc bank_mask:0xf\n\ts_nop 0\n\t"
                     "v_readlane_b32 %2, %0, 63\n\ts_nop 1\n\t"
                     "v_subrev_f32 %1, %2, %3\n\tv_exp_f32 %1, %1\n\ts_nop 0"
                     : "=&v"(mt), "=&v"(p), "=&s"(ms) : "v"(s));
        u32x4 bm[4];
        {
            unsigned pb;
            asm volatile("v_cvt_pk_bf16_f32 %5, %4, %4\n\tds_write_b16 %6, %5\n\tds_write_b16 %6, %5 offset:20\n\t"
                     "ds_read_b128 %0, %7\n\tds_read_b128 %1, %7 offset:272\n\tds_read_b128 %2, %7 offset:544\n\tds_read_b128 %3, %7 offset:816\n\t"
                     "v_add_f32_dpp %4, %4, %4 quad_perm:[1,0,3,2] row_mask:0xf bank_mask:0xf\n\ts_nop 1\n\t"
                     "v_add_f32_dpp %4, %4, %4 quad_perm:[2,3,0,1] row_mask:0xf bank_mask:0xf\n\ts_nop 1\n\t"
                     "v_add_f32_dpp %4, %4, %4 row_half_mirror row_mask:0xf bank_mask:0xf\n\ts_nop 1\n\t"
                     "v_add_f32_dpp %4, %4, %4 row_mirror row_mask:0xf bank_mask:0xf\n\ts_nop 1\n\t"
                     "v_add_f32_dpp %4, %4, %4 row_bcast:15 row_mask:0xa bank_mask:0xf\n\ts_nop 1\n\t"
                     "v_add_f32_dpp %4, %4, %4 row_bcast:31 row_mask:0xc bank_mask:0xf\n\t"
                     "s_mov_b32 exec_lo, 0\n\ts_brev_b32 exec_hi, 1\n\tds_write2_b32 %9, %8, %4 offset1:8\n\ts_mov_b64 exec, -1\n\ts_waitcnt lgkmcnt(0)"
                     : "=&v"(bm[0]), "=&v"(bm[1]), "=&v"(bm[2]), "=&v"(bm[3]), "+v"(p), "=&v"(pb)
                     : "v"(pw), "v"(pr), "v"(mt), "v"(mlw) : "memory");
        }
#define PVP(name) asm volatile(name : "+v"(da) : "v"(bm[0]), "v"(bm[1]), "v"(bm[2]), "v"(bm[3]), "v"(ow), "v"(ix) : "memory")
#define PVB(name) do { f32x4 da = z4; PVP(name); } while (0)
        int w7 = wave;
        if (wave == 7) {
            PVB(ASM_PVB0); PVB(ASM_PVB1); PVB(ASM_PVB2);
            asm volatile("s_waitcnt lgkmcnt(0)\n\ts_mov_b64 exec, 1\n\tds_add_u32 %0, %1 offset:372\n\tds_add_u32 %0, %1 offset:348\n\ts_mov_b64 exec, -1" :: "v"(ma), "v"(1u) : "memory");
        }
        asm volatile("" : "+s"(w7));
        if (w7 != 7) {
            LANES();
            {
                const unsigned v0 = (unsigned)(4 * kq * 384 + n * 16) + to[3];
                if (early) { ldkv<208, 0>(vbase, v0); pace(due, step); ldkv<212, 384>(vbase, v0); VMW(6); }
                if (early) { pace(due, step); ldkv<216, 768>(vbase, v0); }
                PVB(ASM_PVB0);
                if (early) { pace(due, step); ldkv<220, 1152>(vbase, v0); pace(due, step); tail1(); }
            }
            VMW(4);
            PVB(ASM_PVB1);
#define MEET(off) do { unsigned t_; int c_; \
            asm volatile("s_waitcnt lgkmcnt(0)\n\ts_mov_b64 exec, 1\n\tds_add_u32 %2, %3 offset:" #off "\n\ts_mov_b64 exec, -1\n\ts_mov_b32 %1, 0x400000\n1:\n\tds_read_b32 %0, %2 offset:" #off "\n\ts_sub_u32 %1, %1, 1\n\ts_cbranch_scc1 2f\n\t" \
                         "s_waitcnt lgkmcnt(0)\n\tv_cmp_gt_u32 vcc, 8, %0\n\ts_cbranch_vccnz 1b\n2:" : "=&v"(t_), "=&s"(c_) : "v"(ma), "v"(1u) : "memory", "scc", "vcc"); } while (0)
            asm volatile("s_waitcnt lgkmcnt(0)\n\ts_mov_b64 exec, 1\n\tds_add_u32 %0, %1 offset:372\n\ts_mov_b64 exec, -1" :: "v"(ma), "v"(1u) : "memory");
            f32x4 da = z4; int la;
            {
                unsigned hfa = (a_hf(h) ^ CX(0)) + (unsigned)(lane * 128 + ((sub - lane) & 7) * 4), hb = lds_ld(lb + L_CNT + 36);
                const unsigned tw_ = SCH(S_TW);
                asm volatile("" : "+v"(da), "+v"(hfa), "+v"(hb));
                if (early) { tail2(); tail3(); } else dt = 0u;
                unsigned ar = 1u, mx, dv = ((dt * tw_) >> 8) + (is_mlp ? SCH(S_G1) : 0u) - hb; int th;
                asm volatile("s_waitcnt vmcnt(%4)\n\ts_memrealtime vcc\n\ts_mov_b64 exec, 1\n\ts_waitcnt lgkmcnt(0)\n\tv_add_u32 %1, vcc_lo, %3\n\t"
                             "ds_max_u32 %2, %1 offset:%5\n\tds_add_rtn_u32 %0, %2, %0 offset:376\n\tds_read_b32 %1, %2 offset:%5\n\ts_mov_b64 exec, -1"
                             : "+v"(ar), "=&v"(mx) : "v"(ma), "v"(dv), "i"(EHW), "i"(L_POL + 20 - L_ML) : "memory", "scc", "vcc");
                PVP(ASM_PV20); PVP(ASM_PV21); if (EHW <= 2) PVP(ASM_PV22);
                asm volatile("s_waitcnt lgkmcnt(0)\n\tv_add_u32 %3, %3, %4\n\tv_readfirstlane_b32 %0, %2\n\tv_readfirstlane_b32 %1, %3\n\ts_cmp_lg_u32 %0, 7\n\ts_cbranch_scc1 2f\n\t"
                             "s_mov_b64 exec, 31\n\tv_and_b32 %3, -2, %1\n\tglobal_store_dword %5, %3, %7 sc1\n\tv_or_b32 %2, 1, %1\n\tds_write_b32 %6, %2 offset:%8\n\ts_mov_b64 exec, -1\n2:"
                             : "=&s"(la), "=&s"(th), "+v"(ar), "+v"(mx) : "v"(hb), "v"(hfa), "v"(ma), "s"(ws), "i"(L_POL + 12 - L_ML) : "memory", "scc");
            }
            lds_wait_ge(lb + L_CNT + 52, 8u);
            const f32x4 m0 = *(const LAS f32x4*)(size_t)ma, m1 = *(const LAS f32x4*)(size_t)(ma + 16);
            const float M = vmax3(vmax3(m0[0], m0[1], m0[2]), vmax3(m0[3], m1[0], m1[1]), vmax3(m1[2], m1[3], m1[3]));
            float fj = __builtin_amdgcn_exp2f(*(const LAS float*)(size_t)mo - M);
            auto combine = [&]() {
                const f32x4 a = *(const LAS f32x4*)(size_t)r0, c = *(const LAS f32x4*)(size_t)r1;
                const float z = red3((a + c) * fj);
                auto sw = __builtin_amdgcn_permlane16_swap(__float_as_uint(z), __float_as_uint(z), false, false);
                return enc(pk2(__uint_as_float(sw[0]), __uint_as_float(sw[1])));
            };
            if (wave < 4) st4_m<0x000000ffu, 0x000000ffu>(ws, so, combine());
            else if (wave == 6) {
                float L = *(const LAS float*)(size_t)(mo + 32) * fj;
                L += dpp<0x128>(L); L = sw16_add(L, L); L = sw32_add(L, L);
                asm volatile("s_mov_b64 exec, 3\n\tglobal_store_dwordx2 %0, %1, %2 sc1\n\ts_mov_b64 exec, -1" :: "v"(so), "v"((u32x2){enc(__float_as_uint(M)), enc(__float_as_uint(L))}), "s"(ws) : "memory");
            } else la += 8;
            const unsigned h2 = a_h2(h) ^ CX(0); const int ot = (8 * sub + 2 * wave + (q4 & 1)) * 4 + kq, rw = (q4 & 1) ? 3 - p4 : p4;
            unsigned j0 = lb + L_WO + (unsigned)((8 * kq + q4) * 256 + ((2 * wave) ^ (q4 | ((kq & 1) << 2))) * 32 + p4 * 8), j1 = j0 ^ 32u;
            unsigned j2 = h2 + (unsigned)((lane >> 3) * 128 + rw * 32 + (q4 & 1) * 16), j3 = h2 + (unsigned)(3072 + ((n >> 3) * 4 + rw) * MLS);
            unsigned j4 = (a_own(ot) + (unsigned)(6144 + h * 64 + rw * 16)) ^ ((unsigned)((ot & 15) << 7) | CX(0));
            if (OPA) asm volatile("" : "+v"(fj), "+{v0}"(j0), "+{v1}"(j1), "+{v2}"(j2), "+{v3}"(j3), "+{v4}"(j4)); else asm volatile("" : "+v"(fj));
            STAMP(6);
            asm volatile("s_waitcnt vmcnt(2)\n\ts_cmp_eq_u32 %0, 7\n\ts_cbranch_scc1 1f\n\ts_waitcnt vmcnt(1)\n\ts_sub_u32 %0, %0, 8\n\ts_cmp_gt_u32 %0, 6\n\ts_cbranch_scc1 1f\n\ts_waitcnt vmcnt(0)\n1:" : "+s"(la) :: "memory", "scc");
            if (EHW > 2) PVP(ASM_PV22);
            STAMP(20); DMARK(46);
            asm volatile(ASM_PV23 "s_nop 7\n\tds_write_b128 %5, %0 offset:2048" : "+v"(da) : "v"(bm[0]), "v"(bm[1]), "v"(bm[2]), "v"(bm[3]), "v"(ow), "v"(ix) : "memory");
            if ((wave & 6) == 4) { MEET(348); st4_m<0x000000ffu, 0x000000ffu>(ws, so, combine()); STAMP(5); asm volatile("s_mov_b64 exec, 1\n\tds_add_u32 %0, %1 offset:344\n\ts_mov_b64 exec, -1" :: "v"(ma), "v"(1u) : "memory"); }
            else asm volatile("s_mov_b64 exec, 1\n\tds_add_u32 %0, %1 offset:348\n\ts_mov_b64 exec, -1" :: "v"(ma), "v"(1u) : "memory");
            if (is_mlp && wave < 4) {
                LANES();
                NARROW_ADDR();
                unsigned va = nva + (unsigned)(wave * 160 * 3072), vb = nvb + (unsigned)(wave * 160 * 3072), lw = lb + (unsigned)(L_GU + wave * 5120);
                asm volatile("" : "+v"(va), "+v"(vb), "+s"(lw));
                M0_KEEP();
                lds_wait_ge(lb + L_CNT + 24, 2u);
                lds_wait_ge(lb + L_CNT + 40, 1u);
                STAMP(18);
                if (wave >= (int)(SCH(S_X7) >> 3))
#pragma unroll
                for (int c = 0; c < 5; ++c) {
                    unsigned t;
                    asm volatile("s_add_u32 m0, %4, %5\n\tv_add_u32 %0, %6, %1\n\tglobal_load_lds_dwordx4 %0, %2" WPOL "\n\ts_add_u32 m0, %4, %7\n\ts_nop 0\n\tglobal_load_lds_dwordx4 %0, %3" WPOL
                                 : "=&v"(t) : "v"(c == 4 ? vb : va), "s"(wg), "s"(wu), "s"(lw), "i"(c * 1024), "i"((c & 3) * 8 * 3072), "i"(c * 1024 + 20480) : "memory", "scc");
                }
                M0_BACK();
                asm volatile("s_mov_b64 exec, 1\n\tds_add_u32 %0, %1 offset:364\n\ts_mov_b64 exec, -1" :: "v"(ma), "v"(1u) : "memory");
                STAMP(37);
                if (P1ONLY) { VMW(0); STAMP(27); }
            }
            if (!P1ONLY && !is_mlp && wave < 4) {
                int st, rf, ct; unsigned t0;
                asm volatile(ASM_OP : [st] "=&s"(st), [rf] "=&s"(rf), [ct] "=&s"(ct), [t0] "=&{v7}"(t0) : [a0] "{v0}"(j0), [a1] "{v1}"(j1), [gs] "{v2}"(j2), [gm] "{v3}"(j3), [so] "{v4}"(j4),
                             [ws] "s"(ws), [fl] "s"(lb + L_CNT + 48), [tag] "s"(tag), [dbg] "s"(lb + L_DBG + wave * 512 + 256 + 7 * 4) : "memory", "scc", "vcc", CLOB_8_15, CLOB_16_63 OPCL);
                STAMP(7); DMARK(30);
            }
        }
    }
    {
        IDS();
        if (wave == 7) {
            PAGE(1);
            STAMP(15);
            LANES();
            {
                __builtin_amdgcn_s_setprio(3);
                unsigned th = 0u;
                auto hint = [&]() { lds_wait_ge(lb + L_POL + 12, 1u); th = lds_ld(lb + L_POL + 12); };
                unsigned hfa = (a_hf(h) ^ CX(0)) + (unsigned)(lane * 128 + ((sub - lane) & 7) * 4), hb = lds_ld(lb + L_CNT + 36), ma = lb + L_ML;
                asm volatile("" : "+v"(hfa), "+v"(hb), "+v"(ma));
#define ARRIVE(off, W) do { \
                    unsigned ar = 1u, mx, dv = (off) - hb; int la, th_; \
                    asm volatile("s_memrealtime vcc\n\ts_mov_b64 exec, 1\n\ts_waitcnt lgkmcnt(0)\n\tv_add_u32 %1, vcc_lo, %3\n\t" \
                                 "ds_max_u32 %2, %1 offset:%4\n\tds_add_rtn_u32 %0, %2, %0 offset:376\n\tds_read_b32 %1, %2 offset:%4\n\ts_mov_b64 exec, -1" \
                                 : "+v"(ar), "=&v"(mx) : "v"(ma), "v"(dv), "i"(L_POL + 20 - L_ML) : "memory", "scc", "vcc"); \
                    asm volatile("s_waitcnt lgkmcnt(0)\n\tv_add_u32 %3, %3, %4\n\tv_readfirstlane_b32 %0, %2\n\tv_readfirstlane_b32 %1, %3\n\ts_cmp_eq_u32 %0, 7\n\ts_cbranch_scc1 1f\n\ts_waitcnt vmcnt(%9)\n\ts_branch 2f\n" \
                                 "1:\n\ts_mov_b64 exec, 31\n\tv_and_b32 %3, -2, %1\n\tglobal_store_dword %5, %3, %7 sc1\n\tv_or_b32 %2, 1, %1\n\tds_write_b32 %6, %2 offset:%8\n\ts_mov_b64 exec, -1\n\ts_waitcnt vmcnt(%10)\n2:" \
                                 : "=&s"(la), "=&s"(th_), "+v"(ar), "+v"(mx) : "v"(hb), "v"(hfa), "v"(ma), "s"(ws), "i"(L_POL + 12 - L_ML), "i"(W), "i"((W) | 1) : "memory", "scc"); \
                    if (STAMPS && __builtin_amdgcn_readfirstlane(la) == 7) { STAMPV(21, th_); RETRY(20, 1); } \
                } while (0)
                if (!is_mlp) {
                    WO_ADDR();
                    unsigned oa = woa, ob = woa ^ 128u;
                    asm volatile("" : "+v"(oa), "+v"(ob), "+s"(wob));
                    if (SCH(S_FL) & 128) {
                        const u32x4 ones = splat<~0u>();
#pragma unroll
                        for (int i = 0; i < 6; ++i) st16_l(ws, (a_own(oi) ^ CX(0)) + (unsigned)(i * 1024 + lane * 16), ones);
                        if (oi < 96 && lane < 32) st16_l(ws, WS_XB + (unsigned)((oi / 12) * 8192 + (oi % 12) * 512 + lane * 16), ones);
                    }
                    M0_KEEP();
                    if (WPRE) {
                        unsigned dw = (t0 + SCH(S_WD)) << 4;
                        wo_paced<NN7, NN7 + WPRE>(wob, oa, ob, lb, dw, SCH(S_TR));
                    }
                    if (NN7 + WPRE < 48) { if (SCH(S_WT) > 7u) lds_wait_ge(lb + L_CNT + 56, SCH(S_WT) - 7u); else lds_wait_ge(lb + L_CNT + 16, SCH(S_WT)); }
                    STAMP(35);
                    if (WK < 0) { wo_rest<NN7 + WPRE, NN7 + WPRE - WK>(wob, oa, ob, lb); ARRIVE(SCH(S_G3) - 100u, 63); }
                    wo_rest<NN7 + WPRE - (WK < 0 ? WK : 0), 48>(wob, oa, ob, lb);
                    M0_BACK();
                    STAMP(36);
                    if (WK < 0) VMW(0); else { VMW(WK < 0 ? 0 : WK); ARRIVE(SCH(S_G3) - 100u, 0); }
                    STAMP(33); DMARK(44);
                    asm volatile("ds_write_b32 %0, %1" :: "v"(lb + L_CNT + 40), "v"(1u) : "memory");
                    if (lane < 48) st16_sc1(ws, WS_CS + (unsigned)(((h * 6 + (lane >> 3)) * 5 + sub) * 128 + (lane & 7) * 16), tag4(*reinterpret_cast<const u32x4*>(smem + L_CS + 768 + lane * 16), tag & 0xffu));
                } else {
                    ARRIVE(0u, 63);
                    {
                        NARROW_ADDR();
                        unsigned t = now32(); const unsigned ts = SCH(S_TS); const int se = (int)(SCH(S_X7) >> 3) * 10;
#pragma unroll 1
                        for (int s = NM7; s < se; ++s) { narrow<true>(wg, wu, nva, nvb, lb, s, nrot); t += ts; wait_until(t); }
                        STAMP(47);
                    }
                    const int ml = mi - 12 * xg, nl = (int)SCH(S_ND), l3 = lane / 3;
                    unsigned pa = (unsigned)((l3 < nl ? l3 : 0) * 3072 + xg * 384 + (lane - 3 * l3) * 128);
                    const u16* pb = ((ml & 1) ? wu : wg) + (640 - (6 - (ml >> 1)) * LPN * nl) * 1536;
                    auto late = [&]() {
                        unsigned t = now32();
                        if (nl) {
#pragma unroll
                            for (int k = 0; k < LPN; ++k) { pull(pb + k * nl * 1536, pa, lb + L_PF); if (SCH(S_PA) && k < LPN - 1) { t += SCH(S_PA); wait_until(t); } }
                        }
                    };
                    if (SCH(S_PA)) late();
                    const u16* wdb = wd + mi * 16 * DM;
                    auto down = [&]() {
                        unsigned c16 = (unsigned)lane * 16u, t, keep; int k;
                        asm volatile("s_mov_b32 %2, m0\n\ts_mov_b32 m0, %5\n\ts_mov_b32 %3, 20\n"
                                     "1:\n\tv_mul_u32_u24 %1, 0xc7d, %0\n\tv_lshrrev_b32 %1, 22, %1\n\tv_lshlrev_b32 %1, 5, %1\n\tv_sub_u32 %1, %0, %1\n\tv_min_u32 %1, 0x4ff0, %1\n\t"
                                     "global_load_lds_dwordx4 %1, %4" WPOL "\n\ts_add_u32 m0, m0, 0x400\n\tv_add_u32 %0, 0x400, %0\n\ts_sleep %6\n\ts_sub_u32 %3, %3, 1\n\ts_cbranch_scc0 1b\n\ts_mov_b32 m0, %2"
                                     : "+v"(c16), "=&v"(t), "=&s"(keep), "=&s"(k) : "s"(wdb), "s"(lb + L_WD), "i"(DSL) : "memory", "scc");
                    };
                    asm volatile("" : "+v"(pa), "+s"(wdb));
                    if (DSL) { down(); STAMP(46); }
                    if (SCH(S_N1) > 15u) { lds_wait_ge(lb + L_CNT + 24, 2u); if (SCH(S_NT)) wait_until(now32() + SCH(S_NT)); }
                    else if (SCH(S_N1) > 14u) { hint(); wait_until(th + SCH(S_NT)); }
                    else if (SCH(S_N1) > 7u) lds_wait_ge(lb + L_CNT + 56, SCH(S_N1) - 6u); else lds_wait_ge(lb + L_CNT + 16, SCH(S_N1));
                    if (!SCH(S_PA)) late();
                    {
                        if (!DSL) down();
                        VMW(DSL ? 0 : DNW);
                        asm volatile("ds_write_b32 %0, %1" :: "v"(lb + L_CNT + 40), "v"(now32() | 1u) : "memory");
                        VMW(0);
                    }
                    STAMP(34);
                    asm volatile("ds_write_b32 %0, %1" :: "v"(lb + L_CNT + 48), "v"(1u) : "memory");
                }
                __builtin_amdgcn_s_setprio(0);
            }
        }
        if (wave == 6) {
            PAGE(2);
            LANES();
            if (P1ONLY) {} else {
            if (sub < 5) {
                int d = 0; int left;
                const unsigned o = (a_hf(h) ^ CX(0)) + (unsigned)(sub * 128);
                const int tk = sub * 4 + ((lane >> 3) & 3);
                unsigned ta = (a_ts(lane & 7) ^ CX(0)) + (unsigned)((lane < 32 ? tk + (tk / 5) * 3 : (5 + tk + (tk / 3) * 5) & 31) * 128 + h * 4), fa = lb + L_CNT + 48, t;
                asm volatile("" : "+v"(ta), "+v"(fa));
                {
                    u32x8 e; unsigned x;
                    asm volatile("s_movk_i32 %1, 4000\n1:\n\ts_load_dwordx8 s[4:11], %4, %5 glc\n\tds_read_b32 %3, %6 offset:%7\n\ts_memrealtime vcc\n\ts_sub_u32 %1, %1, 1\n\ts_waitcnt lgkmcnt(0)\n\ts_cbranch_scc1 2f\n\t"
                                 "v_readfirstlane_b32 s4, %3\n\ts_or_b32 vcc_hi, s5, s6\n\ts_or_b32 %2, s7, s8\n\ts_or_b32 vcc_hi, vcc_hi, s9\n\ts_or_b32 %2, %2, s10\n\ts_or_b32 vcc_hi, vcc_hi, s11\n\ts_orn2_b32 %2, %2, s4\n\t"
                                 "s_or_b32 vcc_hi, vcc_hi, %2\n\ts_bitcmp1_b32 vcc_hi, 0\n\ts_cbranch_scc1 1b\n2:\n\ts_mov_b32 %2, vcc_lo"
                                 : "=&{s[4:11]}"(e), "=&s"(left), "=&s"(t), "=&v"(x) : "s"(ws), "s"(o), "v"(fa), "i"(L_POL + 12 - L_CNT - 48) : "memory", "scc", "vcc");
                    asm volatile("s_sub_i32 s4, s4, %2\n\ts_sub_i32 s5, s5, %2\n\ts_sub_i32 s6, s6, %2\n\ts_sub_i32 s7, s7, %2\n\ts_max_i32 s4, s4, s5\n\ts_sub_i32 s8, s8, %2\n\ts_max_i32 s6, s6, s7\n\ts_sub_i32 s9, s9, %2\n\t"
                                 "s_max_i32 s4, s4, s6\n\ts_sub_i32 s10, s10, %2\n\ts_max_i32 s8, s8, s9\n\ts_sub_i32 s11, s11, %2\n\ts_max_i32 s4, s4, s8\n\ts_max_i32 s10, s10, s11\n\ts_max_i32 %0, s4, s10"
                                 : "=s"(d), "+{s[4:11]}"(e) : "s"(t) : "scc");
                }
                if (__builtin_expect(left < 0, 0)) {
#pragma unroll 1
                    for (unsigned spins = 0; spins < kSpinCap; ++spins) {
                        if (allok(poll4(ws, o + (unsigned)((lane & 7) * 4)))) break;
                    }
                    t = now32(); d = 0;
                    RETRY(21, 1);
                }
                d += (int)SCH(S_H20);
                if (TSX && d < (int)SCH(S_G2) - TSX) d = (int)SCH(S_G2) - TSX;
                DMARK(45);
                const unsigned tg = (t + (unsigned)(d > (int)SCH(S_G2) && d < 1000 ? d : (int)SCH(S_G2))) | 1u;
                asm volatile("ds_write_b32 %0, %1" :: "v"(fa), "v"(tg) : "memory");
                STAMP(25);
                st4_m<~0u, 0u>(ws, ta, enc(t + (unsigned)d));
                if (sub < 3) { wait_until(tg + 4u); st4_m<0u, ~0u>(ws, ta, enc(t + (unsigned)d)); }
                STAMPV(28, tg);
            } else lds_wait_ge(lb + L_CNT + 28, 8u);
            unsigned t; int d;
            const unsigned fl0 = lds_ld(lb + L_CNT + 36) + SCH(S_WD);
            {
                const unsigned o = (a_ts(xg) ^ CX(0)) + (unsigned)(rr * 128);
                unsigned r, lo; int left;
                asm volatile("s_movk_i32 %2, 4000\n1:\n\ts_load_dwordx16 s[68:83], %4, %5 glc\n\ts_load_dwordx16 s[84:99], %4, %6 glc\n\ts_memrealtime vcc\n\ts_sub_u32 %2, %2, 1\n\ts_waitcnt lgkmcnt(0)\n\ts_cbranch_scc1 2f\n\t"
                             "s_max_u32 s68, s68, s84\n\ts_max_u32 s69, s69, s85\n\ts_max_u32 s70, s70, s86\n\ts_max_u32 s71, s71, s87\n\ts_max_u32 s72, s72, s88\n\ts_max_u32 s73, s73, s89\n\ts_max_u32 s74, s74, s90\n\ts_max_u32 s75, s75, s91\n\ts_max_u32 s76, s76, s92\n\ts_max_u32 s77, s77, s93\n\ts_max_u32 s78, s78, s94\n\ts_max_u32 s79, s79, s95\n\ts_max_u32 s80, s80, s96\n\ts_max_u32 s81, s81, s97\n\ts_max_u32 s82, s82, s98\n\ts_max_u32 s83, s83, s99\n\ts_max_u32 s68, s68, s76\n\ts_max_u32 s69, s69, s77\n\ts_max_u32 s70, s70, s78\n\ts_max_u32 s71, s71, s79\n\ts_max_u32 s72, s72, s80\n\ts_max_u32 s73, s73, s81\n\ts_max_u32 s74, s74, s82\n\ts_max_u32 s75, s75, s83\n\ts_max_u32 s84, s68, s72\n\ts_max_u32 s85, s69, s73\n\ts_max_u32 s86, s70, s74\n\ts_max_u32 s87, s71, s75\n\ts_max_u32 s84, s84, s86\n\ts_max_u32 s85, s85, s87\n\ts_max_u32 %0, s84, s85\n\t"
                             "s_cmp_eq_u32 %0, -1\n\ts_cbranch_scc1 1b\n2:\n\ts_mov_b32 %1, vcc_lo\n\t"
                             "s_min_u32 s68, s68, s69\n\ts_min_u32 s70, s70, s71\n\ts_min_u32 s72, s72, s73\n\ts_min_u32 s74, s74, s75\n\ts_min_u32 s68, s68, s70\n\ts_min_u32 s72, s72, s74\n\ts_min_u32 %[lo], s68, s72"
                             : "=&s"(r), "=&s"(t), "=&s"(left), [lo] "=&s"(lo) : "s"(ws), "s"(o), "s"(o + 64u) : "memory", "scc", "vcc", "s68", "s69", "s70", "s71", "s72", "s73", "s74", "s75", "s76", "s77", "s78", "s79", "s80", "s81", "s82", "s83", "s84", "s85", "s86", "s87", "s88", "s89", "s90", "s91", "s92", "s93", "s94", "s95", "s96", "s97", "s98", "s99");
                if (__builtin_expect(left < 0, 0)) {
#pragma unroll 1
                    for (unsigned spins = 0; spins < kSpinCap; ++spins) {
                        if (allok(poll4(ws, o + (unsigned)((lane & 31) * 4)))) break;
                    }
                    lo = r = t = now32();
                    RETRY(22, 1);
                }
                const unsigned sp = r - lo, ex = sp < SCH(S_Y0) ? ((SCH(S_Y0) - sp) * 5u) >> 3 : 0u;
                d = (int)(r + (ex < SCH(S_Y1) ? ex : SCH(S_Y1)) - t);
                if (SCH(S_WD)) d = imax(d, (int)(fl0 - t));
            }
            asm volatile("ds_write_b32 %0, %1" :: "v"(lb + L_CNT + 8), "v"((t + (unsigned)(d < 150 ? d : 0)) | 1u) : "memory");
            STAMP(23); if (!is_mlp) DMARK(33);
            STAMPV(31, t + (unsigned)(d < 150 ? d : 0));
            if (is_mlp && SCH(S_LT)) {
                lds_wait_ge(lb + L_CNT + 40, 1u);
                const unsigned tr = lds_ld(lb + L_CNT + 40) + SCH(S_LT);
                if ((int)(tr - t - (unsigned)(d < 150 ? d : 0) - SCH(S_XA0)) > 0) { st4_m<0xffu, 0u>(ws, WS_XA + (a_rep(lane & 7) ^ CX(0)) + (unsigned)(5120 + xg * 128 + (20 + mi - 12 * xg) * 4), enc(tr)); RETRY(24, 1); }
            }
            }
        }
    }

    unsigned ref = 0;
#pragma unroll 1
    for (int layer_ = 0; layer_ < (P1ONLY ? 0 : 2); ++layer_) {
        IDS();
#define LAYER() int layer = layer_; asm volatile("" : "+s"(layer)); const unsigned cx = CX(layer), xa = WS_XA + (unsigned)(layer * LB), xm = xa + (a_rep(xg) ^ cx); (void)xa; (void)xm
        auto gather_img = [&](unsigned o, u32x4 (&a)[5], int sid, unsigned off) {
#pragma unroll 1
            for (unsigned spins = 0; spins < kSpinCap; ++spins) {
                a[0] = ld16_sc1o<-2048>(ws, o); a[1] = ld16_sc1o<-1024>(ws, o); a[2] = ld16_sc1o<0>(ws, o); a[3] = ld16_sc1o<1024>(ws, o); a[4] = ld16_sc1o<2048>(ws, o);
                asm volatile("s_waitcnt vmcnt(0)" : "+v"(a[0]), "+v"(a[1]), "+v"(a[2]), "+v"(a[3]), "+v"(a[4]) :: "memory");
                if (allok(umax(mx4(a[0], mx4(a[1])), mx4(a[2], mx4(a[3], mx4(a[4])))))) break;
                RETRY(sid, spins + 1);
                __builtin_amdgcn_s_sleep(10);
            }
        };
        auto gather_tab = [&](unsigned o, u32x4 (&a)[5], u32x4& r, int sid, unsigned t) {
            wait_until(t);
            a[0] = ld16_sc1o<-2048>(ws, o); a[1] = ld16_sc1o<-1024>(ws, o); a[2] = ld16_sc1o<0>(ws, o); a[3] = ld16_sc1o<1024>(ws, o); a[4] = ld16_sc1o<2048>(ws, o);
            wait_until(t + SCH(S_H4));
            r = ld16_sc1o<3072>(ws, o);
            asm volatile("s_waitcnt vmcnt(1)" : "+v"(a[0]), "+v"(a[1]), "+v"(a[2]), "+v"(a[3]), "+v"(a[4]) :: "memory");
            if (__builtin_expect(!allok(umax(mx4(a[0], mx4(a[1])), mx4(a[2], mx4(a[3], mx4(a[4]))))), 0)) {
                RETRY(sid, 1);
                asm volatile("s_waitcnt vmcnt(0)" : "+v"(r) :: "memory");
                __builtin_amdgcn_s_sleep(4);
                gather_img(o, a, sid, 0);
                r = ld16_sc1o<3072>(ws, o);
            }
        };
        auto late = [&](u32x4 v, unsigned base) {
            int d = -1000;
#pragma unroll
            for (int i = 0; i < 4; ++i) d = imax(d, v[i] == ~0u ? -1000 : (int)(v[i] - base));
            d = wave_imax(d);
            return d < 1000 ? d : 1000;
        };
        auto img_stats = [&](const u32x4 (&a)[5], float& mb, float& rden) {
            float s0 = 0.f, s1 = 0.f, q0 = 0.f, q1 = 0.f;
#pragma unroll
            for (int i = 0; i < 10; ++i) {
                const unsigned u = a[i >> 1][(i & 1) * 2], v = a[i >> 1][(i & 1) * 2 + 1];
                s0 = dot2(u, 0x3f803f80u, s0); q0 = dot2(u, u, q0); s1 = dot2(v, 0x3f803f80u, s1); q1 = dot2(v, v, q1);
            }
            const float mean = sum_x4(s0 + s1) * (1.f / DM);
            ln_consts(mean, sum_x4(q0 + q1) * (1.f / DM) - mean * mean, mb, rden);
        };
        if (!is_mlp) {
            if (wave == 5) {
                PAGE(4);
                LANES(); LAYER();
                const unsigned po = (a_rep((lane >> 2) & 7) ^ cx) + (unsigned)((oi >> 1) * 64 + p4 * 16 + (oi & 1) * 8 + (lane >> 5) * 4);
                if (layer == 0) lds_wait_ge(lb + L_CNT + 40, 1u);
                u32x4 w[5];
#pragma unroll
                for (int j = 0; j < 5; ++j) w[j] = *reinterpret_cast<const u32x4*>(smem + L_WR + j * 256 + (lane >> 2) * 16);
                float xr = layer ? *reinterpret_cast<const float*>(smem + L_XR + lane * 4) : __uint_as_float((unsigned)*reinterpret_cast<const u16*>(smem + L_WR + 1280 + p4 * 8 + kq * 2) << 16);
                float cw = 0.f;
#pragma unroll
                for (int j = 0; j < 20; ++j) cw = dot2(w[j >> 2][j & 3], 0x3f803f80u, cw);
                cw = sum_x4(cw);
                const unsigned tw = (a_rep(lane & 7) ^ cx) + (unsigned)(5120 + xg * 128 + ((h & 3) * 5 + sub) * 4);
                {
                    unsigned o = (a_own(oi) ^ cx) + (unsigned)(layer * LB + 6144 + lane * 16), so = xa + po;
                    unsigned t8 = tag & 0xffu;
                    asm volatile("" : "+v"(xr), "+v"(o), "+v"(so), "+v"(cw), "+s"(t8));
                    if (layer == 0) {
                        if (!(SCH(S_FL) & 224)) lds_wait_ge(lb + L_CNT + 44, 1u);
#pragma unroll 1
                        for (unsigned spins = 0; spins < (kSpinCap << 6) && ref == 0u; ++spins) ref = lds_ld(lb + L_CNT + 8);
                    }
                    if (PRI & 1) __builtin_amdgcn_s_setprio(2);
                    if (!layer) DMARK(35);
                    if (layer == 0 && (SCH(S_FL) & 96)) lds_wait_ge(lb + L_CNT + 44, 1u);
                    wait_until(ref + (layer ? SCH(S_H3) : SCH(S_H30)));
                    if (!layer) DMARK(34);
                    CY(c_a);
                    u32x4 a0, a1;
                    GATHER(8 + 32 * layer, { a0 = ld16_sc1o<0>(ws, o); a1 = ld16_sc1o<1024>(ws, o);
                                 asm volatile("s_waitcnt vmcnt(0)" : "+v"(a0), "+v"(a1) :: "memory"); a0 ^= t8; a1 ^= t8; }, tagok(or4(a0) | or4(a1)));
                    CY(c_b);
                    xr += bfr(red4(asf(a0) + asf(a1)));
                    auto sw = __builtin_amdgcn_permlane16_swap(__float_as_uint(xr), __float_as_uint(xr), false, false);
                    st4_sc1(ws, so, enc(pk2(__uint_as_float(sw[0]), __uint_as_float(sw[1]))));
                    xr = bfr(xr);
                    const unsigned t = now32() + SCH(S_GAP);
                    if ((int)(t - ref - (layer ? SCH(S_XA1) : SCH(S_XA0))) > 0) st4_m<0xffu, 0u>(ws, xa + tw, enc(t + SCH(S_EX)));
                    CY(c_c); CYW(55, c_a, c_b); CYW(56, c_b, c_c);
                    STAMP(8 + 32 * layer); if (layer) DMARK(25); else DMARK(31);
                }
                float rs, xn;
                {
                    unsigned o = xm + (unsigned)(2048 + lane * 16);
                    asm volatile("" : "+v"(o), "+v"(cw));
                    u32x4 a[5], lt; gather_tab(o, a, lt, 9 + 32 * layer, ref + (layer ? SCH(S_XA1) : SCH(S_XA0)) + 8u);
                    float mb, rden; img_stats(a, mb, rden);
                    float r0 = 0.f, r1 = 0.f;
#pragma unroll
                    for (int j = 0; j < 10; ++j) { r0 = dot2(a[j >> 1][(j & 1) * 2], w[j >> 1][(j & 1) * 2], r0); r1 = dot2(a[j >> 1][(j & 1) * 2 + 1], w[j >> 1][(j & 1) * 2 + 1], r1); }
                    const float r = bfr((sum_x4(r0 + r1) - mb * cw) * rden);
                    rs = bfr(frcp(1.f + fexp(-r)));
                    xn = bfr(bfr(xr - mb) * rden);
                    asm volatile("s_waitcnt vmcnt(0)" : "+v"(lt), "+v"(rs), "+v"(xn) :: "memory");
                    ref += (unsigned)imax(late(lt, ref + (layer ? SCH(S_XA1) : SCH(S_XA0))), 0);
                    if (!layer) DMARK(38);
                    if (layer == 0) {
                        if (g == 0) st4_m<1u, 0u>(ws, WS_EP, tag);
                    }
                    STAMP(9 + 32 * layer); if (!layer) DMARK(37);
                }
                {
                    unsigned o = (a_own(oi) ^ cx) + (unsigned)(layer * LB + lane * 16 + 2048);
                    unsigned so = layer ? (unsigned)((p4 * DM + oi * 4 + (lane >> 5) * 2) * 2) : WS_XB + po;
                    unsigned fc = lb + L_CNT + 44, fv; int fs, fn;
                    asm volatile("" : "+v"(rs), "+v"(xn), "+v"(o), "+v"(so), "+v"(fc));
                    wait_until(ref + (layer ? SCH(S_H51) : SCH(S_H50)));
                    CY(c_a);
                    u32x4 a[6];
                    GATHER(13 + 32 * layer, { a[0] = ld16_sc1o<-2048>(ws, o); a[1] = ld16_sc1o<-1024>(ws, o); a[2] = ld16_sc1o<0>(ws, o);
                                 a[3] = ld16_sc1o<1024>(ws, o); a[4] = ld16_sc1o<2048>(ws, o); a[5] = ld16_sc1o<3072>(ws, o);
                                 asm volatile("s_mov_b32 %8, 0x1000000\n1:\n\tds_read_b32 %6, %9\n\ts_sub_u32 %8, %8, 1\n\ts_waitcnt lgkmcnt(0)\n\ts_cbranch_scc1 2f\n\tv_readfirstlane_b32 %7, %6\n\ts_cmp_lt_u32 %7, 2\n\ts_cbranch_scc1 1b\n2:\n\ts_waitcnt vmcnt(0)"
                                              : "+v"(a[0]), "+v"(a[1]), "+v"(a[2]), "+v"(a[3]), "+v"(a[4]), "+v"(a[5]), "=&v"(fv), "=&s"(fs), "=&s"(fn) : "v"(fc) : "memory", "scc"); },
                           allok(umax(umax(mx4(a[0], mx4(a[1])), mx4(a[2], mx4(a[3]))), mx4(a[4], mx4(a[5])))));
                    CY(c_b);
                    const float y = bfr(red4(((asf(a[0]) + asf(a[1])) + (asf(a[2]) + asf(a[3]))) + (asf(a[4]) + asf(a[5]))));
                    xr += bfr(xn + bfr(y * rs));
                    auto sw = __builtin_amdgcn_permlane16_swap(__float_as_uint(xr), __float_as_uint(xr), false, false);
                    const unsigned w = pk2(__uint_as_float(sw[0]), __uint_as_float(sw[1]));
                    if (layer == 0) {
                        st4_sc1(ws, so, enc(w));
                        const unsigned t = now32() + SCH(S_GAP), t2 = ref + SCH(S_XB), tx = ((int)(t - t2) > 0 ? t + SCH(S_EXB) : t2) | 1u;
                        asm volatile("ds_write_b32 %0, %1" :: "v"(lb + L_CNT + 60), "v"(tx) : "memory");
                        if ((sub == 0 && (h & 3) == 0) || (int)(t - t2) > 0) st4_m<0xffu, 0u>(ws, WS_XB + tw, enc(tx));
                    }
                    else {
                        asm volatile("s_mov_b32 exec_lo, 15\n\ts_mov_b32 exec_hi, 15\n\tglobal_store_dword %0, %1, %2 " OSX "\n\ts_mov_b64 exec, -1" :: "v"(so), "v"(w), "s"(out) : "memory");
                    }
                    *reinterpret_cast<float*>(smem + L_XR + lane * 4) = bfr(xr);
                    CY(c_c); CYW(57, c_a, c_b); CYW(58, c_b, c_c);
                    STAMP(13 + 32 * layer); if (!layer) DMARK(36);
                }
            } else if (wave == 4 && layer_ == 0) {
                LANES();
                const u32x4 ones = splat<~0u>();
                if (SCH(S_FL) & 32) lds_wait_ge(lb + L_CNT + 8, 1u);
                if (SCH(S_FL) & 64) { lds_wait_ge(lb + L_CNT + 48, 1u); wait_until(lds_ld(lb + L_CNT + 48) + 30u); }
                if (!(SCH(S_FL) & 128)) {
#pragma unroll
                for (int i = 0; i < 6; ++i) st16_l(ws, (a_own(oi) ^ CX(0)) + (unsigned)(i * 1024 + lane * 16), ones);
                if (oi < 96 && lane < 32) st16_l(ws, WS_XB + (unsigned)((oi / 12) * 8192 + (oi % 12) * 512 + lane * 16), ones);
                asm volatile("s_waitcnt vmcnt(0)\n\tds_write_b32 %0, %1" :: "v"(lb + L_CNT + 44), "v"(1u) : "memory");
                }
                lds_wait_ge(lb + L_CNT + 8, 1u); wait_until(lds_ld(lb + L_CNT + 8) + SCH(S_CL) - 100u);
                if (SCH(S_FL) & 2) {
#pragma unroll
                    for (int i = 0; i < 2; ++i) st16_l(ws, a_own(oi) + (unsigned)(LB + 6144 + i * 1024 + lane * 16), (u32x4){tag + 1u, tag + 1u, tag + 1u, tag + 1u});
                }
                if (oi < 128) { st16_l(ws, WS_XA + LB + (unsigned)(oi * 1536 + lane * 16), ones); if (lane < 32) st16_l(ws, WS_XA + LB + (unsigned)(oi * 1536 + 1024 + lane * 16), ones); }
#pragma unroll
                for (int i = 0; i < 3; ++i) st16_l(ws, a_h1(h, lane >> 4) + (unsigned)(((2 * i + ((lane >> 3) & 1)) * 5 + sub) * 128 + (lane & 7) * 16), ones);
                asm volatile("s_waitcnt vmcnt(0)\n\tds_write_b32 %0, %1" :: "v"(lb + L_CNT + 44), "v"(2u) : "memory");
                STAMP(26);
            }
        } else {
            PAGE(3);
            LAYER();
            if (wave < 4) {
                LANES();
                if (layer == 0) { lds_wait_ge(lb + L_CNT + 8, 1u); ref = lds_ld(lb + L_CNT + 8); }
                const unsigned gs = xm + (unsigned)(wave * 1280);
                int st;
                asm volatile(ASM_GU : [st] "=&s"(st) : [a0] "v"(lb + L_GU + (unsigned)((wave * 160 + 8 * kq + q4 + 4 * (kq & 1)) * 32 + p4 * 8)), [gs] "v"(gs + lane * 16),
                             [g2] "v"(gs + (unsigned)(1024 + kq * 64 + p4 * 16 + q4 * 4)), [pw] "v"(lb + L_OP + (unsigned)(n * 784 + kq * 192 + wave * 8)), [gw] "v"(lb + L_OP + (unsigned)(4096 + n * 272 + kq * 64 + wave * 4)),
                             [ws] "s"(ws), [tg] "s"(ref + (layer ? SCH(S_XA1) : SCH(S_XA0))), [dbg] "s"(lb + L_DBG + wave * 512 + 256 + (10 + 32 * layer) * 4) : "memory", "scc", "vcc", "v6", "v7", CLOB_8_15, CLOB_16_63);
                STAMP(11 + 32 * layer);
                if (wave == 1 && layer == 0) {
                    u32x4 lt = ld16_sc1(ws, xm + (unsigned)(5120 + lane * 16));
                    asm volatile("s_waitcnt vmcnt(0)" : "+v"(lt) :: "memory");
                    asm volatile("ds_write_b32 %0, %1" :: "v"(lb + L_CNT + 60), "v"((ref + SCH(S_XB) + (unsigned)imax(late(lt, ref + SCH(S_XA0)), 0)) | 1u) : "memory");
                }
            } else {
                LANES();
                if (layer == 0) {
                    lds_wait_ge(lb + L_CNT + 48, 1u);
                    if (wave == 7) DMARK(39);
                }
                const unsigned wdo = lb + L_WD + (unsigned)(4 * kq * 1312 + (160 * (wave & 3) + 4 * p4) * 2);
                unsigned w0 = wdo + q4 * 1312, w1 = wdo + ((q4 + 1) & 3) * 1312, w2 = wdo + ((q4 + 2) & 3) * 1312, w3 = wdo + ((q4 + 3) & 3) * 1312;
                asm volatile(ASM_HS : [w0] "+{v8}"(w0), [w1] "+{v9}"(w1), [w2] "+{v10}"(w2), [w3] "+{v11}"(w3) :
                             [rp] "v"(lb + L_OP + (unsigned)(p4 * 784 + (4 * kq + q4) * 48)), [cp] "v"(lb + L_OP + (unsigned)(4 * 784 + (4 * kq + q4) * 48)),
                             [sq] "v"(lb + L_OP + (unsigned)(4096 + p4 * 288)), [sx] "v"(lb + L_OP + (unsigned)(4096 + p4 * 272 + 64)),
                             [so] "v"(((unsigned)(layer * LB + ((10 * (wave & 1) + q4) * 4 + kq) * GP + (wave & 2) * 4096 + mi * 64 + p4 * 16)) ^ ((unsigned)((q4 * 4 + kq) << 7) | cx)), [ws] "s"(ws)
                             : "memory", "v12", "v13", CLOB_16_63);
                STAMP(12 + 32 * layer); if (!layer) DMARK(32);
            }
        }
        if (layer_ != 0) break;
        unsigned tz = 0u;
        if (wave == 7) lds_wait_ge(lb + L_CNT + 8, 1u);
        if (is_mlp || wave == 7) tz = (lds_ld(lb + L_CNT + 8) + SCH(S_H50) + SCH(S_Y2)) | 1u;
        if (wave == (is_mlp ? 2 : 7)) {
            LANES();
            unsigned o = WS_CS + (unsigned)((h * 6 + (lane < 48 ? lane >> 3 : 0)) * 640 + (lane & 7) * 16); const unsigned t8 = tag & 0xffu;
            asm volatile("" : "+v"(o));
            { const unsigned tc = tz - SCH(S_H50) - SCH(S_Y2) + SCH(S_TR); while ((int)(now32() - tc) < 0) __builtin_amdgcn_s_sleep(3); }
            u32x4 a[5];
#pragma unroll 1
            for (unsigned spins = 0; spins < kSpinCap; ++spins) {
                a[0] = ld16_sc1o<0>(ws, o); a[1] = ld16_sc1o<128>(ws, o); a[2] = ld16_sc1o<256>(ws, o); a[3] = ld16_sc1o<384>(ws, o); a[4] = ld16_sc1o<512>(ws, o);
                asm volatile("s_waitcnt vmcnt(0)" : "+v"(a[0]), "+v"(a[1]), "+v"(a[2]), "+v"(a[3]), "+v"(a[4]) :: "memory");
#pragma unroll
                for (int i = 0; i < 5; ++i) a[i] ^= t8;
                if (tagok(or4(a[0] | a[1] | a[2] | a[3] | a[4]))) break;
                RETRY(4, spins + 1);
                __builtin_amdgcn_s_sleep(4);
            }
            if (lane < 48) *reinterpret_cast<f32x4*>(smem + L_CS + (lane >> 3) * 128 + (lane & 1) * 64 + ((lane >> 1) & 3) * 16) = ((asf(a[0]) + asf(a[1])) + (asf(a[2]) + asf(a[3]))) + asf(a[4]);
            STAMP(32);
        }
        {
            const unsigned xb = WS_XB + (a_rep(xg) ^ CX(0));
#define QPROJ() do { const int tp = wave == 6 ? 5 : wave; int st_, rf_; \
                    const unsigned a0_ = lb + L_WQ + (unsigned)((8 * kq + q4) * 384 + ((2 * tp) ^ ((q4 >> 1) | ((kq & 1) << 1))) * 32 + p4 * 8); \
                    asm volatile(ASM_QP : [st] "=&s"(st_), [rf] "=&s"(rf_) : [a0] "{v60}"(a0_), [a1] "{v61}"(a0_ ^ 32u), [gs] "{v62}"(xb + (unsigned)(sub * 1024 + lane * 16)), \
                                 [so] "{v63}"((a_h1(h, p4) ^ CX(1)) + (unsigned)((tp * 5 + sub) * 128 + kq * 32 + (q4 & 1) * 16)), \
                                 [ws] "s"(ws), [fl] "s"(lb + L_CNT + 60), [tz] "s"(SCH(S_H50) + SCH(S_Y2)), [dbg] "s"(lb + L_DBG + wave * 512 + 256 + 14 * 4) : "memory", "scc", "vcc", CLOB_16_59); } while (0)
#define ATT_IN() LANES(); \
            ATT_ADDR(1); \
            const unsigned go = (wave < 6 ? (a_h1(h, b) ^ CX(1)) + (unsigned)(wave * 640) : (a_own(oi) ^ CX(1)) + (unsigned)(LB + (wave & 1) * 3072)) + (unsigned)(lane * 16); \
            const unsigned qo = lb + L_Q + (unsigned)((wave & 1) * 192 + (wave >> 1) * 64 + (lane & 1) * 32 + (kq >> 1) * 16 + ((lane >> 1) & 3) * 4 + (kq & 1) * 2); \
            const unsigned cs = lb + L_CS + (unsigned)(wave * 128 + (lane & 1) * 64 + ((lane >> 1) & 3) * 16 + kq * 4); \
            int st
#define ATT_OPS : [st] "=&s"(st) : [qa] "{v8}"(qa), [pw] "{v9}"(pw), [pr] "{v10}"(pr), [ow] "{v11}"(ow), [mo] "{v12}"(mo), [r0] "{v5}"(r0), [so] "{v6}"(so), [go] "{v13}"(go), [qo] "{v14}"(qo), [cs] "{v15}"(cs), [ix] "{v7}"(ix), \
                [mlw] "s"(lb + L_ML + wave * 4), [ma] "s"(lb + L_ML), [ws] "s"(ws), [wv] "s"(wave), \
                [cl] "s"((wave + 2) & (sub - 5) & 8), [tg] "s"(SCH(S_H1)), [tz] "s"(is_mlp && tz ? tz + SCH(S_Y3) : 0u), [dbg] "s"(lb + L_DBG + wave * 512 + 256 + 35 * 4) : "memory", "scc", "vcc", CLOB_16_63 TRCL
            if (wave >= 4) {
                ATT_IN();
                unsigned go_ = go, qo_ = qo, cs_ = cs;
                asm volatile("" : "+{v8}"(qa), "+{v9}"(pw), "+{v10}"(pr), "+{v11}"(ow), "+{v12}"(mo), "+{v5}"(r0), "+{v6}"(so), "+{v13}"(go_), "+{v14}"(qo_), "+{v15}"(cs_), "+{v7}"(ix));
                PAGE(5);
                if (!(wave & 1) && !is_mlp) { QPROJ(); DMARK(21); }
                if (wave == 7) {
                unsigned o = xb + (unsigned)(2048 + lane * 16);
                asm volatile("" : "+v"(o));
                unsigned gx = 0u;
                if (PRI & 2) __builtin_amdgcn_s_setprio(3);
                if (tz) while ((int)(now32() - tz) < 0) __builtin_amdgcn_s_sleep(3);
#pragma unroll 1
                for (unsigned spins = 0; spins < (kSpinCap << 6) && gx == 0u; ++spins) gx = lds_ld(lb + L_CNT + 60);
                u32x4 a[5], r; gather_tab(o, a, r, 14, gx + 4u);
                unsigned k0 = lane & 7 ? 1u : 2u;
                asm volatile("s_waitcnt vmcnt(0)" : "+v"(r), "+v"(k0) :: "memory");
                auto dif = [&](unsigned w, unsigned k) { const unsigned x = w ^ gx, y = w + k; return x < y ? x : y; };
                if (__builtin_expect(__builtin_amdgcn_ballot_w64((dif(r[0], k0) | dif(r[1], 1u) | dif(r[2], 1u) | dif(r[3], 1u)) != 0u) != 0ull, 0)) {
#pragma unroll 1
                    for (unsigned spins = 0; spins < kSpinCap && !allok(lane & 7 ? 0u : r[0]); ++spins) {
                        RETRY(15, spins + 1);
                        __builtin_amdgcn_s_sleep(4);
                        r = ld16_sc1o<3072>(ws, o);
                        asm volatile("s_waitcnt vmcnt(0)" : "+v"(r) :: "memory");
                    }
                    gx = (gx + (unsigned)late(r, gx)) | 1u;
                }
                asm volatile("ds_write_b32 %0, %1" :: "v"(lb + L_CNT + 20), "v"(gx) : "memory");
                STAMP(38); DMARK(28);
                float mb, rden; img_stats(a, mb, rden);
                STAMP(14); DMARK(24);
                if (lane == b) *reinterpret_cast<u32x2*>(smem + L_CNT + 32) = (u32x2){__float_as_uint(mb), __float_as_uint(rden)};
                BAR();
                }
                { const unsigned go = go_, qo = qo_, cs = cs_; asm volatile(ASM_ATT_B ATT_OPS); }
            }
            else if (is_mlp) { PAGE(6); ATT_IN(); asm volatile(ASM_ATT_A ATT_OPS); }
            else {
                ATT_IN();
                const unsigned a0 = lb + L_WO + (unsigned)((8 * kq + q4) * 256 + ((2 * wave) ^ (q4 | ((kq & 1) << 2))) * 32 + p4 * 8), h2 = (a_h2(h) ^ CX(1)) + LB; const int ot = (8 * sub + 2 * wave + (q4 & 1)) * 4 + kq, rw = (q4 & 1) ? 3 - p4 : p4;
                unsigned i0 = qa, i1 = pw, i2 = pr, i3 = ow, i4 = mo, i5 = go, i6 = qo, i7 = cs; int rf;
                unsigned j0 = a0, j1 = a0 ^ 32u, j2 = h2 + (unsigned)((lane >> 3) * 128 + rw * 32 + (q4 & 1) * 16), j3 = h2 + (unsigned)(3072 + ((n >> 3) * 4 + rw) * MLS1);
                unsigned j4 = (a_own(ot) + (unsigned)(LB + 6144 + h * 64 + rw * 16)) ^ ((unsigned)((ot & 15) << 7) | CX(1));
                asm volatile("" : "+{v8}"(i0), "+{v9}"(i1), "+{v10}"(i2), "+{v11}"(i3), "+{v12}"(i4), "+{v13}"(i5), "+{v14}"(i6), "+{v15}"(i7), "+{v0}"(j0), "+{v1}"(j1), "+{v2}"(j2), "+{v3}"(j3), "+{v4}"(j4), "+{v5}"(r0), "+{v6}"(so), "+{v7}"(ix));
                PAGE(7);
                QPROJ();
                asm volatile(ASM_ATT_P : [st] "=&s"(st), [rf] "=&s"(rf), [t0] "+{v7}"(ix), [qa] "+{v8}"(i0), [pw] "+{v9}"(i1), [pr] "+{v10}"(i2), [ow] "+{v11}"(i3), [mo] "+{v12}"(i4), [go] "+{v13}"(i5), [qo] "+{v14}"(i6), [cs] "+{v15}"(i7)
                             : [a0] "{v0}"(j0), [a1] "{v1}"(j1), [gs] "{v2}"(j2), [gm] "{v3}"(j3), [s2] "{v4}"(j4), [r0] "{v5}"(r0), [so] "{v6}"(so),
                               [mlw] "s"(lb + L_ML + wave * 4), [ma] "s"(lb + L_ML), [ws] "s"(ws),
                               [tg] "s"(SCH(S_H1)), [t2] "s"(SCH(S_H2)), [wv] "s"(wave), [tag] "s"(tag), [dbg] "s"(lb + L_DBG + wave * 512 + 256 + 35 * 4) : "memory", "scc", "vcc", CLOB_16_63 TRCL);
                STAMP(39); DMARK(23);
                break;
            }
            if (wave != 7) STAMP(38);
            DMARK(22); if (is_mlp) DMARK(26);
            ref = lds_ld(lb + L_CNT + 20);
        }
    }
    asm volatile("s_endpgm\n\t.fill 128, 4, 0xbf800000\nmega_pf_end:" ::: "memory");
}

extern "C" void kernel_launch(void* const* d_in, const int* in_sizes, int n_in, void* d_out, int out_size, void* d_ws,
                              size_t ws_size, hipStream_t stream) {
    const u16* x0 = (const u16*)d_in[0];
    const u16* kc = (const u16*)d_in[1];
    const u16* vc = (const u16*)d_in[2];
    const u16* wq = (const u16*)d_in[3];
    const u16* wo = (const u16*)d_in[4];
    const u16* wr = (const u16*)d_in[6];
    const u16* wg = (const u16*)d_in[7];
    const u16* wu = (const u16*)d_in[8];
    const u16* wd = (const u16*)d_in[9];
    (void)hipFuncSetAttribute((const void*)mega_kernel, hipFuncAttributeMaxDynamicSharedMemorySize, L_END);
    mega_kernel<<<256, 512, L_END, stream>>>(kc, vc, (unsigned char*)(((size_t)d_ws + LB - 1) & ~(size_t)(LB - 1)), x0, wq, wg, wu, wo, wr, wd, (u16*)d_out
                                             );
}
```

```cpp
#define SCHED {136,243,311,56,205,276,328,430,10,0,490,3,1823,271,14,0,256,16,150,0,80,68,564,60,4,16,0,9,70,40,16,40,0,4,170,230,455,30,70,0,120,33,44,46}
#define WOMODE 0
#define KAL 4096
#define P_LN 7
#define P_QP 9
#define F7 4
#define DHI 4
#define WQP 4
#define EPF 3
#define LDR 38
#define CA 10
#define A7 13
#define Q7 28
#define L7 28
#define QG 37
#define QW 5
#define ECX "sc1"
#define NN7 18
#define WK -4
#define DNW 21
#define NM7 0
#define TSX 1
#define WSP -1
#define WPRE 0
#define MLS 16
#define MLS1 16
#include <hip/hip_runtime.h>
#include <hip/hip_bf16.h>
#include <stdint.h>

typedef unsigned short u16;
typedef unsigned u32x4 __attribute__((ext_vector_type(4)));
typedef unsigned u32x2 __attribute__((ext_vector_type(2)));
typedef unsigned u32x8 __attribute__((ext_vector_type(8)));
typedef float f32x4 __attribute__((ext_vector_type(4)));
typedef short s16x4 __attribute__((ext_vector_type(4)));
typedef __attribute__((ext_vector_type(4))) __bf16 bf16x4;
typedef __attribute__((ext_vector_type(8))) __bf16 bf16x8;
#define LAS __attribute__((address_space(3)))

constexpr int DM = 640, HQ = 32, SK = 4096, DH = 192, WIN = 1024;
constexpr int QD = HQ * DH;
constexpr float SCALE = 0.07216878364870322f;
constexpr float L2E = 1.44269504088896341f;
constexpr unsigned kSpinCap = 1u << 16;
constexpr int NMLP = 96;
#define TDEPTH 4

#define POL 0
#define WPOL " nt"
enum { S_XA0, S_H50, S_XB, S_H1, S_H2, S_H3, S_XA1, S_H51, S_GAP, S_PA, S_PD, S_WT, S_WD, S_CL, S_H4, S_GS, S_TS, S_TW, S_TR, S_C5, S_H30, S_H20, S_PM, S_EX, S_N0, S_N1, S_NT, S_ND, S_NR, S_PT, S_FL, S_BT, S_G1, S_G2, S_G3, S_X3, S_X4, S_X5, S_X6, S_X7, S_Y0, S_Y1, S_Y2, S_Y3, S_N };
#define S_LT S_GS
#define S_EXB S_NR
#define S_PO S_BT
#define S_MO S_PT
#define S_P7 S_X4
#define S_O7 S_X5

constexpr unsigned LB = 2097152, GP = 16384;
constexpr unsigned WS_XA = 80 * GP;
constexpr unsigned WS_H2 = 84 * GP;
constexpr unsigned WS_XB = 92 * GP;
constexpr unsigned WS_TS = 96 * GP;
constexpr unsigned WS_H1B = LB + 92 * GP;
constexpr unsigned WS_PR = 124 * GP;
constexpr unsigned WS_EP = 2 * LB;
constexpr unsigned WS_AR = WS_EP + 1024;
constexpr unsigned WS_GO = WS_EP + 4096;
constexpr unsigned WS_H1A = WS_EP + 8192;
constexpr unsigned WS_CS = WS_H1A + 160 * 1792;
constexpr unsigned WS_SINK = WS_CS + 32 * 3840;
__device__ __forceinline__ unsigned a_rep(int r) { return (unsigned)((r & 3) * GP + (r >> 2) * 8192); }
__device__ __forceinline__ unsigned a_own(int o) { return (unsigned)(o * GP - (o >= 80 ? 80 * GP - 8192 : 0)); }
__device__ __forceinline__ unsigned a_h2(int h) { return WS_H2 + (unsigned)(((h & 15) >> 1) * GP + (h >> 4) * 8192 + (h & 1) * 4096); }
__device__ __forceinline__ unsigned a_hf(int h) { return WS_XA + a_rep(h >> 2) + (unsigned)((h & 2) * 6 * GP + 6144 + (h & 1) * 1024); }
__device__ __forceinline__ unsigned a_ts(int r) { return WS_TS + (unsigned)(((r & 3) >> 1) * GP + (r >> 2) * 8192 + (r & 1) * 4096); }
__device__ __forceinline__ unsigned a_h1(int h, int row) { return WS_H1B + (unsigned)(((h & 15) * 2 + (row >> 1)) * GP + (h >> 4) * 8192 + (row & 1) * 3840); }
#define CX(layer) ((pol << (12 - (layer))) & 8192u)
constexpr int LEADER = LDR;
#define RST 128
constexpr unsigned WS_DBG = 6680576;
static_assert(WS_SINK + 256 * 128 <= WS_DBG, "ws map");

constexpr int L_WQ = 0;
constexpr int L_WO = 49152;
constexpr int L_GU = 0;
constexpr int L_WD = 40960;
constexpr int OPS = 3200;
constexpr int L_OP = 98304;
constexpr int L_P = L_OP + 8 * OPS;
constexpr int L_Q = L_P + 8 * 1088;
constexpr int L_CS = L_Q + 512;
constexpr int L_ML = L_CS + 1536;
constexpr int L_XR = L_ML + 64;
constexpr int L_CNT = L_XR + 256;
constexpr int L_FW = L_CNT + 64;
constexpr int L_PF = L_FW + 64;
constexpr int L_POL = L_PF + 256;
constexpr int L_WR = L_POL + 32;
constexpr int L_DBG = L_WR + 1536;
constexpr int L_END = L_DBG + 4096;
static_assert(L_CNT + 32 - L_ML == 352, "stats offset");
static_assert(L_END <= 163840 && L_END > 81920 && L_Q % 16 == 0, "LDS");

typedef __attribute__((ext_vector_type(2))) __bf16 bf16x2;
typedef float f32x2 __attribute__((ext_vector_type(2)));
__device__ __forceinline__ unsigned pk2(float a, float b) { return __builtin_bit_cast(unsigned, __builtin_convertvector((f32x2){a, b}, bf16x2)); }
__device__ __forceinline__ float lo16(unsigned w) { return __uint_as_float(w << 16); }
__device__ __forceinline__ float hi16(unsigned w) { return __uint_as_float(w & 0xffff0000u); }
__device__ __forceinline__ float bfr(float f) { return lo16(pk2(f, f)); }
template <int CTRL> __device__ __forceinline__ float dpp(float x) {
    return __builtin_bit_cast(float, __builtin_amdgcn_mov_dpp(__builtin_bit_cast(int, x), CTRL, 0xf, 0xf, true));
}
template <int CTRL> __device__ __forceinline__ unsigned dppu(unsigned x) { return (unsigned)__builtin_amdgcn_mov_dpp((int)x, CTRL, 0xf, 0xf, true); }
__device__ __forceinline__ float row16_sum(float v) {
    v += dpp<0xB1>(v); v += dpp<0x4E>(v); v += dpp<0x141>(v); v += dpp<0x140>(v); return v;
}
__device__ __forceinline__ float sw16_add(float a, float b) { auto s = __builtin_amdgcn_permlane16_swap(__float_as_uint(a), __float_as_uint(b), false, false); return __uint_as_float(s[0]) + __uint_as_float(s[1]); }
__device__ __forceinline__ float sw32_add(float a, float b) { auto s = __builtin_amdgcn_permlane32_swap(__float_as_uint(a), __float_as_uint(b), false, false); return __uint_as_float(s[0]) + __uint_as_float(s[1]); }
__device__ __forceinline__ float wave_sum(float v) { v = row16_sum(v); v = sw16_add(v, v); return sw32_add(v, v); }
__device__ __forceinline__ int imax(int a, int b) { return a > b ? a : b; }
__device__ __forceinline__ unsigned umax(unsigned a, unsigned b) { return a > b ? a : b; }
__device__ __forceinline__ int wave_imax(int v) {
    v = imax(v, __builtin_amdgcn_mov_dpp(v, 0xB1, 0xf, 0xf, true)); v = imax(v, __builtin_amdgcn_mov_dpp(v, 0x4E, 0xf, 0xf, true));
    v = imax(v, __builtin_amdgcn_mov_dpp(v, 0x141, 0xf, 0xf, true)); v = imax(v, __builtin_amdgcn_mov_dpp(v, 0x140, 0xf, 0xf, true));
    return imax(imax(__builtin_amdgcn_readlane(v, 0), __builtin_amdgcn_readlane(v, 16)), imax(__builtin_amdgcn_readlane(v, 32), __builtin_amdgcn_readlane(v, 48)));
}
__device__ __forceinline__ int fkey(int bits) { return bits ^ ((bits >> 31) & 0x7fffffff); }

#define BAR() asm volatile("s_waitcnt lgkmcnt(0)\n\ts_barrier" ::: "memory")
#define VMW(n) asm volatile("s_waitcnt vmcnt(%0)" :: "i"(n) : "memory")

__device__ __forceinline__ u32x4 ld16_sc1(const void* sbase, unsigned voff) {
    u32x4 r; asm volatile("global_load_dwordx4 %0, %1, %2 sc1" : "=v"(r) : "v"(voff), "s"(sbase) : "memory"); return r;
}
template <int IMM> __device__ __forceinline__ u32x4 ld16_sc1o(const void* sbase, unsigned voff) {
    u32x4 r; asm volatile("global_load_dwordx4 %0, %1, %2 offset:%3 sc1" : "=v"(r) : "v"(voff), "s"(sbase), "i"(IMM) : "memory"); return r;
}
template <int IMM> __device__ __forceinline__ u32x2 ld8_sc1o(const void* sbase, unsigned voff) {
    u32x2 r; asm volatile("global_load_dwordx2 %0, %1, %2 offset:%3 sc1" : "=v"(r) : "v"(voff), "s"(sbase), "i"(IMM) : "memory"); return r;
}
__device__ __forceinline__ unsigned ld4_sc1(const void* sbase, unsigned voff) {
    unsigned r; asm volatile("global_load_dword %0, %1, %2 sc1" : "=v"(r) : "v"(voff), "s"(sbase) : "memory"); return r;
}
__device__ __forceinline__ void st16_sc1(void* sbase, unsigned voff, u32x4 v) {
    asm volatile("global_store_dwordx4 %0, %1, %2 sc1\n\ts_nop 1" :: "v"(voff), "v"(v), "s"(sbase) : "memory");
}
#define XSX "sc1 nt"
#define XLX "sc1 nt"
#define LCX "sc1"
__device__ __forceinline__ void st16_e(void* sbase, unsigned voff, u32x4 v) {
    asm volatile("global_store_dwordx4 %0, %1, %2 " ECX "\n\ts_nop 1" :: "v"(voff), "v"(v), "s"(sbase) : "memory");
}
__device__ __forceinline__ void st16_l(void* sbase, unsigned voff, u32x4 v) {
    asm volatile("global_store_dwordx4 %0, %1, %2 " LCX "\n\ts_nop 1" :: "v"(voff), "v"(v), "s"(sbase) : "memory");
}
__device__ __forceinline__ void st16_x(void* sbase, unsigned voff, u32x4 v) {
    asm volatile("global_store_dwordx4 %0, %1, %2 " XSX "\n\ts_nop 1" :: "v"(voff), "v"(v), "s"(sbase) : "memory");
}
template <int IMM> __device__ __forceinline__ u32x4 ld16_xo(const void* sbase, unsigned voff) {
    u32x4 r; asm volatile("global_load_dwordx4 %0, %1, %2 offset:%3 " XLX : "=v"(r) : "v"(voff), "s"(sbase), "i"(IMM) : "memory"); return r;
}
typedef unsigned u32x3 __attribute__((ext_vector_type(3)));
__device__ __forceinline__ u32x3 ld12_xo(const void* sbase, unsigned voff) {
    u32x3 r; asm volatile("global_load_dwordx3 %0, %1, %2 " XLX : "=v"(r) : "v"(voff), "s"(sbase) : "memory"); return r;
}
__device__ __forceinline__ void st8_sc1(void* sbase, unsigned voff, u32x2 v) {
    asm volatile("global_store_dwordx2 %0, %1, %2 sc1" :: "v"(voff), "v"(v), "s"(sbase) : "memory");
}
__device__ __forceinline__ void st4_sc1(void* sbase, unsigned voff, unsigned v) {
    asm volatile("global_store_dword %0, %1, %2 sc1" :: "v"(voff), "v"(v), "s"(sbase) : "memory");
}
__device__ __forceinline__ void st8(void* sbase, unsigned voff, u32x2 v) {
    asm volatile("global_store_dwordx2 %0, %1, %2" :: "v"(voff), "v"(v), "s"(sbase) : "memory");
}
template <int IMM> __device__ __forceinline__ u32x4 ld16_nt(const void* sbase, unsigned voff) {
    return __builtin_nontemporal_load(reinterpret_cast<const u32x4*>(reinterpret_cast<const unsigned char*>(sbase) + voff + IMM));
}
__device__ __forceinline__ u32x4 ld16_v(const void* sbase, unsigned voff) {
    return *reinterpret_cast<const u32x4*>(reinterpret_cast<const unsigned char*>(sbase) + voff);
}
__device__ __forceinline__ void dma16(const void* sbase, unsigned voff, unsigned lds_dst) {
    unsigned keep;
    asm volatile("s_mov_b32 %0, m0\n\ts_mov_b32 m0, %3\n\ts_nop 0\n\tglobal_load_lds_dwordx4 %1, %2" WPOL "\n\ts_mov_b32 m0, %0"
                 : "=&s"(keep) : "v"(voff), "s"(sbase), "s"(lds_dst) : "memory");
}
__device__ __forceinline__ void dma16p(const void* sbase, unsigned voff, unsigned lds_dst) {
    unsigned keep;
    asm volatile("s_mov_b32 %0, m0\n\ts_mov_b32 m0, %3\n\ts_nop 0\n\tglobal_load_lds_dwordx4 %1, %2\n\ts_mov_b32 m0, %0"
                 : "=&s"(keep) : "v"(voff), "s"(sbase), "s"(lds_dst) : "memory");
}
__device__ __forceinline__ void dma4(const void* sbase, unsigned voff, unsigned lds_dst) {
    unsigned keep;
    asm volatile("s_mov_b32 %0, m0\n\ts_mov_b32 m0, %3\n\ts_nop 0\n\tglobal_load_lds_dword %1, %2\n\ts_mov_b32 m0, %0"
                 : "=&s"(keep) : "v"(voff), "s"(sbase), "s"(lds_dst) : "memory");
}
__device__ __forceinline__ void dma4n(const void* sbase, unsigned voff, unsigned lds_dst) {
    unsigned keep;
    asm volatile("s_mov_b32 %0, m0\n\ts_mov_b32 m0, %3\n\ts_nop 0\n\tglobal_load_lds_dword %1, %2 nt\n\ts_mov_b32 m0, %0"
                 : "=&s"(keep) : "v"(voff), "s"(sbase), "s"(lds_dst) : "memory");
}
#define KEEPY " sc1"
__device__ __forceinline__ void pull(const void* sbase, unsigned voff, unsigned lds_dst) {
    unsigned keep;
    asm volatile("s_mov_b32 %0, m0\n\ts_mov_b32 m0, %3\n\ts_nop 0\n\tglobal_load_lds_dword %1, %2 nt\n\tglobal_load_lds_dword %1, %2" KEEPY "\n\ts_mov_b32 m0, %0"
                 : "=&s"(keep) : "v"(voff), "s"(sbase), "s"(lds_dst) : "memory");
}
__device__ __forceinline__ unsigned poll4(void* sbase, unsigned voff) {
    unsigned r;
    asm volatile("global_load_dword %0, %1, %2 sc1\n\ts_waitcnt vmcnt(0)" : "=v"(r) : "v"(voff), "s"(sbase) : "memory");
    return r;
}
__device__ __forceinline__ unsigned enc(unsigned w) { return w < 0xfffffffeu ? w : 0xfffffffeu; }
__device__ __forceinline__ u32x4 enc4(f32x4 c) { return (u32x4){enc(__float_as_uint(c[0])), enc(__float_as_uint(c[1])), enc(__float_as_uint(c[2])), enc(__float_as_uint(c[3]))}; }
__device__ __forceinline__ unsigned mx4(u32x4 a) { return umax(umax(umax(a[0], a[1]), a[2]), a[3]); }
__device__ __forceinline__ unsigned mx4(u32x4 a, unsigned m) { return umax(umax(umax(umax(m, a[0]), a[1]), a[2]), a[3]); }
__device__ __forceinline__ bool allok(unsigned m) { return __builtin_amdgcn_ballot_w64(m == 0xffffffffu) == 0ull; }
__device__ __forceinline__ u32x4 tag4(u32x4 a, unsigned t8) { return (a & 0xffffff00u) | t8; }
__device__ __forceinline__ unsigned or4(u32x4 a) { return a[0] | a[1] | a[2] | a[3]; }
__device__ __forceinline__ bool tagok(unsigned m) { return __builtin_amdgcn_ballot_w64((m & 0xffu) != 0u) == 0ull; }
__device__ __forceinline__ f32x4 asf(u32x4 a) { return __builtin_bit_cast(f32x4, a); }

__device__ __forceinline__ bf16x4 tr64(unsigned lds_addr) { return __builtin_amdgcn_ds_read_tr16_b64_v4bf16((LAS bf16x4*)(size_t)lds_addr); }
__device__ __forceinline__ bf16x8 cat8(bf16x4 a, bf16x4 b) { return __builtin_shufflevector(a, b, 0, 1, 2, 3, 4, 5, 6, 7); }
__device__ __forceinline__ f32x4 mfma32(bf16x8 a, bf16x8 b, f32x4 c) { return __builtin_amdgcn_mfma_f32_16x16x32_bf16(a, b, c, 0, 0, 0); }
__device__ __forceinline__ f32x4 mfma32u(u32x4 a, u32x4 b, f32x4 c) { return mfma32(__builtin_bit_cast(bf16x8, a), __builtin_bit_cast(bf16x8, b), c); }
__device__ __forceinline__ f32x4 mfma32w(bf16x8 a, u32x4 b, f32x4 c) { return mfma32(a, __builtin_bit_cast(bf16x8, b), c); }
__device__ __forceinline__ f32x4 mfma16(bf16x4 a, u32x2 b, f32x4 c) {
    return __builtin_amdgcn_mfma_f32_16x16x16bf16_1k(__builtin_bit_cast(s16x4, a), __builtin_bit_cast(s16x4, b), c, 0, 0, 0);
}
__device__ __forceinline__ unsigned now32() { return (unsigned)__builtin_amdgcn_s_memrealtime(); }
#define WSL 1
__device__ __forceinline__ void wait_until(unsigned t) {
    asm volatile("s_branch 2f\n1:\n\ts_sleep 2\n2:\n\ts_memrealtime vcc\n\ts_waitcnt lgkmcnt(0)\n\ts_sub_i32 vcc_lo, vcc_lo, %0\n\ts_cmp_lt_i32 vcc_lo, -14\n\ts_cbranch_scc1 1b\n\ts_cmp_lt_i32 vcc_lo, 0\n\ts_cbranch_scc0 4f\n"
                 "3:\n\ts_memrealtime vcc\n\ts_waitcnt lgkmcnt(0)\n\ts_sub_i32 vcc_lo, vcc_lo, %0\n\ts_cmp_lt_i32 vcc_lo, 0\n\ts_cbranch_scc1 3b\n4:" :: "s"(t) : "memory", "scc", "vcc");
}
__device__ __forceinline__ unsigned lds_ld(unsigned addr) { unsigned t; asm volatile("ds_read_b32 %0, %1\n\ts_waitcnt lgkmcnt(0)\n\ts_nop 0" : "=v"(t) : "v"(addr) : "memory"); return __builtin_amdgcn_readfirstlane(t); }
__device__ __forceinline__ void lds_wait_ge(unsigned addr, unsigned v) {
#pragma unroll 1
    for (unsigned spins = 0; spins < (kSpinCap << 6); ++spins) { if (lds_ld(addr) >= v) break; __builtin_amdgcn_s_sleep(1); }
}

#define STAMPS 0
#define STAMP(i) do {} while (0)
#define STAMPV(i, t) do {} while (0)
#define RETRY(i, n) do {} while (0)
#define RETRY_ASM ""
#define RETRY_ASM3 ""
#define RETRY_ASM2 ""

#define CY(v) do {} while (0)
#define CYW(slot, a, b) do {} while (0)
#define CYS(slot, a, b) do {} while (0)
__device__ __forceinline__ int swz(int r) { return r ^ (((r >> 3) & 1) << 2); }
__device__ __forceinline__ f32x2 unpk(unsigned w) { return (f32x2){lo16(w), hi16(w)}; }
__device__ __forceinline__ unsigned pk2v(f32x2 v) { return __builtin_bit_cast(unsigned, __builtin_convertvector(v, bf16x2)); }
__device__ __forceinline__ float frcp(float x) { return __builtin_amdgcn_rcpf(x); }
__device__ __forceinline__ float fexp(float x) { return __builtin_amdgcn_exp2f(x * L2E); }

__device__ __forceinline__ void ln_consts(float mean, float var, float& mb, float& rden) {
    const unsigned mv = pk2(mean, __builtin_amdgcn_fmed3f(var, 0.f, 3.0e38f));
    mb = lo16(mv);
    rden = frcp(bfr(__builtin_amdgcn_sqrtf(bfr(hi16(mv) + 1e-5f))));
}
__device__ __forceinline__ void row_stats(u32x4 a, u32x4 c, int lane, float& mb, float& rden) {
    const float c0 = __uint_as_float((unsigned)__builtin_amdgcn_readfirstlane((int)a[0]) << 16);
    const f32x2 c2 = {c0, c0};
    f32x2 s2 = {0.f, 0.f}, q2 = s2, t2 = s2, u2 = s2;
#pragma unroll
    for (int i = 0; i < 4; ++i) { const f32x2 d = unpk(a[i]) - c2, e = unpk(c[i]) - c2; s2 += d; q2 += d * d; t2 += e; u2 += e * e; }
    float s = s2[0] + s2[1], q = q2[0] + q2[1];
    if (lane < 16) { s += t2[0] + t2[1]; q += u2[0] + u2[1]; }
    s = wave_sum(s); q = wave_sum(q);
    const float ms = s * (1.f / DM);
    ln_consts(c0 + ms, (q - s * ms) * (1.f / DM), mb, rden);
}
__device__ __forceinline__ float red3(f32x4 v) { float z = sw16_add(sw32_add(v[0], v[2]), sw32_add(v[1], v[3])); return z + dpp<0x128>(z); }
__device__ __forceinline__ float red4(f32x4 v) { float z = red3(v); return z + dpp<0x124>(z); }
__device__ __forceinline__ float sum_x4(float v) { v += dpp<0x124>(v); v += dpp<0x128>(v); v = sw16_add(v, v); return sw32_add(v, v); }
__device__ __forceinline__ float dot2(unsigned a, unsigned b, float c) { return __builtin_amdgcn_fdot2_f32_bf16(__builtin_bit_cast(bf16x2, a), __builtin_bit_cast(bf16x2, b), c, false); }
__device__ __forceinline__ int kpos(int r) { return (r & 7) | (((r >> 5) & 3) << 3) | (((r >> 3) & 3) << 5); }
__device__ __forceinline__ int gpos(int r) { const int qt = r / 160, i = r - 160 * qt; return 160 * qt + (i < 128 ? kpos(i) : i); }
__device__ __forceinline__ float vmax3(float a, float b, float c) { float r; asm("v_max3_f32 %0, %1, %2, %3" : "=v"(r) : "v"(a), "v"(b), "v"(c)); return r; }
template <unsigned LO, unsigned HI> __device__ __forceinline__ void st16_m(void* sbase, unsigned voff, u32x4 v) {
    asm volatile("s_mov_b32 exec_lo, %3\n\ts_mov_b32 exec_hi, %4\n\tglobal_store_dwordx4 %0, %1, %2 sc1\n\ts_mov_b64 exec, -1\n\ts_nop 0" :: "v"(voff), "v"(v), "s"(sbase), "i"(LO), "i"(HI) : "memory");
}
template <unsigned LO, unsigned HI> __device__ __forceinline__ void st4_m(void* sbase, unsigned voff, unsigned v) {
    asm volatile("s_mov_b32 exec_lo, %3\n\ts_mov_b32 exec_hi, %4\n\tglobal_store_dword %0, %1, %2 sc1\n\ts_mov_b64 exec, -1" :: "v"(voff), "v"(v), "s"(sbase), "i"(LO), "i"(HI) : "memory");
}

#define GATHER(sid, LOADS, OK) do { LOADS; if (__builtin_expect(!(OK), 0)) { for (unsigned spins_ = 1; spins_ < kSpinCap; ++spins_) { RETRY(sid, spins_); __builtin_amdgcn_s_sleep(1); LOADS; if (OK) break; } } } while (0)
#define SCH(i) (sched_c[i])
#define PRIO_MODE 0
#define TAILN 8
#define ABL 0
#define DIRECT 0
#define GMODE 0
#define GP_ 0
#define GID blockIdx.x
#define H1ROW 1
#define P_BA 3
#define WO_AT 7

#define ASM_QK \
    "ds_read_b128 v[48:51], %1 offset:0\n\t" \
    "ds_read_b128 v[52:55], %1 offset:128\n\t" \
    "ds_read_b128 v[56:59], %1 offset:256\n\t" \
    "v_mov_b32 v32, 0\n\t" \
    "v_mov_b32 v33, 0\n\t" \
    "v_mov_b32 v36, 0\n\t" \
    "v_mov_b32 v37, 0\n\t" \
    "v_mov_b32 v40, 0\n\t" \
    "v_mov_b32 v41, 0\n\t" \
    "v_mov_b32 v44, 0\n\t" \
    "v_mov_b32 v45, 0\n\t" \
    "s_waitcnt lgkmcnt(2)\n\t" \
    "v_smfmac_f32_16x16x64_bf16 v[32:35], v[48:51], v[64:71], %2\n\t" \
    "v_smfmac_f32_16x16x64_bf16 v[36:39], v[48:51], v[88:95], %2\n\t" \
    "v_smfmac_f32_16x16x64_bf16 v[40:43], v[48:51], v[112:119], %2\n\t" \
    "v_smfmac_f32_16x16x64_bf16 v[44:47], v[48:51], v[136:143], %2\n\t" \
    "s_waitcnt lgkmcnt(1)\n\t" \
    "v_smfmac_f32_16x16x64_bf16 v[32:35], v[52:55], v[72:79], %2\n\t" \
    "v_smfmac_f32_16x16x64_bf16 v[36:39], v[52:55], v[96:103], %2\n\t" \
    "v_smfmac_f32_16x16x64_bf16 v[40:43], v[52:55], v[120:127], %2\n\t" \
    "v_smfmac_f32_16x16x64_bf16 v[44:47], v[52:55], v[144:151], %2\n\t" \
    "s_waitcnt lgkmcnt(0)\n\t" \
    "v_smfmac_f32_16x16x64_bf16 v[32:35], v[56:59], v[80:87], %2\n\t" \
    "v_smfmac_f32_16x16x64_bf16 v[36:39], v[56:59], v[104:111], %2\n\t" \
    "v_smfmac_f32_16x16x64_bf16 v[40:43], v[56:59], v[128:135], %2\n\t" \
    "v_smfmac_f32_16x16x64_bf16 v[44:47], v[56:59], v[152:159], %2\n\t" \
    "s_nop 4\n\t" \
    "v_add_f32_dpp %0, v32, v33 quad_perm:[0,1,2,3] row_mask:0x1 bank_mask:0xf\n\t" \
    "v_add_f32_dpp %0, v36, v37 quad_perm:[0,1,2,3] row_mask:0x2 bank_mask:0xf\n\t" \
    "v_add_f32_dpp %0, v40, v41 quad_perm:[0,1,2,3] row_mask:0x4 bank_mask:0xf\n\t" \
    "v_add_f32_dpp %0, v44, v45 quad_perm:[0,1,2,3] row_mask:0x8 bank_mask:0xf\n\t"
#define ASM_PVB0 \
    "s_nop 1\n\t" \
    "v_smfmac_f32_16x16x64_bf16 %0, %1, v[160:167], %6 abid:1\n\t" \
    "v_smfmac_f32_16x16x64_bf16 %0, %2, v[168:175], %6 abid:1\n\t" \
    "v_smfmac_f32_16x16x64_bf16 %0, %3, v[176:183], %6 abid:1\n\t" \
    "v_smfmac_f32_16x16x64_bf16 %0, %4, v[184:191], %6 abid:1\n\t" \
    "s_nop 7\n\t" \
    "ds_write_b128 %5, %0 offset:0\n\t"
#define ASM_PVB1 \
    "s_nop 1\n\t" \
    "v_smfmac_f32_16x16x64_bf16 %0, %1, v[192:199], %6 abid:1\n\t" \
    "v_smfmac_f32_16x16x64_bf16 %0, %2, v[200:207], %6 abid:1\n\t" \
    "v_smfmac_f32_16x16x64_bf16 %0, %3, v[208:215], %6 abid:1\n\t" \
    "v_smfmac_f32_16x16x64_bf16 %0, %4, v[216:223], %6 abid:1\n\t" \
    "s_nop 7\n\t" \
    "ds_write_b128 %5, %0 offset:1024\n\t"
#define ASM_PVB2 \
    "s_nop 1\n\t" \
    "v_smfmac_f32_16x16x64_bf16 %0, %1, v[224:231], %6 abid:1\n\t" \
    "v_smfmac_f32_16x16x64_bf16 %0, %2, v[232:239], %6 abid:1\n\t" \
    "v_smfmac_f32_16x16x64_bf16 %0, %3, v[240:247], %6 abid:1\n\t" \
    "v_smfmac_f32_16x16x64_bf16 %0, %4, v[248:255], %6 abid:1\n\t" \
    "s_nop 7\n\t" \
    "ds_write_b128 %5, %0 offset:2048\n\t"
#define ASM_PV20 \
    "v_smfmac_f32_16x16x64_bf16 %0, %1, v[224:231], %6 abid:1\n\t"
#define ASM_PV21 \
    "v_smfmac_f32_16x16x64_bf16 %0, %2, v[232:239], %6 abid:1\n\t"
#define ASM_PV22 \
    "v_smfmac_f32_16x16x64_bf16 %0, %3, v[240:247], %6 abid:1\n\t"
#define ASM_PV23 \
    "v_smfmac_f32_16x16x64_bf16 %0, %4, v[248:255], %6 abid:1\n\t"
#define ASM_ATT_A \
    "s_setprio 2\n\t" \
    "v_mov_b32 v28, 0\n\t" \
    "v_mov_b32 v29, 0\n\t" \
    "v_mov_b32 v32, 0\n\t" \
    "v_mov_b32 v33, 0\n\t" \
    "v_mov_b32 v36, 0\n\t" \
    "v_mov_b32 v37, 0\n\t" \
    "v_mov_b32 v40, 0\n\t" \
    "v_mov_b32 v41, 0\n\t" \
    "v_mov_b32 v48, 0\n\t" \
    "v_mov_b32 v49, 0\n\t" \
    "v_mov_b32 v50, 0\n\t" \
    "v_mov_b32 v51, 0\n\t" \
    "v_mov_b32 v52, 0\n\t" \
    "v_mov_b32 v53, 0\n\t" \
    "v_mov_b32 v54, 0\n\t" \
    "v_mov_b32 v55, 0\n\t" \
    "v_mov_b32 v56, 0\n\t" \
    "v_mov_b32 v57, 0\n\t" \
    "v_mov_b32 v58, 0\n\t" \
    "v_mov_b32 v59, 0\n\t" \
    "v_mov_b32 v16, 0\n\t" \
    "v_mov_b32 v17, 0\n\t" \
    "v_mov_b32 v18, 0\n\t" \
    "v_mov_b32 v19, 0\n\t" \
    "v_mov_b32 v20, %[ma]\n\t" \
    "s_cmp_eq_u32 %[tz], 0\n\t" \
    "s_cbranch_scc1 23f\n\t" \
    "s_branch 22f\n\t" \
    "21:\n\t" \
    "s_sleep 3\n\t" \
    "22:\n\t" \
    "s_memrealtime vcc\n\t" \
    "s_waitcnt lgkmcnt(0)\n\t" \
    "s_sub_i32 vcc_lo, vcc_lo, %[tz]\n\t" \
    "s_cmp_lt_i32 vcc_lo, 0\n\t" \
    "s_cbranch_scc1 21b\n\t" \
    "23:\n\t" \
    "s_mov_b32 vcc_hi, 0x400000\n\t" \
    "0:\n\t" \
    "ds_read_b32 v21, v20 offset:340\n\t" \
    "s_sub_u32 vcc_hi, vcc_hi, 1\n\t" \
    "s_cbranch_scc1 4f\n\t" \
    "s_waitcnt lgkmcnt(0)\n\t" \
    "v_readfirstlane_b32 %[st], v21\n\t" \
    "s_cmp_eq_u32 %[st], 0\n\t" \
    "s_cbranch_scc1 0b\n\t" \
    "s_add_u32 %[st], %[st], %[tg]\n\t" \
    "4:\n\t" \
    "s_memrealtime vcc\n\t" \
    "s_waitcnt lgkmcnt(0)\n\t" \
    "s_sub_i32 vcc_lo, vcc_lo, %[st]\n\t" \
    "s_cmp_lt_i32 vcc_lo, -4000\n\t" \
    "s_cbranch_scc1 10f\n\t" \
    "s_cmp_lt_i32 vcc_lo, 0\n\t" \
    "s_cbranch_scc1 4b\n\t" \
    "10:\n\t" \
    "s_mov_b32 exec_hi, 0xff\n\t" \
    "global_load_dwordx4 v[16:19], %[go], %[ws] sc1\n\t" \
    "s_mov_b32 exec_hi, -1\n\t" \
    "s_barrier\n\t" \
    "ds_read_b64 v[22:23], v20 offset:352\n\t" \
    "ds_read_b32 v24, %[cs]\n\t" \
    "s_mov_b32 %[st], 0x10000\n\t" \
    "s_waitcnt lgkmcnt(0)\n\t" \
    "v_mul_f32 v22, v22, v24\n\t" \
    "v_mul_f32_e64 v22, -v22, v23\n\t" \
    "5:\n\t" \
    "s_waitcnt vmcnt(0)\n\t" \
    "v_max3_u32 v24, v16, v17, v18\n\t" \
    "v_max_u32 v24, v24, v19\n\t" \
    "v_cmp_eq_u32 vcc, -1, v24\n\t" \
    "v_permlane32_swap_b32 v16, v18\n\t" \
    "v_permlane32_swap_b32 v17, v19\n\t" \
    "v_pk_add_f32 v[16:17], v[16:17], v[18:19]\n\t" \
    "s_nop 0\n\t" \
    "s_cbranch_vccnz 6f\n\t" \
    "7:\n\t" \
    "v_permlane16_swap_b32 v16, v17\n\t" \
    "v_add_f32 v16, v16, v17\n\t" \
    "s_nop 1\n\t" \
    "v_add_f32_dpp v16, v16, v16 row_ror:8 row_mask:0xf bank_mask:0xf\n\t" \
    "v_fmac_f32 v22, v16, v23\n\t" \
    "v_cvt_pk_bf16_f32 v22, v22, v22\n\t" \
    "ds_write_b16 %[qo], v22\n\t" \
    "s_waitcnt lgkmcnt(0)\n\t" \
    "s_barrier\n\t" \
    "ds_read_b128 v[16:19], %[qa] offset:0\n\t" \
    "ds_read_b128 v[20:23], %[qa] offset:128\n\t" \
    "ds_read_b128 v[24:27], %[qa] offset:256\n\t" \
    "s_waitcnt lgkmcnt(2)\n\t" \
    "v_smfmac_f32_16x16x64_bf16 v[28:31], v[16:19], v[64:71], v7\n\t" \
    "v_smfmac_f32_16x16x64_bf16 v[32:35], v[16:19], v[88:95], v7\n\t" \
    "v_smfmac_f32_16x16x64_bf16 v[36:39], v[16:19], v[112:119], v7\n\t" \
    "v_smfmac_f32_16x16x64_bf16 v[40:43], v[16:19], v[136:143], v7\n\t" \
    "s_waitcnt lgkmcnt(1)\n\t" \
    "v_smfmac_f32_16x16x64_bf16 v[28:31], v[20:23], v[72:79], v7\n\t" \
    "v_smfmac_f32_16x16x64_bf16 v[32:35], v[20:23], v[96:103], v7\n\t" \
    "v_smfmac_f32_16x16x64_bf16 v[36:39], v[20:23], v[120:127], v7\n\t" \
    "v_smfmac_f32_16x16x64_bf16 v[40:43], v[20:23], v[144:151], v7\n\t" \
    "s_waitcnt lgkmcnt(0)\n\t" \
    "v_smfmac_f32_16x16x64_bf16 v[28:31], v[24:27], v[80:87], v7\n\t" \
    "v_smfmac_f32_16x16x64_bf16 v[32:35], v[24:27], v[104:111], v7\n\t" \
    "v_smfmac_f32_16x16x64_bf16 v[36:39], v[24:27], v[128:135], v7\n\t" \
    "v_smfmac_f32_16x16x64_bf16 v[40:43], v[24:27], v[152:159], v7\n\t" \
    "v_mov_b32 v63, %[mlw]\n\t" \
    "s_nop 3\n\t" \
    "v_add_f32_dpp v60, v28, v29 quad_perm:[0,1,2,3] row_mask:0x1 bank_mask:0xf\n\t" \
    "v_add_f32_dpp v60, v32, v33 quad_perm:[0,1,2,3] row_mask:0x2 bank_mask:0xf\n\t" \
    "v_add_f32_dpp v60, v36, v37 quad_perm:[0,1,2,3] row_mask:0x4 bank_mask:0xf\n\t" \
    "v_add_f32_dpp v60, v40, v41 quad_perm:[0,1,2,3] row_mask:0x8 bank_mask:0xf\n\t" \
    "v_mul_f32 v60, 0x3dd53b94, v60\n\t" \
    "s_nop 1\n\t" \
    "v_max_f32_dpp v61, v60, v60 quad_perm:[1,0,3,2] row_mask:0xf bank_mask:0xf\n\t" \
    "s_nop 1\n\t" \
    "v_max_f32_dpp v61, v61, v61 quad_perm:[2,3,0,1] row_mask:0xf bank_mask:0xf\n\t" \
    "s_nop 1\n\t" \
    "v_max_f32_dpp v61, v61, v61 row_half_mirror row_mask:0xf bank_mask:0xf\n\t" \
    "s_nop 1\n\t" \
    "v_max_f32_dpp v61, v61, v61 row_mirror row_mask:0xf bank_mask:0xf\n\t" \
    "s_nop 1\n\t" \
    "v_max_f32_dpp v61, v61, v61 row_bcast:15 row_mask:0xa bank_mask:0xf\n\t" \
    "s_nop 1\n\t" \
    "v_max_f32_dpp v61, v61, v61 row_bcast:31 row_mask:0xc bank_mask:0xf\n\t" \
    "s_nop 0\n\t" \
    "v_readlane_b32 %[st], v61, 63\n\t" \
    "s_mov_b32 exec_lo, 0\n\t" \
    "s_brev_b32 exec_hi, 1\n\t" \
    "ds_write_b32 v63, v61\n\t" \
    "s_mov_b64 exec, -1\n\t" \
    "v_subrev_f32 v62, %[st], v60\n\t" \
    "v_exp_f32 v62, v62\n\t" \
    "s_nop 0\n\t" \
    "v_cvt_pk_bf16_f32 v60, v62, v62\n\t" \
    "ds_write_b16 %[pw], v60\n\t" \
    "ds_write_b16 %[pw], v60 offset:20\n\t" \
    "ds_read_b128 v[16:19], %[pr] offset:0\n\t" \
    "ds_read_b128 v[20:23], %[pr] offset:272\n\t" \
    "ds_read_b128 v[24:27], %[pr] offset:544\n\t" \
    "ds_read_b128 v[28:31], %[pr] offset:816\n\t" \
    "v_mov_b32 v60, %[ma]\n\t" \
    "s_setprio 0\n\t" \
    "v_add_f32_dpp v62, v62, v62 quad_perm:[1,0,3,2] row_mask:0xf bank_mask:0xf\n\t" \
    "s_nop 1\n\t" \
    "v_add_f32_dpp v62, v62, v62 quad_perm:[2,3,0,1] row_mask:0xf bank_mask:0xf\n\t" \
    "s_nop 1\n\t" \
    "v_add_f32_dpp v62, v62, v62 row_half_mirror row_mask:0xf bank_mask:0xf\n\t" \
    "s_nop 1\n\t" \
    "v_add_f32_dpp v62, v62, v62 row_mirror row_mask:0xf bank_mask:0xf\n\t" \
    "s_waitcnt lgkmcnt(3)\n\t" \
    "v_smfmac_f32_16x16x64_bf16 v[48:51], v[16:19], v[160:167], v7 abid:1\n\t" \
    "v_smfmac_f32_16x16x64_bf16 v[52:55], v[16:19], v[192:199], v7 abid:1\n\t" \
    "v_smfmac_f32_16x16x64_bf16 v[56:59], v[16:19], v[224:231], v7 abid:1\n\t" \
    "v_add_f32_dpp v62, v62, v62 row_bcast:15 row_mask:0xa bank_mask:0xf\n\t" \
    "s_waitcnt lgkmcnt(2)\n\t" \
    "v_smfmac_f32_16x16x64_bf16 v[48:51], v[20:23], v[168:175], v7 abid:1\n\t" \
    "v_smfmac_f32_16x16x64_bf16 v[52:55], v[20:23], v[200:207], v7 abid:1\n\t" \
    "v_smfmac_f32_16x16x64_bf16 v[56:59], v[20:23], v[232:239], v7 abid:1\n\t" \
    "v_add_f32_dpp v62, v62, v62 row_bcast:31 row_mask:0xc bank_mask:0xf\n\t" \
    "s_waitcnt lgkmcnt(1)\n\t" \
    "v_smfmac_f32_16x16x64_bf16 v[48:51], v[24:27], v[176:183], v7 abid:1\n\t" \
    "v_smfmac_f32_16x16x64_bf16 v[52:55], v[24:27], v[208:215], v7 abid:1\n\t" \
    "v_smfmac_f32_16x16x64_bf16 v[56:59], v[24:27], v[240:247], v7 abid:1\n\t" \
    "s_waitcnt lgkmcnt(0)\n\t" \
    "v_smfmac_f32_16x16x64_bf16 v[48:51], v[28:31], v[184:191], v7 abid:1\n\t" \
    "v_smfmac_f32_16x16x64_bf16 v[52:55], v[28:31], v[216:223], v7 abid:1\n\t" \
    "v_smfmac_f32_16x16x64_bf16 v[56:59], v[28:31], v[248:255], v7 abid:1\n\t" \
    "s_barrier\n\t" \
    "ds_read_b128 v[16:19], v60\n\t" \
    "ds_read_b128 v[20:23], v60 offset:16\n\t" \
    "ds_read_b32 v24, %[mo]\n\t" \
    "s_mov_b32 exec_lo, 0\n\t" \
    "s_brev_b32 exec_hi, 1\n\t" \
    "ds_write_b32 v63, v62 offset:32\n\t" \
    "s_mov_b64 exec, -1\n\t" \
    "v_mov_b32_dpp v48, v52 quad_perm:[0,1,2,3] row_mask:0xc bank_mask:0xf\n\t" \
    "v_mov_b32_dpp v49, v53 quad_perm:[0,1,2,3] row_mask:0xc bank_mask:0xf\n\t" \
    "v_mov_b32_dpp v50, v54 quad_perm:[0,1,2,3] row_mask:0xc bank_mask:0xf\n\t" \
    "v_mov_b32_dpp v51, v55 quad_perm:[0,1,2,3] row_mask:0xc bank_mask:0xf\n\t" \
    "ds_write_b128 %[ow], v[48:51] offset:0\n\t" \
    "s_mov_b32 exec_lo, 0x00ff00ff\n\t" \
    "s_mov_b32 exec_hi, 0xff00ff00\n\t" \
    "ds_write_b128 %[ow], v[56:59] offset:2048\n\t" \
    "s_mov_b64 exec, -1\n\t" \
    "s_waitcnt lgkmcnt(3)\n\t" \
    "v_max3_f32 v16, v16, v17, v18\n\t" \
    "v_max3_f32 v19, v19, v20, v21\n\t" \
    "v_max3_f32 v16, v16, v22, v23\n\t" \
    "v_max_f32 v26, v16, v19\n\t" \
    "v_sub_f32 v60, v24, v26\n\t" \
    "v_exp_f32 v60, v60\n\t" \
    "s_nop 0\n\t" \
    "ds_write2_b32 %[mo], v60, v26 offset0:96 offset1:104\n\t" \
    "s_waitcnt lgkmcnt(0)\n\t" \
    "s_barrier\n\t" \
    "ds_read_b128 v[16:19], %[r0]\n\t" \
    "ds_read_b128 v[20:23], %[r0] offset:512\n\t" \
    "s_waitcnt lgkmcnt(0)\n\t" \
    "v_pk_add_f32 v[16:17], v[16:17], v[20:21]\n\t" \
    "v_pk_add_f32 v[18:19], v[18:19], v[22:23]\n\t" \
    "v_pk_mul_f32 v[16:17], v[16:17], v[60:61] op_sel_hi:[1,0]\n\t" \
    "v_pk_mul_f32 v[18:19], v[18:19], v[60:61] op_sel_hi:[1,0]\n\t" \
    "s_nop 1\n\t" \
    "v_permlane32_swap_b32 v16, v18\n\t" \
    "v_permlane32_swap_b32 v17, v19\n\t" \
    "v_pk_add_f32 v[16:17], v[16:17], v[18:19]\n\t" \
    "s_nop 1\n\t" \
    "v_permlane16_swap_b32 v16, v17\n\t" \
    "v_add_f32 v16, v16, v17\n\t" \
    "s_nop 1\n\t" \
    "v_add_f32_dpp v17, v16, v16 row_ror:8 row_mask:0xf bank_mask:0xf\n\t" \
    "v_add_f32_dpp v16, v16, v16 row_ror:8 row_mask:0xf bank_mask:0xf\n\t" \
    "s_nop 1\n\t" \
    "v_permlane16_swap_b32 v16, v17\n\t" \
    "s_mov_b32 exec_lo, 0xff\n\t" \
    "v_cvt_pk_bf16_f32 v16, v16, v17\n\t" \
    "s_mov_b32 exec_hi, 0xff\n\t" \
    "v_min_u32 v16, -2, v16\n\t" \
    "global_store_dword %[so], v16, %[ws] sc1\n\t" \
    "s_mov_b64 exec, -1\n\t" \
    "s_branch 2f\n\t" \
    "6:\n\t" \
    "s_sub_u32 %[st], %[st], 1\n\t" \
    "s_cbranch_scc1 7b\n\t" \
    RETRY_ASM \
    "v_mov_b32 v16, 0\n\t" \
    "v_mov_b32 v17, 0\n\t" \
    "v_mov_b32 v18, 0\n\t" \
    "v_mov_b32 v19, 0\n\t" \
    "s_mov_b32 exec_hi, 0xff\n\t" \
    "global_load_dwordx4 v[16:19], %[go], %[ws] sc1\n\t" \
    "s_mov_b32 exec_hi, -1\n\t" \
    "s_branch 5b\n\t" \
    "2:\n\t"
#define ASM_ATT_B \
    "s_setprio 1\n\t" \
    "v_mov_b32 v28, 0\n\t" \
    "v_mov_b32 v29, 0\n\t" \
    "v_mov_b32 v32, 0\n\t" \
    "v_mov_b32 v33, 0\n\t" \
    "v_mov_b32 v36, 0\n\t" \
    "v_mov_b32 v37, 0\n\t" \
    "v_mov_b32 v40, 0\n\t" \
    "v_mov_b32 v41, 0\n\t" \
    "v_mov_b32 v48, 0\n\t" \
    "v_mov_b32 v49, 0\n\t" \
    "v_mov_b32 v50, 0\n\t" \
    "v_mov_b32 v51, 0\n\t" \
    "v_mov_b32 v52, 0\n\t" \
    "v_mov_b32 v53, 0\n\t" \
    "v_mov_b32 v54, 0\n\t" \
    "v_mov_b32 v55, 0\n\t" \
    "v_mov_b32 v56, 0\n\t" \
    "v_mov_b32 v57, 0\n\t" \
    "v_mov_b32 v58, 0\n\t" \
    "v_mov_b32 v59, 0\n\t" \
    "v_mov_b32 v44, -1\n\t" \
    "v_mov_b32 v45, -1\n\t" \
    "v_mov_b32 v46, -1\n\t" \
    "v_mov_b32 v47, -1\n\t" \
    "s_cmp_gt_u32 %[wv], 5\n\t" \
    "s_cbranch_scc0 8f\n\t" \
    "s_cmp_eq_u32 %[wv], 7\n\t" \
    "s_cbranch_scc1 9f\n\t" \
    "s_barrier\n\t" \
    "s_branch 9f\n\t" \
    "8:\n\t" \
    "v_mov_b32 v16, 0\n\t" \
    "v_mov_b32 v17, 0\n\t" \
    "v_mov_b32 v18, 0\n\t" \
    "v_mov_b32 v19, 0\n\t" \
    "v_mov_b32 v20, %[ma]\n\t" \
    "s_cmp_eq_u32 %[tz], 0\n\t" \
    "s_cbranch_scc1 23f\n\t" \
    "s_branch 22f\n\t" \
    "21:\n\t" \
    "s_sleep 3\n\t" \
    "22:\n\t" \
    "s_memrealtime vcc\n\t" \
    "s_waitcnt lgkmcnt(0)\n\t" \
    "s_sub_i32 vcc_lo, vcc_lo, %[tz]\n\t" \
    "s_cmp_lt_i32 vcc_lo, 0\n\t" \
    "s_cbranch_scc1 21b\n\t" \
    "23:\n\t" \
    "s_mov_b32 vcc_hi, 0x400000\n\t" \
    "0:\n\t" \
    "ds_read_b32 v21, v20 offset:340\n\t" \
    "s_sub_u32 vcc_hi, vcc_hi, 1\n\t" \
    "s_cbranch_scc1 4f\n\t" \
    "s_waitcnt lgkmcnt(0)\n\t" \
    "v_readfirstlane_b32 %[st], v21\n\t" \
    "s_cmp_eq_u32 %[st], 0\n\t" \
    "s_cbranch_scc1 0b\n\t" \
    "s_add_u32 %[st], %[st], %[tg]\n\t" \
    "4:\n\t" \
    "s_memrealtime vcc\n\t" \
    "s_waitcnt lgkmcnt(0)\n\t" \
    "s_sub_i32 vcc_lo, vcc_lo, %[st]\n\t" \
    "s_cmp_lt_i32 vcc_lo, -4000\n\t" \
    "s_cbranch_scc1 10f\n\t" \
    "s_cmp_lt_i32 vcc_lo, 0\n\t" \
    "s_cbranch_scc1 4b\n\t" \
    "10:\n\t" \
    "s_mov_b32 exec_hi, 0xff\n\t" \
    "global_load_dwordx4 v[16:19], %[go], %[ws] sc1\n\t" \
    "s_mov_b32 exec_hi, -1\n\t" \
    "s_barrier\n\t" \
    "ds_read_b64 v[22:23], v20 offset:352\n\t" \
    "ds_read_b32 v24, %[cs]\n\t" \
    "s_mov_b32 %[st], 0x10000\n\t" \
    "s_waitcnt lgkmcnt(0)\n\t" \
    "v_mul_f32 v22, v22, v24\n\t" \
    "v_mul_f32_e64 v22, -v22, v23\n\t" \
    "5:\n\t" \
    "s_waitcnt vmcnt(0)\n\t" \
    "v_max3_u32 v24, v16, v17, v18\n\t" \
    "v_max_u32 v24, v24, v19\n\t" \
    "v_cmp_eq_u32 vcc, -1, v24\n\t" \
    "v_permlane32_swap_b32 v16, v18\n\t" \
    "v_permlane32_swap_b32 v17, v19\n\t" \
    "v_pk_add_f32 v[16:17], v[16:17], v[18:19]\n\t" \
    "s_nop 0\n\t" \
    "s_cbranch_vccnz 6f\n\t" \
    "7:\n\t" \
    "v_permlane16_swap_b32 v16, v17\n\t" \
    "v_add_f32 v16, v16, v17\n\t" \
    "s_nop 1\n\t" \
    "v_add_f32_dpp v16, v16, v16 row_ror:8 row_mask:0xf bank_mask:0xf\n\t" \
    "v_fmac_f32 v22, v16, v23\n\t" \
    "v_cvt_pk_bf16_f32 v22, v22, v22\n\t" \
    "ds_write_b16 %[qo], v22\n\t" \
    "9:\n\t" \
    "s_waitcnt lgkmcnt(0)\n\t" \
    "s_barrier\n\t" \
    "ds_read_b128 v[16:19], %[qa] offset:0\n\t" \
    "ds_read_b128 v[20:23], %[qa] offset:128\n\t" \
    "ds_read_b128 v[24:27], %[qa] offset:256\n\t" \
    "s_cmp_eq_u32 %[cl], 0\n\t" \
    "s_cbranch_scc1 3f\n\t" \
    "global_store_dwordx4 %[go], v[44:47], %[ws] offset:0 sc1\n\t" \
    "global_store_dwordx4 %[go], v[44:47], %[ws] offset:1024 sc1\n\t" \
    "global_store_dwordx4 %[go], v[44:47], %[ws] offset:2048 sc1\n\t" \
    "3:\n\t" \
    "s_waitcnt lgkmcnt(2)\n\t" \
    "v_smfmac_f32_16x16x64_bf16 v[28:31], v[16:19], v[64:71], v7\n\t" \
    "v_smfmac_f32_16x16x64_bf16 v[32:35], v[16:19], v[88:95], v7\n\t" \
    "v_smfmac_f32_16x16x64_bf16 v[36:39], v[16:19], v[112:119], v7\n\t" \
    "v_smfmac_f32_16x16x64_bf16 v[40:43], v[16:19], v[136:143], v7\n\t" \
    "s_waitcnt lgkmcnt(1)\n\t" \
    "v_smfmac_f32_16x16x64_bf16 v[28:31], v[20:23], v[72:79], v7\n\t" \
    "v_smfmac_f32_16x16x64_bf16 v[32:35], v[20:23], v[96:103], v7\n\t" \
    "v_smfmac_f32_16x16x64_bf16 v[36:39], v[20:23], v[120:127], v7\n\t" \
    "v_smfmac_f32_16x16x64_bf16 v[40:43], v[20:23], v[144:151], v7\n\t" \
    "s_waitcnt lgkmcnt(0)\n\t" \
    "v_smfmac_f32_16x16x64_bf16 v[28:31], v[24:27], v[80:87], v7\n\t" \
    "v_smfmac_f32_16x16x64_bf16 v[32:35], v[24:27], v[104:111], v7\n\t" \
    "v_smfmac_f32_16x16x64_bf16 v[36:39], v[24:27], v[128:135], v7\n\t" \
    "v_smfmac_f32_16x16x64_bf16 v[40:43], v[24:27], v[152:159], v7\n\t" \
    "v_mov_b32 v63, %[mlw]\n\t" \
    "s_nop 3\n\t" \
    "v_add_f32_dpp v60, v28, v29 quad_perm:[0,1,2,3] row_mask:0x1 bank_mask:0xf\n\t" \
    "v_add_f32_dpp v60, v32, v33 quad_perm:[0,1,2,3] row_mask:0x2 bank_mask:0xf\n\t" \
    "v_add_f32_dpp v60, v36, v37 quad_perm:[0,1,2,3] row_mask:0x4 bank_mask:0xf\n\t" \
    "v_add_f32_dpp v60, v40, v41 quad_perm:[0,1,2,3] row_mask:0x8 bank_mask:0xf\n\t" \
    "v_mul_f32 v60, 0x3dd53b94, v60\n\t" \
    "s_nop 1\n\t" \
    "v_max_f32_dpp v61, v60, v60 quad_perm:[1,0,3,2] row_mask:0xf bank_mask:0xf\n\t" \
    "s_nop 1\n\t" \
    "v_max_f32_dpp v61, v61, v61 quad_perm:[2,3,0,1] row_mask:0xf bank_mask:0xf\n\t" \
    "s_nop 1\n\t" \
    "v_max_f32_dpp v61, v61, v61 row_half_mirror row_mask:0xf bank_mask:0xf\n\t" \
    "s_nop 1\n\t" \
    "v_max_f32_dpp v61, v61, v61 row_mirror row_mask:0xf bank_mask:0xf\n\t" \
    "s_nop 1\n\t" \
    "v_max_f32_dpp v61, v61, v61 row_bcast:15 row_mask:0xa bank_mask:0xf\n\t" \
    "s_nop 1\n\t" \
    "v_max_f32_dpp v61, v61, v61 row_bcast:31 row_mask:0xc bank_mask:0xf\n\t" \
    "s_nop 0\n\t" \
    "v_readlane_b32 %[st], v61, 63\n\t" \
    "s_mov_b32 exec_lo, 0\n\t" \
    "s_brev_b32 exec_hi, 1\n\t" \
    "ds_write_b32 v63, v61\n\t" \
    "s_mov_b64 exec, -1\n\t" \
    "v_subrev_f32 v62, %[st], v60\n\t" \
    "v_exp_f32 v62, v62\n\t" \
    "s_nop 0\n\t" \
    "v_cvt_pk_bf16_f32 v60, v62, v62\n\t" \
    "ds_write_b16 %[pw], v60\n\t" \
    "ds_write_b16 %[pw], v60 offset:20\n\t" \
    "ds_read_b128 v[16:19], %[pr] offset:0\n\t" \
    "ds_read_b128 v[20:23], %[pr] offset:272\n\t" \
    "ds_read_b128 v[24:27], %[pr] offset:544\n\t" \
    "ds_read_b128 v[28:31], %[pr] offset:816\n\t" \
    "v_mov_b32 v60, %[ma]\n\t" \
    "s_setprio 0\n\t" \
    "v_add_f32_dpp v62, v62, v62 quad_perm:[1,0,3,2] row_mask:0xf bank_mask:0xf\n\t" \
    "s_nop 1\n\t" \
    "v_add_f32_dpp v62, v62, v62 quad_perm:[2,3,0,1] row_mask:0xf bank_mask:0xf\n\t" \
    "s_nop 1\n\t" \
    "v_add_f32_dpp v62, v62, v62 row_half_mirror row_mask:0xf bank_mask:0xf\n\t" \
    "s_nop 1\n\t" \
    "v_add_f32_dpp v62, v62, v62 row_mirror row_mask:0xf bank_mask:0xf\n\t" \
    "s_waitcnt lgkmcnt(3)\n\t" \
    "s_barrier\n\t" \
    "v_smfmac_f32_16x16x64_bf16 v[48:51], v[16:19], v[160:167], v7 abid:1\n\t" \
    "v_smfmac_f32_16x16x64_bf16 v[52:55], v[16:19], v[192:199], v7 abid:1\n\t" \
    "v_smfmac_f32_16x16x64_bf16 v[56:59], v[16:19], v[224:231], v7 abid:1\n\t" \
    "v_add_f32_dpp v62, v62, v62 row_bcast:15 row_mask:0xa bank_mask:0xf\n\t" \
    "s_waitcnt lgkmcnt(2)\n\t" \
    "v_smfmac_f32_16x16x64_bf16 v[48:51], v[20:23], v[168:175], v7 abid:1\n\t" \
    "v_smfmac_f32_16x16x64_bf16 v[52:55], v[20:23], v[200:207], v7 abid:1\n\t" \
    "v_smfmac_f32_16x16x64_bf16 v[56:59], v[20:23], v[232:239], v7 abid:1\n\t" \
    "v_add_f32_dpp v62, v62, v62 row_bcast:31 row_mask:0xc bank_mask:0xf\n\t" \
    "s_waitcnt lgkmcnt(1)\n\t" \
    "v_smfmac_f32_16x16x64_bf16 v[48:51], v[24:27], v[176:183], v7 abid:1\n\t" \
    "v_smfmac_f32_16x16x64_bf16 v[52:55], v[24:27], v[208:215], v7 abid:1\n\t" \
    "v_smfmac_f32_16x16x64_bf16 v[56:59], v[24:27], v[240:247], v7 abid:1\n\t" \
    "s_waitcnt lgkmcnt(0)\n\t" \
    "v_smfmac_f32_16x16x64_bf16 v[48:51], v[28:31], v[184:191], v7 abid:1\n\t" \
    "v_smfmac_f32_16x16x64_bf16 v[52:55], v[28:31], v[216:223], v7 abid:1\n\t" \
    "v_smfmac_f32_16x16x64_bf16 v[56:59], v[28:31], v[248:255], v7 abid:1\n\t" \
    "s_mov_b32 exec_lo, 0\n\t" \
    "s_brev_b32 exec_hi, 1\n\t" \
    "ds_write_b32 v63, v62 offset:32\n\t" \
    "s_mov_b64 exec, -1\n\t" \
    "s_cmp_gt_u32 %[wv], 5\n\t" \
    "s_nop 1\n\t" \
    "v_mov_b32_dpp v48, v52 quad_perm:[0,1,2,3] row_mask:0xc bank_mask:0xf\n\t" \
    "v_mov_b32_dpp v49, v53 quad_perm:[0,1,2,3] row_mask:0xc bank_mask:0xf\n\t" \
    "v_mov_b32_dpp v50, v54 quad_perm:[0,1,2,3] row_mask:0xc bank_mask:0xf\n\t" \
    "v_mov_b32_dpp v51, v55 quad_perm:[0,1,2,3] row_mask:0xc bank_mask:0xf\n\t" \
    "ds_write_b128 %[ow], v[48:51] offset:0\n\t" \
    "s_mov_b32 exec_lo, 0x00ff00ff\n\t" \
    "s_mov_b32 exec_hi, 0xff00ff00\n\t" \
    "ds_write_b128 %[ow], v[56:59] offset:2048\n\t" \
    "s_mov_b64 exec, -1\n\t" \
    "s_cbranch_scc1 1f\n\t" \
    "s_waitcnt lgkmcnt(0)\n\t" \
    "s_barrier\n\t" \
    "ds_read_b128 v[16:19], %[r0]\n\t" \
    "ds_read_b128 v[20:23], %[r0] offset:640\n\t" \
    "ds_read_b32 v60, %[mo] offset:384\n\t" \
    "s_waitcnt lgkmcnt(1)\n\t" \
    "v_pk_add_f32 v[16:17], v[16:17], v[20:21]\n\t" \
    "v_pk_add_f32 v[18:19], v[18:19], v[22:23]\n\t" \
    "s_waitcnt lgkmcnt(0)\n\t" \
    "v_pk_mul_f32 v[16:17], v[16:17], v[60:61] op_sel_hi:[1,0]\n\t" \
    "v_pk_mul_f32 v[18:19], v[18:19], v[60:61] op_sel_hi:[1,0]\n\t" \
    "s_nop 1\n\t" \
    "v_permlane32_swap_b32 v16, v18\n\t" \
    "v_permlane32_swap_b32 v17, v19\n\t" \
    "v_pk_add_f32 v[16:17], v[16:17], v[18:19]\n\t" \
    "s_nop 1\n\t" \
    "v_permlane16_swap_b32 v16, v17\n\t" \
    "v_add_f32 v16, v16, v17\n\t" \
    "s_nop 1\n\t" \
    "v_add_f32_dpp v17, v16, v16 row_ror:8 row_mask:0xf bank_mask:0xf\n\t" \
    "v_add_f32_dpp v16, v16, v16 row_ror:8 row_mask:0xf bank_mask:0xf\n\t" \
    "s_nop 1\n\t" \
    "v_permlane16_swap_b32 v16, v17\n\t" \
    "s_mov_b32 exec_lo, 0xff\n\t" \
    "v_cvt_pk_bf16_f32 v16, v16, v17\n\t" \
    "s_mov_b32 exec_hi, 0xff\n\t" \
    "v_min_u32 v16, -2, v16\n\t" \
    "global_store_dword %[so], v16, %[ws] sc1\n\t" \
    "s_mov_b64 exec, -1\n\t" \
    "s_branch 2f\n\t" \
    "6:\n\t" \
    "s_sub_u32 %[st], %[st], 1\n\t" \
    "s_cbranch_scc1 7b\n\t" \
    RETRY_ASM \
    "v_mov_b32 v16, 0\n\t" \
    "v_mov_b32 v17, 0\n\t" \
    "v_mov_b32 v18, 0\n\t" \
    "v_mov_b32 v19, 0\n\t" \
    "s_mov_b32 exec_hi, 0xff\n\t" \
    "global_load_dwordx4 v[16:19], %[go], %[ws] sc1\n\t" \
    "s_mov_b32 exec_hi, -1\n\t" \
    "s_branch 5b\n\t" \
    "1:\n\t" \
    "s_cmp_eq_u32 %[wv], 7\n\t" \
    "s_waitcnt vmcnt(0) lgkmcnt(0)\n\t" \
    "s_barrier\n\t" \
    "s_cbranch_scc1 2f\n\t" \
    "ds_read_b32 v16, %[mo] offset:32\n\t" \
    "ds_read_b32 v60, %[mo] offset:384\n\t" \
    "ds_read_b32 v26, %[mo] offset:416\n\t" \
    "s_waitcnt lgkmcnt(0)\n\t" \
    "v_min_u32 v26, -2, v26\n\t" \
    "v_mul_f32 v16, v16, v60\n\t" \
    "s_nop 1\n\t" \
    "v_add_f32_dpp v17, v16, v16 row_ror:8 row_mask:0xf bank_mask:0xf\n\t" \
    "v_add_f32_dpp v16, v16, v16 row_ror:8 row_mask:0xf bank_mask:0xf\n\t" \
    "s_nop 1\n\t" \
    "v_permlane16_swap_b32 v16, v17\n\t" \
    "v_add_f32 v18, v16, v17\n\t" \
    "v_add_f32 v16, v16, v17\n\t" \
    "s_nop 1\n\t" \
    "v_permlane32_swap_b32 v16, v18\n\t" \
    "s_mov_b64 exec, 3\n\t" \
    "v_add_f32 v27, v16, v18\n\t" \
    "v_min_u32 v27, -2, v27\n\t" \
    "global_store_dwordx2 %[so], v[26:27], %[ws] sc1\n\t" \
    "s_mov_b64 exec, -1\n\t" \
    "2:\n\t"
#define ASM_QP \
    "s_mov_b32 %[st], 0x10000\n\t" \
    "5:\n\t" \
    "ds_read_b64_tr_b16 v[16:17], %[a0] offset:0\n\t" \
    "ds_read_b64_tr_b16 v[18:19], %[a0] offset:1536\n\t" \
    "ds_read_b64_tr_b16 v[20:21], %[a1] offset:0\n\t" \
    "ds_read_b64_tr_b16 v[22:23], %[a1] offset:1536\n\t" \
    "ds_read_b64_tr_b16 v[24:25], %[a0] offset:12288\n\t" \
    "ds_read_b64_tr_b16 v[26:27], %[a0] offset:13824\n\t" \
    "ds_read_b64_tr_b16 v[28:29], %[a1] offset:12288\n\t" \
    "ds_read_b64_tr_b16 v[30:31], %[a1] offset:13824\n\t" \
    "ds_read_b64_tr_b16 v[32:33], %[a0] offset:24576\n\t" \
    "ds_read_b64_tr_b16 v[34:35], %[a0] offset:26112\n\t" \
    "ds_read_b64_tr_b16 v[36:37], %[a1] offset:24576\n\t" \
    "ds_read_b64_tr_b16 v[38:39], %[a1] offset:26112\n\t" \
    "ds_read_b64_tr_b16 v[40:41], %[a0] offset:36864\n\t" \
    "ds_read_b64_tr_b16 v[42:43], %[a0] offset:38400\n\t" \
    "ds_read_b64_tr_b16 v[44:45], %[a1] offset:36864\n\t" \
    "ds_read_b64_tr_b16 v[46:47], %[a1] offset:38400\n\t" \
    "v_mov_b32 v48, %[fl]\n\t" \
    "v_add_u32 v50, -52, v48\n\t" \
    "s_mov_b32 vcc_hi, 0x100000\n\t" \
    "s_branch 25f\n\t" \
    "24:\n\t" \
    "s_sleep 4\n\t" \
    "25:\n\t" \
    "ds_read_b32 v49, v50\n\t" \
    "s_sub_u32 vcc_hi, vcc_hi, 1\n\t" \
    "s_cbranch_scc1 23f\n\t" \
    "s_waitcnt lgkmcnt(0)\n\t" \
    "v_readfirstlane_b32 %[rf], v49\n\t" \
    "s_cmp_eq_u32 %[rf], 0\n\t" \
    "s_cbranch_scc1 24b\n\t" \
    "s_add_u32 %[rf], %[rf], %[tz]\n\t" \
    "s_branch 22f\n\t" \
    "21:\n\t" \
    "s_sleep 3\n\t" \
    "22:\n\t" \
    "s_memrealtime vcc\n\t" \
    "s_waitcnt lgkmcnt(0)\n\t" \
    "s_sub_i32 vcc_lo, vcc_lo, %[rf]\n\t" \
    "s_cmp_lt_i32 vcc_lo, 0\n\t" \
    "s_cbranch_scc1 21b\n\t" \
    "23:\n\t" \
    "s_mov_b32 vcc_hi, 0x400000\n\t" \
    "0:\n\t" \
    "ds_read_b32 v49, v48\n\t" \
    "s_sub_u32 vcc_hi, vcc_hi, 1\n\t" \
    "s_cbranch_scc1 10f\n\t" \
    "s_waitcnt lgkmcnt(0)\n\t" \
    "v_readfirstlane_b32 %[rf], v49\n\t" \
    "s_cmp_eq_u32 %[rf], 0\n\t" \
    "s_cbranch_scc1 0b\n\t" \
    "4:\n\t" \
    "s_memrealtime vcc\n\t" \
    "s_waitcnt lgkmcnt(0)\n\t" \
    "s_sub_i32 vcc_lo, vcc_lo, %[rf]\n\t" \
    "s_cmp_lt_i32 vcc_lo, 0\n\t" \
    "s_cbranch_scc1 4b\n\t" \
    "10:\n\t" \
    "global_load_dwordx4 v[48:51], %[gs], %[ws] sc1\n\t" \
    "s_waitcnt vmcnt(0)\n\t" \
    "v_mfma_f32_16x16x32_bf16 v[16:19], v[16:19], v[48:51], 0\n\t" \
    "v_mfma_f32_16x16x32_bf16 v[20:23], v[20:23], v[48:51], 0\n\t" \
    "v_mov_b32_dpp v52, v48 row_shl:4 row_mask:0xf bank_mask:0xf\n\t" \
    "v_mov_b32_dpp v53, v49 row_shl:4 row_mask:0xf bank_mask:0xf\n\t" \
    "v_mov_b32_dpp v54, v50 row_shl:4 row_mask:0xf bank_mask:0xf\n\t" \
    "v_mov_b32_dpp v55, v51 row_shl:4 row_mask:0xf bank_mask:0xf\n\t" \
    "v_mov_b32_dpp v56, v48 row_shl:8 row_mask:0xf bank_mask:0xf\n\t" \
    "v_mov_b32_dpp v57, v49 row_shl:8 row_mask:0xf bank_mask:0xf\n\t" \
    "v_mfma_f32_16x16x32_bf16 v[16:19], v[24:27], v[52:55], v[16:19]\n\t" \
    "v_mov_b32_dpp v58, v50 row_shl:8 row_mask:0xf bank_mask:0xf\n\t" \
    "v_mov_b32_dpp v59, v51 row_shl:8 row_mask:0xf bank_mask:0xf\n\t" \
    "v_mfma_f32_16x16x32_bf16 v[20:23], v[28:31], v[52:55], v[20:23]\n\t" \
    "v_mov_b32_dpp v52, v48 row_shl:12 row_mask:0xf bank_mask:0xf\n\t" \
    "v_mov_b32_dpp v53, v49 row_shl:12 row_mask:0xf bank_mask:0xf\n\t" \
    "v_mfma_f32_16x16x32_bf16 v[16:19], v[32:35], v[56:59], v[16:19]\n\t" \
    "v_mov_b32_dpp v54, v50 row_shl:12 row_mask:0xf bank_mask:0xf\n\t" \
    "v_mov_b32_dpp v55, v51 row_shl:12 row_mask:0xf bank_mask:0xf\n\t" \
    "v_mfma_f32_16x16x32_bf16 v[20:23], v[36:39], v[56:59], v[20:23]\n\t" \
    "v_max3_u32 v24, v48, v49, v50\n\t" \
    "v_max_u32 v24, v24, v51\n\t" \
    "v_mfma_f32_16x16x32_bf16 v[16:19], v[40:43], v[52:55], v[16:19]\n\t" \
    "v_cmp_eq_u32 vcc, -1, v24\n\t" \
    "v_mfma_f32_16x16x32_bf16 v[20:23], v[44:47], v[52:55], v[20:23]\n\t" \
    "s_mov_b32 exec_lo, 0x00ff00ff\n\t" \
    "s_mov_b32 exec_hi, 0x00ff00ff\n\t" \
    "s_cbranch_vccnz 6f\n\t" \
    "s_nop 4\n\t" \
    "v_mov_b32_dpp v16, v20 row_shr:4 row_mask:0xf bank_mask:0x2\n\t" \
    "v_mov_b32_dpp v17, v21 row_shr:4 row_mask:0xf bank_mask:0x2\n\t" \
    "v_mov_b32_dpp v18, v22 row_shr:4 row_mask:0xf bank_mask:0x2\n\t" \
    "v_mov_b32_dpp v19, v23 row_shr:4 row_mask:0xf bank_mask:0x2\n\t" \
    "v_min_u32 v16, -2, v16\n\t" \
    "v_min_u32 v17, -2, v17\n\t" \
    "v_min_u32 v18, -2, v18\n\t" \
    "v_min_u32 v19, -2, v19\n\t" \
    "global_store_dwordx4 %[so], v[16:19], %[ws] sc1\n\t" \
    "s_mov_b64 exec, -1\n\t" \
    "s_branch 2f\n\t" \
    "6:\n\t" \
    "s_mov_b64 exec, -1\n\t" \
    "s_sub_u32 %[st], %[st], 1\n\t" \
    "s_cbranch_scc1 2f\n\t" \
    RETRY_ASM \
    "s_sleep 6\n\t" \
    "s_branch 5b\n\t" \
    "2:\n\t"
#define ASM_ATT_P \
    "s_setprio 2\n\t" \
    "v_mov_b32 v28, 0\n\t" \
    "v_mov_b32 v29, 0\n\t" \
    "v_mov_b32 v32, 0\n\t" \
    "v_mov_b32 v33, 0\n\t" \
    "v_mov_b32 v36, 0\n\t" \
    "v_mov_b32 v37, 0\n\t" \
    "v_mov_b32 v40, 0\n\t" \
    "v_mov_b32 v41, 0\n\t" \
    "v_mov_b32 v48, 0\n\t" \
    "v_mov_b32 v49, 0\n\t" \
    "v_mov_b32 v50, 0\n\t" \
    "v_mov_b32 v51, 0\n\t" \
    "v_mov_b32 v52, 0\n\t" \
    "v_mov_b32 v53, 0\n\t" \
    "v_mov_b32 v54, 0\n\t" \
    "v_mov_b32 v55, 0\n\t" \
    "v_mov_b32 v56, 0\n\t" \
    "v_mov_b32 v57, 0\n\t" \
    "v_mov_b32 v58, 0\n\t" \
    "v_mov_b32 v59, 0\n\t" \
    "v_mov_b32 v16, 0\n\t" \
    "v_mov_b32 v17, 0\n\t" \
    "v_mov_b32 v18, 0\n\t" \
    "v_mov_b32 v19, 0\n\t" \
    "v_mov_b32 v20, %[ma]\n\t" \
    "s_mov_b32 vcc_hi, 0x400000\n\t" \
    "0:\n\t" \
    "ds_read_b32 v21, v20 offset:340\n\t" \
    "s_sub_u32 vcc_hi, vcc_hi, 1\n\t" \
    "s_cbranch_scc1 4f\n\t" \
    "s_waitcnt lgkmcnt(0)\n\t" \
    "v_readfirstlane_b32 %[rf], v21\n\t" \
    "s_cmp_eq_u32 %[rf], 0\n\t" \
    "s_cbranch_scc1 0b\n\t" \
    "s_add_u32 %[st], %[rf], %[tg]\n\t" \
    "4:\n\t" \
    "s_memrealtime vcc\n\t" \
    "s_waitcnt lgkmcnt(0)\n\t" \
    "s_sub_i32 vcc_lo, vcc_lo, %[st]\n\t" \
    "s_cmp_lt_i32 vcc_lo, -4000\n\t" \
    "s_cbranch_scc1 10f\n\t" \
    "s_cmp_lt_i32 vcc_lo, 0\n\t" \
    "s_cbranch_scc1 4b\n\t" \
    "10:\n\t" \
    "s_mov_b32 exec_hi, 0xff\n\t" \
    "global_load_dwordx4 v[16:19], %[go], %[ws] sc1\n\t" \
    "s_mov_b32 exec_hi, -1\n\t" \
    "s_barrier\n\t" \
    "ds_read_b64 v[22:23], v20 offset:352\n\t" \
    "ds_read_b32 v24, %[cs]\n\t" \
    "s_mov_b32 %[st], 0x10000\n\t" \
    "s_waitcnt lgkmcnt(0)\n\t" \
    "v_mul_f32 v22, v22, v24\n\t" \
    "v_mul_f32_e64 v22, -v22, v23\n\t" \
    "5:\n\t" \
    "s_waitcnt vmcnt(0)\n\t" \
    "v_max3_u32 v24, v16, v17, v18\n\t" \
    "v_max_u32 v24, v24, v19\n\t" \
    "v_cmp_eq_u32 vcc, -1, v24\n\t" \
    "v_permlane32_swap_b32 v16, v18\n\t" \
    "v_permlane32_swap_b32 v17, v19\n\t" \
    "v_pk_add_f32 v[16:17], v[16:17], v[18:19]\n\t" \
    "s_nop 0\n\t" \
    "s_cbranch_vccnz 6f\n\t" \
    "7:\n\t" \
    "v_permlane16_swap_b32 v16, v17\n\t" \
    "v_add_f32 v16, v16, v17\n\t" \
    "s_nop 1\n\t" \
    "v_add_f32_dpp v16, v16, v16 row_ror:8 row_mask:0xf bank_mask:0xf\n\t" \
    "v_fmac_f32 v22, v16, v23\n\t" \
    "v_cvt_pk_bf16_f32 v22, v22, v22\n\t" \
    "ds_write_b16 %[qo], v22\n\t" \
    "s_waitcnt lgkmcnt(0)\n\t" \
    "s_barrier\n\t" \
    "ds_read_b128 v[16:19], %[qa] offset:0\n\t" \
    "ds_read_b128 v[20:23], %[qa] offset:128\n\t" \
    "ds_read_b128 v[24:27], %[qa] offset:256\n\t" \
    "s_waitcnt lgkmcnt(2)\n\t" \
    "v_smfmac_f32_16x16x64_bf16 v[28:31], v[16:19], v[64:71], v7\n\t" \
    "v_smfmac_f32_16x16x64_bf16 v[32:35], v[16:19], v[88:95], v7\n\t" \
    "v_smfmac_f32_16x16x64_bf16 v[36:39], v[16:19], v[112:119], v7\n\t" \
    "v_smfmac_f32_16x16x64_bf16 v[40:43], v[16:19], v[136:143], v7\n\t" \
    "s_waitcnt lgkmcnt(1)\n\t" \
    "v_smfmac_f32_16x16x64_bf16 v[28:31], v[20:23], v[72:79], v7\n\t" \
    "v_smfmac_f32_16x16x64_bf16 v[32:35], v[20:23], v[96:103], v7\n\t" \
    "v_smfmac_f32_16x16x64_bf16 v[36:39], v[20:23], v[120:127], v7\n\t" \
    "v_smfmac_f32_16x16x64_bf16 v[40:43], v[20:23], v[144:151], v7\n\t" \
    "s_waitcnt lgkmcnt(0)\n\t" \
    "v_smfmac_f32_16x16x64_bf16 v[28:31], v[24:27], v[80:87], v7\n\t" \
    "v_smfmac_f32_16x16x64_bf16 v[32:35], v[24:27], v[104:111], v7\n\t" \
    "v_smfmac_f32_16x16x64_bf16 v[36:39], v[24:27], v[128:135], v7\n\t" \
    "v_smfmac_f32_16x16x64_bf16 v[40:43], v[24:27], v[152:159], v7\n\t" \
    "v_mov_b32 v63, %[mlw]\n\t" \
    "s_nop 3\n\t" \
    "v_add_f32_dpp v60, v28, v29 quad_perm:[0,1,2,3] row_mask:0x1 bank_mask:0xf\n\t" \
    "v_add_f32_dpp v60, v32, v33 quad_perm:[0,1,2,3] row_mask:0x2 bank_mask:0xf\n\t" \
    "v_add_f32_dpp v60, v36, v37 quad_perm:[0,1,2,3] row_mask:0x4 bank_mask:0xf\n\t" \
    "v_add_f32_dpp v60, v40, v41 quad_perm:[0,1,2,3] row_mask:0x8 bank_mask:0xf\n\t" \
    "v_mul_f32 v60, 0x3dd53b94, v60\n\t" \
    "s_nop 1\n\t" \
    "v_max_f32_dpp v61, v60, v60 quad_perm:[1,0,3,2] row_mask:0xf bank_mask:0xf\n\t" \
    "s_nop 1\n\t" \
    "v_max_f32_dpp v61, v61, v61 quad_perm:[2,3,0,1] row_mask:0xf bank_mask:0xf\n\t" \
    "s_nop 1\n\t" \
    "v_max_f32_dpp v61, v61, v61 row_half_mirror row_mask:0xf bank_mask:0xf\n\t" \
    "s_nop 1\n\t" \
    "v_max_f32_dpp v61, v61, v61 row_mirror row_mask:0xf bank_mask:0xf\n\t" \
    "s_nop 1\n\t" \
    "v_max_f32_dpp v61, v61, v61 row_bcast:15 row_mask:0xa bank_mask:0xf\n\t" \
    "s_nop 1\n\t" \
    "v_max_f32_dpp v61, v61, v61 row_bcast:31 row_mask:0xc bank_mask:0xf\n\t" \
    "s_nop 0\n\t" \
    "v_readlane_b32 %[st], v61, 63\n\t" \
    "s_mov_b32 exec_lo, 0\n\t" \
    "s_brev_b32 exec_hi, 1\n\t" \
    "ds_write_b32 v63, v61\n\t" \
    "s_mov_b64 exec, -1\n\t" \
    "v_subrev_f32 v62, %[st], v60\n\t" \
    "v_exp_f32 v62, v62\n\t" \
    "s_nop 0\n\t" \
    "v_cvt_pk_bf16_f32 v60, v62, v62\n\t" \
    "ds_write_b16 %[pw], v60\n\t" \
    "ds_write_b16 %[pw], v60 offset:20\n\t" \
    "ds_read_b128 v[16:19], %[pr] offset:0\n\t" \
    "ds_read_b128 v[20:23], %[pr] offset:272\n\t" \
    "ds_read_b128 v[24:27], %[pr] offset:544\n\t" \
    "ds_read_b128 v[28:31], %[pr] offset:816\n\t" \
    "v_mov_b32 v60, %[ma]\n\t" \
    "s_setprio 0\n\t" \
    "v_add_f32_dpp v62, v62, v62 quad_perm:[1,0,3,2] row_mask:0xf bank_mask:0xf\n\t" \
    "s_nop 1\n\t" \
    "v_add_f32_dpp v62, v62, v62 quad_perm:[2,3,0,1] row_mask:0xf bank_mask:0xf\n\t" \
    "s_nop 1\n\t" \
    "v_add_f32_dpp v62, v62, v62 row_half_mirror row_mask:0xf bank_mask:0xf\n\t" \
    "s_nop 1\n\t" \
    "v_add_f32_dpp v62, v62, v62 row_mirror row_mask:0xf bank_mask:0xf\n\t" \
    "s_waitcnt lgkmcnt(3)\n\t" \
    "v_smfmac_f32_16x16x64_bf16 v[48:51], v[16:19], v[160:167], v7 abid:1\n\t" \
    "v_smfmac_f32_16x16x64_bf16 v[52:55], v[16:19], v[192:199], v7 abid:1\n\t" \
    "v_smfmac_f32_16x16x64_bf16 v[56:59], v[16:19], v[224:231], v7 abid:1\n\t" \
    "v_add_f32_dpp v62, v62, v62 row_bcast:15 row_mask:0xa bank_mask:0xf\n\t" \
    "s_waitcnt lgkmcnt(2)\n\t" \
    "v_smfmac_f32_16x16x64_bf16 v[48:51], v[20:23], v[168:175], v7 abid:1\n\t" \
    "v_smfmac_f32_16x16x64_bf16 v[52:55], v[20:23], v[200:207], v7 abid:1\n\t" \
    "v_smfmac_f32_16x16x64_bf16 v[56:59], v[20:23], v[232:239], v7 abid:1\n\t" \
    "v_add_f32_dpp v62, v62, v62 row_bcast:31 row_mask:0xc bank_mask:0xf\n\t" \
    "s_waitcnt lgkmcnt(1)\n\t" \
    "v_smfmac_f32_16x16x64_bf16 v[48:51], v[24:27], v[176:183], v7 abid:1\n\t" \
    "v_smfmac_f32_16x16x64_bf16 v[52:55], v[24:27], v[208:215], v7 abid:1\n\t" \
    "v_smfmac_f32_16x16x64_bf16 v[56:59], v[24:27], v[240:247], v7 abid:1\n\t" \
    "s_waitcnt lgkmcnt(0)\n\t" \
    "v_smfmac_f32_16x16x64_bf16 v[48:51], v[28:31], v[184:191], v7 abid:1\n\t" \
    "v_smfmac_f32_16x16x64_bf16 v[52:55], v[28:31], v[216:223], v7 abid:1\n\t" \
    "v_smfmac_f32_16x16x64_bf16 v[56:59], v[28:31], v[248:255], v7 abid:1\n\t" \
    "s_barrier\n\t" \
    "ds_read_b128 v[16:19], v60\n\t" \
    "ds_read_b128 v[20:23], v60 offset:16\n\t" \
    "ds_read_b32 v24, %[mo]\n\t" \
    "s_mov_b32 exec_lo, 0\n\t" \
    "s_brev_b32 exec_hi, 1\n\t" \
    "ds_write_b32 v63, v62 offset:32\n\t" \
    "s_mov_b64 exec, -1\n\t" \
    "v_mov_b32_dpp v48, v52 quad_perm:[0,1,2,3] row_mask:0xc bank_mask:0xf\n\t" \
    "v_mov_b32_dpp v49, v53 quad_perm:[0,1,2,3] row_mask:0xc bank_mask:0xf\n\t" \
    "v_mov_b32_dpp v50, v54 quad_perm:[0,1,2,3] row_mask:0xc bank_mask:0xf\n\t" \
    "v_mov_b32_dpp v51, v55 quad_perm:[0,1,2,3] row_mask:0xc bank_mask:0xf\n\t" \
    "ds_write_b128 %[ow], v[48:51] offset:0\n\t" \
    "s_mov_b32 exec_lo, 0x00ff00ff\n\t" \
    "s_mov_b32 exec_hi, 0xff00ff00\n\t" \
    "ds_write_b128 %[ow], v[56:59] offset:2048\n\t" \
    "s_mov_b64 exec, -1\n\t" \
    "s_waitcnt lgkmcnt(3)\n\t" \
    "v_max3_f32 v16, v16, v17, v18\n\t" \
    "v_max3_f32 v19, v19, v20, v21\n\t" \
    "v_max3_f32 v16, v16, v22, v23\n\t" \
    "v_max_f32 v26, v16, v19\n\t" \
    "v_sub_f32 v60, v24, v26\n\t" \
    "v_exp_f32 v60, v60\n\t" \
    "s_nop 0\n\t" \
    "ds_write2_b32 %[mo], v60, v26 offset0:96 offset1:104\n\t" \
    "ds_read_b64_tr_b16 v[28:29], %[a0] offset:0\n\t" \
    "ds_read_b64_tr_b16 v[30:31], %[a0] offset:1024\n\t" \
    "ds_read_b64_tr_b16 v[32:33], %[a1] offset:8192\n\t" \
    "ds_read_b64_tr_b16 v[34:35], %[a1] offset:9216\n\t" \
    "ds_read_b64_tr_b16 v[36:37], %[a0] offset:8192\n\t" \
    "ds_read_b64_tr_b16 v[38:39], %[a0] offset:9216\n\t" \
    "ds_read_b64_tr_b16 v[40:41], %[a1] offset:0\n\t" \
    "ds_read_b64_tr_b16 v[42:43], %[a1] offset:1024\n\t" \
    "ds_read_b64_tr_b16 v[44:45], %[a0] offset:16384\n\t" \
    "ds_read_b64_tr_b16 v[46:47], %[a0] offset:17408\n\t" \
    "ds_read_b64_tr_b16 v[48:49], %[a1] offset:24576\n\t" \
    "ds_read_b64_tr_b16 v[50:51], %[a1] offset:25600\n\t" \
    "ds_read_b64_tr_b16 v[52:53], %[a0] offset:24576\n\t" \
    "ds_read_b64_tr_b16 v[54:55], %[a0] offset:25600\n\t" \
    "ds_read_b64_tr_b16 v[56:57], %[a1] offset:16384\n\t" \
    "ds_read_b64_tr_b16 v[58:59], %[a1] offset:17408\n\t" \
    "s_waitcnt lgkmcnt(0)\n\t" \
    "s_barrier\n\t" \
    "ds_read_b128 v[16:19], %[r0]\n\t" \
    "ds_read_b128 v[20:23], %[r0] offset:512\n\t" \
    "s_waitcnt lgkmcnt(0)\n\t" \
    "v_pk_add_f32 v[16:17], v[16:17], v[20:21]\n\t" \
    "v_pk_add_f32 v[18:19], v[18:19], v[22:23]\n\t" \
    "v_pk_mul_f32 v[16:17], v[16:17], v[60:61] op_sel_hi:[1,0]\n\t" \
    "v_pk_mul_f32 v[18:19], v[18:19], v[60:61] op_sel_hi:[1,0]\n\t" \
    "s_nop 1\n\t" \
    "v_permlane32_swap_b32 v16, v18\n\t" \
    "v_permlane32_swap_b32 v17, v19\n\t" \
    "v_pk_add_f32 v[16:17], v[16:17], v[18:19]\n\t" \
    "s_nop 1\n\t" \
    "v_permlane16_swap_b32 v16, v17\n\t" \
    "v_add_f32 v16, v16, v17\n\t" \
    "s_nop 1\n\t" \
    "v_add_f32_dpp v17, v16, v16 row_ror:8 row_mask:0xf bank_mask:0xf\n\t" \
    "v_add_f32_dpp v16, v16, v16 row_ror:8 row_mask:0xf bank_mask:0xf\n\t" \
    "s_nop 1\n\t" \
    "v_permlane16_swap_b32 v16, v17\n\t" \
    "s_mov_b32 exec_lo, 0xff\n\t" \
    "v_cvt_pk_bf16_f32 v16, v16, v17\n\t" \
    "s_mov_b32 exec_hi, 0xff\n\t" \
    "v_min_u32 v16, -2, v16\n\t" \
    "global_store_dword %[so], v16, %[ws] sc1\n\t" \
    "s_mov_b64 exec, -1\n\t" \
    "ds_read_b64_tr_b16 v[60:61], %[a0] offset:32768\n\t" \
    "ds_read_b64_tr_b16 v[62:63], %[a0] offset:33792\n\t" \
    "s_add_u32 %[rf], %[rf], %[t2]\n\t" \
    "s_mov_b32 %[st], 0x10000\n\t" \
    "14:\n\t" \
    "s_memrealtime vcc\n\t" \
    "s_waitcnt lgkmcnt(0)\n\t" \
    "s_sub_i32 vcc_lo, vcc_lo, %[rf]\n\t" \
    "s_cmp_lt_i32 vcc_lo, -4000\n\t" \
    "s_cbranch_scc1 15f\n\t" \
    "s_cmp_lt_i32 vcc_lo, 0\n\t" \
    "s_cbranch_scc1 14b\n\t" \
    "15:\n\t" \
    "global_load_dwordx4 v[8:11], %[gs], %[ws] offset:0 sc1\n\t" \
    "global_load_dwordx4 v[12:15], %[gs], %[ws] offset:1024 sc1\n\t" \
    "global_load_dwordx4 v[16:19], %[gs], %[ws] offset:2048 sc1\n\t" \
    "global_load_dwordx4 v[24:27], %[gm], %[ws] sc1\n\t" \
    "s_waitcnt vmcnt(3)\n\t" \
    "v_mfma_f32_16x16x32_bf16 v[28:31], v[28:31], v[8:11], 0\n\t" \
    "v_mov_b32_dpp v20, v8 row_half_mirror row_mask:0xf bank_mask:0xf\n\t" \
    "v_mov_b32_dpp v21, v9 row_half_mirror row_mask:0xf bank_mask:0xf\n\t" \
    "v_mfma_f32_16x16x32_bf16 v[32:35], v[32:35], v[8:11], 0\n\t" \
    "v_mov_b32_dpp v22, v10 row_half_mirror row_mask:0xf bank_mask:0xf\n\t" \
    "v_mov_b32_dpp v23, v11 row_half_mirror row_mask:0xf bank_mask:0xf\n\t" \
    "v_max3_u32 %[t0], v8, v9, v10\n\t" \
    "s_waitcnt vmcnt(2)\n\t" \
    "v_mfma_f32_16x16x32_bf16 v[28:31], v[36:39], v[20:23], v[28:31]\n\t" \
    "v_max3_u32 %[t0], %[t0], v11, v12\n\t" \
    "ds_read_b64_tr_b16 v[36:37], %[a1] offset:40960\n\t" \
    "v_mfma_f32_16x16x32_bf16 v[32:35], v[40:43], v[20:23], v[32:35]\n\t" \
    "ds_read_b64_tr_b16 v[38:39], %[a1] offset:41984\n\t" \
    "ds_read_b64_tr_b16 v[40:41], %[a0] offset:40960\n\t" \
    "v_mfma_f32_16x16x32_bf16 v[28:31], v[44:47], v[12:15], v[28:31]\n\t" \
    "v_mov_b32_dpp v20, v12 row_half_mirror row_mask:0xf bank_mask:0xf\n\t" \
    "v_mov_b32_dpp v21, v13 row_half_mirror row_mask:0xf bank_mask:0xf\n\t" \
    "v_mfma_f32_16x16x32_bf16 v[32:35], v[48:51], v[12:15], v[32:35]\n\t" \
    "v_mov_b32_dpp v22, v14 row_half_mirror row_mask:0xf bank_mask:0xf\n\t" \
    "v_mov_b32_dpp v23, v15 row_half_mirror row_mask:0xf bank_mask:0xf\n\t" \
    "ds_read_b64_tr_b16 v[42:43], %[a0] offset:41984\n\t" \
    "ds_read_b64_tr_b16 v[44:45], %[a1] offset:32768\n\t" \
    "v_mfma_f32_16x16x32_bf16 v[28:31], v[52:55], v[20:23], v[28:31]\n\t" \
    "ds_read_b64_tr_b16 v[46:47], %[a1] offset:33792\n\t" \
    "s_waitcnt vmcnt(0)\n\t" \
    "v_max3_u32 %[t0], %[t0], v13, v14\n\t" \
    "v_mfma_f32_16x16x32_bf16 v[32:35], v[56:59], v[20:23], v[32:35]\n\t" \
    "v_max3_u32 v8, v24, v25, v26\n\t" \
    "v_sub_f32 v24, v26, v24\n\t" \
    "v_mfma_f32_16x16x32_bf16 v[28:31], v[60:63], v[16:19], v[28:31]\n\t" \
    "v_mov_b32_dpp v20, v16 row_half_mirror row_mask:0xf bank_mask:0xf\n\t" \
    "v_mov_b32_dpp v21, v17 row_half_mirror row_mask:0xf bank_mask:0xf\n\t" \
    "s_waitcnt lgkmcnt(4)\n\t" \
    "v_mfma_f32_16x16x32_bf16 v[32:35], v[36:39], v[16:19], v[32:35]\n\t" \
    "v_mov_b32_dpp v22, v18 row_half_mirror row_mask:0xf bank_mask:0xf\n\t" \
    "v_mov_b32_dpp v23, v19 row_half_mirror row_mask:0xf bank_mask:0xf\n\t" \
    "v_exp_f32 v24, v24\n\t" \
    "v_max3_u32 %[t0], %[t0], v15, v16\n\t" \
    "s_waitcnt lgkmcnt(0)\n\t" \
    "v_mfma_f32_16x16x32_bf16 v[28:31], v[40:43], v[20:23], v[28:31]\n\t" \
    "v_fmac_f32 v25, v24, v27\n\t" \
    "v_max3_u32 v8, v8, v27, v17\n\t" \
    "v_mfma_f32_16x16x32_bf16 v[32:35], v[44:47], v[20:23], v[32:35]\n\t" \
    "v_rcp_f32 v24, v25\n\t" \
    "v_max3_u32 %[t0], %[t0], v18, v19\n\t" \
    "v_max_u32 v8, v8, %[t0]\n\t" \
    "v_cmp_eq_u32 vcc, -1, v8\n\t" \
    "v_mov_b32 v9, 0xff\n\t" \
    "s_nop 1\n\t" \
    "s_cbranch_vccnz 16f\n\t" \
    "v_mov_b32_dpp v28, v32 quad_perm:[0,1,2,3] row_mask:0xf bank_mask:0xa\n\t" \
    "v_mov_b32_dpp v29, v33 quad_perm:[0,1,2,3] row_mask:0xf bank_mask:0xa\n\t" \
    "v_mov_b32_dpp v30, v34 quad_perm:[0,1,2,3] row_mask:0xf bank_mask:0xa\n\t" \
    "v_mov_b32_dpp v31, v35 quad_perm:[0,1,2,3] row_mask:0xf bank_mask:0xa\n\t" \
    "v_pk_mul_f32 v[28:29], v[28:29], v[24:25] op_sel_hi:[1,0]\n\t" \
    "v_pk_mul_f32 v[30:31], v[30:31], v[24:25] op_sel_hi:[1,0]\n\t" \
    "s_nop 0\n\t" \
    "v_add_f32_dpp v28, v28, v28 row_shl:8 row_mask:0xf bank_mask:0xf\n\t" \
    "v_add_f32_dpp v29, v29, v29 row_shl:8 row_mask:0xf bank_mask:0xf\n\t" \
    "v_add_f32_dpp v30, v30, v30 row_shl:8 row_mask:0xf bank_mask:0xf\n\t" \
    "v_add_f32_dpp v31, v31, v31 row_shl:8 row_mask:0xf bank_mask:0xf\n\t" \
    "s_mov_b32 exec_lo, 0x00ff00ff\n\t" \
    "s_mov_b32 exec_hi, 0x00ff00ff\n\t" \
    "v_bfi_b32 v28, v9, %[tag], v28\n\t" \
    "v_bfi_b32 v29, v9, %[tag], v29\n\t" \
    "v_bfi_b32 v30, v9, %[tag], v30\n\t" \
    "v_bfi_b32 v31, v9, %[tag], v31\n\t" \
    "global_store_dwordx4 %[s2], v[28:31], %[ws] sc1\n\t" \
    "s_mov_b64 exec, -1\n\t" \
    "s_branch 2f\n\t" \
    "6:\n\t" \
    "s_sub_u32 %[st], %[st], 1\n\t" \
    "s_cbranch_scc1 7b\n\t" \
    RETRY_ASM \
    "v_mov_b32 v16, 0\n\t" \
    "v_mov_b32 v17, 0\n\t" \
    "v_mov_b32 v18, 0\n\t" \
    "v_mov_b32 v19, 0\n\t" \
    "s_mov_b32 exec_hi, 0xff\n\t" \
    "global_load_dwordx4 v[16:19], %[go], %[ws] sc1\n\t" \
    "s_mov_b32 exec_hi, -1\n\t" \
    "s_branch 5b\n\t" \
    "16:\n\t" \
    "s_mov_b64 exec, -1\n\t" \
    "s_sub_u32 %[st], %[st], 1\n\t" \
    "s_cbranch_scc1 2f\n\t" \
    RETRY_ASM3 \
    "s_sleep 3\n\t" \
    "ds_read_b64_tr_b16 v[28:29], %[a0] offset:0\n\t" \
    "ds_read_b64_tr_b16 v[30:31], %[a0] offset:1024\n\t" \
    "ds_read_b64_tr_b16 v[32:33], %[a1] offset:8192\n\t" \
    "ds_read_b64_tr_b16 v[34:35], %[a1] offset:9216\n\t" \
    "ds_read_b64_tr_b16 v[36:37], %[a0] offset:8192\n\t" \
    "ds_read_b64_tr_b16 v[38:39], %[a0] offset:9216\n\t" \
    "ds_read_b64_tr_b16 v[40:41], %[a1] offset:0\n\t" \
    "ds_read_b64_tr_b16 v[42:43], %[a1] offset:1024\n\t" \
    "ds_read_b64_tr_b16 v[44:45], %[a0] offset:16384\n\t" \
    "ds_read_b64_tr_b16 v[46:47], %[a0] offset:17408\n\t" \
    "ds_read_b64_tr_b16 v[48:49], %[a1] offset:24576\n\t" \
    "ds_read_b64_tr_b16 v[50:51], %[a1] offset:25600\n\t" \
    "ds_read_b64_tr_b16 v[52:53], %[a0] offset:24576\n\t" \
    "ds_read_b64_tr_b16 v[54:55], %[a0] offset:25600\n\t" \
    "ds_read_b64_tr_b16 v[56:57], %[a1] offset:16384\n\t" \
    "ds_read_b64_tr_b16 v[58:59], %[a1] offset:17408\n\t" \
    "ds_read_b64_tr_b16 v[60:61], %[a0] offset:32768\n\t" \
    "ds_read_b64_tr_b16 v[62:63], %[a0] offset:33792\n\t" \
    "s_waitcnt lgkmcnt(0)\n\t" \
    "s_branch 15b\n\t" \
    "2:\n\t"
#define ASM_OP \
    "s_mov_b32 %[st], 0x10000\n\t" \
    "5:\n\t" \
    "v_mov_b32 v8, %[fl]\n\t" \
    "s_mov_b32 %[ct], 0x400000\n\t" \
    "0:\n\t" \
    "ds_read_b32 v9, v8\n\t" \
    "s_memrealtime vcc\n\t" \
    "s_sub_u32 %[ct], %[ct], 1\n\t" \
    "s_waitcnt lgkmcnt(0)\n\t" \
    "s_cbranch_scc1 10f\n\t" \
    "v_readfirstlane_b32 %[rf], v9\n\t" \
    "s_cmp_eq_u32 %[rf], 0\n\t" \
    "s_cbranch_scc1 0b\n\t" \
    "s_sub_i32 vcc_lo, vcc_lo, %[rf]\n\t" \
    "s_cmp_ge_i32 vcc_lo, -4\n\t" \
    "s_cbranch_scc1 10f\n\t" \
    "4:\n\t" \
    "s_memrealtime vcc\n\t" \
    "s_waitcnt lgkmcnt(0)\n\t" \
    "s_sub_i32 vcc_lo, vcc_lo, %[rf]\n\t" \
    "s_cmp_lt_i32 vcc_lo, 0\n\t" \
    "s_cbranch_scc1 4b\n\t" \
    "10:\n\t" \
    "global_load_dwordx4 v[8:11], %[gs], %[ws] offset:0 sc1\n\t" \
    "global_load_dwordx4 v[12:15], %[gs], %[ws] offset:1024 sc1\n\t" \
    "global_load_dwordx4 v[16:19], %[gs], %[ws] offset:2048 sc1\n\t" \
    "global_load_dwordx4 v[24:27], %[gm], %[ws] sc1\n\t" \
    "v_mov_b32 v28, %[fl]\n\t" \
    "v_add_u32 v28, -8, v28\n\t" \
    "s_mov_b32 %[ct], 0x400000\n\t" \
    "11:\n\t" \
    "ds_read_b32 v29, v28\n\t" \
    "s_sub_u32 %[ct], %[ct], 1\n\t" \
    "s_waitcnt lgkmcnt(0)\n\t" \
    "s_cbranch_scc1 12f\n\t" \
    "v_cmp_eq_u32 vcc, 0, v29\n\t" \
    "s_cbranch_vccnz 11b\n\t" \
    "12:\n\t" \
    "ds_read_b64_tr_b16 v[28:29], %[a0] offset:0\n\t" \
    "ds_read_b64_tr_b16 v[30:31], %[a0] offset:1024\n\t" \
    "ds_read_b64_tr_b16 v[32:33], %[a1] offset:8192\n\t" \
    "ds_read_b64_tr_b16 v[34:35], %[a1] offset:9216\n\t" \
    "ds_read_b64_tr_b16 v[36:37], %[a0] offset:8192\n\t" \
    "ds_read_b64_tr_b16 v[38:39], %[a0] offset:9216\n\t" \
    "ds_read_b64_tr_b16 v[40:41], %[a1] offset:0\n\t" \
    "ds_read_b64_tr_b16 v[42:43], %[a1] offset:1024\n\t" \
    "ds_read_b64_tr_b16 v[44:45], %[a0] offset:16384\n\t" \
    "ds_read_b64_tr_b16 v[46:47], %[a0] offset:17408\n\t" \
    "ds_read_b64_tr_b16 v[48:49], %[a1] offset:24576\n\t" \
    "ds_read_b64_tr_b16 v[50:51], %[a1] offset:25600\n\t" \
    "ds_read_b64_tr_b16 v[52:53], %[a0] offset:24576\n\t" \
    "ds_read_b64_tr_b16 v[54:55], %[a0] offset:25600\n\t" \
    "ds_read_b64_tr_b16 v[56:57], %[a1] offset:16384\n\t" \
    "ds_read_b64_tr_b16 v[58:59], %[a1] offset:17408\n\t" \
    "ds_read_b64_tr_b16 v[60:61], %[a0] offset:32768\n\t" \
    "ds_read_b64_tr_b16 v[62:63], %[a0] offset:33792\n\t" \
    "s_waitcnt vmcnt(3) lgkmcnt(0)\n\t" \
    "v_mfma_f32_16x16x32_bf16 v[28:31], v[28:31], v[8:11], 0\n\t" \
    "v_mov_b32_dpp v20, v8 row_half_mirror row_mask:0xf bank_mask:0xf\n\t" \
    "v_mov_b32_dpp v21, v9 row_half_mirror row_mask:0xf bank_mask:0xf\n\t" \
    "v_mfma_f32_16x16x32_bf16 v[32:35], v[32:35], v[8:11], 0\n\t" \
    "v_mov_b32_dpp v22, v10 row_half_mirror row_mask:0xf bank_mask:0xf\n\t" \
    "v_mov_b32_dpp v23, v11 row_half_mirror row_mask:0xf bank_mask:0xf\n\t" \
    "v_max3_u32 %[t0], v8, v9, v10\n\t" \
    "s_waitcnt vmcnt(2)\n\t" \
    "v_mfma_f32_16x16x32_bf16 v[28:31], v[36:39], v[20:23], v[28:31]\n\t" \
    "v_max3_u32 %[t0], %[t0], v11, v12\n\t" \
    "ds_read_b64_tr_b16 v[36:37], %[a1] offset:40960\n\t" \
    "v_mfma_f32_16x16x32_bf16 v[32:35], v[40:43], v[20:23], v[32:35]\n\t" \
    "ds_read_b64_tr_b16 v[38:39], %[a1] offset:41984\n\t" \
    "ds_read_b64_tr_b16 v[40:41], %[a0] offset:40960\n\t" \
    "v_mfma_f32_16x16x32_bf16 v[28:31], v[44:47], v[12:15], v[28:31]\n\t" \
    "v_mov_b32_dpp v20, v12 row_half_mirror row_mask:0xf bank_mask:0xf\n\t" \
    "v_mov_b32_dpp v21, v13 row_half_mirror row_mask:0xf bank_mask:0xf\n\t" \
    "v_mfma_f32_16x16x32_bf16 v[32:35], v[48:51], v[12:15], v[32:35]\n\t" \
    "v_mov_b32_dpp v22, v14 row_half_mirror row_mask:0xf bank_mask:0xf\n\t" \
    "v_mov_b32_dpp v23, v15 row_half_mirror row_mask:0xf bank_mask:0xf\n\t" \
    "ds_read_b64_tr_b16 v[42:43], %[a0] offset:41984\n\t" \
    "ds_read_b64_tr_b16 v[44:45], %[a1] offset:32768\n\t" \
    "v_mfma_f32_16x16x32_bf16 v[28:31], v[52:55], v[20:23], v[28:31]\n\t" \
    "ds_read_b64_tr_b16 v[46:47], %[a1] offset:33792\n\t" \
    "s_waitcnt vmcnt(0)\n\t" \
    "v_max3_u32 %[t0], %[t0], v13, v14\n\t" \
    "v_mfma_f32_16x16x32_bf16 v[32:35], v[56:59], v[20:23], v[32:35]\n\t" \
    "v_max3_u32 v8, v24, v25, v26\n\t" \
    "v_sub_f32 v24, v26, v24\n\t" \
    "v_mfma_f32_16x16x32_bf16 v[28:31], v[60:63], v[16:19], v[28:31]\n\t" \
    "v_mov_b32_dpp v20, v16 row_half_mirror row_mask:0xf bank_mask:0xf\n\t" \
    "v_mov_b32_dpp v21, v17 row_half_mirror row_mask:0xf bank_mask:0xf\n\t" \
    "s_waitcnt lgkmcnt(4)\n\t" \
    "v_mfma_f32_16x16x32_bf16 v[32:35], v[36:39], v[16:19], v[32:35]\n\t" \
    "v_mov_b32_dpp v22, v18 row_half_mirror row_mask:0xf bank_mask:0xf\n\t" \
    "v_mov_b32_dpp v23, v19 row_half_mirror row_mask:0xf bank_mask:0xf\n\t" \
    "v_exp_f32 v24, v24\n\t" \
    "v_max3_u32 %[t0], %[t0], v15, v16\n\t" \
    "s_waitcnt lgkmcnt(0)\n\t" \
    "v_mfma_f32_16x16x32_bf16 v[28:31], v[40:43], v[20:23], v[28:31]\n\t" \
    "v_fmac_f32 v25, v24, v27\n\t" \
    "v_max3_u32 v8, v8, v27, v17\n\t" \
    "v_mfma_f32_16x16x32_bf16 v[32:35], v[44:47], v[20:23], v[32:35]\n\t" \
    "v_rcp_f32 v24, v25\n\t" \
    "v_max3_u32 %[t0], %[t0], v18, v19\n\t" \
    "v_max_u32 v8, v8, %[t0]\n\t" \
    "v_cmp_eq_u32 vcc, -1, v8\n\t" \
    "v_mov_b32 v9, 0xff\n\t" \
    "s_nop 1\n\t" \
    "s_cbranch_vccnz 6f\n\t" \
    "v_mov_b32_dpp v28, v32 quad_perm:[0,1,2,3] row_mask:0xf bank_mask:0xa\n\t" \
    "v_mov_b32_dpp v29, v33 quad_perm:[0,1,2,3] row_mask:0xf bank_mask:0xa\n\t" \
    "v_mov_b32_dpp v30, v34 quad_perm:[0,1,2,3] row_mask:0xf bank_mask:0xa\n\t" \
    "v_mov_b32_dpp v31, v35 quad_perm:[0,1,2,3] row_mask:0xf bank_mask:0xa\n\t" \
    "v_pk_mul_f32 v[28:29], v[28:29], v[24:25] op_sel_hi:[1,0]\n\t" \
    "v_pk_mul_f32 v[30:31], v[30:31], v[24:25] op_sel_hi:[1,0]\n\t" \
    "s_nop 0\n\t" \
    "v_add_f32_dpp v28, v28, v28 row_shl:8 row_mask:0xf bank_mask:0xf\n\t" \
    "v_add_f32_dpp v29, v29, v29 row_shl:8 row_mask:0xf bank_mask:0xf\n\t" \
    "v_add_f32_dpp v30, v30, v30 row_shl:8 row_mask:0xf bank_mask:0xf\n\t" \
    "v_add_f32_dpp v31, v31, v31 row_shl:8 row_mask:0xf bank_mask:0xf\n\t" \
    "s_mov_b32 exec_lo, 0x00ff00ff\n\t" \
    "s_mov_b32 exec_hi, 0x00ff00ff\n\t" \
    "v_bfi_b32 v28, v9, %[tag], v28\n\t" \
    "v_bfi_b32 v29, v9, %[tag], v29\n\t" \
    "v_bfi_b32 v30, v9, %[tag], v30\n\t" \
    "v_bfi_b32 v31, v9, %[tag], v31\n\t" \
    "global_store_dwordx4 %[so], v[28:31], %[ws] sc1\n\t" \
    "s_mov_b64 exec, -1\n\t" \
    "s_branch 2f\n\t" \
    "6:\n\t" \
    "s_mov_b64 exec, -1\n\t" \
    "s_sub_u32 %[st], %[st], 1\n\t" \
    "s_cbranch_scc1 2f\n\t" \
    RETRY_ASM \
    "s_sleep 6\n\t" \
    "s_branch 5b\n\t" \
    "2:\n\t"
#define ASM_GU \
    "s_mov_b32 %[st], 0x10000\n\t" \
    "5:\n\t" \
    "v_xor_b32 v11, 0x80, %[a0]\n\t" \
    "v_mov_b32 v12, 0x3f803f80\n\t" \
    "v_mov_b32 v13, 0x3f803f80\n\t" \
    "v_mov_b32 v14, 0x3f803f80\n\t" \
    "v_mov_b32 v15, 0x3f803f80\n\t" \
    "v_mov_b32 v56, 0x3f803f80\n\t" \
    "v_mov_b32 v57, 0x3f803f80\n\t" \
    "v_mov_b32 v58, 0x3f803f80\n\t" \
    "v_mov_b32 v59, 0x3f803f80\n\t" \
    "s_mov_b32 vcc_hi, 0\n\t" \
    "s_branch 8f\n\t" \
    "9:\n\t" \
    "4:\n\t" \
    "s_memrealtime vcc\n\t" \
    "s_waitcnt lgkmcnt(0)\n\t" \
    "s_sub_i32 vcc_lo, vcc_lo, %[tg]\n\t" \
    "s_cmp_lt_i32 vcc_lo, 0\n\t" \
    "s_cbranch_scc1 4b\n\t" \
    "global_load_dwordx4 v[6:9], %[gs], %[ws] sc1\n\t" \
    "global_load_dword v10, %[g2], %[ws] sc1\n\t" \
    "s_mov_b32 vcc_hi, 1\n\t" \
    "s_waitcnt vmcnt(2)\n\t" \
    "8:\n\t" \
    "ds_read_b64_tr_b16 v[16:17], %[a0] offset:0\n\t" \
    "ds_read_b64_tr_b16 v[18:19], v11 offset:0\n\t" \
    "ds_read_b64_tr_b16 v[36:37], %[a0] offset:20480\n\t" \
    "ds_read_b64_tr_b16 v[38:39], v11 offset:20480\n\t" \
    "ds_read_b64_tr_b16 v[20:21], %[a0] offset:1024\n\t" \
    "ds_read_b64_tr_b16 v[22:23], v11 offset:1024\n\t" \
    "ds_read_b64_tr_b16 v[40:41], %[a0] offset:21504\n\t" \
    "ds_read_b64_tr_b16 v[42:43], v11 offset:21504\n\t" \
    "ds_read_b64_tr_b16 v[24:25], %[a0] offset:2048\n\t" \
    "ds_read_b64_tr_b16 v[26:27], v11 offset:2048\n\t" \
    "ds_read_b64_tr_b16 v[44:45], %[a0] offset:22528\n\t" \
    "ds_read_b64_tr_b16 v[46:47], v11 offset:22528\n\t" \
    "ds_read_b64_tr_b16 v[28:29], %[a0] offset:3072\n\t" \
    "ds_read_b64_tr_b16 v[30:31], v11 offset:3072\n\t" \
    "ds_read_b64_tr_b16 v[48:49], %[a0] offset:23552\n\t" \
    "ds_read_b64_tr_b16 v[50:51], v11 offset:23552\n\t" \
    "ds_read_b64_tr_b16 v[32:33], %[a0] offset:4096\n\t" \
    "ds_read_b64_tr_b16 v[34:35], v11 offset:4096\n\t" \
    "ds_read_b64_tr_b16 v[52:53], %[a0] offset:24576\n\t" \
    "ds_read_b64_tr_b16 v[54:55], v11 offset:24576\n\t" \
    "s_cmp_eq_u32 vcc_hi, 0\n\t" \
    "s_cbranch_scc1 9b\n\t" \
    "s_waitcnt vmcnt(1) lgkmcnt(0)\n\t" \
    "v_mov_b32_dpp v12, v6 quad_perm:[0,1,2,3] row_mask:0xf bank_mask:0x1\n\t" \
    "v_mov_b32_dpp v13, v7 quad_perm:[0,1,2,3] row_mask:0xf bank_mask:0x1\n\t" \
    "v_mov_b32_dpp v14, v8 quad_perm:[0,1,2,3] row_mask:0xf bank_mask:0x1\n\t" \
    "v_mov_b32_dpp v15, v9 quad_perm:[0,1,2,3] row_mask:0xf bank_mask:0x1\n\t" \
    "v_mov_b32_dpp v56, v6 row_shl:4 row_mask:0xf bank_mask:0x1\n\t" \
    "v_mov_b32_dpp v57, v7 row_shl:4 row_mask:0xf bank_mask:0x1\n\t" \
    "v_mfma_f32_16x16x32_bf16 v[16:19], v[16:19], v[12:15], 0\n\t" \
    "v_mov_b32_dpp v58, v8 row_shl:4 row_mask:0xf bank_mask:0x1\n\t" \
    "v_mov_b32_dpp v59, v9 row_shl:4 row_mask:0xf bank_mask:0x1\n\t" \
    "v_mfma_f32_16x16x32_bf16 v[36:39], v[36:39], v[12:15], 0\n\t" \
    "v_max3_u32 v11, v6, v7, v8\n\t" \
    "v_mfma_f32_16x16x32_bf16 v[60:63], v[12:15], v[12:15], 0\n\t" \
    "v_mfma_f32_16x16x32_bf16 v[16:19], v[20:23], v[56:59], v[16:19]\n\t" \
    "v_mov_b32_dpp v12, v6 row_shl:8 row_mask:0xf bank_mask:0x1\n\t" \
    "v_mov_b32_dpp v13, v7 row_shl:8 row_mask:0xf bank_mask:0x1\n\t" \
    "v_mfma_f32_16x16x32_bf16 v[36:39], v[40:43], v[56:59], v[36:39]\n\t" \
    "v_mov_b32_dpp v14, v8 row_shl:8 row_mask:0xf bank_mask:0x1\n\t" \
    "v_mov_b32_dpp v15, v9 row_shl:8 row_mask:0xf bank_mask:0x1\n\t" \
    "v_mfma_f32_16x16x32_bf16 v[60:63], v[56:59], v[56:59], v[60:63]\n\t" \
    "s_nop 0\n\t" \
    "v_mfma_f32_16x16x32_bf16 v[16:19], v[24:27], v[12:15], v[16:19]\n\t" \
    "v_mov_b32_dpp v56, v6 row_shl:12 row_mask:0xf bank_mask:0x1\n\t" \
    "v_mov_b32_dpp v57, v7 row_shl:12 row_mask:0xf bank_mask:0x1\n\t" \
    "v_mfma_f32_16x16x32_bf16 v[36:39], v[44:47], v[12:15], v[36:39]\n\t" \
    "v_mov_b32_dpp v58, v8 row_shl:12 row_mask:0xf bank_mask:0x1\n\t" \
    "v_mov_b32_dpp v59, v9 row_shl:12 row_mask:0xf bank_mask:0x1\n\t" \
    "v_mfma_f32_16x16x32_bf16 v[60:63], v[12:15], v[12:15], v[60:63]\n\t" \
    "s_nop 0\n\t" \
    "v_mfma_f32_16x16x32_bf16 v[16:19], v[28:31], v[56:59], v[16:19]\n\t" \
    "s_waitcnt vmcnt(0)\n\t" \
    "v_mov_b32_dpp v12, v10 quad_perm:[0,1,2,3] row_mask:0xf bank_mask:0x1\n\t" \
    "v_mov_b32_dpp v13, v10 row_shl:4 row_mask:0xf bank_mask:0x1\n\t" \
    "v_mfma_f32_16x16x32_bf16 v[36:39], v[48:51], v[56:59], v[36:39]\n\t" \
    "v_mov_b32_dpp v14, v10 row_shl:8 row_mask:0xf bank_mask:0x1\n\t" \
    "v_mov_b32_dpp v15, v10 row_shl:12 row_mask:0xf bank_mask:0x1\n\t" \
    "v_mfma_f32_16x16x32_bf16 v[60:63], v[56:59], v[56:59], v[60:63]\n\t" \
    "v_max3_u32 v11, v11, v9, v10\n\t" \
    "v_mfma_f32_16x16x32_bf16 v[16:19], v[32:35], v[12:15], v[16:19]\n\t" \
    "v_cmp_eq_u32 vcc, -1, v11\n\t" \
    "v_mfma_f32_16x16x32_bf16 v[36:39], v[52:55], v[12:15], v[36:39]\n\t" \
    "v_mfma_f32_16x16x32_bf16 v[60:63], v[12:15], v[12:15], v[60:63]\n\t" \
    "s_mov_b32 exec_lo, 0x001f001f\n\t" \
    "s_mov_b32 exec_hi, 0x001f001f\n\t" \
    "s_cbranch_vccnz 6f\n\t" \
    "7:\n\t" \
    "s_nop 3\n\t" \
    "ds_write2_b32 %[pw], v16, v36 offset0:0 offset1:1\n\t" \
    "ds_write2_b32 %[pw], v17, v37 offset0:12 offset1:13\n\t" \
    "ds_write2_b32 %[pw], v18, v38 offset0:24 offset1:25\n\t" \
    "ds_write2_b32 %[pw], v19, v39 offset0:36 offset1:37\n\t" \
    "ds_write2_b32 %[gw], v60, v61 offset1:4\n\t" \
    "ds_write2_b32 %[gw], v62, v63 offset0:8 offset1:12\n\t" \
    "s_mov_b64 exec, -1\n\t" \
    "s_waitcnt lgkmcnt(0)\n\t" \
    "s_barrier\n\t" \
    "s_branch 2f\n\t" \
    "6:\n\t" \
    "s_sub_u32 %[st], %[st], 1\n\t" \
    "s_cbranch_scc1 7b\n\t" \
    "s_mov_b64 exec, -1\n\t" \
    RETRY_ASM \
    "s_sleep 1\n\t" \
    "s_branch 5b\n\t" \
    "2:\n\t"
#define ASM_HS \
    "ds_read_b64_tr_b16 v[16:17], %[w0] offset:0\n\t" \
    "ds_read_b64_tr_b16 v[18:19], %[w1] offset:32\n\t" \
    "ds_read_b64_tr_b16 v[20:21], %[w2] offset:64\n\t" \
    "ds_read_b64_tr_b16 v[22:23], %[w3] offset:96\n\t" \
    "ds_read_b64_tr_b16 v[24:25], %[w0] offset:128\n\t" \
    "ds_read_b64_tr_b16 v[26:27], %[w1] offset:160\n\t" \
    "ds_read_b64_tr_b16 v[28:29], %[w2] offset:192\n\t" \
    "ds_read_b64_tr_b16 v[30:31], %[w3] offset:224\n\t" \
    "ds_read_b64_tr_b16 v[32:33], %[w0] offset:256\n\t" \
    "ds_read_b64_tr_b16 v[34:35], %[w1] offset:288\n\t" \
    "v_mov_b32 v36, 0\n\t" \
    "v_mov_b32 v37, 0\n\t" \
    "v_mov_b32 v38, 0\n\t" \
    "v_mov_b32 v39, 0\n\t" \
    "v_mov_b32 v40, 0\n\t" \
    "v_mov_b32 v41, 0\n\t" \
    "v_mov_b32 v42, 0\n\t" \
    "v_mov_b32 v43, 0\n\t" \
    "v_add_u32 v12, 0x40000, %[so]\n\t" \
    "v_add_u32 v13, 0x80000, %[so]\n\t" \
    "s_waitcnt lgkmcnt(0)\n\t" \
    "s_barrier\n\t" \
    "ds_read_b128 v[44:47], %[sx]\n\t" \
    "ds_read_b128 v[48:51], %[sq]\n\t" \
    "ds_read_b128 v[52:55], %[cp]\n\t" \
    "ds_read_b128 v[56:59], %[cp] offset:16\n\t" \
    "ds_read_b128 v[60:63], %[rp]\n\t" \
    "ds_read_b128 v[8:11], %[rp] offset:16\n\t" \
    "s_waitcnt lgkmcnt(5)\n\t" \
    "v_pk_add_f32 v[44:45], v[44:45], v[46:47]\n\t" \
    "s_waitcnt lgkmcnt(4)\n\t" \
    "v_pk_add_f32 v[48:49], v[48:49], v[50:51]\n\t" \
    "v_add_f32 v44, v44, v45\n\t" \
    "v_add_f32 v48, v48, v49\n\t" \
    "v_mul_f32 v44, 0x3acccccd, v44\n\t" \
    "v_mul_f32 v48, 0x3acccccd, v48\n\t" \
    "v_fma_f32 v48, -v44, v44, v48\n\t" \
    "v_max_f32 v48, 0, v48\n\t" \
    "v_cvt_pk_bf16_f32 v44, v44, v48\n\t" \
    "v_and_b32 v48, 0xffff0000, v44\n\t" \
    "v_add_f32 v48, 0x3727c5ac, v48\n\t" \
    "v_cvt_pk_bf16_f32 v48, v48, v48\n\t" \
    "v_lshlrev_b32 v48, 16, v48\n\t" \
    "v_sqrt_f32 v48, v48\n\t" \
    "s_waitcnt lgkmcnt(2)\n\t" \
    "v_pk_add_f32 v[52:53], v[52:53], v[54:55]\n\t" \
    "v_cvt_pk_bf16_f32 v48, v48, v48\n\t" \
    "v_lshlrev_b32 v48, 16, v48\n\t" \
    "v_rcp_f32 v48, v48\n\t" \
    "v_pk_add_f32 v[56:57], v[56:57], v[58:59]\n\t" \
    "v_lshlrev_b32 v44, 16, v44\n\t" \
    "s_waitcnt lgkmcnt(0)\n\t" \
    "v_pk_add_f32 v[60:61], v[60:61], v[62:63]\n\t" \
    "v_pk_add_f32 v[8:9], v[8:9], v[10:11]\n\t" \
    "v_pk_add_f32 v[52:53], v[52:53], v[56:57]\n\t" \
    "v_pk_add_f32 v[60:61], v[60:61], v[8:9]\n\t" \
    "v_pk_fma_f32 v[60:61], v[52:53], v[44:45], v[60:61] op_sel_hi:[1,0,1] neg_lo:[1,0,0] neg_hi:[1,0,0]\n\t" \
    "v_pk_mul_f32 v[60:61], v[60:61], v[48:49] op_sel_hi:[1,0]\n\t" \
    "v_cvt_pk_bf16_f32 v60, v60, v61\n\t" \
    "v_lshlrev_b32 v61, 16, v60\n\t" \
    "v_mul_f32 v62, 0xbfb8aa3b, v61\n\t" \
    "v_exp_f32 v62, v62\n\t" \
    "v_and_b32 v60, 0xffff0000, v60\n\t" \
    "v_add_f32 v62, 1.0, v62\n\t" \
    "v_rcp_f32 v62, v62\n\t" \
    "s_nop 0\n\t" \
    "v_mul_f32 v61, v62, v61\n\t" \
    "v_cvt_pk_bf16_f32 v61, v61, v61\n\t" \
    "v_lshlrev_b32 v61, 16, v61\n\t" \
    "v_mul_f32 v60, v60, v61\n\t" \
    "s_nop 1\n\t" \
    "v_mov_b32_dpp v61, v60 row_ror:12 row_mask:0xf bank_mask:0xf\n\t" \
    "v_cvt_pk_bf16_f32 v60, v60, v61\n\t" \
    "s_nop 1\n\t" \
    "v_mov_b32_dpp v36, v60 quad_perm:[0,1,2,3] row_mask:0xf bank_mask:0x1\n\t" \
    "v_mov_b32_dpp v37, v60 row_ror:8 row_mask:0xf bank_mask:0x1\n\t" \
    "v_mov_b32_dpp v38, v60 quad_perm:[0,1,2,3] row_mask:0xf bank_mask:0x2\n\t" \
    "v_mov_b32_dpp v39, v60 row_ror:8 row_mask:0xf bank_mask:0x2\n\t" \
    "v_mov_b32_dpp v40, v60 quad_perm:[0,1,2,3] row_mask:0xf bank_mask:0x4\n\t" \
    "v_mov_b32_dpp v41, v60 row_ror:8 row_mask:0xf bank_mask:0x4\n\t" \
    "v_mfma_f32_16x16x32_bf16 v[44:47], v[16:19], v[36:39], 0\n\t" \
    "v_mov_b32_dpp v42, v60 quad_perm:[0,1,2,3] row_mask:0xf bank_mask:0x8\n\t" \
    "v_mov_b32_dpp v43, v60 row_ror:8 row_mask:0xf bank_mask:0x8\n\t" \
    "v_mfma_f32_16x16x32_bf16 v[48:51], v[24:27], v[36:39], 0\n\t" \
    "v_mfma_f32_16x16x32_bf16 v[52:55], v[32:35], v[36:39], 0\n\t" \
    "v_mfma_f32_16x16x32_bf16 v[44:47], v[20:23], v[40:43], v[44:47]\n\t" \
    "v_mfma_f32_16x16x32_bf16 v[48:51], v[28:31], v[40:43], v[48:51]\n\t" \
    "s_nop 5\n\t" \
    "v_min_u32 v52, -2, v52\n\t" \
    "v_min_u32 v53, -2, v53\n\t" \
    "v_min_u32 v54, -2, v54\n\t" \
    "v_min_u32 v55, -2, v55\n\t" \
    "v_min_u32 v44, -2, v44\n\t" \
    "v_min_u32 v45, -2, v45\n\t" \
    "v_min_u32 v46, -2, v46\n\t" \
    "v_min_u32 v47, -2, v47\n\t" \
    "global_store_dwordx4 %[so], v[44:47], %[ws] sc1\n\t" \
    "v_min_u32 v48, -2, v48\n\t" \
    "v_min_u32 v49, -2, v49\n\t" \
    "v_min_u32 v50, -2, v50\n\t" \
    "v_min_u32 v51, -2, v51\n\t" \
    "global_store_dwordx4 v12, v[48:51], %[ws] sc1\n\t" \
    "s_mov_b32 exec_lo, 0x00ff00ff\n\t" \
    "s_mov_b32 exec_hi, 0x00ff00ff\n\t" \
    "global_store_dwordx4 v13, v[52:55], %[ws] sc1\n\t" \
    "s_mov_b64 exec, -1\n\t"
#define CLOB_32_59 "v32", "v33", "v34", "v35", "v36", "v37", "v38", "v39", "v40", "v41", "v42", "v43", "v44", "v45", "v46", "v47", "v48", "v49", "v50", "v51", "v52", "v53", "v54", "v55", "v56", "v57", "v58", "v59"
#define CLOB_8_15 "v8", "v9", "v10", "v11", "v12", "v13", "v14", "v15"
#define CLOB_16_59 "v16", "v17", "v18", "v19", "v20", "v21", "v22", "v23", "v24", "v25", "v26", "v27", "v28", "v29", "v30", "v31", "v32", "v33", "v34", "v35", "v36", "v37", "v38", "v39", "v40", "v41", "v42", "v43", "v44", "v45", "v46", "v47", "v48", "v49", "v50", "v51", "v52", "v53", "v54", "v55", "v56", "v57", "v58", "v59"
#define CLOB_16_63 "v16", "v17", "v18", "v19", "v20", "v21", "v22", "v23", "v24", "v25", "v26", "v27", "v28", "v29", "v30", "v31", "v32", "v33", "v34", "v35", "v36", "v37", "v38", "v39", "v40", "v41", "v42", "v43", "v44", "v45", "v46", "v47", "v48", "v49", "v50", "v51", "v52", "v53", "v54", "v55", "v56", "v57", "v58", "v59", "v60", "v61", "v62", "v63"
template <int R, int IMM> __device__ __forceinline__ void ldkv(const void* sbase, unsigned voff) {
    if constexpr ((R < 160 && (POL & 1)) || (R >= 160 && (POL & 2)))
        asm volatile("global_load_dwordx4 v[%c2:%c3], %0, %1 offset:%c4" :: "v"(voff), "s"(sbase), "i"(R), "i"(R + 3), "i"(IMM) : "memory");
    else
        asm volatile("global_load_dwordx4 v[%c2:%c3], %0, %1 offset:%c4 nt" :: "v"(voff), "s"(sbase), "i"(R), "i"(R + 3), "i"(IMM) : "memory");
}
template <int A> __device__ __forceinline__ void fix_k() {
    unsigned t0, t1, t2, t3;
    asm volatile("v_mov_b32 %0, v%c4\n\tv_mov_b32 %1, v%c5\n\tv_mov_b32 %2, v%c6\n\tv_mov_b32 %3, v%c7\n\t"
                 "v_mov_b32_dpp v%c4, v%c8 row_ror:8 row_mask:0xf bank_mask:0xc\n\tv_mov_b32_dpp v%c5, v%c9 row_ror:8 row_mask:0xf bank_mask:0xc\n\t"
                 "v_mov_b32_dpp v%c6, v%c10 row_ror:8 row_mask:0xf bank_mask:0xc\n\tv_mov_b32_dpp v%c7, v%c11 row_ror:8 row_mask:0xf bank_mask:0xc\n\t"
                 "v_mov_b32_dpp v%c8, %0 row_ror:8 row_mask:0xf bank_mask:0x3\n\tv_mov_b32_dpp v%c9, %1 row_ror:8 row_mask:0xf bank_mask:0x3\n\t"
                 "v_mov_b32_dpp v%c10, %2 row_ror:8 row_mask:0xf bank_mask:0x3\n\tv_mov_b32_dpp v%c11, %3 row_ror:8 row_mask:0xf bank_mask:0x3"
                 : "=&v"(t0), "=&v"(t1), "=&v"(t2), "=&v"(t3) : "i"(A), "i"(A + 1), "i"(A + 2), "i"(A + 3), "i"(A + 12), "i"(A + 13), "i"(A + 14), "i"(A + 15));
}
#define PACE 1
#define D7 16
template <int D = DHI> __device__ __forceinline__ void pace(unsigned& due, unsigned step) {
    asm volatile("s_waitcnt vmcnt(%2)\n\ts_add_u32 %0, %0, %1\n1:\n\ts_memrealtime vcc\n\ts_waitcnt lgkmcnt(0)\n\ts_lshl_b32 vcc_lo, vcc_lo, 4\n\ts_sub_i32 vcc_lo, vcc_lo, %0\n\ts_cmp_ge_i32 vcc_lo, 0\n\ts_cbranch_scc0 1b"
                 : "+s"(due) : "s"(step), "i"(D) : "memory", "scc", "vcc");
}
__device__ __forceinline__ unsigned pace_t(unsigned& due, unsigned step) {
    unsigned t;
    asm volatile("s_waitcnt vmcnt(%3)\n\ts_add_u32 %0, %0, %2\n1:\n\ts_memrealtime vcc\n\ts_waitcnt lgkmcnt(0)\n\ts_lshl_b32 %1, vcc_lo, 4\n\ts_sub_i32 vcc_lo, %1, %0\n\ts_cmp_ge_i32 vcc_lo, 0\n\ts_cbranch_scc0 1b"
                 : "+s"(due), "=&s"(t) : "s"(step), "i"(DHI) : "memory", "scc", "vcc");
    return t;
}
#define MID 0
#define MIDN 7
#define JE 0
#define PW 0
#define CPF 0
#define PADN 0
#define KF 0
#define W7F 0
#define EPF_PULL 0
#define FIRSTD 0
#define EPM 248
#define EPN 16
#define PULLM 0
#define KEEPX " sc1"
#define LKEEP 0
#define LPN 4
#define TPN 0
#define OPCL
#define STG 2
#define AB(x) 0
#define OWN 0
#define OWNER(h, sub) ((OWN & 1) ? ((h) >> 4) * 80 + ((0x03142 >> (4 * (sub))) & 7) * 16 + ((h) & 15) : (h) * 5 + (sub))
#define PGM 255
#define PAGE(k) do { if (PGM & (1 << (k))) asm volatile("s_branch 1f\n\t.p2align 12\n1:" ::: "memory"); } while (0)
#define TRCL
#define QSP 0
#define XI 14
#define OPA 1
#define EHW 2
#define GPOL 1
#define QZ 0
#define PRI 3
#define P1ONLY 0
#undef NET
#define NET 0
#define NPS 0
#define NP0 0
#define MP0 24
#define MPS 3
static_assert(!WOMODE || (NET <= 3 && MP0 >= 11 && MP0 + (6 - NET) * MPS < 36), "extras");
template <int I, int HI, bool THR, class F> __device__ __forceinline__ void kv_stream_x(const void* kbase, const void* vbase, unsigned ko, unsigned v0, const unsigned (&to)[4], unsigned& due, unsigned& step, F&& extra) {
    if constexpr (I < HI) {
        if constexpr (NPS > 0 && I >= NP0 && (I - NP0) % (NPS > 0 ? NPS : 1) == 0 && (I - NP0) / (NPS > 0 ? NPS : 1) < 6) extra(100 + (I - NP0) / NPS);
        if constexpr (WOMODE && I >= MP0 && (I - MP0) % MPS == 0 && (I - MP0) / MPS < 7 - NET) { extra((I - MP0) / MPS); pace(due, step); }
        if constexpr (I < 24) ldkv<64 + 4 * I, (I % 3) * 128 + ((I / 3) & 1) * 3072>(kbase, ko + to[I / 6]);
        else ldkv<160 + 4 * (I - 24), ((I - 24) & 3) * 384>(vbase, v0 + to[(I - 24) >> 2]);
        if constexpr (I >= 14 && I < 38 && (I - 14) % 6 < 3) fix_k<64 + 4 * (I - 14)>();
        pace(due, step);
        if constexpr (I == XI || I == XI + 5) extra(300 + I - XI);
        if constexpr (I == P_QP || I == 14 || I == 18 || I == 26 || I == 34) extra(I == P_QP ? 216 : I == 14 ? 220 : I == 18 ? 224 : I == 26 ? 228 : 232);
        kv_stream_x<I + 1, HI, THR>(kbase, vbase, ko, v0, to, due, step, extra);
    }
}
template <int I, int HI, int THR> __device__ __forceinline__ void kv_stream(const void* kbase, const void* vbase, unsigned ko, unsigned v0, const unsigned (&to)[4], unsigned& due, unsigned step) {
    if constexpr (I < HI) {
        if constexpr (I < 24) ldkv<64 + 4 * I, (I % 3) * 128 + ((I / 3) & 1) * 3072>(kbase, ko + to[I / 6]);
        else ldkv<160 + 4 * (I - 24), ((I - 24) & 3) * 384>(vbase, v0 + to[(I - 24) >> 2]);
        if constexpr (THR == 1 && I >= P_BA) pace(due, step);
        if constexpr (THR == 2) pace<D7>(due, step);
        if constexpr (THR == 3) pace(due, step);
        kv_stream<I + 1, HI, THR>(kbase, vbase, ko, v0, to, due, step);
    }
}
#define NMS 22
#define NNE 48
#define DSL 0
#define NME 46
static_assert((NM7 == 0 || NN7 % 6 == 0) && NM7 <= NME - NMS && NN7 <= NNE - F7 && NN7 <= 48 - F7, "slots");
template <int I> struct IC { static constexpr int v = I; };
template <int I, int HI, class F> __device__ __forceinline__ void kv_stream7(const void* kbase, const void* vbase, unsigned ko, unsigned v0, unsigned v1, unsigned v2, const unsigned (&to)[4], unsigned& due, unsigned step, F&& nar) {
    if constexpr (I < HI) {
        if constexpr (I < 24) ldkv<64 + 4 * I, (I % 3) * 128 + ((I / 3) & 1) * 3072>(kbase, ko + to[I / 6]);
        else if constexpr (I < 40) ldkv<160 + 4 * (I - 24), ((I - 24) & 3) * 384>(vbase, v0 + to[(I - 24) >> 2]);
        else ldkv<160 + 4 * (I - 24), (I & 3) * 384>(vbase, I < 44 ? v1 : v2);
        if constexpr (((I - F7) * NN7) % (48 - F7) < NN7) nar(IC<((I - F7) * NN7) / (48 - F7)>{});
        if constexpr (NNE < 48 && I < NNE && ((I - F7) * NN7) % (NNE - F7) < NN7) nar(IC<200 + ((I - F7) * NN7) / (NNE - F7)>{});
        if constexpr (I >= NMS && I < NME && ((I - NMS) * NM7) % (NME - NMS) < NM7) nar(IC<100 + ((I - NMS) * NM7) / (NME - NMS)>{});
        if constexpr (I < 47) pace<D7>(due, step);
        kv_stream7<I + 1, HI>(kbase, vbase, ko, v0, v1, v2, to, due, step, nar);
    }
}
template <int I> __device__ __forceinline__ void fix_all() { if constexpr (I < 12) { fix_k<64 + 4 * ((I / 3) * 6 + I % 3)>(); fix_all<I + 1>(); } }
#define LANES() int lane; asm volatile("v_mbcnt_lo_u32_b32 %0, -1, 0\n\tv_mbcnt_hi_u32_b32 %0, -1, %0" : "=v"(lane)); __builtin_assume(lane >= 0 && lane < 64); \
    const int n = lane & 15, kq = lane >> 4, q4 = n >> 2, p4 = lane & 3; (void)n; (void)kq; (void)q4; (void)p4
template <unsigned C> __device__ __forceinline__ u32x4 splat() { unsigned a, b, c, d; asm volatile("v_mov_b32 %0, %4\n\tv_mov_b32 %1, %4\n\tv_mov_b32 %2, %4\n\tv_mov_b32 %3, %4" : "=v"(a), "=v"(b), "=v"(c), "=v"(d) : "i"(C)); return (u32x4){a, b, c, d}; }
static_assert(TDEPTH <= 8 && DHI <= 8, "fix_k schedule");
__device__ __forceinline__ u32x4 ld16_a(const void* sbase, unsigned voff) {
    u32x4 r; asm volatile("global_load_dwordx4 %0, %1, %2" : "=v"(r) : "v"(voff), "s"(sbase) : "memory"); return r;
}

template <bool KEEP = false> __device__ __forceinline__ void narrow(const u16* wg, const u16* wu, unsigned va, unsigned vb, unsigned lb, int s, int rot) {
    const int s10 = (s * 26) >> 8, r = s - 10 * s10, m = r >= 5, c = r - 5 * m, qt = (s10 + rot) & 3;
    if (KEEP) dma16p((m ? wu : wg) + (qt * 160 + (c & 3) * 8) * 1536, c == 4 ? vb : va, lb + L_GU + (unsigned)(m * 20480 + (qt * 5 + c) * 1024)); else
    dma16((m ? wu : wg) + (qt * 160 + (c & 3) * 8) * 1536, c == 4 ? vb : va, lb + L_GU + (unsigned)(m * 20480 + (qt * 5 + c) * 1024));
}
#define NROT 0
#define ROTS(s) (NROT ? ((s) + 7 * (mi & 3)) % (int)SCH(S_N1) : (s))
#define OSX ""
#define LATED 0
#define LDSL 2
template <int P> __device__ __forceinline__ void wo_piece(const u16* wob, unsigned oa, unsigned lb) {
    constexpr int j = P / 6, i = P % 6;
    dma16(wob + (i * 32 + (j & 1) * 16 + ((j >> 1) & 1) * 4 + (j >> 2) * 2) * DM, (j & 2) ? oa ^ 128u : oa, lb + L_WO + (j + 8 * i) * 1024);
}
template <int P, int HI, int SP = WSP> __device__ __forceinline__ void wo_rest(const u16* wob, unsigned oa, unsigned ob, unsigned lb) {
    if constexpr (P < HI) {
        constexpr int j = P / 6, i = P % 6; unsigned t;
        asm volatile("s_add_u32 m0, %3, %4\n\tv_add_u32 %0, %5, %1\n\tglobal_load_lds_dwordx4 %0, %2" WPOL
                     : "=&v"(t) : "v"((j & 2) ? ob : oa), "s"(wob), "s"(lb), "i"(L_WO + (j + 8 * i) * 1024), "i"((i * 32 + (j & 1) * 16 + ((j >> 1) & 1) * 4 + (j >> 2) * 2) * DM * 2) : "memory", "scc");
        if (SP > 0 && P < 47) __builtin_amdgcn_s_sleep(SP);
        if (SP < 0 && (P & 1) && P < 47) __builtin_amdgcn_s_sleep(-SP);
        wo_rest<P + 1, HI, SP>(wob, oa, ob, lb);
    }
}
#define WDP 8
template <int P, int HI> __device__ __forceinline__ void wo_paced(const u16* wob, unsigned oa, unsigned ob, unsigned lb, unsigned& due, unsigned step) {
    if constexpr (P < HI) { pace<WDP>(due, step); wo_rest<P, P + 1, 0>(wob, oa, ob, lb); wo_paced<P + 1, HI>(wob, oa, ob, lb, due, step); }
}
#define M0_KEEP() unsigned m0_; asm volatile("s_mov_b32 %0, m0" : "=s"(m0_) :: "memory")
#define M0_BACK() asm volatile("s_mov_b32 m0, %0" :: "s"(m0_) : "memory")
#define WO_ADDR() const u16* wob = wo + h * DH * DM + sub * 128; const unsigned woa = (unsigned)(((kq >> 1) * 8 + (kq & 1)) * DM * 2 + ((n >> 1) ^ kq) * 32 + (n & 1) * 16)
#define NARROW_ADDR() const int r5_ = swz(lane >> 1); const unsigned nva = (unsigned)(((r5_ & 7) + ((r5_ >> 3) << 5)) * 3072 + mi * 32 + (lane & 1) * 16), \
        nvb = (unsigned)((128 + r5_) * 3072 + mi * 32 + (lane & 1) * 16); const int nrot = 0

#define SLOWL(j) (((((unsigned)(size_t)wg >> 7) + (unsigned)(j)) & 7u) == 3u)
#define N1W ((int)(SLOWL(mi >> 2) ? SCH(S_N0) : SCH(S_N1)))
#define GUPF(rot) do { const int nd = (int)SCH(S_ND); int sh = (rr >> 3) * 5 + sub + (int)(rot); if (sh >= 20) sh -= 20; \
        _Pragma("unroll 1") for (int k_ = 0; k_ < nd; k_ += 21) { const int rw = k_ + lane / 3; \
        const unsigned o = (unsigned)((640 - 20 * nd + sh * nd + (rw < nd ? rw : nd - 1)) * 3072 + xg * 384 + (lane % 3) * 128); \
        if (GPOL) { pull(wg, o, lb + L_PF); pull(wu, o, lb + L_PF); } else { dma4(wg, o, lb + L_PF); dma4(wu, o, lb + L_PF); } } } while (0)
#define EXTRA(k) do { LANES(); \
        if (!is_mlp) { \
            const int j = (k) < 6 ? wave : 7, i = (k) < 6 ? (k) : wave < 5 ? wave : 5, pp = j * 4 + kq, f = (pp & 3) | (((pp >> 3) & 1) << 2); \
            const int dim = ((pp >> 4) & 1) * 16 + (pp & 1) * 8 + ((pp >> 1) & 7); \
            dma16(wo + (h * DH + i * 32) * DM + sub * 128, (unsigned)(dim * DM * 2 + ((n >> 1) ^ f) * 32 + (n & 1) * 16), lb + L_WO + (j + 8 * i) * 1024); \
        } else if ((k) < 4) { \
            const int s = (int)SCH(S_N0) + wave + 7 * (k); \
            if (s < (int)SCH(S_N1)) { NARROW_ADDR(); narrow(wg, wu, nva, nvb, lb, s, nrot); } \
        } else { \
            const int ii = wave + 7 * ((k) - 4), c = ii * 64 + lane; int row = c / 82, cc = c - 82 * row; if (cc > 79) cc = 79; if (row > 15) row = 15; \
            dma16(wd + mi * 16 * DM, (unsigned)((row * DM + cc * 8) * 2), lb + L_WD + ii * 1024); \
        } } while (0)

__global__ void __launch_bounds__(512, 2) __attribute__((amdgpu_num_vgpr(32), aligned(KAL)))
mega_kernel(const u16* __restrict__ kc, const u16* __restrict__ vc, unsigned char* __restrict__ ws, const u16* __restrict__ x0, const u16* __restrict__ wq,
            const u16* __restrict__ wg, const u16* __restrict__ wu, const u16* __restrict__ wo, const u16* __restrict__ wr,
            const u16* __restrict__ wd, u16* __restrict__ out
            ) {
    extern __shared__ __attribute__((aligned(16))) unsigned char smem[];
    constexpr unsigned sched_c[S_N] = SCHED; (void)sched_c;
    const unsigned lb = (unsigned)(size_t)smem;
#define STAMP0() do {} while (0)
    unsigned long long st64 = 0; (void)st64;
    const int wave0 = __builtin_amdgcn_readfirstlane(threadIdx.x >> 6);
    {
        unsigned long long kp, pc; unsigned s0, s1, s2, a0, a1;
        asm volatile("s_load_dwordx2 %0, %7, 0x0\n\ts_getpc_b64 %1\nmega_pf_here:\n\ts_lshr_b32 %2, %8, 3\n\tv_lshl_add_u32 %5, %9, 5, %2\n\tv_lshlrev_b32 %5, 7, %5\n\t"
                     "v_min_u32 %5, mega_pf_end - mega_pf_here - 4, %5\n\tglobal_load_dword v255, %5, %1\n\t"
                     "s_and_b32 %3, %8, 48\n\ts_and_b32 %4, %8, 7\n\ts_lshl1_add_u32 %3, %4, %3\n\ts_and_b32 %4, %2, 25\n\ts_lshl4_add_u32 %3, %3, %4\n\ts_add_u32 %3, %3, 6\n\ts_lshl_b32 %3, %3, 5\n\t"
                     "v_lshrrev_b32 %5, 6, %9\n\tv_add_u32 %5, %2, %5\n\tv_and_or_b32 %5, %5, 31, %3\n\tv_and_b32 %6, 7, %9\n\tv_lshl_add_u32 %5, %5, 4, %6\n\tv_mul_u32_u24 %5, 0x180, %5\n\t"
                     "v_and_or_b32 %5, %9, 48, %5\n\tv_lshlrev_b32 %6, 3, %9\n\tv_and_or_b32 %5, %6, 64, %5\n\ts_waitcnt lgkmcnt(0)\n\t"
                     "global_load_dwordx4 v[64:67], %5, %0 nt\n\tglobal_load_dwordx4 v[68:71], %5, %0 offset:128 nt\n\tglobal_load_dwordx4 v[72:75], %5, %0 offset:256 nt\n\tglobal_load_dwordx4 v[76:79], %5, %0 offset:3072 nt\n\t"
                     "s_memrealtime %1\n\ts_waitcnt lgkmcnt(0)"
                     : "=&s"(kp), "=&s"(pc), "=&s"(s0), "=&s"(s1), "=&s"(s2), "=&v"(a0), "=&v"(a1) : "s"(__builtin_amdgcn_kernarg_segment_ptr()), "s"(blockIdx.x), "v"(threadIdx.x) : "memory", "scc");
        st64 = pc;
    }
    const u32x4 ones = {~0u, ~0u, ~0u, ~0u};
    const f32x4 z4 = {0.f, 0.f, 0.f, 0.f};
    asm volatile("" ::: "v255");
#define WSOFF 0

#define DMARK(k) do {} while (0)
    unsigned to[4];
#pragma unroll
    for (int t = 0; t < 4; ++t) to[t] = ((t * 8 + wave0 + (GID >> 3)) & 31u) * 6144u;
    unsigned step = wave0 == 7 ? SCH(S_P7) : ((GID >> 3) & 7) >= 5 ? SCH(S_PM) : SCH(S_PD);
    unsigned t_start, due, t0, pol = 0u, tag = 0u;
#define START(off) do { t0 = t_start = (unsigned)st64; due = ((t_start + (off)) << 4) + ((((wave0 + STG) & 7) * step) >> 3); STAMPV(10, t_start); STAMP0(); } while (0)
    {
        const int wave = wave0, lane = threadIdx.x & 63;
        const int g = GID, xg = g & 7, rr = g >> 3;
        const int h = xg * 4 + (rr >> 3), sub = rr & 7, b = sub >> 1, half = sub & 1;
        const bool is_mlp = sub >= 5; const int mi = h * 3 + sub - 5, oi = OWNER(h, sub);
        const int n = lane & 15, kq = lane >> 4, q4 = n >> 2, p4 = lane & 3;
        const u16* kbase = kc + ((size_t)(b * HQ + h) * SK + (SK - WIN) + half * 512) * DH;
        const u16* vbase = vc + ((size_t)(b * HQ + h) * SK + (SK - WIN) + half * 512) * DH;
        const unsigned ko = (unsigned)((lane & 7) * 384 + (((lane >> 3) & 1) * 4 + (lane >> 4)) * 16);
        const unsigned v0 = (unsigned)(4 * kq * 384 + n * 16);
        const unsigned v1 = (unsigned)(4 * kq * 384 + 256 + (n & 7) * 16) + (n >= 8 ? to[2] : to[0]), v2 = (unsigned)(4 * kq * 384 + 256 + (n & 7) * 16) + (n >= 8 ? to[3] : to[1]);
        auto lds_zero_nb = [&]() {
            if (threadIdx.x < 16) *reinterpret_cast<unsigned*>(smem + L_CNT + threadIdx.x * 4) = 0u;
            *reinterpret_cast<f32x4*>(smem + L_P + threadIdx.x * 16) = z4;
            if (threadIdx.x < 32) *reinterpret_cast<f32x4*>(smem + L_P + 8192 + threadIdx.x * 16) = z4;
        };
        auto lds_zero = [&]() { lds_zero_nb(); BAR(); };
        auto lds_inc = [&](int idx) {
            asm volatile("s_waitcnt lgkmcnt(0)" ::: "memory");
            if (lane == 0) __hip_atomic_fetch_add(reinterpret_cast<unsigned*>(smem + L_CNT) + idx, 1u, __ATOMIC_RELAXED, __HIP_MEMORY_SCOPE_WORKGROUP);
        };
        const unsigned po = WS_PR + (unsigned)((mi & 1) * LB + ((mi >> 1) & 1) * 8192 + (mi >> 1) * 128);
        auto clean = [&]() {
            if (is_mlp) {
#pragma unroll
                for (int i = 0; i < 3; ++i) {
                    const int c = mi * 3 + i;
                    const int lim = c >= 256 ? (AB(64) ? 0 : 64) : c >= 64 && c < 192 ? ((c & 3) == 3 ? MLS / 2 : AB(4) ? 0 : 64) : (c & 7) >= 6 ? 40 : c < 64 || AB(32) ? (AB(2) ? 0 : 64) : 0;
                    if (lane < lim) st16_e(ws, WS_XA + (unsigned)(c * 1024 + lane * 16), ones);
                }
                if (lane < 16 && !AB(8)) st16_sc1(ws, (po ^ (unsigned)((lane & 8) << 10)) + (unsigned)((lane & 7) * 16), ones);
            }
        };
        auto first7 = [&]() {
            if (CA == 0) clean();
            if (EPF != 3) kv_stream<0, F7, 0>(kbase, vbase, ko, v0, to, due, step);
            START(SCH(S_O7));
            lds_zero_nb();
            __builtin_amdgcn_s_setprio(3);
            {
                unsigned d;
                asm volatile("s_waitcnt lgkmcnt(0)\n\ts_load_dword %0, %2, %3\n\ts_load_dword %1, %2, %4\n\ts_barrier\n\ts_waitcnt lgkmcnt(0)"
                             : "=&s"(tag), "=&s"(d) : "s"(ws), "s"(WS_EP), "s"(is_mlp ? (po | (2 * GP)) & ~16383u : WS_EP) : "memory");
                tag += 0x01010101u;
            }
            STAMP(24);
        };
        int w7 = wave;
        { if (wave == 7) first7(); asm volatile("" : "+s"(w7)); }
        if (w7 != 7) {
            const unsigned o2 = (unsigned)((wave & 3) * 1280 + lane * 16);
            u32x4 xa_ = ones, xc_ = ones;
            START(is_mlp ? SCH(S_MO) : SCH(S_PO));
            if (FIRSTD) { VMW(3); STAMP(36); VMW(0); STAMP(37); }
            lds_zero();
            if (!is_mlp) {
                xa_ = ld16_a(x0, o2); xc_ = ld16_a(x0, lane < 16 ? o2 + 1024 : o2);
#pragma unroll
                for (int i = 0; i < 7; ++i) {
                    int ii = wave + 7 * i; if (ii > 47) ii = 47;
                    const int c = ii * 64 + lane, row = c / 24, u = c - 24 * row;
                    const int f = ((row >> 1) & 1) | (((row >> 3) & 1) << 1);
                    unsigned o = (unsigned)(((sub * 128 + kpos(row)) * QD + h * DH) * 2 + ((u >> 1) ^ f) * 32 + (u & 1) * 16);
                    asm volatile("" : "+v"(o));
                    if (i < 2) pace<DHI + 2>(due, step); else pace(due, step);
                    dma16(wq, o, lb + L_WQ + ii * 1024);
                }
            }
            pace(due, step);
            kv_stream<4, P_LN, 3>(kbase, vbase, ko, v0, to, due, step);
            if (!is_mlp) {
                asm volatile("s_waitcnt vmcnt(%2)" : "+v"(xa_), "+v"(xc_) : "i"(P_LN - (EPF == 3 ? 4 : 0)) : "memory");
                if (wave >= 3) {
                    float mb, rden; row_stats(xa_, xc_, lane, mb, rden);
                    const f32x2 mb2 = {mb, mb}, rd2 = {rden, rden};
#pragma unroll
                    for (int j = 0; j < 4; ++j) { xa_[j] = pk2v(unpk(pk2v(unpk(xa_[j]) - mb2)) * rd2); xc_[j] = pk2v(unpk(pk2v(unpk(xc_[j]) - mb2)) * rd2); }
                    *reinterpret_cast<u32x4*>(smem + L_OP + (wave & 3) * 1344 + lane * 16) = xa_;
                    if (lane < 16) *reinterpret_cast<u32x4*>(smem + L_OP + (wave & 3) * 1344 + 1024 + lane * 16) = xc_;
                }
            }
            lds_inc(1);
            kv_stream<P_LN, P_QP, 1>(kbase, vbase, ko, v0, to, due, step);
            if (!is_mlp && wave < 6) {
                lds_wait_ge(lb + L_CNT + 4, 7u);
                STAMP(1);
                const unsigned a0 = lb + L_WQ + (unsigned)((8 * kq + q4) * 384 + ((2 * wave) ^ ((q4 >> 1) | ((kq & 1) << 1))) * 32 + p4 * 8), a1 = a0 ^ 32u;
                const unsigned char* xp = smem + L_OP + ((n >> 1) & 3) * 1344 + sub * 256 + kq * 64;
                f32x4 c0 = z4, c1 = z4;
#pragma unroll
                for (int s = 0; s < 4; ++s) {
                    const u32x4 bf = n >= 8 ? (u32x4){0x3f803f80u, 0x3f803f80u, 0x3f803f80u, 0x3f803f80u} : *reinterpret_cast<const u32x4*>(xp + s * 16);
                    c0 = mfma32w(cat8(tr64(a0 + s * 12288), tr64(a0 + s * 12288 + 1536)), bf, c0);
                    c1 = mfma32w(cat8(tr64(a1 + s * 12288), tr64(a1 + s * 12288 + 1536)), bf, c1);
                }
                if (n & 1) c0 = c1;
                if (n < 10) *reinterpret_cast<f32x4*>(smem + (n < 8 ? L_OP + 6144 + (n >> 1) * 768 : L_CS + 768) + wave * 128 + kq * 32 + (n & 1) * 16) = c0;
                lds_inc(3);
            }
            kv_stream_x<P_QP, 36, true>(kbase, vbase, ko, v0, to, due, step, [&](int k) {
                if (k >= 300) { if ((k == 305) == is_mlp) { } } else
                if (k >= 200) STAMPV(56 + (k - 216) / 4, now32() - (due >> 4) + t_start + 100u - ((now32() - (due >> 4) + 0x8000000u) & 0xf0000000u)); });
        } else {
            const unsigned t8 = tag & 0xffu;
            WO_ADDR();
            NARROW_ADDR(); (void)nva; (void)nvb; (void)nrot;
            const int ml_ = mi - 12 * xg, l3_ = lane / 3, ne_ = (int)SCH(S_N0), r0e_ = 640 - 6 * LPN * (int)SCH(S_ND);
            const unsigned pe_ = (unsigned)((l3_ < ne_ ? l3_ : 0) * 3072 + xg * 384 + (lane - 3 * l3_) * 128);
            auto nar = [&](auto ic) {
                constexpr int k = decltype(ic)::v;
                auto rows = [&](int j) { const int row = (j * 6 + (ml_ >> 1)) * ne_; if (ne_ && row < r0e_) pull(((ml_ & 1) ? wu : wg) + (row < 640 - ne_ ? row : 640 - ne_) * 1536, pe_, lb + L_PF); };
                if constexpr (k >= 200) { if (is_mlp) rows(k - 200); }
                else if constexpr (k >= 100) { if (is_mlp) narrow<true>(wg, wu, nva, nvb, lb, k - 100, 0); }
                else if (is_mlp) { if (NNE == 48) rows(k); }
                else wo_piece<k>(wob, woa, lb); };
            auto qstore = [&]() {
                if (!is_mlp) {
                    lds_wait_ge(lb + L_CNT + 12, 6u);
                    f32x4 v[3];
#pragma unroll
                    for (int t = 0; t < 3; ++t) v[t] = *reinterpret_cast<const f32x4*>(smem + L_OP + 6144 + lane * 48 + t * 16);
                    unsigned am = 14u << 23;
#pragma unroll
                    for (int i = 0; i < 12; ++i) am = umax(am, __float_as_uint(v[i >> 2][i & 3]) & 0x7fffffffu);
                    am >>= 23;
                    const float sc = __uint_as_float((268u - am) << 23);
                    unsigned u[12], w[6];
#pragma unroll
                    for (int i = 0; i < 12; ++i) u[i] = __float_as_uint(__builtin_amdgcn_fmed3f(__builtin_fmaf(v[i >> 2][i & 3], sc, 12615680.f), 12582912.f, 12648447.f));
#pragma unroll
                    for (int i = 0; i < 6; ++i) w[i] = (u[2 * i] << 16) | (u[2 * i + 1] & 0xfff0u) | (i < 4 ? (((am >> (2 * i)) & 3u) << 2) | (t8 & 3u) : t8 & 15u);
                    unsigned lo = t8;
#pragma unroll
                    for (int i = 0; i < 6; ++i) lo |= (u[2 * i + 1] & 15u) << (8 + 4 * i);
                    const u32x4 d = {w[0], w[1], w[2], w[3]}; const u32x3 e = {w[4], w[5], lo};
                    const unsigned o = WS_H1A + (unsigned)((h * 5 + sub) * 1792);
                    if (!AB(16)) {
                        st16_x(ws, o + (unsigned)(lane * 16), d);
                        asm volatile("global_store_dwordx3 %0, %1, %2 offset:1024 " XSX "\n\ts_nop 1" :: "v"(o + (unsigned)(lane * 12)), "v"(e), "s"(ws) : "memory");
                    }
                }
                STAMP(17);
            };
            if (EPF == 3) pace<D7>(due, step);
            kv_stream7<F7, (CA > F7 ? CA : F7)>(kbase, vbase, ko, v0, v1, v2, to, due, step, nar);
            if (CA) clean();
            kv_stream7<(CA > F7 ? CA : F7), A7>(kbase, vbase, ko, v0, v1, v2, to, due, step, nar);
            if (A7 >= Q7) qstore();
            if (is_mlp) due -= 4 * step;
            kv_stream7<A7, A7 + 4>(kbase, vbase, ko, v0, v1, v2, to, due, step, nar);
            if (is_mlp) {
                unsigned long long c0, c1, c2; unsigned d;
                due += 4 * step;
                VMW(A7 + 4 - CA);
                asm volatile(
                             "s_memtime %0\n\ts_load_dword %3, %4, %5\n\ts_waitcnt lgkmcnt(0)\n\t"
                             "s_memtime %1\n\ts_load_dword %3, %4, %6\n\ts_waitcnt lgkmcnt(0)\n\t"
                             "s_memtime %2\n\ts_waitcnt lgkmcnt(0)"
                             : "=&s"(c0), "=&s"(c1), "=&s"(c2), "=&s"(d) : "s"(ws), "s"(AB(8) ? WS_EP : po), "s"(AB(8) ? WS_EP : po ^ 8192u) : "memory");
                RETRY(48, (unsigned)c1 - (unsigned)c0); RETRY(49, (unsigned)c2 - (unsigned)c1);
                const unsigned arv = (t_start & ~1u) | ((((int)((unsigned)c2 + (unsigned)c0 - 2u * (unsigned)c1) > 0 ? 0u : 1u) ^ (unsigned)((mi >> 1) ^ (xg >> 2))) & 1u);
                if (lane == 0) st8_sc1(ws, WS_AR + (unsigned)((mi >> 2) * 128 + (mi & 3) * 8), (u32x2){tag, arv});
            }
            STAMP(16);
            if (A7 < Q7) { kv_stream7<A7 + 4, Q7>(kbase, vbase, ko, v0, v1, v2, to, due, step, nar); qstore(); }
            kv_stream7<(A7 < Q7 ? Q7 : A7 + 4), L7>(kbase, vbase, ko, v0, v1, v2, to, due, step, nar);
            u32x4 t = ones;
            const unsigned oa = WS_AR + (unsigned)((lane < 48 ? lane >> 1 : 0) * 128 + (lane & 1) * 16);
            if (g == LEADER) t = ld16_sc1(ws, oa);
            kv_stream7<L7, L7 + 4>(kbase, vbase, ko, v0, v1, v2, to, due, step, nar);
            if (g == LEADER) {
                asm volatile("s_waitcnt vmcnt(4)" : "+v"(t) :: "memory");
#pragma unroll 1
                for (unsigned spins = 0; spins < (kSpinCap << 4); ++spins) {
                    if (__builtin_amdgcn_ballot_w64(t[0] != tag || t[2] != tag) == 0ull) break;
                    RETRY(3, spins + 1);
                    __builtin_amdgcn_s_sleep(8);
                    t = ld16_sc1(ws, oa);
                    asm volatile("s_waitcnt vmcnt(0)" : "+v"(t) :: "memory");
                }
                t0 = t_start + (unsigned)wave_imax(imax((int)(t[1] - t_start), (int)(t[3] - t_start)));
                const int v0_ = __builtin_popcountll(__builtin_amdgcn_ballot_w64((t[1] & 1u) != 0u) & 0xffffffffffffull), v1_ = __builtin_popcountll(__builtin_amdgcn_ballot_w64((t[3] & 1u) != 0u) & 0xffffffffffffull);
                RETRY(50, v0_ + 100); RETRY(51, v1_ + 100);
                t0 = (t0 & ~7u) | (v0_ > 24 ? 2u : 0u) | (v1_ > 24 ? 4u : 0u);
                if (SCH(S_FL) & 256) { RETRY(5, (t0 ^ (SCH(S_FL) >> 8)) & 2u); RETRY(6, (t0 ^ (SCH(S_FL) >> 8)) & 4u); t0 = (t0 & ~7u) | ((SCH(S_FL) >> 8) & 6u); }
                if (lane < 8) st8_sc1(ws, WS_GO + (unsigned)(lane * 128), (u32x2){tag, t0});
                STAMP(29);
            }
#define LDQ() do { a4 = ld16_xo<0>(ws, o4); a1 = ld12_xo(ws, o1); b4 = ld16_xo<0>(ws, p4_); b1 = ld12_xo(ws, p1); } while (0)
#define WTQ(n) asm volatile("s_waitcnt vmcnt(%4)" : "+v"(a4), "+v"(a1), "+v"(b4), "+v"(b1) : "i"(n) : "memory")
            kv_stream7<L7 + 4, QG>(kbase, vbase, ko, v0, v1, v2, to, due, step, nar);
            {
                u32x2 r = ld8_sc1o<0>(ws, WS_GO + (unsigned)(xg * 128));
                const unsigned ob = WS_H1A + (unsigned)(h * 8960), o4 = ob + (unsigned)(kq * 1792 + (b * 16 + n) * 16), o1 = ob + (unsigned)(kq * 1792 + 1024 + (b * 16 + n) * 12);
                const unsigned p4_ = ob + (unsigned)(7168 + (b * 16 + n) * 16), p1 = ob + (unsigned)(8192 + (b * 16 + n) * 12);
                u32x4 a4, b4; u32x3 a1, b1;
                LDQ();
                kv_stream7<QG, QG + QW>(kbase, vbase, ko, v0, v1, v2, to, due, step, nar);
                WTQ(QW);
#pragma unroll 1
                for (unsigned spins = 0; spins < (kSpinCap << 4); ++spins) {
                    if (tagok((or4((a4 ^ t8) | (b4 ^ t8)) & 3u) | (((a1[0] ^ t8) | (a1[1] ^ t8) | (b1[0] ^ t8) | (b1[1] ^ t8)) & 15u) | (a1[2] ^ t8) | (b1[2] ^ t8)) || AB(16)) break;
                    RETRY(1, spins + 1);
                    __builtin_amdgcn_s_sleep(8);
                    LDQ();
                    WTQ(0);
                }
                float q[12];
                auto dec = [&](u32x4 d, u32x3 e, bool first) {
                    const unsigned x = ((d[0] >> 2) & 3u) | (d[1] & 12u) | ((d[2] & 12u) << 2) | ((d[3] & 12u) << 4);
                    float sc = __uint_as_float((x - 14u) << 23);
                    if (!first && kq) sc = 0.f;
                    const float of = -32768.f * sc;
#pragma unroll
                    for (int i = 0; i < 12; ++i) {
                        const unsigned w = i < 8 ? d[i >> 1] : e[(i >> 1) - 4];
                        const float y = __builtin_fmaf((float)((i & 1) ? (w & 0xfff0u) | ((e[2] >> (8 + 4 * (i >> 1))) & 15u) : w >> 16), sc, of);
                        q[i] = first ? y : q[i] + y;
                    }
                };
                dec(a4, a1, true); dec(b4, b1, false);
#pragma unroll
                for (int t = 0; t < 3; ++t) {
                    const float z = sw16_add(sw32_add(q[4 * t], q[4 * t + 2]), sw32_add(q[4 * t + 1], q[4 * t + 3]));
                    const int c = 3 * n + t;
                    *reinterpret_cast<u16*>(smem + L_Q + ((c >> 3) & 1) * 192 + (c >> 4) * 64 + (c & 1) * 32 + ((c >> 1) & 3) * 4 + (kq >> 1) * 16 + (kq & 1) * 2) = (u16)pk2(z, z);
                }
                STAMP(30);
                asm volatile("" : "+v"(r));
#pragma unroll 1
                for (unsigned spins = 0; spins < (kSpinCap << 4); ++spins) {
                    t0 = __builtin_amdgcn_readfirstlane(r[1]);
                    if (__builtin_amdgcn_readfirstlane(r[0]) == tag) break;
                    RETRY(2, spins + 1);
                    __builtin_amdgcn_s_sleep(8);
                    r = ld8_sc1o<0>(ws, WS_GO + (unsigned)(xg * 128));
                    asm volatile("s_waitcnt vmcnt(0)" : "+v"(r) :: "memory");
                }
            }
            pol = t0;
            asm volatile("ds_write2_b32 %0, %1, %2 offset1:3\n\tds_write2_b32 %0, %3, %2 offset0:1 offset1:5" :: "v"(lb + L_POL), "v"(pol), "v"(0u), "v"(tag) : "memory");
            if (t0 - t_start + 8u > 500u || (SCH(S_FL) & 4096)) t0 = t_start;
            asm volatile("ds_write_b32 %0, %1" :: "v"(lb + L_CNT + 36), "v"(t0 | 1u) : "memory");
            lds_inc(0);
            STAMP(2);
            kv_stream7<QG + QW, 48>(kbase, vbase, ko, v0, v1, v2, to, due, step, nar);
            if (AB(1024)) {
                unsigned long long pc; unsigned len;
                asm volatile("s_getpc_b64 %0\nmega_pf_two:\n\ts_mov_b32 %1, mega_pf_end - mega_pf_two - 4" : "=s"(pc), "=s"(len));
                const unsigned o = (unsigned)((lane * 16 + (rr & 15)) * 128);
                dma4((const void*)pc, o < len ? o : len, lb + L_PF);
            }
            if (!is_mlp) {
                dma16p(wr, (unsigned)(lane * 16), lb + L_WR); dma4(wr, (unsigned)(1024 + lane * 4), lb + L_WR + 1024);
                dma4(x0, (unsigned)((((lane >> 1) & 3) * DM + oi * 4) * 2 + (lane & 1) * 4), lb + L_WR + 1280);
                dma4n(out, (unsigned)(xg * 128 + (lane & 1) * 4096), lb + L_PF);
            } else if (!AB(8192)) {
                dma4n(wd + mi * 16 * DM, (unsigned)((lane & 1) * 20476), lb + L_PF);
                if (!AB(16384)) { const unsigned o = (unsigned)(((lane & 7) * 91 + 2) * 3072 + mi * 32); dma4n(wg, o, lb + L_PF); dma4n(wu, o, lb + L_PF); }
            }
            VMW(16);
            fix_all<0>();
            VMW(0);
            STAMP(19);
            __builtin_amdgcn_s_setprio(0);
        }
    }

#define IDS() int g = GID; asm volatile("" : "+s"(g)); int wave = wave0; asm volatile("" : "+s"(wave)); \
        const int xg = g & 7, rr = g >> 3, h = xg * 4 + (rr >> 3), sub = rr & 7, b = sub >> 1; \
        const bool is_mlp = sub >= 5; const int mi = h * 3 + sub - 5, oi = OWNER(h, sub); (void)mi; (void)oi; (void)b
#define ATT_ADDR(layer) \
        unsigned qa = lb + (unsigned)(L_Q + kq * 32 + (lane & 1) * 16), ix = (unsigned)((p4 & 1) * 0xaaaa + p4 * 0x55550000) ^ 0x44444444u; \
        unsigned pw = lb + L_P + (unsigned)(wave * 1088 + ((kq & 1) * 2 + (p4 >> 1)) * 272 + (p4 & 1) * 128 + (q4 >> 1) * 64 + (kq >> 1) * 32 + (q4 & 1) * 8); \
        unsigned pr = lb + L_P + (unsigned)(wave * 1088 + (lane >> 2) * 16); \
        unsigned ow = lb + L_OP + (unsigned)(wave * OPS + lane * 16); \
        unsigned mlw = lb + L_ML + (unsigned)(wave * 4), mo = lb + L_ML + (unsigned)((lane >> 3) * 4), ma = lb + L_ML; \
        unsigned r0 = lb + L_OP + (unsigned)((lane >> 3) * OPS + (wave < 4 ? (wave >> 1) * 128 : 2048) + (wave & 1) * 256 + (lane & 7) * 16), r1 = r0 + (wave < 4 ? 1536u : 640u); \
        unsigned so = (a_h2(h) ^ (layer ? CX(1) : 0u)) + (unsigned)(layer * LB + (wave < 6 ? ((wave >> 1) * 8 + (lane >> 5) * 4 + (wave & 1) * 2 + (sub & 1)) * 128 + b * 32 + (lane & 7) * 4 \
                                                                                  : 3072 + (((sub & 1) ^ (lane & 1)) * 4 + b) * (layer ? MLS1 : MLS) + (lane & 1) * 8)); \
        (void)mlw; (void)ma; (void)r1

    if (PADN) asm volatile("s_branch 1f\n\t.fill %0, 4, 0xbf800000\n1:" :: "i"(PADN) : "memory");
    {
        IDS(); LANES();
        ATT_ADDR(0);
        asm volatile("" : "+v"(qa), "+v"(pw), "+v"(pr), "+v"(ow), "+v"(mlw), "+v"(mo), "+v"(ma), "+v"(r0), "+v"(r1), "+v"(so), "+v"(ix));
        const u16* vbase = vc + ((size_t)(b * HQ + h) * SK + (SK - WIN) + (sub & 1) * 512) * DH;
        unsigned v1 = (unsigned)(4 * kq * 384 + 256 + (n & 7) * 16), v2 = v1 + (n >= 8 ? to[3] : to[1]);
        v1 += n >= 8 ? to[2] : to[0];
        auto tail1 = [&]() { ldkv<224, 0>(vbase, v1); pace(due, step); ldkv<228, 384>(vbase, v1); pace(due, step); ldkv<232, 768>(vbase, v1); pace(due, step); ldkv<236, 1152>(vbase, v1); };
        unsigned dt = 0u;
        auto tail2 = [&]() { if (EHW == 3) dt = pace_t(due, step); else pace(due, step); ldkv<240, 0>(vbase, v2); };
        auto tail3 = [&]() {
            if (EHW == 2) dt = pace_t(due, step); else pace(due, step);
            ldkv<244, 384>(vbase, v2);
            if (EHW == 1) dt = pace_t(due, step); else pace(due, step);
            ldkv<248, 768>(vbase, v2); dt = pace_t(due, step) - dt; ldkv<252, 1152>(vbase, v2);
            asm volatile("s_mov_b64 exec, 1\n\tds_add_u32 %0, %1 offset:336\n\ts_mov_b64 exec, -1" :: "v"(ma), "v"(1u) : "memory");
            STAMP(3);
        };
        if (wave != 7) {
            const unsigned t = lds_ld(lb + L_CNT + 36);
            if (t) due += (t - t_start) << 4;
            step = (step * SCH(S_X3)) >> 8;
            if (wave == 6 && !is_mlp) step -= SCH(S_X6);
        }
        const bool early = wave == 7 || lds_ld(lb + L_CNT) != 0u;
        if (!early) {
            const unsigned v0 = (unsigned)(4 * kq * 384 + n * 16) + to[3];
            ldkv<208, 0>(vbase, v0); pace(due, step); ldkv<212, 384>(vbase, v0); pace(due, step); ldkv<216, 768>(vbase, v0); pace(due, step); ldkv<220, 1152>(vbase, v0); pace(due, step);
            tail1(); tail2(); tail3();
            lds_wait_ge(lb + L_CNT, 1u);
        }
        pol = lds_ld(lb + L_POL); tag = lds_ld(lb + L_POL + 4);
        so ^= CX(0);
        STAMP(4);
        PAGE(0);
        float sa;
        asm volatile(ASM_QK : "=&v"(sa) : "v"(qa), "v"(ix) : "memory", CLOB_32_59);
        const float s = bfr(bfr(sa) * SCALE) * L2E;
        float mt, p; int ms;
        asm volatile("s_nop 1\n\tv_max_f32_dpp %0, %3, %3 quad_perm:[1,0,3,2] row_mask:0xf bank_mask:0xf\n\ts_nop 1\n\t"
                     "v_max_f32_dpp %0, %0, %0 quad_perm:[2,3,0,1] row_mask:0xf bank_mask:0xf\n\ts_nop 1\n\t"
                     "v_max_f32_dpp %0, %0, %0 row_half_mirror row_mask:0xf bank_mask:0xf\n\ts_nop 1\n\t"
                     "v_max_f32_dpp %0, %0, %0 row_mirror row_mask:0xf bank_mask:0xf\n\ts_nop 1\n\t"
                     "v_max_f32_dpp %0, %0, %0 row_bcast:15 row_mask:0xa bank_mask:0xf\n\ts_nop 1\n\t"
                     "v_max_f32_dpp %0, %0, %0 row_bcast:31 row_mask:0xc bank_mask:0xf\n\ts_nop 0\n\t"
                     "v_readlane_b32 %2, %0, 63\n\ts_nop 1\n\t"
                     "v_subrev_f32 %1, %2, %3\n\tv_exp_f32 %1, %1\n\ts_nop 0"
                     : "=&v"(mt), "=&v"(p), "=&s"(ms) : "v"(s));
        u32x4 bm[4];
        {
            unsigned pb;
            asm volatile("v_cvt_pk_bf16_f32 %5, %4, %4\n\tds_write_b16 %6, %5\n\tds_write_b16 %6, %5 offset:20\n\t"
                     "ds_read_b128 %0, %7\n\tds_read_b128 %1, %7 offset:272\n\tds_read_b128 %2, %7 offset:544\n\tds_read_b128 %3, %7 offset:816\n\t"
                     "v_add_f32_dpp %4, %4, %4 quad_perm:[1,0,3,2] row_mask:0xf bank_mask:0xf\n\ts_nop 1\n\t"
                     "v_add_f32_dpp %4, %4, %4 quad_perm:[2,3,0,1] row_mask:0xf bank_mask:0xf\n\ts_nop 1\n\t"
                     "v_add_f32_dpp %4, %4, %4 row_half_mirror row_mask:0xf bank_mask:0xf\n\ts_nop 1\n\t"
                     "v_add_f32_dpp %4, %4, %4 row_mirror row_mask:0xf bank_mask:0xf\n\ts_nop 1\n\t"
                     "v_add_f32_dpp %4, %4, %4 row_bcast:15 row_mask:0xa bank_mask:0xf\n\ts_nop 1\n\t"
                     "v_add_f32_dpp %4, %4, %4 row_bcast:31 row_mask:0xc bank_mask:0xf\n\t"
                     "s_mov_b32 exec_lo, 0\n\ts_brev_b32 exec_hi, 1\n\tds_write2_b32 %9, %8, %4 offset1:8\n\ts_mov_b64 exec, -1\n\ts_waitcnt lgkmcnt(0)"
                     : "=&v"(bm[0]), "=&v"(bm[1]), "=&v"(bm[2]), "=&v"(bm[3]), "+v"(p), "=&v"(pb)
                     : "v"(pw), "v"(pr), "v"(mt), "v"(mlw) : "memory");
        }
#define PVP(name) asm volatile(name : "+v"(da) : "v"(bm[0]), "v"(bm[1]), "v"(bm[2]), "v"(bm[3]), "v"(ow), "v"(ix) : "memory")
#define PVB(name) do { f32x4 da = z4; PVP(name); } while (0)
        int w7 = wave;
        if (wave == 7) {
            PVB(ASM_PVB0); PVB(ASM_PVB1); PVB(ASM_PVB2);
            asm volatile("s_waitcnt lgkmcnt(0)\n\ts_mov_b64 exec, 1\n\tds_add_u32 %0, %1 offset:372\n\tds_add_u32 %0, %1 offset:348\n\ts_mov_b64 exec, -1" :: "v"(ma), "v"(1u) : "memory");
        }
        asm volatile("" : "+s"(w7));
        if (w7 != 7) {
            LANES();
            {
                const unsigned v0 = (unsigned)(4 * kq * 384 + n * 16) + to[3];
                if (early) { ldkv<208, 0>(vbase, v0); pace(due, step); ldkv<212, 384>(vbase, v0); VMW(6); }
                if (early) { pace(due, step); ldkv<216, 768>(vbase, v0); }
                PVB(ASM_PVB0);
                if (early) { pace(due, step); ldkv<220, 1152>(vbase, v0); pace(due, step); tail1(); }
            }
            VMW(4);
            PVB(ASM_PVB1);
#define MEET(off) do { unsigned t_; int c_; \
            asm volatile("s_waitcnt lgkmcnt(0)\n\ts_mov_b64 exec, 1\n\tds_add_u32 %2, %3 offset:" #off "\n\ts_mov_b64 exec, -1\n\ts_mov_b32 %1, 0x400000\n1:\n\tds_read_b32 %0, %2 offset:" #off "\n\ts_sub_u32 %1, %1, 1\n\ts_cbranch_scc1 2f\n\t" \
                         "s_waitcnt lgkmcnt(0)\n\tv_cmp_gt_u32 vcc, 8, %0\n\ts_cbranch_vccnz 1b\n2:" : "=&v"(t_), "=&s"(c_) : "v"(ma), "v"(1u) : "memory", "scc", "vcc"); } while (0)
            asm volatile("s_waitcnt lgkmcnt(0)\n\ts_mov_b64 exec, 1\n\tds_add_u32 %0, %1 offset:372\n\ts_mov_b64 exec, -1" :: "v"(ma), "v"(1u) : "memory");
            f32x4 da = z4; int la;
            {
                unsigned hfa = (a_hf(h) ^ CX(0)) + (unsigned)(lane * 128 + ((sub - lane) & 7) * 4), hb = lds_ld(lb + L_CNT + 36);
                const unsigned tw_ = SCH(S_TW);
                asm volatile("" : "+v"(da), "+v"(hfa), "+v"(hb));
                if (early) { tail2(); tail3(); } else dt = 0u;
                unsigned ar = 1u, mx, dv = ((dt * tw_) >> 8) + (is_mlp ? SCH(S_G1) : 0u) - hb; int th;
                asm volatile("s_waitcnt vmcnt(%4)\n\ts_memrealtime vcc\n\ts_mov_b64 exec, 1\n\ts_waitcnt lgkmcnt(0)\n\tv_add_u32 %1, vcc_lo, %3\n\t"
                             "ds_max_u32 %2, %1 offset:%5\n\tds_add_rtn_u32 %0, %2, %0 offset:376\n\tds_read_b32 %1, %2 offset:%5\n\ts_mov_b64 exec, -1"
                             : "+v"(ar), "=&v"(mx) : "v"(ma), "v"(dv), "i"(EHW), "i"(L_POL + 20 - L_ML) : "memory", "scc", "vcc");
                PVP(ASM_PV20); PVP(ASM_PV21); if (EHW <= 2) PVP(ASM_PV22);
                asm volatile("s_waitcnt lgkmcnt(0)\n\tv_add_u32 %3, %3, %4\n\tv_readfirstlane_b32 %0, %2\n\tv_readfirstlane_b32 %1, %3\n\ts_cmp_lg_u32 %0, 7\n\ts_cbranch_scc1 2f\n\t"
                             "s_mov_b64 exec, 31\n\tv_and_b32 %3, -2, %1\n\tglobal_store_dword %5, %3, %7 sc1\n\tv_or_b32 %2, 1, %1\n\tds_write_b32 %6, %2 offset:%8\n\ts_mov_b64 exec, -1\n2:"
                             : "=&s"(la), "=&s"(th), "+v"(ar), "+v"(mx) : "v"(hb), "v"(hfa), "v"(ma), "s"(ws), "i"(L_POL + 12 - L_ML) : "memory", "scc");
            }
            lds_wait_ge(lb + L_CNT + 52, 8u);
            const f32x4 m0 = *(const LAS f32x4*)(size_t)ma, m1 = *(const LAS f32x4*)(size_t)(ma + 16);
            const float M = vmax3(vmax3(m0[0], m0[1], m0[2]), vmax3(m0[3], m1[0], m1[1]), vmax3(m1[2], m1[3], m1[3]));
            float fj = __builtin_amdgcn_exp2f(*(const LAS float*)(size_t)mo - M);
            auto combine = [&]() {
                const f32x4 a = *(const LAS f32x4*)(size_t)r0, c = *(const LAS f32x4*)(size_t)r1;
                const float z = red3((a + c) * fj);
                auto sw = __builtin_amdgcn_permlane16_swap(__float_as_uint(z), __float_as_uint(z), false, false);
                return enc(pk2(__uint_as_float(sw[0]), __uint_as_float(sw[1])));
            };
            if (wave < 4) st4_m<0x000000ffu, 0x000000ffu>(ws, so, combine());
            else if (wave == 6) {
                float L = *(const LAS float*)(size_t)(mo + 32) * fj;
                L += dpp<0x128>(L); L = sw16_add(L, L); L = sw32_add(L, L);
                asm volatile("s_mov_b64 exec, 3\n\tglobal_store_dwordx2 %0, %1, %2 sc1\n\ts_mov_b64 exec, -1" :: "v"(so), "v"((u32x2){enc(__float_as_uint(M)), enc(__float_as_uint(L))}), "s"(ws) : "memory");
            } else la += 8;
            const unsigned h2 = a_h2(h) ^ CX(0); const int ot = (8 * sub + 2 * wave + (q4 & 1)) * 4 + kq, rw = (q4 & 1) ? 3 - p4 : p4;
            unsigned j0 = lb + L_WO + (unsigned)((8 * kq + q4) * 256 + ((2 * wave) ^ (q4 | ((kq & 1) << 2))) * 32 + p4 * 8), j1 = j0 ^ 32u;
            unsigned j2 = h2 + (unsigned)((lane >> 3) * 128 + rw * 32 + (q4 & 1) * 16), j3 = h2 + (unsigned)(3072 + ((n >> 3) * 4 + rw) * MLS);
            unsigned j4 = (a_own(ot) + (unsigned)(6144 + h * 64 + rw * 16)) ^ ((unsigned)((ot & 15) << 7) | CX(0));
            if (OPA) asm volatile("" : "+v"(fj), "+{v0}"(j0), "+{v1}"(j1), "+{v2}"(j2), "+{v3}"(j3), "+{v4}"(j4)); else asm volatile("" : "+v"(fj));
            STAMP(6);
            asm volatile("s_waitcnt vmcnt(2)\n\ts_cmp_eq_u32 %0, 7\n\ts_cbranch_scc1 1f\n\ts_waitcnt vmcnt(1)\n\ts_sub_u32 %0, %0, 8\n\ts_cmp_gt_u32 %0, 6\n\ts_cbranch_scc1 1f\n\ts_waitcnt vmcnt(0)\n1:" : "+s"(la) :: "memory", "scc");
            if (EHW > 2) PVP(ASM_PV22);
            STAMP(20); DMARK(46);
            asm volatile(ASM_PV23 "s_nop 7\n\tds_write_b128 %5, %0 offset:2048" : "+v"(da) : "v"(bm[0]), "v"(bm[1]), "v"(bm[2]), "v"(bm[3]), "v"(ow), "v"(ix) : "memory");
            if ((wave & 6) == 4) { MEET(348); st4_m<0x000000ffu, 0x000000ffu>(ws, so, combine()); STAMP(5); asm volatile("s_mov_b64 exec, 1\n\tds_add_u32 %0, %1 offset:344\n\ts_mov_b64 exec, -1" :: "v"(ma), "v"(1u) : "memory"); }
            else asm volatile("s_mov_b64 exec, 1\n\tds_add_u32 %0, %1 offset:348\n\ts_mov_b64 exec, -1" :: "v"(ma), "v"(1u) : "memory");
            if (is_mlp && wave < 4) {
                LANES();
                NARROW_ADDR();
                unsigned va = nva + (unsigned)(wave * 160 * 3072), vb = nvb + (unsigned)(wave * 160 * 3072), lw = lb + (unsigned)(L_GU + wave * 5120);
                asm volatile("" : "+v"(va), "+v"(vb), "+s"(lw));
                M0_KEEP();
                lds_wait_ge(lb + L_CNT + 24, 2u);
                lds_wait_ge(lb + L_CNT + 40, 1u);
                STAMP(18);
                if (wave >= (int)(SCH(S_X7) >> 3))
#pragma unroll
                for (int c = 0; c < 5; ++c) {
                    unsigned t;
                    asm volatile("s_add_u32 m0, %4, %5\n\tv_add_u32 %0, %6, %1\n\tglobal_load_lds_dwordx4 %0, %2" WPOL "\n\ts_add_u32 m0, %4, %7\n\ts_nop 0\n\tglobal_load_lds_dwordx4 %0, %3" WPOL
                                 : "=&v"(t) : "v"(c == 4 ? vb : va), "s"(wg), "s"(wu), "s"(lw), "i"(c * 1024), "i"((c & 3) * 8 * 3072), "i"(c * 1024 + 20480) : "memory", "scc");
                }
                M0_BACK();
                asm volatile("s_mov_b64 exec, 1\n\tds_add_u32 %0, %1 offset:364\n\ts_mov_b64 exec, -1" :: "v"(ma), "v"(1u) : "memory");
                STAMP(37);
                if (P1ONLY) { VMW(0); STAMP(27); }
            }
            if (!P1ONLY && !is_mlp && wave < 4) {
                int st, rf, ct; unsigned t0;
                asm volatile(ASM_OP : [st] "=&s"(st), [rf] "=&s"(rf), [ct] "=&s"(ct), [t0] "=&{v7}"(t0) : [a0] "{v0}"(j0), [a1] "{v1}"(j1), [gs] "{v2}"(j2), [gm] "{v3}"(j3), [so] "{v4}"(j4),
                             [ws] "s"(ws), [fl] "s"(lb + L_CNT + 48), [tag] "s"(tag), [dbg] "s"(lb + L_DBG + wave * 512 + 256 + 7 * 4) : "memory", "scc", "vcc", CLOB_8_15, CLOB_16_63 OPCL);
                STAMP(7); DMARK(30);
            }
        }
    }
    {
        IDS();
        if (wave == 7) {
            PAGE(1);
            STAMP(15);
            LANES();
            {
                __builtin_amdgcn_s_setprio(3);
                unsigned th = 0u;
                auto hint = [&]() { lds_wait_ge(lb + L_POL + 12, 1u); th = lds_ld(lb + L_POL + 12); };
                unsigned hfa = (a_hf(h) ^ CX(0)) + (unsigned)(lane * 128 + ((sub - lane) & 7) * 4), hb = lds_ld(lb + L_CNT + 36), ma = lb + L_ML;
                asm volatile("" : "+v"(hfa), "+v"(hb), "+v"(ma));
#define ARRIVE(off, W) do { \
                    unsigned ar = 1u, mx, dv = (off) - hb; int la, th_; \
                    asm volatile("s_memrealtime vcc\n\ts_mov_b64 exec, 1\n\ts_waitcnt lgkmcnt(0)\n\tv_add_u32 %1, vcc_lo, %3\n\t" \
                                 "ds_max_u32 %2, %1 offset:%4\n\tds_add_rtn_u32 %0, %2, %0 offset:376\n\tds_read_b32 %1, %2 offset:%4\n\ts_mov_b64 exec, -1" \
                                 : "+v"(ar), "=&v"(mx) : "v"(ma), "v"(dv), "i"(L_POL + 20 - L_ML) : "memory", "scc", "vcc"); \
                    asm volatile("s_waitcnt lgkmcnt(0)\n\tv_add_u32 %3, %3, %4\n\tv_readfirstlane_b32 %0, %2\n\tv_readfirstlane_b32 %1, %3\n\ts_cmp_eq_u32 %0, 7\n\ts_cbranch_scc1 1f\n\ts_waitcnt vmcnt(%9)\n\ts_branch 2f\n" \
                                 "1:\n\ts_mov_b64 exec, 31\n\tv_and_b32 %3, -2, %1\n\tglobal_store_dword %5, %3, %7 sc1\n\tv_or_b32 %2, 1, %1\n\tds_write_b32 %6, %2 offset:%8\n\ts_mov_b64 exec, -1\n\ts_waitcnt vmcnt(%10)\n2:" \
                                 : "=&s"(la), "=&s"(th_), "+v"(ar), "+v"(mx) : "v"(hb), "v"(hfa), "v"(ma), "s"(ws), "i"(L_POL + 12 - L_ML), "i"(W), "i"((W) | 1) : "memory", "scc"); \
                    if (STAMPS && __builtin_amdgcn_readfirstlane(la) == 7) { STAMPV(21, th_); RETRY(20, 1); } \
                } while (0)
                if (!is_mlp) {
                    WO_ADDR();
                    unsigned oa = woa, ob = woa ^ 128u;
                    asm volatile("" : "+v"(oa), "+v"(ob), "+s"(wob));
                    if (SCH(S_FL) & 128) {
                        const u32x4 ones = splat<~0u>();
#pragma unroll
                        for (int i = 0; i < 6; ++i) st16_l(ws, (a_own(oi) ^ CX(0)) + (unsigned)(i * 1024 + lane * 16), ones);
                        if (oi < 96 && lane < 32) st16_l(ws, WS_XB + (unsigned)((oi / 12) * 8192 + (oi % 12) * 512 + lane * 16), ones);
                    }
                    M0_KEEP();
                    if (WPRE) {
                        unsigned dw = (t0 + SCH(S_WD)) << 4;
                        wo_paced<NN7, NN7 + WPRE>(wob, oa, ob, lb, dw, SCH(S_TR));
                    }
                    if (NN7 + WPRE < 48) { if (SCH(S_WT) > 7u) lds_wait_ge(lb + L_CNT + 56, SCH(S_WT) - 7u); else lds_wait_ge(lb + L_CNT + 16, SCH(S_WT)); }
                    STAMP(35);
                    if (WK < 0) { wo_rest<NN7 + WPRE, NN7 + WPRE - WK>(wob, oa, ob, lb); ARRIVE(SCH(S_G3) - 100u, 63); }
                    wo_rest<NN7 + WPRE - (WK < 0 ? WK : 0), 48>(wob, oa, ob, lb);
                    M0_BACK();
                    STAMP(36);
                    if (WK < 0) VMW(0); else { VMW(WK < 0 ? 0 : WK); ARRIVE(SCH(S_G3) - 100u, 0); }
                    STAMP(33); DMARK(44);
                    asm volatile("ds_write_b32 %0, %1" :: "v"(lb + L_CNT + 40), "v"(1u) : "memory");
                    if (lane < 48) st16_sc1(ws, WS_CS + (unsigned)(((h * 6 + (lane >> 3)) * 5 + sub) * 128 + (lane & 7) * 16), tag4(*reinterpret_cast<const u32x4*>(smem + L_CS + 768 + lane * 16), tag & 0xffu));
                } else {
                    ARRIVE(0u, 63);
                    {
                        NARROW_ADDR();
                        unsigned t = now32(); const unsigned ts = SCH(S_TS); const int se = (int)(SCH(S_X7) >> 3) * 10;
#pragma unroll 1
                        for (int s = NM7; s < se; ++s) { narrow<true>(wg, wu, nva, nvb, lb, s, nrot); t += ts; wait_until(t); }
                        STAMP(47);
                    }
                    const int ml = mi - 12 * xg, nl = (int)SCH(S_ND), l3 = lane / 3;
                    unsigned pa = (unsigned)((l3 < nl ? l3 : 0) * 3072 + xg * 384 + (lane - 3 * l3) * 128);
                    const u16* pb = ((ml & 1) ? wu : wg) + (640 - (6 - (ml >> 1)) * LPN * nl) * 1536;
                    auto late = [&]() {
                        unsigned t = now32();
                        if (nl) {
#pragma unroll
                            for (int k = 0; k < LPN; ++k) { pull(pb + k * nl * 1536, pa, lb + L_PF); if (SCH(S_PA) && k < LPN - 1) { t += SCH(S_PA); wait_until(t); } }
                        }
                    };
                    if (SCH(S_PA)) late();
                    const u16* wdb = wd + mi * 16 * DM;
                    auto down = [&]() {
                        unsigned c16 = (unsigned)lane * 16u, t, keep; int k;
                        asm volatile("s_mov_b32 %2, m0\n\ts_mov_b32 m0, %5\n\ts_mov_b32 %3, 20\n"
                                     "1:\n\tv_mul_u32_u24 %1, 0xc7d, %0\n\tv_lshrrev_b32 %1, 22, %1\n\tv_lshlrev_b32 %1, 5, %1\n\tv_sub_u32 %1, %0, %1\n\tv_min_u32 %1, 0x4ff0, %1\n\t"
                                     "global_load_lds_dwordx4 %1, %4" WPOL "\n\ts_add_u32 m0, m0, 0x400\n\tv_add_u32 %0, 0x400, %0\n\ts_sleep %6\n\ts_sub_u32 %3, %3, 1\n\ts_cbranch_scc0 1b\n\ts_mov_b32 m0, %2"
                                     : "+v"(c16), "=&v"(t), "=&s"(keep), "=&s"(k) : "s"(wdb), "s"(lb + L_WD), "i"(DSL) : "memory", "scc");
                    };
                    asm volatile("" : "+v"(pa), "+s"(wdb));
                    if (DSL) { down(); STAMP(46); }
                    if (SCH(S_N1) > 15u) { lds_wait_ge(lb + L_CNT + 24, 2u); if (SCH(S_NT)) wait_until(now32() + SCH(S_NT)); }
                    else if (SCH(S_N1) > 14u) { hint(); wait_until(th + SCH(S_NT)); }
                    else if (SCH(S_N1) > 7u) lds_wait_ge(lb + L_CNT + 56, SCH(S_N1) - 6u); else lds_wait_ge(lb + L_CNT + 16, SCH(S_N1));
                    if (!SCH(S_PA)) late();
                    {
                        if (!DSL) down();
                        VMW(DSL ? 0 : DNW);
                        asm volatile("ds_write_b32 %0, %1" :: "v"(lb + L_CNT + 40), "v"(now32() | 1u) : "memory");
                        VMW(0);
                    }
                    STAMP(34);
                    asm volatile("ds_write_b32 %0, %1" :: "v"(lb + L_CNT + 48), "v"(1u) : "memory");
                }
                __builtin_amdgcn_s_setprio(0);
            }
        }
        if (wave == 6) {
            PAGE(2);
            LANES();
            if (P1ONLY) {} else {
            if (sub < 5) {
                int d = 0; int left;
                const unsigned o = (a_hf(h) ^ CX(0)) + (unsigned)(sub * 128);
                const int tk = sub * 4 + ((lane >> 3) & 3);
                unsigned ta = (a_ts(lane & 7) ^ CX(0)) + (unsigned)((lane < 32 ? tk + (tk / 5) * 3 : (5 + tk + (tk / 3) * 5) & 31) * 128 + h * 4), fa = lb + L_CNT + 48, t;
                asm volatile("" : "+v"(ta), "+v"(fa));
                {
                    u32x8 e; unsigned x;
                    asm volatile("s_movk_i32 %1, 4000\n1:\n\ts_load_dwordx8 s[4:11], %4, %5 glc\n\tds_read_b32 %3, %6 offset:%7\n\ts_memrealtime vcc\n\ts_sub_u32 %1, %1, 1\n\ts_waitcnt lgkmcnt(0)\n\ts_cbranch_scc1 2f\n\t"
                                 "v_readfirstlane_b32 s4, %3\n\ts_or_b32 vcc_hi, s5, s6\n\ts_or_b32 %2, s7, s8\n\ts_or_b32 vcc_hi, vcc_hi, s9\n\ts_or_b32 %2, %2, s10\n\ts_or_b32 vcc_hi, vcc_hi, s11\n\ts_orn2_b32 %2, %2, s4\n\t"
                                 "s_or_b32 vcc_hi, vcc_hi, %2\n\ts_bitcmp1_b32 vcc_hi, 0\n\ts_cbranch_scc1 1b\n2:\n\ts_mov_b32 %2, vcc_lo"
                                 : "=&{s[4:11]}"(e), "=&s"(left), "=&s"(t), "=&v"(x) : "s"(ws), "s"(o), "v"(fa), "i"(L_POL + 12 - L_CNT - 48) : "memory", "scc", "vcc");
                    asm volatile("s_sub_i32 s4, s4, %2\n\ts_sub_i32 s5, s5, %2\n\ts_sub_i32 s6, s6, %2\n\ts_sub_i32 s7, s7, %2\n\ts_max_i32 s4, s4, s5\n\ts_sub_i32 s8, s8, %2\n\ts_max_i32 s6, s6, s7\n\ts_sub_i32 s9, s9, %2\n\t"
                                 "s_max_i32 s4, s4, s6\n\ts_sub_i32 s10, s10, %2\n\ts_max_i32 s8, s8, s9\n\ts_sub_i32 s11, s11, %2\n\ts_max_i32 s4, s4, s8\n\ts_max_i32 s10, s10, s11\n\ts_max_i32 %0, s4, s10"
                                 : "=s"(d), "+{s[4:11]}"(e) : "s"(t) : "scc");
                }
                if (__builtin_expect(left < 0, 0)) {
#pragma unroll 1
                    for (unsigned spins = 0; spins < kSpinCap; ++spins) {
                        if (allok(poll4(ws, o + (unsigned)((lane & 7) * 4)))) break;
                    }
                    t = now32(); d = 0;
                    RETRY(21, 1);
                }
                d += (int)SCH(S_H20);
                if (TSX && d < (int)SCH(S_G2) - TSX) d = (int)SCH(S_G2) - TSX;
                DMARK(45);
                const unsigned tg = (t + (unsigned)(d > (int)SCH(S_G2) && d < 1000 ? d : (int)SCH(S_G2))) | 1u;
                asm volatile("ds_write_b32 %0, %1" :: "v"(fa), "v"(tg) : "memory");
                STAMP(25);
                st4_m<~0u, 0u>(ws, ta, enc(t + (unsigned)d));
                if (sub < 3) { wait_until(tg + 4u); st4_m<0u, ~0u>(ws, ta, enc(t + (unsigned)d)); }
                STAMPV(28, tg);
            } else lds_wait_ge(lb + L_CNT + 28, 8u);
            unsigned t; int d;
            const unsigned fl0 = lds_ld(lb + L_CNT + 36) + SCH(S_WD);
            {
                const unsigned o = (a_ts(xg) ^ CX(0)) + (unsigned)(rr * 128);
                unsigned r, lo; int left;
                asm volatile("s_movk_i32 %2, 4000\n1:\n\ts_load_dwordx16 s[68:83], %4, %5 glc\n\ts_load_dwordx16 s[84:99], %4, %6 glc\n\ts_memrealtime vcc\n\ts_sub_u32 %2, %2, 1\n\ts_waitcnt lgkmcnt(0)\n\ts_cbranch_scc1 2f\n\t"
                             "s_max_u32 s68, s68, s84\n\ts_max_u32 s69, s69, s85\n\ts_max_u32 s70, s70, s86\n\ts_max_u32 s71, s71, s87\n\ts_max_u32 s72, s72, s88\n\ts_max_u32 s73, s73, s89\n\ts_max_u32 s74, s74, s90\n\ts_max_u32 s75, s75, s91\n\ts_max_u32 s76, s76, s92\n\ts_max_u32 s77, s77, s93\n\ts_max_u32 s78, s78, s94\n\ts_max_u32 s79, s79, s95\n\ts_max_u32 s80, s80, s96\n\ts_max_u32 s81, s81, s97\n\ts_max_u32 s82, s82, s98\n\ts_max_u32 s83, s83, s99\n\ts_max_u32 s68, s68, s76\n\ts_max_u32 s69, s69, s77\n\ts_max_u32 s70, s70, s78\n\ts_max_u32 s71, s71, s79\n\ts_max_u32 s72, s72, s80\n\ts_max_u32 s73, s73, s81\n\ts_max_u32 s74, s74, s82\n\ts_max_u32 s75, s75, s83\n\ts_max_u32 s84, s68, s72\n\ts_max_u32 s85, s69, s73\n\ts_max_u32 s86, s70, s74\n\ts_max_u32 s87, s71, s75\n\ts_max_u32 s84, s84, s86\n\ts_max_u32 s85, s85, s87\n\ts_max_u32 %0, s84, s85\n\t"
                             "s_cmp_eq_u32 %0, -1\n\ts_cbranch_scc1 1b\n2:\n\ts_mov_b32 %1, vcc_lo\n\t"
                             "s_min_u32 s68, s68, s69\n\ts_min_u32 s70, s70, s71\n\ts_min_u32 s72, s72, s73\n\ts_min_u32 s74, s74, s75\n\ts_min_u32 s68, s68, s70\n\ts_min_u32 s72, s72, s74\n\ts_min_u32 %[lo], s68, s72"
                             : "=&s"(r), "=&s"(t), "=&s"(left), [lo] "=&s"(lo) : "s"(ws), "s"(o), "s"(o + 64u) : "memory", "scc", "vcc", "s68", "s69", "s70", "s71", "s72", "s73", "s74", "s75", "s76", "s77", "s78", "s79", "s80", "s81", "s82", "s83", "s84", "s85", "s86", "s87", "s88", "s89", "s90", "s91", "s92", "s93", "s94", "s95", "s96", "s97", "s98", "s99");
                if (__builtin_expect(left < 0, 0)) {
#pragma unroll 1
                    for (unsigned spins = 0; spins < kSpinCap; ++spins) {
                        if (allok(poll4(ws, o + (unsigned)((lane & 31) * 4)))) break;
                    }
                    lo = r = t = now32();
                    RETRY(22, 1);
                }
                const unsigned sp = r - lo, ex = sp < SCH(S_Y0) ? ((SCH(S_Y0) - sp) * 5u) >> 3 : 0u;
                d = (int)(r + (ex < SCH(S_Y1) ? ex : SCH(S_Y1)) - t);
                if (SCH(S_WD)) d = imax(d, (int)(fl0 - t));
            }
            asm volatile("ds_write_b32 %0, %1" :: "v"(lb + L_CNT + 8), "v"((t + (unsigned)(d < 250 ? d : 0)) | 1u) : "memory");
            STAMP(23); if (!is_mlp) DMARK(33);
            STAMPV(31, t + (unsigned)(d < 250 ? d : 0));
            if (is_mlp && SCH(S_LT)) {
                lds_wait_ge(lb + L_CNT + 40, 1u);
                const unsigned tr = lds_ld(lb + L_CNT + 40) + SCH(S_LT);
                if ((int)(tr - t - (unsigned)(d < 250 ? d : 0) - SCH(S_XA0)) > 0) { st4_m<0xffu, 0u>(ws, WS_XA + (a_rep(lane & 7) ^ CX(0)) + (unsigned)(5120 + xg * 128 + (20 + mi - 12 * xg) * 4), enc(tr)); RETRY(24, 1); }
            }
            }
        }
    }

    unsigned ref = 0;
#pragma unroll 1
    for (int layer_ = 0; layer_ < (P1ONLY ? 0 : 2); ++layer_) {
        IDS();
#define LAYER() int layer = layer_; asm volatile("" : "+s"(layer)); const unsigned cx = CX(layer), xa = WS_XA + (unsigned)(layer * LB), xm = xa + (a_rep(xg) ^ cx); (void)xa; (void)xm
        auto gather_img = [&](unsigned o, u32x4 (&a)[5], int sid, unsigned off) {
#pragma unroll 1
            for (unsigned spins = 0; spins < kSpinCap; ++spins) {
                a[0] = ld16_sc1o<-2048>(ws, o); a[1] = ld16_sc1o<-1024>(ws, o); a[2] = ld16_sc1o<0>(ws, o); a[3] = ld16_sc1o<1024>(ws, o); a[4] = ld16_sc1o<2048>(ws, o);
                asm volatile("s_waitcnt vmcnt(0)" : "+v"(a[0]), "+v"(a[1]), "+v"(a[2]), "+v"(a[3]), "+v"(a[4]) :: "memory");
                if (allok(umax(mx4(a[0], mx4(a[1])), mx4(a[2], mx4(a[3], mx4(a[4])))))) break;
                RETRY(sid, spins + 1);
                __builtin_amdgcn_s_sleep(10);
            }
        };
        auto gather_tab = [&](unsigned o, u32x4 (&a)[5], u32x4& r, int sid, unsigned t) {
            wait_until(t);
            a[0] = ld16_sc1o<-2048>(ws, o); a[1] = ld16_sc1o<-1024>(ws, o); a[2] = ld16_sc1o<0>(ws, o); a[3] = ld16_sc1o<1024>(ws, o); a[4] = ld16_sc1o<2048>(ws, o);
            wait_until(t + SCH(S_H4));
            r = ld16_sc1o<3072>(ws, o);
            asm volatile("s_waitcnt vmcnt(1)" : "+v"(a[0]), "+v"(a[1]), "+v"(a[2]), "+v"(a[3]), "+v"(a[4]) :: "memory");
            if (__builtin_expect(!allok(umax(mx4(a[0], mx4(a[1])), mx4(a[2], mx4(a[3], mx4(a[4]))))), 0)) {
                RETRY(sid, 1);
                asm volatile("s_waitcnt vmcnt(0)" : "+v"(r) :: "memory");
                __builtin_amdgcn_s_sleep(4);
                gather_img(o, a, sid, 0);
                r = ld16_sc1o<3072>(ws, o);
            }
        };
        auto late = [&](u32x4 v, unsigned base) {
            int d = -1000;
#pragma unroll
            for (int i = 0; i < 4; ++i) d = imax(d, v[i] == ~0u ? -1000 : (int)(v[i] - base));
            d = wave_imax(d);
            return d < 1000 ? d : 1000;
        };
        auto img_stats = [&](const u32x4 (&a)[5], float& mb, float& rden) {
            float s0 = 0.f, s1 = 0.f, q0 = 0.f, q1 = 0.f;
#pragma unroll
            for (int i = 0; i < 10; ++i) {
                const unsigned u = a[i >> 1][(i & 1) * 2], v = a[i >> 1][(i & 1) * 2 + 1];
                s0 = dot2(u, 0x3f803f80u, s0); q0 = dot2(u, u, q0); s1 = dot2(v, 0x3f803f80u, s1); q1 = dot2(v, v, q1);
            }
            const float mean = sum_x4(s0 + s1) * (1.f / DM);
            ln_consts(mean, sum_x4(q0 + q1) * (1.f / DM) - mean * mean, mb, rden);
        };
        if (!is_mlp) {
            if (wave == 5) {
                PAGE(4);
                LANES(); LAYER();
                const unsigned po = (a_rep((lane >> 2) & 7) ^ cx) + (unsigned)((oi >> 1) * 64 + p4 * 16 + (oi & 1) * 8 + (lane >> 5) * 4);
                if (layer == 0) lds_wait_ge(lb + L_CNT + 40, 1u);
                u32x4 w[5];
#pragma unroll
                for (int j = 0; j < 5; ++j) w[j] = *reinterpret_cast<const u32x4*>(smem + L_WR + j * 256 + (lane >> 2) * 16);
                float xr = layer ? *reinterpret_cast<const float*>(smem + L_XR + lane * 4) : __uint_as_float((unsigned)*reinterpret_cast<const u16*>(smem + L_WR + 1280 + p4 * 8 + kq * 2) << 16);
                float cw = 0.f;
#pragma unroll
                for (int j = 0; j < 20; ++j) cw = dot2(w[j >> 2][j & 3], 0x3f803f80u, cw);
                cw = sum_x4(cw);
                const unsigned tw = (a_rep(lane & 7) ^ cx) + (unsigned)(5120 + xg * 128 + ((h & 3) * 5 + sub) * 4);
                {
                    unsigned o = (a_own(oi) ^ cx) + (unsigned)(layer * LB + 6144 + lane * 16), so = xa + po;
                    unsigned t8 = tag & 0xffu;
                    asm volatile("" : "+v"(xr), "+v"(o), "+v"(so), "+v"(cw), "+s"(t8));
                    if (layer == 0) {
                        if (!(SCH(S_FL) & 224)) lds_wait_ge(lb + L_CNT + 44, 1u);
#pragma unroll 1
                        for (unsigned spins = 0; spins < (kSpinCap << 6) && ref == 0u; ++spins) ref = lds_ld(lb + L_CNT + 8);
                    }
                    if (PRI & 1) __builtin_amdgcn_s_setprio(2);
                    if (!layer) DMARK(35);
                    if (layer == 0 && (SCH(S_FL) & 96)) lds_wait_ge(lb + L_CNT + 44, 1u);
                    wait_until(ref + (layer ? SCH(S_H3) : SCH(S_H30)));
                    if (!layer) DMARK(34);
                    CY(c_a);
                    u32x4 a0, a1;
                    GATHER(8 + 32 * layer, { a0 = ld16_sc1o<0>(ws, o); a1 = ld16_sc1o<1024>(ws, o);
                                 asm volatile("s_waitcnt vmcnt(0)" : "+v"(a0), "+v"(a1) :: "memory"); a0 ^= t8; a1 ^= t8; }, tagok(or4(a0) | or4(a1)));
                    CY(c_b);
                    xr += bfr(red4(asf(a0) + asf(a1)));
                    auto sw = __builtin_amdgcn_permlane16_swap(__float_as_uint(xr), __float_as_uint(xr), false, false);
                    st4_sc1(ws, so, enc(pk2(__uint_as_float(sw[0]), __uint_as_float(sw[1]))));
                    xr = bfr(xr);
                    const unsigned t = now32() + SCH(S_GAP);
                    if ((int)(t - ref - (layer ? SCH(S_XA1) : SCH(S_XA0))) > 0) st4_m<0xffu, 0u>(ws, xa + tw, enc(t + SCH(S_EX)));
                    CY(c_c); CYW(55, c_a, c_b); CYW(56, c_b, c_c);
                    STAMP(8 + 32 * layer); if (layer) DMARK(25); else DMARK(31);
                }
                float rs, xn;
                {
                    unsigned o = xm + (unsigned)(2048 + lane * 16);
                    asm volatile("" : "+v"(o), "+v"(cw));
                    u32x4 a[5], lt; gather_tab(o, a, lt, 9 + 32 * layer, ref + (layer ? SCH(S_XA1) : SCH(S_XA0)) + 8u);
                    float mb, rden; img_stats(a, mb, rden);
                    float r0 = 0.f, r1 = 0.f;
#pragma unroll
                    for (int j = 0; j < 10; ++j) { r0 = dot2(a[j >> 1][(j & 1) * 2], w[j >> 1][(j & 1) * 2], r0); r1 = dot2(a[j >> 1][(j & 1) * 2 + 1], w[j >> 1][(j & 1) * 2 + 1], r1); }
                    const float r = bfr((sum_x4(r0 + r1) - mb * cw) * rden);
                    rs = bfr(frcp(1.f + fexp(-r)));
                    xn = bfr(bfr(xr - mb) * rden);
                    asm volatile("s_waitcnt vmcnt(0)" : "+v"(lt), "+v"(rs), "+v"(xn) :: "memory");
                    ref += (unsigned)imax(late(lt, ref + (layer ? SCH(S_XA1) : SCH(S_XA0))), 0);
                    if (!layer) DMARK(38);
                    if (layer == 0) {
                        if (g == 0) st4_m<1u, 0u>(ws, WS_EP, tag);
                    }
                    STAMP(9 + 32 * layer); if (!layer) DMARK(37);
                }
                {
                    unsigned o = (a_own(oi) ^ cx) + (unsigned)(layer * LB + lane * 16 + 2048);
                    unsigned so = layer ? (unsigned)((p4 * DM + oi * 4 + (lane >> 5) * 2) * 2) : WS_XB + po;
                    unsigned fc = lb + L_CNT + 44, fv; int fs, fn;
                    asm volatile("" : "+v"(rs), "+v"(xn), "+v"(o), "+v"(so), "+v"(fc));
                    wait_until(ref + (layer ? SCH(S_H51) : SCH(S_H50)));
                    CY(c_a);
                    u32x4 a[6];
                    GATHER(13 + 32 * layer, { a[0] = ld16_sc1o<-2048>(ws, o); a[1] = ld16_sc1o<-1024>(ws, o); a[2] = ld16_sc1o<0>(ws, o);
                                 a[3] = ld16_sc1o<1024>(ws, o); a[4] = ld16_sc1o<2048>(ws, o); a[5] = ld16_sc1o<3072>(ws, o);
                                 asm volatile("s_mov_b32 %8, 0x1000000\n1:\n\tds_read_b32 %6, %9\n\ts_sub_u32 %8, %8, 1\n\ts_waitcnt lgkmcnt(0)\n\ts_cbranch_scc1 2f\n\tv_readfirstlane_b32 %7, %6\n\ts_cmp_lt_u32 %7, 2\n\ts_cbranch_scc1 1b\n2:\n\ts_waitcnt vmcnt(0)"
                                              : "+v"(a[0]), "+v"(a[1]), "+v"(a[2]), "+v"(a[3]), "+v"(a[4]), "+v"(a[5]), "=&v"(fv), "=&s"(fs), "=&s"(fn) : "v"(fc) : "memory", "scc"); },
                           allok(umax(umax(mx4(a[0], mx4(a[1])), mx4(a[2], mx4(a[3]))), mx4(a[4], mx4(a[5])))));
                    CY(c_b);
                    const float y = bfr(red4(((asf(a[0]) + asf(a[1])) + (asf(a[2]) + asf(a[3]))) + (asf(a[4]) + asf(a[5]))));
                    xr += bfr(xn + bfr(y * rs));
                    auto sw = __builtin_amdgcn_permlane16_swap(__float_as_uint(xr), __float_as_uint(xr), false, false);
                    const unsigned w = pk2(__uint_as_float(sw[0]), __uint_as_float(sw[1]));
                    if (layer == 0) {
                        st4_sc1(ws, so, enc(w));
                        const unsigned t = now32() + SCH(S_GAP), t2 = ref + SCH(S_XB), tx = ((int)(t - t2) > 0 ? t + SCH(S_EXB) : t2) | 1u;
                        asm volatile("ds_write_b32 %0, %1" :: "v"(lb + L_CNT + 60), "v"(tx) : "memory");
                        if ((sub == 0 && (h & 3) == 0) || (int)(t - t2) > 0) st4_m<0xffu, 0u>(ws, WS_XB + tw, enc(tx));
                    }
                    else {
                        asm volatile("s_mov_b32 exec_lo, 15\n\ts_mov_b32 exec_hi, 15\n\tglobal_store_dword %0, %1, %2 " OSX "\n\ts_mov_b64 exec, -1" :: "v"(so), "v"(w), "s"(out) : "memory");
                    }
                    *reinterpret_cast<float*>(smem + L_XR + lane * 4) = bfr(xr);
                    CY(c_c); CYW(57, c_a, c_b); CYW(58, c_b, c_c);
                    STAMP(13 + 32 * layer); if (!layer) DMARK(36);
                }
            } else if (wave == 4 && layer_ == 0) {
                LANES();
                const u32x4 ones = splat<~0u>();
                if (SCH(S_FL) & 32) lds_wait_ge(lb + L_CNT + 8, 1u);
                if (SCH(S_FL) & 64) { lds_wait_ge(lb + L_CNT + 48, 1u); wait_until(lds_ld(lb + L_CNT + 48) + 30u); }
                if (!(SCH(S_FL) & 128)) {
#pragma unroll
                for (int i = 0; i < 6; ++i) st16_l(ws, (a_own(oi) ^ CX(0)) + (unsigned)(i * 1024 + lane * 16), ones);
                if (oi < 96 && lane < 32) st16_l(ws, WS_XB + (unsigned)((oi / 12) * 8192 + (oi % 12) * 512 + lane * 16), ones);
                asm volatile("s_waitcnt vmcnt(0)\n\tds_write_b32 %0, %1" :: "v"(lb + L_CNT + 44), "v"(1u) : "memory");
                }
                lds_wait_ge(lb + L_CNT + 8, 1u); wait_until(lds_ld(lb + L_CNT + 8) + SCH(S_CL) - 100u);
                if (SCH(S_FL) & 2) {
#pragma unroll
                    for (int i = 0; i < 2; ++i) st16_l(ws, a_own(oi) + (unsigned)(LB + 6144 + i * 1024 + lane * 16), (u32x4){tag + 1u, tag + 1u, tag + 1u, tag + 1u});
                }
                if (oi < 128) { st16_l(ws, WS_XA + LB + (unsigned)(oi * 1536 + lane * 16), ones); if (lane < 32) st16_l(ws, WS_XA + LB + (unsigned)(oi * 1536 + 1024 + lane * 16), ones); }
#pragma unroll
                for (int i = 0; i < 3; ++i) st16_l(ws, a_h1(h, lane >> 4) + (unsigned)(((2 * i + ((lane >> 3) & 1)) * 5 + sub) * 128 + (lane & 7) * 16), ones);
                asm volatile("s_waitcnt vmcnt(0)\n\tds_write_b32 %0, %1" :: "v"(lb + L_CNT + 44), "v"(2u) : "memory");
                STAMP(26);
            }
        } else {
            PAGE(3);
            LAYER();
            if (wave < 4) {
                LANES();
                if (layer == 0) { lds_wait_ge(lb + L_CNT + 8, 1u); ref = lds_ld(lb + L_CNT + 8); }
                const unsigned gs = xm + (unsigned)(wave * 1280);
                int st;
                asm volatile(ASM_GU : [st] "=&s"(st) : [a0] "v"(lb + L_GU + (unsigned)((wave * 160 + 8 * kq + q4 + 4 * (kq & 1)) * 32 + p4 * 8)), [gs] "v"(gs + lane * 16),
                             [g2] "v"(gs + (unsigned)(1024 + kq * 64 + p4 * 16 + q4 * 4)), [pw] "v"(lb + L_OP + (unsigned)(n * 784 + kq * 192 + wave * 8)), [gw] "v"(lb + L_OP + (unsigned)(4096 + n * 272 + kq * 64 + wave * 4)),
                             [ws] "s"(ws), [tg] "s"(ref + (layer ? SCH(S_XA1) : SCH(S_XA0))), [dbg] "s"(lb + L_DBG + wave * 512 + 256 + (10 + 32 * layer) * 4) : "memory", "scc", "vcc", "v6", "v7", CLOB_8_15, CLOB_16_63);
                STAMP(11 + 32 * layer);
                if (wave == 1 && layer == 0) {
                    u32x4 lt = ld16_sc1(ws, xm + (unsigned)(5120 + lane * 16));
                    asm volatile("s_waitcnt vmcnt(0)" : "+v"(lt) :: "memory");
                    asm volatile("ds_write_b32 %0, %1" :: "v"(lb + L_CNT + 60), "v"((ref + SCH(S_XB) + (unsigned)imax(late(lt, ref + SCH(S_XA0)), 0)) | 1u) : "memory");
                }
            } else {
                LANES();
                if (layer == 0) {
                    lds_wait_ge(lb + L_CNT + 48, 1u);
                    if (wave == 7) DMARK(39);
                }
                const unsigned wdo = lb + L_WD + (unsigned)(4 * kq * 1312 + (160 * (wave & 3) + 4 * p4) * 2);
                unsigned w0 = wdo + q4 * 1312, w1 = wdo + ((q4 + 1) & 3) * 1312, w2 = wdo + ((q4 + 2) & 3) * 1312, w3 = wdo + ((q4 + 3) & 3) * 1312;
                asm volatile(ASM_HS : [w0] "+{v8}"(w0), [w1] "+{v9}"(w1), [w2] "+{v10}"(w2), [w3] "+{v11}"(w3) :
                             [rp] "v"(lb + L_OP + (unsigned)(p4 * 784 + (4 * kq + q4) * 48)), [cp] "v"(lb + L_OP + (unsigned)(4 * 784 + (4 * kq + q4) * 48)),
                             [sq] "v"(lb + L_OP + (unsigned)(4096 + p4 * 288)), [sx] "v"(lb + L_OP + (unsigned)(4096 + p4 * 272 + 64)),
                             [so] "v"(((unsigned)(layer * LB + ((10 * (wave & 1) + q4) * 4 + kq) * GP + (wave & 2) * 4096 + mi * 64 + p4 * 16)) ^ ((unsigned)((q4 * 4 + kq) << 7) | cx)), [ws] "s"(ws)
                             : "memory", "v12", "v13", CLOB_16_63);
                STAMP(12 + 32 * layer); if (!layer) DMARK(32);
            }
        }
        if (layer_ != 0) break;
        unsigned tz = 0u;
        if (wave == 7) lds_wait_ge(lb + L_CNT + 8, 1u);
        if (is_mlp || wave == 7) tz = (lds_ld(lb + L_CNT + 8) + SCH(S_H50) + SCH(S_Y2)) | 1u;
        if (wave == (is_mlp ? 2 : 7)) {
            LANES();
            unsigned o = WS_CS + (unsigned)((h * 6 + (lane < 48 ? lane >> 3 : 0)) * 640 + (lane & 7) * 16); const unsigned t8 = tag & 0xffu;
            asm volatile("" : "+v"(o));
            { const unsigned tc = tz - SCH(S_H50) - SCH(S_Y2) + SCH(S_TR); while ((int)(now32() - tc) < 0) __builtin_amdgcn_s_sleep(3); }
            u32x4 a[5];
#pragma unroll 1
            for (unsigned spins = 0; spins < kSpinCap; ++spins) {
                a[0] = ld16_sc1o<0>(ws, o); a[1] = ld16_sc1o<128>(ws, o); a[2] = ld16_sc1o<256>(ws, o); a[3] = ld16_sc1o<384>(ws, o); a[4] = ld16_sc1o<512>(ws, o);
                asm volatile("s_waitcnt vmcnt(0)" : "+v"(a[0]), "+v"(a[1]), "+v"(a[2]), "+v"(a[3]), "+v"(a[4]) :: "memory");
#pragma unroll
                for (int i = 0; i < 5; ++i) a[i] ^= t8;
                if (tagok(or4(a[0] | a[1] | a[2] | a[3] | a[4]))) break;
                RETRY(4, spins + 1);
                __builtin_amdgcn_s_sleep(4);
            }
            if (lane < 48) *reinterpret_cast<f32x4*>(smem + L_CS + (lane >> 3) * 128 + (lane & 1) * 64 + ((lane >> 1) & 3) * 16) = ((asf(a[0]) + asf(a[1])) + (asf(a[2]) + asf(a[3]))) + asf(a[4]);
            STAMP(32);
        }
        {
            const unsigned xb = WS_XB + (a_rep(xg) ^ CX(0));
#define QPROJ() do { const int tp = wave == 6 ? 5 : wave; int st_, rf_; \
                    const unsigned a0_ = lb + L_WQ + (unsigned)((8 * kq + q4) * 384 + ((2 * tp) ^ ((q4 >> 1) | ((kq & 1) << 1))) * 32 + p4 * 8); \
                    asm volatile(ASM_QP : [st] "=&s"(st_), [rf] "=&s"(rf_) : [a0] "{v60}"(a0_), [a1] "{v61}"(a0_ ^ 32u), [gs] "{v62}"(xb + (unsigned)(sub * 1024 + lane * 16)), \
                                 [so] "{v63}"((a_h1(h, p4) ^ CX(1)) + (unsigned)((tp * 5 + sub) * 128 + kq * 32 + (q4 & 1) * 16)), \
                                 [ws] "s"(ws), [fl] "s"(lb + L_CNT + 60), [tz] "s"(SCH(S_H50) + SCH(S_Y2)), [dbg] "s"(lb + L_DBG + wave * 512 + 256 + 14 * 4) : "memory", "scc", "vcc", CLOB_16_59); } while (0)
#define ATT_IN() LANES(); \
            ATT_ADDR(1); \
            const unsigned go = (wave < 6 ? (a_h1(h, b) ^ CX(1)) + (unsigned)(wave * 640) : (a_own(oi) ^ CX(1)) + (unsigned)(LB + (wave & 1) * 3072)) + (unsigned)(lane * 16); \
            const unsigned qo = lb + L_Q + (unsigned)((wave & 1) * 192 + (wave >> 1) * 64 + (lane & 1) * 32 + (kq >> 1) * 16 + ((lane >> 1) & 3) * 4 + (kq & 1) * 2); \
            const unsigned cs = lb + L_CS + (unsigned)(wave * 128 + (lane & 1) * 64 + ((lane >> 1) & 3) * 16 + kq * 4); \
            int st
#define ATT_OPS : [st] "=&s"(st) : [qa] "{v8}"(qa), [pw] "{v9}"(pw), [pr] "{v10}"(pr), [ow] "{v11}"(ow), [mo] "{v12}"(mo), [r0] "{v5}"(r0), [so] "{v6}"(so), [go] "{v13}"(go), [qo] "{v14}"(qo), [cs] "{v15}"(cs), [ix] "{v7}"(ix), \
                [mlw] "s"(lb + L_ML + wave * 4), [ma] "s"(lb + L_ML), [ws] "s"(ws), [wv] "s"(wave), \
                [cl] "s"((wave + 2) & (sub - 5) & 8), [tg] "s"(SCH(S_H1)), [tz] "s"(is_mlp && tz ? tz + SCH(S_Y3) : 0u), [dbg] "s"(lb + L_DBG + wave * 512 + 256 + 35 * 4) : "memory", "scc", "vcc", CLOB_16_63 TRCL
            if (wave >= 4) {
                ATT_IN();
                unsigned go_ = go, qo_ = qo, cs_ = cs;
                asm volatile("" : "+{v8}"(qa), "+{v9}"(pw), "+{v10}"(pr), "+{v11}"(ow), "+{v12}"(mo), "+{v5}"(r0), "+{v6}"(so), "+{v13}"(go_), "+{v14}"(qo_), "+{v15}"(cs_), "+{v7}"(ix));
                PAGE(5);
                if (!(wave & 1) && !is_mlp) { QPROJ(); DMARK(21); }
                if (wave == 7) {
                unsigned o = xb + (unsigned)(2048 + lane * 16);
                asm volatile("" : "+v"(o));
                unsigned gx = 0u;
                if (PRI & 2) __builtin_amdgcn_s_setprio(3);
                if (tz) while ((int)(now32() - tz) < 0) __builtin_amdgcn_s_sleep(3);
#pragma unroll 1
                for (unsigned spins = 0; spins < (kSpinCap << 6) && gx == 0u; ++spins) gx = lds_ld(lb + L_CNT + 60);
                u32x4 a[5], r; gather_tab(o, a, r, 14, gx + 4u);
                unsigned k0 = lane & 7 ? 1u : 2u;
                asm volatile("s_waitcnt vmcnt(0)" : "+v"(r), "+v"(k0) :: "memory");
                auto dif = [&](unsigned w, unsigned k) { const unsigned x = w ^ gx, y = w + k; return x < y ? x : y; };
                if (__builtin_expect(__builtin_amdgcn_ballot_w64((dif(r[0], k0) | dif(r[1], 1u) | dif(r[2], 1u) | dif(r[3], 1u)) != 0u) != 0ull, 0)) {
#pragma unroll 1
                    for (unsigned spins = 0; spins < kSpinCap && !allok(lane & 7 ? 0u : r[0]); ++spins) {
                        RETRY(15, spins + 1);
                        __builtin_amdgcn_s_sleep(4);
                        r = ld16_sc1o<3072>(ws, o);
                        asm volatile("s_waitcnt vmcnt(0)" : "+v"(r) :: "memory");
                    }
                    gx = (gx + (unsigned)late(r, gx)) | 1u;
                }
                asm volatile("ds_write_b32 %0, %1" :: "v"(lb + L_CNT + 20), "v"(gx) : "memory");
                STAMP(38); DMARK(28);
                float mb, rden; img_stats(a, mb, rden);
                STAMP(14); DMARK(24);
                if (lane == b) *reinterpret_cast<u32x2*>(smem + L_CNT + 32) = (u32x2){__float_as_uint(mb), __float_as_uint(rden)};
                BAR();
                }
                { const unsigned go = go_, qo = qo_, cs = cs_; asm volatile(ASM_ATT_B ATT_OPS); }
            }
            else if (is_mlp) { PAGE(6); ATT_IN(); asm volatile(ASM_ATT_A ATT_OPS); }
            else {
                ATT_IN();
                const unsigned a0 = lb + L_WO + (unsigned)((8 * kq + q4) * 256 + ((2 * wave) ^ (q4 | ((kq & 1) << 2))) * 32 + p4 * 8), h2 = (a_h2(h) ^ CX(1)) + LB; const int ot = (8 * sub + 2 * wave + (q4 & 1)) * 4 + kq, rw = (q4 & 1) ? 3 - p4 : p4;
                unsigned i0 = qa, i1 = pw, i2 = pr, i3 = ow, i4 = mo, i5 = go, i6 = qo, i7 = cs; int rf;
                unsigned j0 = a0, j1 = a0 ^ 32u, j2 = h2 + (unsigned)((lane >> 3) * 128 + rw * 32 + (q4 & 1) * 16), j3 = h2 + (unsigned)(3072 + ((n >> 3) * 4 + rw) * MLS1);
                unsigned j4 = (a_own(ot) + (unsigned)(LB + 6144 + h * 64 + rw * 16)) ^ ((unsigned)((ot & 15) << 7) | CX(1));
                asm volatile("" : "+{v8}"(i0), "+{v9}"(i1), "+{v10}"(i2), "+{v11}"(i3), "+{v12}"(i4), "+{v13}"(i5), "+{v14}"(i6), "+{v15}"(i7), "+{v0}"(j0), "+{v1}"(j1), "+{v2}"(j2), "+{v3}"(j3), "+{v4}"(j4), "+{v5}"(r0), "+{v6}"(so), "+{v7}"(ix));
                PAGE(7);
                QPROJ();
                asm volatile(ASM_ATT_P : [st] "=&s"(st), [rf] "=&s"(rf), [t0] "+{v7}"(ix), [qa] "+{v8}"(i0), [pw] "+{v9}"(i1), [pr] "+{v10}"(i2), [ow] "+{v11}"(i3), [mo] "+{v12}"(i4), [go] "+{v13}"(i5), [qo] "+{v14}"(i6), [cs] "+{v15}"(i7)
                             : [a0] "{v0}"(j0), [a1] "{v1}"(j1), [gs] "{v2}"(j2), [gm] "{v3}"(j3), [s2] "{v4}"(j4), [r0] "{v5}"(r0), [so] "{v6}"(so),
                               [mlw] "s"(lb + L_ML + wave * 4), [ma] "s"(lb + L_ML), [ws] "s"(ws),
                               [tg] "s"(SCH(S_H1)), [t2] "s"(SCH(S_H2)), [wv] "s"(wave), [tag] "s"(tag), [dbg] "s"(lb + L_DBG + wave * 512 + 256 + 35 * 4) : "memory", "scc", "vcc", CLOB_16_63 TRCL);
                STAMP(39); DMARK(23);
                break;
            }
            if (wave != 7) STAMP(38);
            DMARK(22); if (is_mlp) DMARK(26);
            ref = lds_ld(lb + L_CNT + 20);
        }
    }
    asm volatile("s_endpgm\n\t.fill 128, 4, 0xbf800000\nmega_pf_end:" ::: "memory");
}

extern "C" void kernel_launch(void* const* d_in, const int* in_sizes, int n_in, void* d_out, int out_size, void* d_ws,
                              size_t ws_size, hipStream_t stream) {
    const u16* x0 = (const u16*)d_in[0];
    const u16* kc = (const u16*)d_in[1];
    const u16* vc = (const u16*)d_in[2];
    const u16* wq = (const u16*)d_in[3];
    const u16* wo = (const u16*)d_in[4];
    const u16* wr = (const u16*)d_in[6];
    const u16* wg = (const u16*)d_in[7];
    const u16* wu = (const u16*)d_in[8];
    const u16* wd = (const u16*)d_in[9];
    (void)hipFuncSetAttribute((const void*)mega_kernel, hipFuncAttributeMaxDynamicSharedMemorySize, L_END);
    mega_kernel<<<256, 512, L_END, stream>>>(kc, vc, (unsigned char*)(((size_t)d_ws + LB - 1) & ~(size_t)(LB - 1)), x0, wq, wg, wu, wo, wr, wd, (u16*)d_out
                                             );
}
```
